```python
import torch

def _softmax(x, axis=-1):
    return torch.softmax(x, dim=axis)

def _gelu(x):
    return torch.nn.functional.gelu(x, approximate='tanh')

def _silu(x):
    return torch.nn.functional.silu(x)

def _sigmoid(x):
    return torch.sigmoid(x)

def _layernorm(x, axis=-1, eps=1e-05):
    m = x.mean(dim=axis, keepdim=True)
    v = x.var(dim=axis, keepdim=True, unbiased=False)
    return (x - m) / torch.sqrt(v + eps)

def _scatter_add(tgt, idx, src, axis=0):
    return tgt.clone().index_add_(axis, idx, src)

def _segment_sum(src, seg, n):
    out = torch.zeros((n,) + src.shape[1:], dtype=src.dtype)
    return out.index_add_(0, seg, src)

def _pad(x, pw):
    flat = tuple((v for pair in reversed(pw) for v in pair))
    return torch.nn.functional.pad(x, flat)
kernel_name = 'adaLN_cond__gqa_block_cached__swiglu_ffn__prompt'

def setup_inputs():

    _w = lambda a: (lambda t: t.to(torch.bfloat16) if t.is_floating_point() else t)(torch.as_tensor(a))

    def _A_setup():
        B, Sq, D = (1, 2048, 1024)
        Hq, Hkv, Sk, Dh = (32, 4, 2048, 128)
        assert Hq % Hkv == 0, (Hq, Hkv)
        return {'x': _w((lambda *s: torch.randn(*s, dtype=torch.bfloat16) * 0.05)(B, Sq, D)), 'k_cache': _w((lambda *s: torch.randn(*s, dtype=torch.bfloat16) * 3.2)(B, Hkv, Sk, Dh)), 'v_cache': _w((lambda *s: torch.randn(*s, dtype=torch.bfloat16) * 0.4)(B, Hkv, Sk, Dh)), 'wq': _w((lambda *s: torch.randn(*s, dtype=torch.bfloat16) * 3.2)(D, Hq * Dh) * D ** (-0.5)), 'wo': _w((lambda *s: torch.randn(*s, dtype=torch.bfloat16) * 0.4)(Hq * Dh, D) * (Hq * Dh) ** (-0.5))}
    _A_in = _A_setup()

    def _B_setup():
        B, S, D, F = (1, 2048, 1024, 3584)
        return {'x': _w((lambda *s: torch.randn(*s, dtype=torch.bfloat16) * 0.05)(B, S, D)), 'w_gate': _w((lambda *s: torch.randn(*s, dtype=torch.bfloat16) * 0.05)(D, F)), 'w_up': _w((lambda *s: torch.randn(*s, dtype=torch.bfloat16) * 0.05)(D, F)), 'w_down': _w((lambda *s: torch.randn(*s, dtype=torch.bfloat16) * 0.05)(F, D))}
    _B_in = _B_setup()
    return {'A_x': _A_in['x'], 'A_k_cache': _A_in['k_cache'], 'A_v_cache': _A_in['v_cache'], 'A_wq': _A_in['wq'], 'A_wo': _A_in['wo'], 'B_x': _B_in['x'], 'B_w_gate': _B_in['w_gate'], 'B_w_up': _B_in['w_up'], 'B_w_down': _B_in['w_down'], 'c': _w((lambda *s: torch.randn(*s, dtype=torch.bfloat16) * 0.1)(1, 1024) * 8.0), 'w_mod': _w((lambda *s: torch.randn(*s, dtype=torch.bfloat16) * 0.1)(1024, 6 * 1024) * 1024 ** (-0.5) * 8.0)}

def reference(A_x, A_k_cache, A_v_cache, A_wq, A_wo, B_x, B_w_gate, B_w_up, B_w_down, c, w_mod):

    def _A_ref(x, k_cache, v_cache, wq, wo):
        B, Sq, D = x.shape
        Hq, Dh = (32, 128)
        Hkv = k_cache.shape[1]
        rep = Hq // Hkv
        q = (lambda x, *d: x.permute(*d))((x @ wq).reshape(B, Sq, Hq, Dh), 0, 2, 1, 3)
        k = (lambda x, n, axis: torch.repeat_interleave(x, n, dim=axis))(k_cache, rep, axis=1)
        v = (lambda x, n, axis: torch.repeat_interleave(x, n, dim=axis))(v_cache, rep, axis=1)
        scores = torch.einsum('bhqd,bhkd->bhqk', q, k) * Dh ** (-0.5)
        p = _softmax(scores, axis=-1)
        o = torch.einsum('bhqk,bhkd->bhqd', p, v)
        return (lambda x, *d: x.permute(*d))(o, 0, 2, 1, 3).reshape(B, Sq, Hq * Dh) @ wo

    def _B_ref(x, w_gate, w_up, w_down):
        g = _silu(x @ w_gate)
        u = x @ w_up
        return g * u @ w_down
    x = A_x
    mod = (_silu(c) @ w_mod).reshape(c.shape[0], 6, 1, -1)
    sa, ca, ga, sb, cb, gb = (mod[:, 0].clone(), mod[:, 1].clone(), mod[:, 2].clone(), mod[:, 3].clone(), mod[:, 4].clone(), mod[:, 5].clone())
    h = _layernorm(x, axis=-1) * (1 + ca) + sa
    x = x + ga * _A_ref(h, A_k_cache, A_v_cache, A_wq, A_wo)
    h = _layernorm(x, axis=-1) * (1 + cb) + sb
    x = x + gb * _B_ref(h, B_w_gate, B_w_up, B_w_down)
    return x
```

```cpp
#include <hip/hip_runtime.h>
#include <hip/hip_bf16.h>
#include <cstdint>

typedef unsigned short u16;
typedef short bf16x8 __attribute__((ext_vector_type(8)));
typedef short s16x4 __attribute__((ext_vector_type(4)));
typedef float f32x4 __attribute__((ext_vector_type(4)));
typedef float f32x16 __attribute__((ext_vector_type(16)));
typedef unsigned u32x4 __attribute__((ext_vector_type(4)));
typedef unsigned u32x2 __attribute__((ext_vector_type(2)));
typedef float f32x2 __attribute__((ext_vector_type(2)));
typedef __bf16 bf16x2_t __attribute__((ext_vector_type(2)));
#define DI __device__ __forceinline__

constexpr int S = 2048, DM = 1024, HQ = 32, HKV = 4, SK = 2048, DH = 128, FF = 3584;

DI float bf2f(u16 v) { return __uint_as_float(((unsigned)v) << 16); }
DI unsigned cvtpk(float lo, float hi) { f32x2 v = {lo, hi}; bf16x2_t b = __builtin_convertvector(v, bf16x2_t); return __builtin_bit_cast(unsigned, b); }
DI u16 f2bf(float f) { return (u16)(cvtpk(f, 0.f) & 0xffffu); }
DI float rbf(float f) { return bf2f(f2bf(f)); }

__global__ __launch_bounds__(256) void k_mod(const u16* __restrict__ c, const u16* __restrict__ wmod, float* __restrict__ mod) {
  __shared__ float sc[1024];
  __shared__ float red[32][64 + 1];
  const int tid = threadIdx.x;
  for (int i = tid; i < 1024; i += 256) { float x = bf2f(c[i]); float s = x / (1.f + __expf(-x)); sc[i] = rbf(s); }
  __syncthreads();
  const int cg = tid & 7, kg = tid >> 3;
  const int col0 = blockIdx.x * 64 + cg * 8;
  float acc[8] = {0, 0, 0, 0, 0, 0, 0, 0};
#pragma unroll 8
  for (int k = kg; k < 1024; k += 32) {
    u32x4 w = *(const u32x4*)(wmod + (size_t)k * 6144 + col0);
    float s = sc[k];
#pragma unroll
    for (int j = 0; j < 4; ++j) { acc[2 * j] += s * __uint_as_float(w[j] << 16); acc[2 * j + 1] += s * __uint_as_float(w[j] & 0xffff0000u); }
  }
#pragma unroll
  for (int j = 0; j < 8; ++j) red[kg][cg * 8 + j] = acc[j];
  __syncthreads();
  if (tid < 64) { float s = 0; for (int g = 0; g < 32; ++g) s += red[g][tid]; mod[blockIdx.x * 64 + tid] = rbf(s); }
}

__global__ __launch_bounds__(256) void k_ln(const u16* __restrict__ x, const float* __restrict__ shift, const float* __restrict__ scale, u16* __restrict__ h) {
  const int wid = threadIdx.x >> 6, lane = threadIdx.x & 63;
  const int row = blockIdx.x * 4 + wid;
  const u16* xr = x + (size_t)row * DM;
  float v[16];
#pragma unroll
  for (int i = 0; i < 2; ++i) {
    u32x4 w = *(const u32x4*)(xr + i * 512 + lane * 8);
#pragma unroll
    for (int j = 0; j < 4; ++j) { v[i * 8 + 2 * j] = __uint_as_float(w[j] << 16); v[i * 8 + 2 * j + 1] = __uint_as_float(w[j] & 0xffff0000u); }
  }
  float s = 0;
#pragma unroll
  for (int i = 0; i < 16; ++i) s += v[i];
#pragma unroll
  for (int o = 32; o >= 1; o >>= 1) s += __shfl_xor(s, o);
  const float mean = s * (1.f / DM);
  float q = 0;
#pragma unroll
  for (int i = 0; i < 16; ++i) { float d = v[i] - mean; q += d * d; }
#pragma unroll
  for (int o = 32; o >= 1; o >>= 1) q += __shfl_xor(q, o);
  const float var = rbf(q * (1.f / DM));
  const float mb = rbf(mean);
  const float sd = rbf(sqrtf(rbf(var + 1e-5f)));
#pragma unroll
  for (int i = 0; i < 2; ++i) {
    const int c0 = i * 512 + lane * 8;
    f32x4 sh0 = *(const f32x4*)(shift + c0), sh1 = *(const f32x4*)(shift + c0 + 4);
    f32x4 sc0 = *(const f32x4*)(scale + c0), sc1 = *(const f32x4*)(scale + c0 + 4);
    float o[8];
#pragma unroll
    for (int j = 0; j < 8; ++j) {
      float sh = j < 4 ? sh0[j & 3] : sh1[j & 3], sc = j < 4 ? sc0[j & 3] : sc1[j & 3];
      float n = rbf(rbf(v[i * 8 + j] - mb) / sd);
      o[j] = rbf(n * rbf(1.f + sc)) + sh;
    }
    u32x4 w = {cvtpk(o[0], o[1]), cvtpk(o[2], o[3]), cvtpk(o[4], o[5]), cvtpk(o[6], o[7])};
    *(u32x4*)(h + (size_t)row * DM + c0) = w;
  }
}

template <int EPI>
__global__ __launch_bounds__(256) void k_gemm(const u16* __restrict__ A, const u16* __restrict__ B, const u16* __restrict__ B2, u16* __restrict__ C,
                                              const u16* __restrict__ res, const float* __restrict__ gate, int M, int N, int K) {
  __shared__ __attribute__((aligned(16))) u16 As[128][40];
  __shared__ __attribute__((aligned(16))) u16 Bs[128][40];
  const int tid = threadIdx.x, wid = tid >> 6, lane = tid & 63, wr = wid >> 1, wc = wid & 1, fr = lane & 15, fq = lane >> 4;
  const int BNE = (EPI == 2) ? 64 : 128;
  const int m0 = blockIdx.y * 128, n0 = blockIdx.x * BNE;
  f32x4 acc[4][4] = {};
  for (int k0 = 0; k0 < K; k0 += 32) {
    for (int i = 0; i < 2; ++i) { int ch = tid + i * 256, r = ch >> 2, c = (ch & 3) * 8; *(u32x4*)&As[r][c] = *(const u32x4*)(A + (size_t)(m0 + r) * K + k0 + c); }
    for (int i = 0; i < 2; ++i) {
      int ch = tid + i * 256, kk = ch >> 4, c = (ch & 15) * 8;
      const u16* src;
      if (EPI == 2) {
        int blk = c >> 4, within = c & 15; src = ((blk & 1) ? B2 : B) + (size_t)(k0 + kk) * N + n0 + (blk >> 1) * 16 + within;
      } else src = B + (size_t)(k0 + kk) * N + n0 + c;
      u32x4 w = *(const u32x4*)src;
      for (int j = 0; j < 4; ++j) { Bs[c + 2 * j][kk] = (u16)(w[j] & 0xffff); Bs[c + 2 * j + 1][kk] = (u16)(w[j] >> 16); }
    }
    __syncthreads();
    bf16x8 a[4], b[4];
    for (int m = 0; m < 4; ++m) a[m] = *(const bf16x8*)&As[wr * 64 + m * 16 + fr][fq * 8];
    for (int n = 0; n < 4; ++n) b[n] = *(const bf16x8*)&Bs[wc * 64 + n * 16 + fr][fq * 8];
    for (int m = 0; m < 4; ++m) for (int n = 0; n < 4; ++n) acc[m][n] = __builtin_amdgcn_mfma_f32_16x16x32_bf16(b[n], a[m], acc[m][n], 0, 0, 0);
    __syncthreads();
  }
  for (int m = 0; m < 4; ++m) {
    const int row = m0 + wr * 64 + m * 16 + fr;
    if (EPI == 2) {
      for (int n = 0; n < 4; n += 2) {
        const int col = n0 + wc * 32 + (n >> 1) * 16 + fq * 4;
        float o[4];
        for (int j = 0; j < 4; ++j) { float g = rbf(acc[m][n][j]), u = rbf(acc[m][n + 1][j]); float sg = rbf(g / (1.f + __expf(-g))); o[j] = sg * u; }
        u32x2 w = {cvtpk(o[0], o[1]), cvtpk(o[2], o[3])};
        *(u32x2*)(C + (size_t)row * N + col) = w;
      }
    } else {
      for (int n = 0; n < 4; ++n) {
        const int col = n0 + wc * 64 + n * 16 + fq * 4;
        float o[4];
        for (int j = 0; j < 4; ++j) o[j] = acc[m][n][j];
        if (EPI == 1) {
          u32x2 rw = *(const u32x2*)(res + (size_t)row * N + col);
          f32x4 g = *(const f32x4*)(gate + col);
          float r[4] = {__uint_as_float(rw[0] << 16), __uint_as_float(rw[0] & 0xffff0000u), __uint_as_float(rw[1] << 16), __uint_as_float(rw[1] & 0xffff0000u)};
          for (int j = 0; j < 4; ++j) o[j] = r[j] + rbf(g[j] * rbf(o[j]));
        }
        u32x2 w = {cvtpk(o[0], o[1]), cvtpk(o[2], o[3])};
        *(u32x2*)(C + (size_t)row * N + col) = w;
      }
    }
  }
}

constexpr int D = 128, NW = 8, QBLK = 32, KVBLK = 64;
constexpr float SCALE = 0.088388347648318440f;
constexpr float THR = 8.f;
constexpr int LDQ = HQ * D, LDK = D, LDO = HQ * D;
constexpr size_t SHM_V = KVBLK * D * 2, SHM_K = KVBLK * D * 2, SHM_ATTN = 2 * SHM_V + 2 * SHM_K + NW * 64 * 4;
#define KSWZ(row, colB) ((row) * 256 + ((colB) ^ (((row) & 7) << 4)))
#define SBAR() __builtin_amdgcn_sched_barrier(0)
DI int crow(int r, int hi) { return (r & 3) + 8 * (r >> 2) + 4 * hi; }
DI unsigned cvtpk_a(float lo, float hi) { unsigned r; asm volatile("v_cvt_pk_bf16_f32 %0, %1, %2" : "=v"(r) : "v"(lo), "v"(hi)); return r; }
DI void partialSM(f32x16& p0, f32x16& p1, float& m_reg, float& mn, float& alpha) {
  constexpr float C = SCALE * 1.4426950408889634f;
  float pmax = p0[0]; for (int r = 1; r < 16; ++r) pmax = fmaxf(pmax, p0[r]); for (int r = 0; r < 16; ++r) pmax = fmaxf(pmax, p1[r]);
  { auto rr = __builtin_amdgcn_permlane32_swap(__float_as_uint(pmax), __float_as_uint(pmax), false, false);
    pmax = fmaxf(__uint_as_float(rr[0]), __uint_as_float(rr[1])); }
  if (__builtin_expect(__all(pmax - m_reg <= THR / SCALE), 1)) { mn = m_reg; alpha = 1.f; }
  else { mn = fmaxf(m_reg, pmax); alpha = __builtin_amdgcn_exp2f((m_reg - mn) * C); m_reg = mn; }
  float mnC = -mn * C;
  for (int r = 0; r < 16; ++r) p0[r] = fmaf(p0[r], C, mnC); for (int r = 0; r < 16; ++r) p1[r] = fmaf(p1[r], C, mnC);
  for (int r = 0; r < 16; ++r) p0[r] = __builtin_amdgcn_exp2f(p0[r]);
}
DI void finishSM(f32x16& p0, f32x16& p1, float alpha, float& l_reg, bf16x8& pa0, bf16x8& pa1, bf16x8& pa2, bf16x8& pa3) {
  for (int r = 0; r < 16; ++r) p1[r] = __builtin_amdgcn_exp2f(p1[r]);
  float ps = 0; for (int r = 0; r < 16; ++r) ps += p0[r]; for (int r = 0; r < 16; ++r) ps += p1[r];
  { auto rr = __builtin_amdgcn_permlane32_swap(__float_as_uint(ps), __float_as_uint(ps), false, false);
    ps = __uint_as_float(rr[0]) + __uint_as_float(rr[1]); }
  l_reg = l_reg * alpha + ps;
#define PK4(P, BASE, OUT) do { unsigned a0 = cvtpk_a(P[BASE + 0], P[BASE + 1]), a1 = cvtpk_a(P[BASE + 2], P[BASE + 3]);   \
    unsigned b0 = cvtpk_a(P[BASE + 4], P[BASE + 5]), b1 = cvtpk_a(P[BASE + 6], P[BASE + 7]);                              \
    auto r0 = __builtin_amdgcn_permlane32_swap(a0, b0, false, false); auto r1 = __builtin_amdgcn_permlane32_swap(a1, b1, false, false); \
    u32x4 w = {r0[0], r1[0], r0[1], r1[1]}; OUT = *reinterpret_cast<bf16x8*>(&w); } while (0)
  PK4(p0, 0, pa0); PK4(p0, 8, pa1); PK4(p1, 0, pa2); PK4(p1, 8, pa3);
#undef PK4
}
DI void qkt(f32x16& p0, f32x16& p1, const u16* Ks, const bf16x8* qr, int r32, int hi) {
  p0 = f32x16{}; p1 = f32x16{};
  for (int d0 = 0; d0 < 8; ++d0) { int cb = (d0 * 16 + hi * 8) * 2;
    bf16x8 b0 = *reinterpret_cast<const bf16x8*>((const char*)Ks + KSWZ(r32, cb));
    bf16x8 b1 = *reinterpret_cast<const bf16x8*>((const char*)Ks + KSWZ(32 + r32, cb));
    p0 = __builtin_amdgcn_mfma_f32_32x32x16_bf16(b0, qr[d0], p0, 0, 0, 0);
    p1 = __builtin_amdgcn_mfma_f32_32x32x16_bf16(b1, qr[d0], p1, 0, 0, 0); }
}
DI int v_st(int k, int c) { const int kk = (k & ~0xC) | ((k & 4) << 1) | ((k & 8) >> 1); return ((kk >> 3) * 4 + (c >> 5)) * 512 + ((kk & 7) * 32 + (c & 31)) * 2; }
DI int v_rd_base(int lane) { return ((lane & 3) << 3) | (((lane >> 2) & 3) << 6) | (((lane >> 4) & 1) << 5) | (((lane >> 5) & 1) << 8); }
constexpr int v_rd_off(int d0, int ks, int half) { return d0 * 512 + ks * 4096 + half * 2048; }
template <int OFF> DI s16x4 tr_read(int vb) { s16x4 r; asm volatile("ds_read_b64_tr_b16 %0, %1 offset:%2" : "=&v"(r) : "v"(vb), "i"(OFF) : "memory"); return r; }
template <int D0> DI void pv_one(f32x16& od, int vb, bf16x8 pa0, bf16x8 pa1, bf16x8 pa2, bf16x8 pa3) {
  const s16x4 l0 = tr_read<v_rd_off(D0, 0, 0)>(vb), h0 = tr_read<v_rd_off(D0, 0, 1)>(vb), l1 = tr_read<v_rd_off(D0, 1, 0)>(vb), h1 = tr_read<v_rd_off(D0, 1, 1)>(vb);
  const s16x4 l2 = tr_read<v_rd_off(D0, 2, 0)>(vb), h2 = tr_read<v_rd_off(D0, 2, 1)>(vb), l3 = tr_read<v_rd_off(D0, 3, 0)>(vb), h3 = tr_read<v_rd_off(D0, 3, 1)>(vb);
  asm volatile("s_waitcnt lgkmcnt(0)" ::: "memory"); SBAR();
#define PK(L, H) (bf16x8){L[0], L[1], L[2], L[3], H[0], H[1], H[2], H[3]}
  od = __builtin_amdgcn_mfma_f32_32x32x16_bf16(pa0, PK(l0, h0), od, 0, 0, 0);
  od = __builtin_amdgcn_mfma_f32_32x32x16_bf16(pa1, PK(l1, h1), od, 0, 0, 0);
  od = __builtin_amdgcn_mfma_f32_32x32x16_bf16(pa2, PK(l2, h2), od, 0, 0, 0);
  od = __builtin_amdgcn_mfma_f32_32x32x16_bf16(pa3, PK(l3, h3), od, 0, 0, 0);
#undef PK
}
DI void pv_d0(f32x16* o, int vb, bf16x8 pa0, bf16x8 pa1, bf16x8 pa2, bf16x8 pa3) {
  pv_one<0>(o[0], vb, pa0, pa1, pa2, pa3); pv_one<1>(o[1], vb, pa0, pa1, pa2, pa3); pv_one<2>(o[2], vb, pa0, pa1, pa2, pa3); pv_one<3>(o[3], vb, pa0, pa1, pa2, pa3);
}
__global__ __launch_bounds__(512, 1) void k_attn(const u16* __restrict__ Q, const u16* __restrict__ Kc, const u16* __restrict__ Vc, u16* __restrict__ O) {
  extern __shared__ __attribute__((aligned(16))) char lds[];
  const int h = blockIdx.y, kvh = h / (HQ / HKV), i0 = blockIdx.x * 256;
  const u16* Qb = Q + (size_t)i0 * LDQ + h * D; const u16* Kh = Kc + (size_t)kvh * SK * D; const u16* Vh = Vc + (size_t)kvh * SK * D;
  u16* Ob = O + (size_t)i0 * LDO + h * D;
  const int seq = SK;
  const int tid = threadIdx.x, wid = tid >> 6, lane = tid & 63, r32 = lane & 31, hi = lane >> 5;
  u16* V_lds = (u16*)lds; u16* K_lds = (u16*)(lds + 2 * SHM_V);
  float* ws = (float*)(lds + 2 * SHM_V + 2 * SHM_K) + wid * 64; float* li_l = ws; float* al_l = ws + 32;
  float m_reg = -1e30f, l_reg = 0; f32x16 o[4] = {}; bf16x8 qr[8];
  const u16* Qw = Qb + (long)(wid * QBLK + r32) * LDQ + hi * 8;
#pragma unroll
  for (int d0 = 0; d0 < 8; ++d0) qr[d0] = *(const bf16x8*)(Qw + d0 * 16);
  const int sr = tid >> 4, sc = (tid & 15) * 8, vst0 = v_st(sr, sc), vst1 = v_st(32 + sr, sc);
  const int vb0 = (int)(uintptr_t)V_lds + v_rd_base(lane);
  struct { bf16x8 vs0, vs1, ks0, ks1; } sr_[2];
#define SLOAD(i, k0) do { sr_[i].vs0 = *(const bf16x8*)(&Vh[(long)((k0) + sr) * LDK + sc]); sr_[i].vs1 = *(const bf16x8*)(&Vh[(long)((k0) + 32 + sr) * LDK + sc]); \
    sr_[i].ks0 = *(const bf16x8*)(&Kh[(long)((k0) + sr) * LDK + sc]); sr_[i].ks1 = *(const bf16x8*)(&Kh[(long)((k0) + 32 + sr) * LDK + sc]); } while (0)
#define SWRITE(b, i) do { *(bf16x8*)((char*)V_lds + (b) * SHM_V + vst0) = sr_[i].vs0;          \
    *(bf16x8*)((char*)V_lds + (b) * SHM_V + vst1) = sr_[i].vs1; int kc = sc * 2;               \
    *(bf16x8*)((char*)K_lds + (b) * SHM_K + KSWZ(sr, kc)) = sr_[i].ks0;                       \
    *(bf16x8*)((char*)K_lds + (b) * SHM_K + KSWZ(32 + sr, kc)) = sr_[i].ks1; } while (0)
#define SWAIT() asm volatile("s_waitcnt vmcnt(4)" ::: "memory")
#define RESC(a) do { if (__any((a) < 1.f)) { if (hi == 0) al_l[r32] = (a); asm volatile("s_waitcnt lgkmcnt(0)" ::: "memory"); \
    for (int d = 0; d < 4; ++d) for (int r = 0; r < 16; ++r) o[d][r] *= al_l[crow(r, hi)]; } } while (0)
  f32x16 pA0, pA1, pB0, pB1; float mnA, mnB, alA, alB; bf16x8 pa0, pa1, pa2, pa3; const int NT = seq / KVBLK;
  constexpr int SE = 0, SO = 1;
  SLOAD(SE, 0); asm volatile("s_waitcnt vmcnt(0)" ::: "memory"); SWRITE(0, SE); __syncthreads();
  qkt(pA0, pA1, K_lds, qr, r32, hi); partialSM(pA0, pA1, m_reg, mnA, alA);
  SLOAD(SO, KVBLK); if (2 < NT) SLOAD(SE, 2 * KVBLK);
  SWAIT(); SWRITE(1, SO); __syncthreads();
  for (int j = 1; j + 1 < NT; j += 2) {
    SBAR(); qkt(pB0, pB1, (u16*)((char*)K_lds + SHM_K), qr, r32, hi);
    finishSM(pA0, pA1, alA, l_reg, pa0, pa1, pa2, pa3); SBAR();
    SLOAD(SO, (j + 2) * KVBLK); SBAR();
    pv_d0(o, vb0, pa0, pa1, pa2, pa3); partialSM(pB0, pB1, m_reg, mnB, alB);
    __syncthreads(); SWAIT(); SWRITE(0, SE);
    RESC(alB); __syncthreads();
    SBAR(); qkt(pA0, pA1, K_lds, qr, r32, hi);
    finishSM(pB0, pB1, alB, l_reg, pa0, pa1, pa2, pa3); SBAR();
    if (j + 3 < NT) SLOAD(SE, (j + 3) * KVBLK); SBAR();
    pv_d0(o, vb0 + (int)SHM_V, pa0, pa1, pa2, pa3); partialSM(pA0, pA1, m_reg, mnA, alA);
    __syncthreads(); SWAIT(); SWRITE(1, SO);
    RESC(alA); __syncthreads();
  }
  SBAR(); qkt(pB0, pB1, (u16*)((char*)K_lds + SHM_K), qr, r32, hi);
  finishSM(pA0, pA1, alA, l_reg, pa0, pa1, pa2, pa3); SBAR();
  pv_d0(o, vb0, pa0, pa1, pa2, pa3); partialSM(pB0, pB1, m_reg, mnB, alB);
  __syncthreads(); RESC(alB);
  finishSM(pB0, pB1, alB, l_reg, pa0, pa1, pa2, pa3); SBAR();
  pv_d0(o, vb0 + (int)SHM_V, pa0, pa1, pa2, pa3);
  if (hi == 0) li_l[r32] = l_reg; asm volatile("s_waitcnt lgkmcnt(0)" ::: "memory");
  float rli[16];
#pragma unroll
  for (int r = 0; r < 16; ++r) rli[r] = __builtin_amdgcn_rcpf(li_l[crow(r, hi)]);
  u16* Ow = Ob + (long)(wid * QBLK) * LDO;
#pragma unroll
  for (int r = 0; r < 16; ++r) { int orow = crow(r, hi);
    for (int d0 = 0; d0 < 4; ++d0) Ow[(long)orow * LDO + d0 * 32 + r32] = f2bf(o[d0][r] * rli[r]); }
}

extern "C" void kernel_launch(void* const* d_in, const int* in_sizes, int n_in, void* d_out, int out_size, void* d_ws, size_t ws_size, hipStream_t stream) {
  const u16* x = (const u16*)d_in[0]; const u16* kc = (const u16*)d_in[1]; const u16* vc = (const u16*)d_in[2];
  const u16* wq = (const u16*)d_in[3]; const u16* wo = (const u16*)d_in[4];
  const u16* wg = (const u16*)d_in[6]; const u16* wu = (const u16*)d_in[7]; const u16* wd = (const u16*)d_in[8];
  const u16* c = (const u16*)d_in[9]; const u16* wmod = (const u16*)d_in[10];
  u16* out = (u16*)d_out;
  char* ws = (char*)d_ws;
  float* mod = (float*)ws;
  u16* h = (u16*)(ws + (1 << 20));
  u16* q = (u16*)(ws + (8 << 20));
  u16* o = (u16*)(ws + (32 << 20));
  u16* x1 = (u16*)(ws + (64 << 20));
  u16* act = (u16*)(ws + (80 << 20));
  hipFuncSetAttribute((const void*)k_attn, hipFuncAttributeMaxDynamicSharedMemorySize, (int)SHM_ATTN);
  k_mod<<<96, 256, 0, stream>>>(c, wmod, mod);
  k_ln<<<S / 4, 256, 0, stream>>>(x, mod, mod + 1024, h);
  k_gemm<0><<<dim3(4096 / 128, S / 128), 256, 0, stream>>>(h, wq, nullptr, q, nullptr, nullptr, S, 4096, 1024);
  k_attn<<<dim3(S / 256, HQ), 512, SHM_ATTN, stream>>>(q, kc, vc, o);
  k_gemm<1><<<dim3(1024 / 128, S / 128), 256, 0, stream>>>(o, wo, nullptr, x1, x, mod + 2048, S, 1024, 4096);
  k_ln<<<S / 4, 256, 0, stream>>>(x1, mod + 3072, mod + 4096, h);
  k_gemm<2><<<dim3(FF / 64, S / 128), 256, 0, stream>>>(h, wg, wu, act, nullptr, nullptr, S, FF, 1024);
  k_gemm<1><<<dim3(1024 / 128, S / 128), 256, 0, stream>>>(act, wd, nullptr, out, x1, mod + 5120, S, 1024, FF);
}
```

```cpp
#include <hip/hip_runtime.h>
#include <utility>
#include <type_traits>
typedef unsigned short u16;
typedef short bf16x8 __attribute__((ext_vector_type(8)));
typedef short s16x4 __attribute__((ext_vector_type(4)));
typedef float f32x4 __attribute__((ext_vector_type(4)));
typedef float f32x16 __attribute__((ext_vector_type(16)));
typedef unsigned u32x4 __attribute__((ext_vector_type(4)));
typedef unsigned u32x2 __attribute__((ext_vector_type(2)));
typedef float f32x2 __attribute__((ext_vector_type(2)));
typedef __bf16 bf16x2_t __attribute__((ext_vector_type(2)));
#define DI __device__ __forceinline__
#define LAS __attribute__((address_space(3)))
#define GAP() __builtin_amdgcn_sched_barrier(0)
template <int... Is> using iseq = std::integer_sequence<int, Is...>;
template <int N> using make_iseq = std::make_integer_sequence<int, N>;

DI unsigned rfl(unsigned x) { return __builtin_amdgcn_readfirstlane(x); }
DI float bf2f(u16 v) { return __uint_as_float(((unsigned)v) << 16); }
DI unsigned cvtpk(float lo, float hi) { f32x2 v = {lo, hi}; bf16x2_t b = __builtin_convertvector(v, bf16x2_t); return __builtin_bit_cast(unsigned, b); }
DI float rbf(float f) { return __uint_as_float(cvtpk(f, 0.f) << 16); }
DI __amdgpu_buffer_rsrc_t mk_srd(const void* p, unsigned bytes) { return __builtin_amdgcn_make_buffer_rsrc((void*)p, (short)0, (int)bytes, 0x00020000); }

#define W4_ACLOB "a0","a1","a2","a3","a4","a5","a6","a7","a8","a9","a10","a11","a12","a13","a14","a15","a16","a17","a18","a19","a20","a21","a22","a23","a24","a25","a26","a27","a28","a29","a30","a31","a32","a33","a34","a35","a36","a37","a38","a39","a40","a41","a42","a43","a44","a45","a46","a47","a48","a49","a50","a51","a52","a53","a54","a55","a56","a57","a58","a59","a60","a61","a62","a63","a64","a65","a66","a67","a68","a69","a70","a71","a72","a73","a74","a75","a76","a77","a78","a79","a80","a81","a82","a83","a84","a85","a86","a87","a88","a89","a90","a91","a92","a93","a94","a95","a96","a97","a98","a99","a100","a101","a102","a103","a104","a105","a106","a107","a108","a109","a110","a111","a112","a113","a114","a115","a116","a117","a118","a119","a120","a121","a122","a123","a124","a125","a126","a127"
#define W4_ACLOB_HI "a128","a129","a130","a131","a132","a133","a134","a135","a136","a137","a138","a139","a140","a141","a142","a143","a144","a145","a146","a147","a148","a149","a150","a151","a152","a153","a154","a155","a156","a157","a158","a159","a160","a161","a162","a163","a164","a165","a166","a167","a168","a169","a170","a171","a172","a173","a174","a175","a176","a177","a178","a179","a180","a181","a182","a183","a184","a185","a186","a187","a188","a189","a190","a191","a192","a193","a194","a195","a196","a197","a198","a199","a200","a201","a202","a203","a204","a205","a206","a207","a208","a209","a210","a211","a212","a213","a214","a215","a216","a217","a218","a219","a220","a221","a222","a223","a224","a225","a226","a227","a228","a229","a230","a231","a232","a233","a234","a235","a236","a237","a238","a239","a240","a241","a242","a243","a244","a245","a246","a247","a248","a249","a250","a251","a252","a253","a254","a255"
template <int SH_, int NW_, int WR_, int NB16_, bool GU_, int KORD_, int NS_, int PDT_, int FD_, int BARPOS_, bool ASMACC_ = false>
struct GCfg {
  static constexpr bool ASMACC = ASMACC_;
  static constexpr int SH = SH_, NW = NW_, WR = WR_, NB16 = NB16_, KORD = KORD_, NS = NS_, PDT = PDT_, FD = FD_, BARPOS = BARPOS_;
  static constexpr bool GU = GU_;
  static constexpr int STEPS = SH == 32 ? 4 : 2;
  static constexpr int MR = WR / SH;
  static constexpr int NB = SH == 32 ? NB16 / 2 : NB16;
  static constexpr int NR16 = GU ? NB16 / 2 : NB16;
  static constexpr int PIECES = NB16 * 2;
  static constexpr int PB = (PIECES + NW - 1) / NW;
  static constexpr int STAGE = PB * NW * 1024;
  static constexpr int REGION = STAGE / 2;
  static constexpr int LDSB = NS * STAGE;
  static constexpr int NOPS = STEPS * MR + PB;
  static constexpr int GS = NB * MR;
  static constexpr int G = STEPS * GS;
  static constexpr int BSTEP = STEPS - FD;
  static constexpr int GB = BSTEP * GS + (BARPOS + 1) * MR - 1;
  static constexpr int dma_lo(int ks) { return (PB * ks) / STEPS; }
  static constexpr int step_ops(int ks) { return MR + dma_lo(ks + 1) - dma_lo(ks); }
  static constexpr int step_first(int ks) { return ks * MR + dma_lo(ks); }
  static constexpr int op_step(int j) { int ks = 0; while (ks < STEPS - 1 && j >= step_first(ks + 1)) ++ks; return ks; }
  static constexpr int op_gap(int j) { const int ks = op_step(j), i = j - step_first(ks), n = step_ops(ks); return ks * GS + (i * GS) / n; }
  static constexpr int ops_before(int g) { int c = 0; for (int j = 0; j < NOPS; ++j) if (op_gap(j) < g) ++c; return c; }
  static constexpr int NA(int ks) {
    const int ts = (ks + 1) % STEPS, jt = step_first(ts) + MR - 1;
    const int full = (ks == STEPS - 1) ? PDT - 1 : PDT;
    return (NOPS - 1 - jt) + (full - 1) * NOPS + ops_before(ks * GS);
  }
  static constexpr int NBW() { return (NS - 3) * NOPS + ops_before(GB); }
  static_assert(NS >= 3 && PDT >= 2 && FD <= STEPS - 0 && BSTEP >= 0, "");
  using acc_t = std::conditional_t<SH == 32, f32x16, f32x4>;
};

template <class C>
struct GState {
  typename C::acc_t acc[C::NB][C::MR];
  bf16x8 a[C::PDT * C::STEPS][C::MR];
  s16x4 blo[C::FD][C::NB], bhi[C::FD][C::NB];
  __amdgpu_buffer_rsrc_t srdA, srdB;
  unsigned voffA[C::MR];
  unsigned voffB[C::GU ? C::PB : 1];
  unsigned sB[C::GU ? 1 : C::PB];
  unsigned m0base, vrd, ktileB;
};
struct GIter { unsigned va_cur, va_nxt, sA, sA0, m0_fill, sK_fill; };

template <class C, int SL, int OFF, int MRI> DI void a_load1(GState<C>& g, unsigned sA) {
  asm volatile("buffer_load_dwordx4 %0, %1, %2, %3 offen offset:%4" : "=v"(g.a[SL][MRI]) : "v"(g.voffA[MRI]), "s"(g.srdA), "s"(sA), "i"(OFF) : "memory");
}
template <class C, int Q> DI void b_dma1(GState<C>& g, unsigned m0v, unsigned sK) {
  if constexpr (C::GU)
    asm volatile("s_mov_b32 m0, %0\n\ts_nop 0\n\tbuffer_load_dwordx4 %1, %2, %3 offen lds" :: "s"(m0v + 1024u * Q), "v"(g.voffB[Q]), "s"(g.srdB), "s"(sK) : "memory");
  else
    asm volatile("s_mov_b32 m0, %0\n\ts_nop 0\n\tbuffer_load_dwordx4 %1, %2, %3 offen lds" :: "s"(m0v + 1024u * Q), "v"(g.voffB[0]), "s"(g.srdB), "s"(sK + g.sB[Q]) : "memory");
}
template <class C> DI void g_dummy(GState<C>& g) {
  asm volatile("s_mov_b32 m0, %0\n\ts_nop 0\n\tbuffer_load_dwordx4 %1, %2, %3 offen lds" :: "s"(g.m0base + (unsigned)(C::NS - 1) * C::STAGE), "v"(g.voffA[0]), "s"(g.srdA), "s"(0x80000000u) : "memory");
}
template <class C, int TI, int J, bool VA, bool VB> DI void g_op(GState<C>& g, const GIter& it) {
  constexpr int ks = C::op_step(J), i = J - C::step_first(ks);
  if constexpr (i < C::MR) {
    constexpr int PD = C::PDT * C::STEPS, SL = (TI * C::STEPS + ks - 1 + PD) % PD;
    if constexpr (VA) { if constexpr (ks == 0) a_load1<C, SL, 0, i>(g, it.sA0); else a_load1<C, SL, (ks - 1) * 1024, i>(g, it.sA); } else g_dummy<C>(g);
  } else {
    if constexpr (VB) b_dma1<C, C::dma_lo(ks) + i - C::MR>(g, it.m0_fill, it.sK_fill); else g_dummy<C>(g);
  }
}
template <class C, int TI, int GI, int... Js> DI void g_ops_at(GState<C>& g, const GIter& it, iseq<Js...>) {
  ((C::op_gap(Js) == GI ? g_op<C, TI, Js, true, true>(g, it) : (void)0), ...);
}
template <class C> constexpr int b_off(int KS, int NBI, int t) {
  if (C::SH == 32) {
    return ((2 * KS + t) * C::NR16 + (C::GU ? NBI : 2 * NBI)) * 256;
  } else {
    const int kb = 4 * KS + (C::KORD == 0 ? 2 * t : t);
    return (kb * C::NR16 + (C::GU ? NBI / 2 : NBI)) * 256 + ((C::GU && (NBI & 1)) ? C::REGION : 0);
  }
}
template <class C, int BUF, int KS, int NBI> DI void b_read(GState<C>& g, unsigned va) {
  g.blo[BUF][NBI] = __builtin_amdgcn_ds_read_tr16_b64_v4i16((LAS s16x4*)(size_t)(va + b_off<C>(KS, NBI, 0)));
  g.bhi[BUF][NBI] = __builtin_amdgcn_ds_read_tr16_b64_v4i16((LAS s16x4*)(size_t)(va + b_off<C>(KS, NBI, 1)));
}
template <class C, int BUF, int KS, int... Ns> DI void b_read_all(GState<C>& g, unsigned va, iseq<Ns...>) { (b_read<C, BUF, KS, Ns>(g, va), ...); }

template <class C, int N, int SL> DI void a_wait(GState<C>& g) {
  static_assert(N >= 0 && N < 64, "vmcnt range");
  if constexpr (C::MR == 1) asm volatile("s_waitcnt vmcnt(%1)" : "+v"(g.a[SL][0]) : "i"(N) : "memory");
  else if constexpr (C::MR == 2) asm volatile("s_waitcnt vmcnt(%2)" : "+v"(g.a[SL][0]), "+v"(g.a[SL][1]) : "i"(N) : "memory");
  else asm volatile("s_waitcnt vmcnt(%4)" : "+v"(g.a[SL][0]), "+v"(g.a[SL][1]), "+v"(g.a[SL][2]), "+v"(g.a[SL][3]) : "i"(N) : "memory");
}

template <class C, int TI, int GI> DI void g_gap(GState<C>& g, const GIter& it) {
  constexpr int KS = GI / C::GS, r = GI % C::GS, NBI = r / C::MR, MRI = r % C::MR;
  constexpr int SL = TI * C::STEPS + KS, BUF = (C::FD == 1) ? 0 : (KS & 1), KN = (KS + C::FD) % C::STEPS;
  if constexpr (r == 0) { a_wait<C, C::NA(KS), SL>(g); GAP(); }
  {
    const bf16x8 bf = __builtin_shufflevector(g.blo[BUF][NBI], g.bhi[BUF][NBI], 0, 1, 2, 3, 4, 5, 6, 7);
    if constexpr (C::ASMACC) { constexpr int ao = 16 * (C::MR * NBI + MRI);
      asm volatile("v_mfma_f32_32x32x16_bf16 a[%c2:%c3], %0, %1, a[%c2:%c3]" :: "v"(bf), "v"(g.a[SL][MRI]), "i"(ao), "i"(ao + 15) : W4_ACLOB); }
    else if constexpr (C::SH == 32) g.acc[NBI][MRI] = __builtin_amdgcn_mfma_f32_32x32x16_bf16(bf, g.a[SL][MRI], g.acc[NBI][MRI], 0, 0, 0);
    else g.acc[NBI][MRI] = __builtin_amdgcn_mfma_f32_16x16x32_bf16(bf, g.a[SL][MRI], g.acc[NBI][MRI], 0, 0, 0);
  }
  if constexpr (GI == C::GB) {
    GAP();
    static_assert(C::NBW() < 64, "");
    asm volatile("s_waitcnt vmcnt(%0) lgkmcnt(0)\n\ts_barrier" :: "i"(C::NBW()) : "memory");
    GAP();
    b_read_all<C, BUF, KN>(g, it.va_nxt, make_iseq<C::BARPOS + 1>{});
  } else if constexpr (MRI == C::MR - 1) {
    if constexpr (KS == C::BSTEP) { if constexpr (NBI > C::BARPOS) b_read<C, BUF, KN, NBI>(g, it.va_nxt); }
    else if constexpr (KS > C::BSTEP) b_read<C, BUF, KN, NBI>(g, it.va_nxt);
    else b_read<C, BUF, KN, NBI>(g, it.va_cur);
  }
  g_ops_at<C, TI, GI>(g, it, make_iseq<C::NOPS>{});
  GAP();
}
template <class C, int TI, int... Gs> DI void g_gaps(GState<C>& g, const GIter& it, iseq<Gs...>) { (g_gap<C, TI, Gs>(g, it), ...); }
template <class C, int TI> DI void g_tile(GState<C>& g, int t, unsigned& stg, unsigned& pstg) {
  unsigned nstg = stg + C::STAGE; if (nstg == C::NS * C::STAGE) nstg = 0;
  GIter it;
  it.va_cur = g.vrd + stg; it.va_nxt = g.vrd + nstg;
  it.sA = (unsigned)(C::STEPS * 1024) * (unsigned)(t + C::PDT); it.sA0 = it.sA - 1024u;
  it.m0_fill = g.m0base + pstg; it.sK_fill = g.ktileB * (unsigned)(t - 1 + C::NS);
  g_gaps<C, TI>(g, it, make_iseq<C::G>{});
  pstg = stg; stg = nstg;
}
template <class C, int... TIs> DI void g_body(GState<C>& g, int t, unsigned& stg, unsigned& pstg, iseq<TIs...>) { (g_tile<C, TIs>(g, t + TIs, stg, pstg), ...); }

template <class C, int V, int J> DI void g_virt_op(GState<C>& g, const GIter& it) {
  constexpr int ks = C::op_step(J);
  constexpr int TI = ((V % C::PDT) + C::PDT) % C::PDT;
  constexpr bool VA = (C::STEPS * (V + C::PDT) + ks - 1) >= 0, VB = (V - 1 + C::NS) >= 0;
  if constexpr (V == -1 && J == C::ops_before(C::GB)) asm volatile("s_waitcnt vmcnt(%0)\n\ts_barrier" :: "i"(C::NBW()) : "memory");
  g_op<C, TI, J, VA, VB>(g, it);
}
template <class C, int V, int... Js> DI void g_virt(GState<C>& g, iseq<Js...>) {
  GIter it; constexpr int tb = V - 1 + C::NS;
  it.va_cur = it.va_nxt = 0; it.sA = (unsigned)(C::STEPS * 1024) * (unsigned)(V + C::PDT >= 0 ? V + C::PDT : 0); it.sA0 = it.sA - 1024u;
  it.m0_fill = g.m0base + (unsigned)(tb >= 0 ? tb : 0) * C::STAGE; it.sK_fill = g.ktileB * (unsigned)(tb >= 0 ? tb : 0);
  (g_virt_op<C, V, Js>(g, it), ...);
  if constexpr (V == -1 && C::ops_before(C::GB) == C::NOPS) asm volatile("s_waitcnt vmcnt(%0)\n\ts_barrier" :: "i"(C::NBW()) : "memory");
}
template <class C, int... Vs> DI void g_prologue(GState<C>& g, iseq<Vs...>) { constexpr int NV = sizeof...(Vs); (g_virt<C, Vs - NV>(g, make_iseq<C::NOPS>{}), ...); }

template <class C, bool PSWAP = false> DI void g_setup(GState<C>& g, unsigned ldsb, unsigned lane, unsigned wid, unsigned ldb) {
  g.m0base = rfl(ldsb + wid * (unsigned)C::PB * 1024u);
  g.ktileB = 64u * ldb;
  const unsigned q = (lane & 15u) >> 2, p0 = lane & 3u, p = PSWAP ? (((p0 & 1u) << 1) | (p0 >> 1)) : p0, g4 = lane >> 4;
  if constexpr (C::SH == 32) {
    const unsigned h = lane >> 5, blk = g4 & 1u;
    g.vrd = ldsb + blk * (C::GU ? (unsigned)C::REGION : 256u) + ((4u * h + q) ^ (4u * blk)) * 32u + 8u * p;
  } else {
    g.vrd = ldsb + (g4 >> 1) * (C::KORD == 0 ? 1u : 2u) * (unsigned)(C::NR16 * 256) + (4u * (g4 & 1u) + q) * 32u + 8u * p;
  }
  if constexpr (C::GU) {
    const unsigned wr = wid % (C::NW / 2), up = wid / (C::NW / 2);
#pragma unroll
    for (int qq = 0; qq < C::PB; ++qq) {
      const unsigned P = wr * C::PB + qq, s = 4u * P + g4 * 0u + (lane >> 4), kb = s / (unsigned)C::NR16, nb = s % (unsigned)C::NR16;
      const unsigned row = ((lane >> 1) & 7u) ^ ((C::SH == 32 && up) ? 4u : 0u);
      g.voffB[qq] = (P < (unsigned)(C::NR16 * 2)) ? (8u * kb + row) * ldb + 32u * nb + 16u * (lane & 1u) : 0x80000000u;
    }
  } else {
    static_assert(C::GU || (C::NR16 % 4 == 0), "");
    const unsigned sub = lane >> 4;
    const unsigned row = ((lane >> 1) & 7u) ^ ((C::SH == 32 && (sub & 1u)) ? 4u : 0u);
    g.voffB[0] = row * ldb + 32u * sub + 16u * (lane & 1u);
#pragma unroll
    for (int qq = 0; qq < C::PB; ++qq) { const unsigned P = wid * C::PB + qq, kb = P / (unsigned)(C::NR16 / 4), j = P % (unsigned)(C::NR16 / 4); g.sB[qq] = rfl(8u * kb * ldb + 128u * j); }
  }
}

template <int R> DI void acc_zero1() { asm volatile("v_accvgpr_write_b32 a[%c0], 0" :: "i"(R) : W4_ACLOB); }
template <int... Rs> DI void acc_zero(iseq<Rs...>) { (acc_zero1<Rs>(), ...); asm volatile("s_nop 1"); }
template <class C> DI void gemm_run(GState<C>& g, int NT) {
  constexpr int NV = C::PDT > C::NS - 1 ? C::PDT : C::NS - 1;
  if constexpr (!C::ASMACC) {
#pragma unroll
    for (int n = 0; n < C::NB; ++n)
#pragma unroll
      for (int m = 0; m < C::MR; ++m)
#pragma unroll
        for (int e = 0; e < (C::SH == 32 ? 16 : 4); ++e) g.acc[n][m][e] = 0.f;
  }
  g_prologue<C>(g, make_iseq<NV>{});
  if constexpr (C::ASMACC) acc_zero(make_iseq<C::NB * C::MR * 16>{});
  b_read_all<C, 0, 0>(g, g.vrd, make_iseq<C::NB>{});
  if constexpr (C::FD == 2) b_read_all<C, 1, 1>(g, g.vrd, make_iseq<C::NB>{});
  GAP();
  unsigned stg = 0, pstg = (C::NS - 1) * C::STAGE;
  for (int t = 0; t < NT; t += C::PDT) g_body<C>(g, t, stg, pstg, make_iseq<C::PDT>{});
  asm volatile("s_waitcnt vmcnt(0) lgkmcnt(0)" ::: "memory");
#pragma unroll
  for (int i = 0; i < C::PDT * C::STEPS; ++i)
#pragma unroll
    for (int m = 0; m < C::MR; ++m) asm volatile("" :: "v"(g.a[i][m]));
  GAP();
}
typedef __bf16 abf16x8 __attribute__((ext_vector_type(8)));
typedef __bf16 abf16x4 __attribute__((ext_vector_type(4)));
#define ATTN_THR 32.0f
constexpr unsigned KRS = 256u, KTILEB = 64u * KRS;
#define KRING 0x0000u
#define VRING 0x8000u
#define QGEMM_LDS 0x10000u
DI float max3f(float a, float b, float c) { float r; asm("v_max3_f32 %0, %1, %2, %3" : "=v"(r) : "v"(a), "v"(b), "v"(c)); return r; }
DI float max2f(float a, float b) { float r; asm("v_max_f32_e32 %0, %1, %2" : "=v"(r) : "v"(a), "v"(b)); return r; }
DI float fadd_s(float a, float b) { float r; asm("v_add_f32_e32 %0, %1, %2" : "=v"(r) : "v"(a), "v"(b)); return r; }
DI float fmul_s(float a, float b) { float r; asm("v_mul_f32_e32 %0, %1, %2" : "=v"(r) : "v"(a), "v"(b)); return r; }
DI float fsub_s(float a, float b) { float r; asm("v_sub_f32_e32 %0, %1, %2" : "=v"(r) : "v"(a), "v"(b)); return r; }
DI float w4_aone() { unsigned l; asm volatile("v_mbcnt_lo_u32_b32 %0, -1, 0\n\tv_mbcnt_hi_u32_b32 %0, -1, %0" : "=v"(l)); return l < 32u ? 1.f : 0.f; }
DI void negm_bcast(f32x16& negm, float aone, float neg_mhat) {
  asm volatile("s_nop 1\n\tv_mfma_f32_32x32x2_f32 %0, %1, %2, 0\n\ts_nop 7\n\ts_nop 7\n\ts_nop 3" : "+v"(negm) : "v"(aone), "v"(neg_mhat));
}
struct W4C {
  unsigned vkb[4], vvb, vksrc[2], vvsrc, m0k, m0v, sofk, sofv;
  __amdgpu_buffer_rsrc_t srdK, srdV;
  f32x16 S[2][2][2];
  f32x16 NEGM[2];
  abf16x8 Vf[4][4];
  abf16x8 Pk[2][4];
  abf16x4 vlo[4][4], vup[4][4];
  float mhat[2], lsum[2], fct[2]; bool pend;
  float rs0[2], rs1[2], pmx0[2], pmx1[2], rmax[2];
  unsigned pm0[8], psf[8];
};
constexpr int AO_(int db, int qb) { return 16 * (2 * db + qb); }
constexpr int AQ_(int qb, int d0) { return 128 + 32 * qb + 4 * d0; }
constexpr int AK_(int kb, int d0) { return 192 + 32 * kb + 4 * d0; }

template <int I> DI void tr_read(W4C& c, unsigned vslot) {
  constexpr int ks = I >> 3, db = (I >> 1) & 3;
  if constexpr ((I & 1) == 0) asm volatile("ds_read_b64_tr_b16 %0, %1 offset:%2" : "=v"(c.vlo[db][ks]) : "v"(c.vvb + vslot), "i"(4096 * ks + 512 * db) : "memory");
  else                        asm volatile("ds_read_b64_tr_b16 %0, %1 offset:%2" : "=v"(c.vup[db][ks]) : "v"(c.vvb + vslot), "i"(4096 * ks + 2048 + 512 * db) : "memory");
}
template <int I> DI void k_read(W4C& c, unsigned slot) {
  constexpr int kb = I >> 3, d0 = I & 7;
  asm volatile("ds_read_b128 a[%c1:%c2], %0 offset:%c3" :: "v"(c.vkb[d0 & 3] + slot), "i"(AK_(kb, d0)), "i"(AK_(kb, d0) + 3), "i"(8192 * kb + 128 * (d0 >> 2)) : "memory", W4_ACLOB_HI);
}
template <int X, bool FIRST, int M> DI void qk_mfma(W4C& c) {
  constexpr int d0 = M >> 2, kb = (M >> 1) & 1, qb = M & 1, ak = AK_(kb, d0), aq = AQ_(qb, d0);
  if constexpr (d0 == 0) {
    if constexpr (FIRST) asm volatile("v_mfma_f32_32x32x16_bf16 %0, a[%c1:%c2], a[%c3:%c4], 0" : "=&v"(c.S[X][kb][qb]) : "i"(ak), "i"(ak + 3), "i"(aq), "i"(aq + 3) : W4_ACLOB, W4_ACLOB_HI);
    else                 asm volatile("v_mfma_f32_32x32x16_bf16 %0, a[%c2:%c3], a[%c4:%c5], %1" : "=&v"(c.S[X][kb][qb]) : "v"(c.NEGM[qb]), "i"(ak), "i"(ak + 3), "i"(aq), "i"(aq + 3) : W4_ACLOB, W4_ACLOB_HI);
  } else asm volatile("v_mfma_f32_32x32x16_bf16 %0, a[%c1:%c2], a[%c3:%c4], %0" : "+v"(c.S[X][kb][qb]) : "i"(ak), "i"(ak + 3), "i"(aq), "i"(aq + 3) : W4_ACLOB, W4_ACLOB_HI);
}
template <int M, bool ZERO> DI void pv_mfma(W4C& c) {
  constexpr int ks = M >> 3, db = (M >> 1) & 3, qb = M & 1, ao = AO_(db, qb);
  if constexpr (ZERO && ks == 0) asm volatile("v_mfma_f32_32x32x16_bf16 a[%c2:%c3], %0, %1, 0" :: "v"(c.Vf[db][ks]), "v"(c.Pk[qb][ks]), "i"(ao), "i"(ao + 15) : W4_ACLOB);
  else                         asm volatile("v_mfma_f32_32x32x16_bf16 a[%c2:%c3], %0, %1, a[%c2:%c3]" :: "v"(c.Vf[db][ks]), "v"(c.Pk[qb][ks]), "i"(ao), "i"(ao + 15) : W4_ACLOB);
}
template <int X, int QB, int KS, int P> DI void pack1(W4C& c) {
  u32x4 w = __builtin_bit_cast(u32x4, c.Pk[QB][KS]);
  w[P] = cvtpk(c.S[X][KS >> 1][QB][8 * (KS & 1) + 2 * P], c.S[X][KS >> 1][QB][8 * (KS & 1) + 2 * P + 1]);
  c.Pk[QB][KS] = __builtin_bit_cast(abf16x8, w);
}
template <int X, int J> DI void rowsum1(W4C& c) {
  constexpr int qb = J >> 4, ks = (J >> 2) & 3, p = J & 3;
  c.rs0[qb] = fadd_s(c.rs0[qb], c.S[X][ks >> 1][qb][8 * (ks & 1) + 2 * p]); c.rs1[qb] = fadd_s(c.rs1[qb], c.S[X][ks >> 1][qb][8 * (ks & 1) + 2 * p + 1]);
}
template <int X, bool FIRST, int M> DI void gapA(W4C& c, unsigned vslot, const int t) {
  constexpr int Xp = X ^ 1;
  qk_mfma<X, FIRST, M>(c);
  if constexpr (M >= 16 && M <= 31) {
    constexpr int q = (M - 16) >> 1;
    if constexpr (((M - 16) & 1) == 0) {
      if constexpr (q < 4) c.pm0[q] = c.m0k + (unsigned)((t + 2) & 1) * 0x4000u + 1024u * q; else c.pm0[q] = c.m0v + (unsigned)((t + 1) & 1) * 0x4000u + 1024u * (q - 4);
      asm volatile("" : "+s"(c.pm0[q]));
    } else {
      if constexpr (q < 4) c.psf[q] = KTILEB * (unsigned)(t + 2) + c.sofk + KRS * 4u * q; else c.psf[q] = KTILEB * (unsigned)(t + 1) + c.sofv + KRS * 8u * ((q - 4) >> 1) + 128u * ((q - 4) & 1);
      asm volatile("" : "+s"(c.psf[q]));
    }
  }
  if constexpr (M == 31) asm volatile("s_nop 4" : "+s"(c.pm0[0]), "+s"(c.pm0[1]), "+s"(c.pm0[2]), "+s"(c.pm0[3]), "+s"(c.pm0[4]), "+s"(c.pm0[5]), "+s"(c.pm0[6]), "+s"(c.pm0[7]),
                                                  "+s"(c.psf[0]), "+s"(c.psf[1]), "+s"(c.psf[2]), "+s"(c.psf[3]), "+s"(c.psf[4]), "+s"(c.psf[5]), "+s"(c.psf[6]), "+s"(c.psf[7]));
  if constexpr (!FIRST && M == 8) asm volatile("" :: "v"(c.NEGM[0]), "v"(c.NEGM[1]));
  if constexpr (!FIRST) {
    constexpr int base = M + (M >= 4) + (M >= 8) + (M >= 12) + (M >= 16) + (M >= 20) + (M >= 24);
    if constexpr (M <= 25) tr_read<base>(c, vslot);
    if constexpr (M == 3 || M == 7 || M == 11 || M == 15 || M == 19 || M == 23) tr_read<base + 1>(c, vslot);
    if constexpr (M < 16) {
      constexpr int e0 = 2 * M, e1 = 2 * M + 1;
      c.S[Xp][1][e0 >> 4][e0 & 15] = __builtin_amdgcn_exp2f(c.S[Xp][1][e0 >> 4][e0 & 15]);
      c.S[Xp][1][e1 >> 4][e1 & 15] = __builtin_amdgcn_exp2f(c.S[Xp][1][e1 >> 4][e1 & 15]);
      pack1<Xp, (M >> 3), ((M >> 2) & 1), (M & 3)>(c);
    } else {
      constexpr int cc = M - 16;
      pack1<Xp, (cc >> 3), 2 + ((cc >> 2) & 1), (cc & 3)>(c);
      rowsum1<Xp, 16 * (cc >> 3) + 8 + 4 * ((cc >> 2) & 1) + (cc & 3)>(c);
    }
  }
  GAP();
}
#define W4_VLO "+v"(c.vlo[0][0]), "+v"(c.vlo[0][1]), "+v"(c.vlo[0][2]), "+v"(c.vlo[0][3]), "+v"(c.vlo[1][0]), "+v"(c.vlo[1][1]), "+v"(c.vlo[1][2]), "+v"(c.vlo[1][3]), \
               "+v"(c.vlo[2][0]), "+v"(c.vlo[2][1]), "+v"(c.vlo[2][2]), "+v"(c.vlo[2][3]), "+v"(c.vlo[3][0]), "+v"(c.vlo[3][1]), "+v"(c.vlo[3][2]), "+v"(c.vlo[3][3])
#define W4_VUP "+v"(c.vup[0][0]), "+v"(c.vup[0][1]), "+v"(c.vup[0][2]), "+v"(c.vup[0][3]), "+v"(c.vup[1][0]), "+v"(c.vup[1][1]), "+v"(c.vup[1][2]), "+v"(c.vup[1][3]), \
               "+v"(c.vup[2][0]), "+v"(c.vup[2][1]), "+v"(c.vup[2][2]), "+v"(c.vup[2][3]), "+v"(c.vup[3][0]), "+v"(c.vup[3][1]), "+v"(c.vup[3][2]), "+v"(c.vup[3][3])
template <int X, bool FIRST, int... Ms> DI void phaseA_(W4C& c, const int t, iseq<Ms...>) {
  const unsigned vslot = (unsigned)((t - 1) & 1) * 0x4000u;
  (gapA<X, FIRST, Ms>(c, vslot, t), ...);
  if constexpr (!FIRST) {
#pragma unroll
    for (int qb = 0; qb < 2; ++qb) {
      const float rs = fadd_s(c.rs0[qb], c.rs1[qb]);
      auto pr = __builtin_amdgcn_permlane32_swap(__float_as_uint(rs), __float_as_uint(rs), false, false);
      c.lsum[qb] = fadd_s(c.lsum[qb], fadd_s(__uint_as_float(pr[0]), __uint_as_float(pr[1])));
    }
  }
  if constexpr (FIRST) asm volatile("s_waitcnt vmcnt(0) lgkmcnt(0)\n\ts_barrier" ::: "memory");
  else { asm volatile("s_waitcnt vmcnt(0) lgkmcnt(0)" : W4_VLO :: "memory"); asm volatile("s_barrier" : W4_VUP :: "memory"); }
  GAP();
  if constexpr (!FIRST) {
#pragma unroll
    for (int ks = 0; ks < 4; ++ks)
#pragma unroll
      for (int db = 0; db < 4; ++db) c.Vf[db][ks] = __builtin_shufflevector(c.vlo[db][ks], c.vup[db][ks], 0, 1, 2, 3, 4, 5, 6, 7);
  }
}
template <int X, bool FIRST> DI void phaseA(W4C& c, const int t) { phaseA_<X, FIRST>(c, t, make_iseq<32>{}); }

template <int X, bool PEEL, bool ZERO1, int M> DI void gapB(W4C& c, const unsigned kslot_rd) {
  if constexpr (!PEEL) pv_mfma<M, ZERO1>(c);
  if constexpr (M < 4)      asm volatile("s_mov_b32 m0, %0\n\ts_nop 0\n\tbuffer_load_dwordx4 %1, %2, %3 offen lds" :: "s"(c.pm0[M]), "v"(c.vksrc[M & 1]), "s"(c.srdK), "s"(c.psf[M]) : "memory");
  else if constexpr (M < 8) asm volatile("s_mov_b32 m0, %0\n\ts_nop 0\n\tbuffer_load_dwordx4 %1, %2, %3 offen lds" :: "s"(c.pm0[M]), "v"(c.vvsrc), "s"(c.srdV), "s"(c.psf[M]) : "memory");
  if constexpr (M >= 1 && M <= 16) k_read<M - 1>(c, kslot_rd);
  if constexpr (M >= 8 && M < 16) {
    constexpr int i = M - 8, q = i >> 2, r = 4 * (i & 3);
    if constexpr ((i & 3) == 0) { c.pmx0[q] = max3f(c.S[X][0][q][0], c.S[X][0][q][1], c.S[X][1][q][0]); c.pmx1[q] = max3f(c.S[X][0][q][2], c.S[X][0][q][3], c.S[X][1][q][1]);
                                  c.pmx0[q] = max3f(c.pmx0[q], c.S[X][1][q][2], c.S[X][1][q][3]); }
    else                        { c.pmx0[q] = max3f(c.pmx0[q], c.S[X][0][q][r], c.S[X][0][q][r + 1]); c.pmx1[q] = max3f(c.pmx1[q], c.S[X][0][q][r + 2], c.S[X][0][q][r + 3]);
                                  c.pmx0[q] = max3f(c.pmx0[q], c.S[X][1][q][r], c.S[X][1][q][r + 1]); c.pmx1[q] = max3f(c.pmx1[q], c.S[X][1][q][r + 2], c.S[X][1][q][r + 3]); }
  }
  if constexpr (M == 16 || M == 17) {
    constexpr int q = M - 16; const float mm = max2f(c.pmx0[q], c.pmx1[q]);
    auto pr = __builtin_amdgcn_permlane32_swap(__float_as_uint(mm), __float_as_uint(mm), false, false);
    c.rmax[q] = max2f(__uint_as_float(pr[0]), __uint_as_float(pr[1]));
  }
  if constexpr (M == 18) {
    if constexpr (PEEL) {
#pragma unroll
      for (int q = 0; q < 2; ++q) { c.mhat[q] = c.rmax[q]; c.lsum[q] = 0.f; c.fct[q] = 1.f;
#pragma unroll
        for (int kb = 0; kb < 2; ++kb) { _Pragma("unroll") for (int e = 0; e < 16; ++e) c.S[X][kb][q][e] = fsub_s(c.S[X][kb][q][e], c.rmax[q]); }
        negm_bcast(c.NEGM[q], w4_aone(), -c.mhat[q]);
      }
    } else {
      const bool grew = (c.rmax[0] > ATTN_THR) | (c.rmax[1] > ATTN_THR);
      if (__builtin_expect(__any((int)grew), 0)) {
#pragma unroll
        for (int q = 0; q < 2; ++q) { const float dl = max2f(c.rmax[q], 0.f); c.mhat[q] = fadd_s(c.mhat[q], dl); c.fct[q] = __builtin_amdgcn_exp2f(-dl);
#pragma unroll
          for (int kb = 0; kb < 2; ++kb) { _Pragma("unroll") for (int e = 0; e < 16; ++e) c.S[X][kb][q][e] = fsub_s(c.S[X][kb][q][e], dl); }
          negm_bcast(c.NEGM[q], w4_aone(), -c.mhat[q]);
        }
        c.pend = true;
      }
    }
  }
  if constexpr (M == 18) { c.rs0[0] = c.rs0[1] = c.rs1[0] = c.rs1[1] = 0.f; }
  if constexpr (M >= 19) {
    constexpr int g = M - 19, e_lo = (32 * g) / 13, e_hi = (32 * (g + 1)) / 13;
    {
      constexpr int p_lo = g ? (32 * (g - 1)) / 13 : 0, p_hi = g ? (32 * g) / 13 : 0;
      if constexpr (p_lo + 0 < p_hi) { constexpr int e = p_lo + 0; if constexpr (e & 1) c.rs1[e >> 4] = fadd_s(c.rs1[e >> 4], c.S[X][0][e >> 4][e & 15]); else c.rs0[e >> 4] = fadd_s(c.rs0[e >> 4], c.S[X][0][e >> 4][e & 15]); }
      if constexpr (p_lo + 1 < p_hi) { constexpr int e = p_lo + 1; if constexpr (e & 1) c.rs1[e >> 4] = fadd_s(c.rs1[e >> 4], c.S[X][0][e >> 4][e & 15]); else c.rs0[e >> 4] = fadd_s(c.rs0[e >> 4], c.S[X][0][e >> 4][e & 15]); }
      if constexpr (p_lo + 2 < p_hi) { constexpr int e = p_lo + 2; if constexpr (e & 1) c.rs1[e >> 4] = fadd_s(c.rs1[e >> 4], c.S[X][0][e >> 4][e & 15]); else c.rs0[e >> 4] = fadd_s(c.rs0[e >> 4], c.S[X][0][e >> 4][e & 15]); }
    }
    if constexpr (e_lo + 0 < e_hi) c.S[X][0][(e_lo + 0) >> 4][(e_lo + 0) & 15] = __builtin_amdgcn_exp2f(c.S[X][0][(e_lo + 0) >> 4][(e_lo + 0) & 15]);
    if constexpr (e_lo + 1 < e_hi) c.S[X][0][(e_lo + 1) >> 4][(e_lo + 1) & 15] = __builtin_amdgcn_exp2f(c.S[X][0][(e_lo + 1) >> 4][(e_lo + 1) & 15]);
    if constexpr (e_lo + 2 < e_hi) c.S[X][0][(e_lo + 2) >> 4][(e_lo + 2) & 15] = __builtin_amdgcn_exp2f(c.S[X][0][(e_lo + 2) >> 4][(e_lo + 2) & 15]);
  }
  if constexpr (M == 31) asm volatile("" :: "v"(c.S[X][0][0]), "v"(c.S[X][0][1]), "v"(c.NEGM[0]), "v"(c.NEGM[1]), "v"(c.rs0[0]), "v"(c.rs0[1]), "v"(c.rs1[0]), "v"(c.rs1[1]));
  GAP();
}
template <int R> DI void resc4(float f) {
  float t0, t1, t2, t3;
  asm volatile("v_accvgpr_read_b32 %0, a[%c5]\n\tv_accvgpr_read_b32 %1, a[%c6]\n\tv_accvgpr_read_b32 %2, a[%c7]\n\tv_accvgpr_read_b32 %3, a[%c8]\n\t"
               "v_mul_f32 %0, %4, %0\n\tv_mul_f32 %1, %4, %1\n\tv_mul_f32 %2, %4, %2\n\tv_mul_f32 %3, %4, %3\n\t"
               "v_accvgpr_write_b32 a[%c5], %0\n\tv_accvgpr_write_b32 a[%c6], %1\n\tv_accvgpr_write_b32 a[%c7], %2\n\tv_accvgpr_write_b32 a[%c8], %3"
               : "=&v"(t0), "=&v"(t1), "=&v"(t2), "=&v"(t3) : "v"(f), "i"(R), "i"(R + 1), "i"(R + 2), "i"(R + 3) : W4_ACLOB);
}
template <int... Is> DI void resc_all(W4C& c, iseq<Is...>) { (resc4<4 * Is>(c.fct[(Is >> 2) & 1]), ...); }
template <int X, bool PEEL, bool ZERO1, int... Ms> DI void phaseB_(W4C& c, const int t, iseq<Ms...>) {
  const unsigned kslot_rd = (unsigned)((t + 1) & 1) * 0x4000u;
  if constexpr (!PEEL) asm volatile("s_nop 1" ::: "memory");
  GAP();
  (gapB<X, PEEL, ZERO1, Ms>(c, kslot_rd), ...);
  {
#pragma unroll
    for (int e = (32 * 12) / 13; e < 32; ++e) { if (e & 1) c.rs1[e >> 4] = fadd_s(c.rs1[e >> 4], c.S[X][0][e >> 4][e & 15]); else c.rs0[e >> 4] = fadd_s(c.rs0[e >> 4], c.S[X][0][e >> 4][e & 15]); }
  }
  if constexpr (!PEEL) {
    if (__builtin_expect(c.pend, 0)) {
      asm volatile("s_nop 7\n\ts_nop 7" ::: W4_ACLOB);
      resc_all(c, make_iseq<32>{});
      asm volatile("s_nop 1" ::: W4_ACLOB);
      c.lsum[0] = fmul_s(c.lsum[0], c.fct[0]); c.lsum[1] = fmul_s(c.lsum[1], c.fct[1]); c.pend = false;
    }
  }
  asm volatile("s_waitcnt lgkmcnt(0)" ::: "memory");
  GAP();
}
template <int X, bool PEEL, bool ZERO1> DI void phaseB(W4C& c, const int t) { phaseB_<X, PEEL, ZERO1>(c, t, make_iseq<32>{}); }
template <int... Is> DI void k_reads(W4C& c, unsigned slot, iseq<Is...>) { (k_read<Is>(c, slot), ...); }
template <int... Is> DI void tr_reads(W4C& c, unsigned vslot, iseq<Is...>) { (tr_read<Is>(c, vslot), ...); }
template <int X, int... Cs> DI void packs(W4C& c, iseq<Cs...>) { (pack1<X, ((Cs >> 3) & 1), 2 * (Cs >> 4) + ((Cs >> 2) & 1), (Cs & 3)>(c), ...); }
template <int X, int J> DI void rowsum1_padded(W4C& c) { rowsum1<X, J>(c); asm volatile("s_nop 1"); }
template <int X, int... Js> DI void rowsums(W4C& c, iseq<Js...>) { (rowsum1_padded<X, Js>(c), ...); }
template <int... Ms> DI void pv_all(W4C& c, iseq<Ms...>) { (pv_mfma<Ms, false>(c), ...); }
template <int R> DI u32x4 acc_pack8(float f) {
  float o[8];
  asm volatile("v_accvgpr_read_b32 %0, a[%c8]\n\tv_accvgpr_read_b32 %1, a[%c9]\n\tv_accvgpr_read_b32 %2, a[%c10]\n\tv_accvgpr_read_b32 %3, a[%c11]\n\t"
               "v_accvgpr_read_b32 %4, a[%c12]\n\tv_accvgpr_read_b32 %5, a[%c13]\n\tv_accvgpr_read_b32 %6, a[%c14]\n\tv_accvgpr_read_b32 %7, a[%c15]"
               : "=v"(o[0]), "=v"(o[1]), "=v"(o[2]), "=v"(o[3]), "=v"(o[4]), "=v"(o[5]), "=v"(o[6]), "=v"(o[7])
               : "i"(R), "i"(R + 1), "i"(R + 2), "i"(R + 3), "i"(R + 4), "i"(R + 5), "i"(R + 6), "i"(R + 7));
  u32x4 w = {cvtpk(o[0] * f, o[1] * f), cvtpk(o[2] * f, o[3] * f), cvtpk(o[4] * f, o[5] * f), cvtpk(o[6] * f, o[7] * f)};
  return w;
}
template <int I> DI void q_conv1(float C) {
  constexpr int qb = I >> 3, d0 = I & 7, nb = d0 >> 1, s = d0 & 1, aq = AQ_(qb, d0);
  const u32x4 w = acc_pack8<AO_(nb, qb) + 8 * s>(C);
  asm volatile("v_accvgpr_write_b32 a[%c4], %0\n\tv_accvgpr_write_b32 a[%c5], %1\n\tv_accvgpr_write_b32 a[%c6], %2\n\tv_accvgpr_write_b32 a[%c7], %3"
               :: "v"(w[0]), "v"(w[1]), "v"(w[2]), "v"(w[3]), "i"(aq), "i"(aq + 1), "i"(aq + 2), "i"(aq + 3) : W4_ACLOB_HI);
}
template <int... Is> DI void q_conv(float C, iseq<Is...>) { (q_conv1<Is>(C), ...); }
template <int I> DI void o_store1(char* base, float il0, float il1) {
  constexpr int qb = I >> 3, db = (I >> 1) & 3, s = I & 1;
  const u32x4 w = acc_pack8<AO_(db, qb) + 8 * s>(qb ? il1 : il0);
  *(u32x4*)(base + (size_t)qb * (256u * 1024u) + (2 * db + s) * 1024) = w;
}
template <int... Is> DI void o_store(char* base, float il0, float il1, iseq<Is...>) { (o_store1<Is>(base, il0, il1), ...); }

using QCfg = GCfg<32, 4, 64, 8, false, 0, 6, 2, 1, 1, true>;
constexpr int SHM_ATTN = 0x10000 + QCfg::LDSB;

__global__ __launch_bounds__(256, 1) void k_attn(const u16* __restrict__ H1f, const u16* __restrict__ Wq, const u16* __restrict__ Kc, const u16* __restrict__ Vc, u16* __restrict__ Of) {
  extern __shared__ __attribute__((aligned(16))) char lds[];
  const unsigned tid = threadIdx.x, wid = rfl(tid >> 6);
  const int bid = blockIdx.x, xcd = bid & 7, j = bid >> 3;
  const int head = 4 * xcd + (j & 3), y = j >> 2, kvh = head >> 3;
  const unsigned ldsb = (unsigned)(size_t)lds;
  {
    const unsigned lane = tid & 63u;
    GState<QCfg> g;
    const int mb0 = y * 8 + wid * 2;
    constexpr unsigned mbstride = 1024u * 32u * 2u;
    g.srdA = mk_srd((const char*)H1f + (size_t)mb0 * mbstride, 2 * mbstride);
    g.voffA[0] = lane * 16u; g.voffA[1] = lane * 16u + mbstride;
    constexpr unsigned ldb = 4096u * 2u;
    g.srdB = mk_srd((const char*)Wq + (size_t)head * 256, 1024u * ldb - (unsigned)head * 256u);
    g_setup<QCfg, true>(g, ldsb + QGEMM_LDS, lane, wid, ldb);
    gemm_run<QCfg>(g, 16);
  }
  unsigned lane; asm volatile("v_mbcnt_lo_u32_b32 %0, -1, 0\n\tv_mbcnt_hi_u32_b32 %0, -1, %0" : "=v"(lane));
  const unsigned l32 = lane & 31u, hi = lane >> 5;
  constexpr int T = 32;
  W4C c;
#pragma unroll
  for (int jj = 0; jj < 4; ++jj) c.vkb[jj] = ldsb + KRING + 256u * l32 + 16u * ((2u * jj + hi) ^ (l32 & 7u));
  c.vvb = ldsb + VRING + (((lane & 3u) << 3) | (((lane >> 2) & 3u) << 6) | (((lane >> 4) & 1u) << 5) | (hi << 8));
#pragma unroll
  for (int par = 0; par < 2; ++par) c.vksrc[par] = KRS * (lane >> 4) + 16u * ((lane & 15u) ^ (4u * par + (lane >> 4)));
  c.vvsrc = KRS * ((lane >> 2) & 7u) + 64u * hi + 16u * (lane & 3u);
  c.m0k = rfl(ldsb + KRING + 4096u * wid); c.m0v = rfl(ldsb + VRING + 4096u * wid);
  c.sofk = rfl(KRS * 16u * wid); c.sofv = rfl(KRS * 16u * wid);
  c.srdK = mk_srd((const char*)Kc + (size_t)kvh * (2048u * 256u), 2048u * 256u);
  c.srdV = mk_srd((const char*)Vc + (size_t)kvh * (2048u * 256u), 2048u * 256u);
  auto dma_k = [&](int tk) {
    const unsigned slot = (unsigned)(tk & 1) * 0x4000u, skv = KTILEB * (unsigned)tk;
#pragma unroll
    for (int q = 0; q < 4; ++q)
      asm volatile("s_nop 4\n\ts_mov_b32 m0, %0\n\ts_nop 0\n\tbuffer_load_dwordx4 %1, %2, %3 offen lds"
                   :: "s"(rfl(c.m0k + slot + 1024u * q)), "v"(c.vksrc[q & 1]), "s"(c.srdK), "s"(rfl(skv + c.sofk + KRS * 4u * q)) : "memory");
  };
  auto dma_v = [&](int tv) {
    const unsigned slot = (unsigned)(tv & 1) * 0x4000u, skv = KTILEB * (unsigned)tv;
#pragma unroll
    for (int q = 0; q < 4; ++q)
      asm volatile("s_nop 4\n\ts_mov_b32 m0, %0\n\ts_nop 0\n\tbuffer_load_dwordx4 %1, %2, %3 offen lds"
                   :: "s"(rfl(c.m0v + slot + 1024u * q)), "v"(c.vvsrc), "s"(c.srdV), "s"(rfl(skv + c.sofv + KRS * 8u * (q >> 1) + 128u * (q & 1))) : "memory");
  };
  dma_k(0); dma_v(0); dma_k(1);
  asm volatile("s_nop 7\n\ts_nop 7" ::: "memory");
  q_conv(0.12751743082f, make_iseq<16>{});
  asm volatile("s_waitcnt vmcnt(0)\n\ts_barrier" ::: "memory");
  k_reads(c, 0u, make_iseq<16>{});
  asm volatile("s_waitcnt lgkmcnt(0)\n\ts_nop 1" ::: "memory");
  c.mhat[0] = c.mhat[1] = 0.f; c.lsum[0] = c.lsum[1] = 0.f; c.fct[0] = c.fct[1] = 1.f; c.pend = false;
  phaseA<0, true>(c, 0);  phaseB<0, true, false>(c, 0);
  phaseA<1, false>(c, 1); phaseB<1, false, true>(c, 1);
  for (int t = 2; t < T; t += 2) {
    phaseA<0, false>(c, t);     phaseB<0, false, false>(c, t);
    phaseA<1, false>(c, t + 1); phaseB<1, false, false>(c, t + 1);
  }
  {
    const unsigned vslot = (unsigned)((T - 1) & 1) * 0x4000u;
    tr_reads(c, vslot, make_iseq<32>{});
#pragma unroll
    for (int e = 0; e < 32; ++e) c.S[1][1][e >> 4][e & 15] = __builtin_amdgcn_exp2f(c.S[1][1][e >> 4][e & 15]);
    packs<1>(c, make_iseq<32>{});
    GAP(); asm volatile("s_nop 1"); GAP();
    rowsums<1>(c, iseq<8, 9, 10, 11, 12, 13, 14, 15, 24, 25, 26, 27, 28, 29, 30, 31>{});
#pragma unroll
    for (int qb = 0; qb < 2; ++qb) { const float rs = fadd_s(c.rs0[qb], c.rs1[qb]);
      auto pr = __builtin_amdgcn_permlane32_swap(__float_as_uint(rs), __float_as_uint(rs), false, false);
      c.lsum[qb] = fadd_s(c.lsum[qb], fadd_s(__uint_as_float(pr[0]), __uint_as_float(pr[1]))); }
    asm volatile("s_waitcnt lgkmcnt(0)" : W4_VLO :: "memory"); asm volatile("" : W4_VUP :: "memory");
    GAP();
#pragma unroll
    for (int ks = 0; ks < 4; ++ks)
#pragma unroll
      for (int db = 0; db < 4; ++db) c.Vf[db][ks] = __builtin_shufflevector(c.vlo[db][ks], c.vup[db][ks], 0, 1, 2, 3, 4, 5, 6, 7);
    asm volatile("s_nop 1" ::: "memory");
    pv_all(c, make_iseq<32>{});
    asm volatile("s_nop 7\n\ts_nop 7" ::: "memory");
  }
  {
    unsigned lb; asm volatile("v_mbcnt_lo_u32_b32 %0, -1, 0\n\tv_mbcnt_hi_u32_b32 %0, -1, %0" : "=v"(lb));
    const float il0 = __builtin_amdgcn_rcpf(c.lsum[0]), il1 = __builtin_amdgcn_rcpf(c.lsum[1]);
    char* base = (char*)Of + ((size_t)(y * 8 + wid * 2) * 256 + head * 8) * 1024 + lb * 16u;
    o_store(base, il0, il1, make_iseq<16>{});
    asm volatile("s_waitcnt vmcnt(0)" ::: "memory");
  }
}
__global__ __launch_bounds__(256) void k_mod(const u16* __restrict__ c, const u16* __restrict__ wmod, float* __restrict__ mod) {
  __shared__ float sc[1024];
  __shared__ float red[64][33];
  const int tid = threadIdx.x;
  for (int i = tid; i < 1024; i += 256) { const float x = bf2f(c[i]); sc[i] = rbf(x / (1.f + __expf(-x))); }
  const int cg = tid & 3, kg = tid >> 2;
  const int col0 = blockIdx.x * 32 + cg * 8;
  u32x4 w[16];
#pragma unroll
  for (int i = 0; i < 16; ++i) w[i] = *(const u32x4*)(wmod + (size_t)(kg + 64 * i) * 6144 + col0);
  __syncthreads();
  float acc[8] = {0, 0, 0, 0, 0, 0, 0, 0};
#pragma unroll
  for (int i = 0; i < 16; ++i) {
    const float s = sc[kg + 64 * i];
#pragma unroll
    for (int jj = 0; jj < 4; ++jj) { acc[2 * jj] += s * __uint_as_float(w[i][jj] << 16); acc[2 * jj + 1] += s * __uint_as_float(w[i][jj] & 0xffff0000u); }
  }
#pragma unroll
  for (int jj = 0; jj < 8; ++jj) red[kg][cg * 8 + jj] = acc[jj];
  __syncthreads();
  if (tid < 32) { float s = 0; for (int g = 0; g < 64; ++g) s += red[g][tid]; mod[blockIdx.x * 32 + tid] = rbf(s); }
}

template <int MODE>
__global__ __launch_bounds__(256) void k_ln(const u16* __restrict__ x, const float* __restrict__ P, const float* __restrict__ mod, u16* __restrict__ x1f, u16* __restrict__ hf) {
  __shared__ float red[2][4][8];
  const int tid = threadIdx.x, r = tid & 7, h = (tid >> 3) & 1, grp = tid >> 4, wid = tid >> 6;
  const int row = blockIdx.x * 8 + r, mb = row >> 5, ml = row & 31;
  const float* shift = mod + (MODE == 0 ? 0 : 3072); const float* scale = mod + (MODE == 0 ? 1024 : 4096); const float* gate = mod + 2048;
  float v[2][16];
#pragma unroll
  for (int i = 0; i < 2; ++i) {
    const int nb = grp + 16 * i;
#pragma unroll
    for (int q = 0; q < 4; ++q) {
      const int n = 32 * nb + 8 * q + 4 * h;
      const u32x2 xw = *(const u32x2*)(x + (size_t)row * 1024 + n);
      float xv[4] = {__uint_as_float(xw[0] << 16), __uint_as_float(xw[0] & 0xffff0000u), __uint_as_float(xw[1] << 16), __uint_as_float(xw[1] & 0xffff0000u)};
      if (MODE == 1) {
        const size_t po = (((size_t)mb * 32 + nb) * 4 + q) * 256 + (h * 32 + ml) * 4;
        const f32x4 p0 = *(const f32x4*)(P + po), p1 = *(const f32x4*)(P + po + (size_t)2048 * 1024);
        const f32x4 gv = *(const f32x4*)(gate + n);
#pragma unroll
        for (int e = 0; e < 4; ++e) xv[e] = rbf(xv[e] + rbf(gv[e] * rbf(p0[e] + p1[e])));
      }
#pragma unroll
      for (int e = 0; e < 4; ++e) v[i][4 * q + e] = xv[e];
    }
  }
  float s = 0;
#pragma unroll
  for (int i = 0; i < 2; ++i)
#pragma unroll
    for (int e = 0; e < 16; ++e) s += v[i][e];
  s += __shfl_xor(s, 8); s += __shfl_xor(s, 16); s += __shfl_xor(s, 32);
  if ((tid & 63) < 8) red[0][wid][r] = s;
  __syncthreads();
  const float mean = (red[0][0][r] + red[0][1][r] + red[0][2][r] + red[0][3][r]) * (1.f / 1024.f);
  float qv = 0;
#pragma unroll
  for (int i = 0; i < 2; ++i)
#pragma unroll
    for (int e = 0; e < 16; ++e) { const float d = v[i][e] - mean; qv += d * d; }
  qv += __shfl_xor(qv, 8); qv += __shfl_xor(qv, 16); qv += __shfl_xor(qv, 32);
  if ((tid & 63) < 8) red[1][wid][r] = qv;
  __syncthreads();
  const float var = rbf((red[1][0][r] + red[1][1][r] + red[1][2][r] + red[1][3][r]) * (1.f / 1024.f));
  const float mb16 = rbf(mean), sd = rbf(sqrtf(rbf(var + 1e-5f)));
#pragma unroll
  for (int i = 0; i < 2; ++i) {
    const int nb = grp + 16 * i;
#pragma unroll
    for (int s2 = 0; s2 < 2; ++s2) {
      float o[8];
#pragma unroll
      for (int t = 0; t < 2; ++t) {
        const int q = 2 * s2 + t, n = 32 * nb + 8 * q + 4 * h;
        const f32x4 sh = *(const f32x4*)(shift + n), sc = *(const f32x4*)(scale + n);
#pragma unroll
        for (int e = 0; e < 4; ++e) { const float nn = rbf(rbf(v[i][4 * q + e] - mb16) / sd); o[4 * t + e] = rbf(nn * rbf(1.f + sc[e])) + sh[e]; }
      }
      const size_t fo = ((size_t)mb * 64 + 2 * nb + s2) * 512 + (h * 32 + ml) * 8;
      u32x4 w = {cvtpk(o[0], o[1]), cvtpk(o[2], o[3]), cvtpk(o[4], o[5]), cvtpk(o[6], o[7])};
      *(u32x4*)(hf + fo) = w;
      if (MODE == 1) {
        const float* vv = &v[i][8 * s2];
        u32x4 w1 = {cvtpk(vv[0], vv[1]), cvtpk(vv[2], vv[3]), cvtpk(vv[4], vv[5]), cvtpk(vv[6], vv[7])};
        *(u32x4*)(x1f + fo) = w1;
      }
    }
  }
}
__global__ __launch_bounds__(256) void k_fin(const u16* __restrict__ x1f, const float* __restrict__ P, const float* __restrict__ mod, u16* __restrict__ out) {
  const int tid = threadIdx.x, r = tid & 7, h = (tid >> 3) & 1, grp = tid >> 4;
  const int row = blockIdx.x * 8 + r, mb = row >> 5, ml = row & 31;
  const float* gate = mod + 5120;
#pragma unroll
  for (int i = 0; i < 2; ++i) {
    const int nb = grp + 16 * i;
#pragma unroll
    for (int s2 = 0; s2 < 2; ++s2) {
      const u32x4 xw = *(const u32x4*)(x1f + ((size_t)mb * 64 + 2 * nb + s2) * 512 + (h * 32 + ml) * 8);
      unsigned ow[2][2];
#pragma unroll
      for (int t = 0; t < 2; ++t) {
        const int q = 2 * s2 + t, n = 32 * nb + 8 * q + 4 * h;
        const size_t po = (((size_t)mb * 32 + nb) * 4 + q) * 256 + (h * 32 + ml) * 4;
        const f32x4 p0 = *(const f32x4*)(P + po), p1 = *(const f32x4*)(P + po + (size_t)2048 * 1024);
        const f32x4 gv = *(const f32x4*)(gate + n);
        const float xv[4] = {__uint_as_float(xw[2 * t] << 16), __uint_as_float(xw[2 * t] & 0xffff0000u), __uint_as_float(xw[2 * t + 1] << 16), __uint_as_float(xw[2 * t + 1] & 0xffff0000u)};
        float o[4];
#pragma unroll
        for (int e = 0; e < 4; ++e) o[e] = xv[e] + rbf(gv[e] * rbf(p0[e] + p1[e]));
        ow[t][0] = cvtpk(o[0], o[1]); ow[t][1] = cvtpk(o[2], o[3]);
      }
      const unsigned s0 = h ? ow[0][0] : ow[1][0], s1 = h ? ow[0][1] : ow[1][1];
      const unsigned g0 = __shfl_xor(s0, 8), g1 = __shfl_xor(s1, 8);
      u32x4 w; if (h == 0) w = (u32x4){ow[0][0], ow[0][1], g0, g1}; else w = (u32x4){g0, g1, ow[1][0], ow[1][1]};
      *(u32x4*)(out + (size_t)row * 1024 + 32 * nb + 16 * s2 + 8 * h) = w;
    }
  }
}

using SCfg = GCfg<32, 4, 32, 8, false, 0, 5, 4, 2, 1>;
template <int K>
__global__ __launch_bounds__(256, 1) void k_gemm_sk(const u16* __restrict__ Af, const u16* __restrict__ B, float* __restrict__ P) {
  using C = SCfg;
  extern __shared__ __attribute__((aligned(16))) char lds[];
  constexpr int N = 1024, KSPLIT = 2;
  const unsigned tid = threadIdx.x, lane = tid & 63u, wid = rfl(tid >> 6);
  const int bid = blockIdx.x, xcd = bid & 7, j = bid >> 3;
  const int kh = xcd & 1, mg = xcd >> 1;
  const int mt = mg * 4 + (j & 3), nt = j >> 2;
  constexpr int klen = K / KSPLIT; const int k0 = kh * klen;
  GState<C> g;
  const int mb0 = mt * 4 + wid;
  constexpr unsigned mbstride = (unsigned)K * 64u;
  g.srdA = mk_srd((const char*)Af + (size_t)mb0 * mbstride + (size_t)k0 * 64, (unsigned)klen * 64u);
  g.voffA[0] = lane * 16u;
  constexpr unsigned ldb = (unsigned)N * 2u;
  g.srdB = mk_srd((const char*)B + (size_t)k0 * ldb + (size_t)nt * 256, (unsigned)klen * ldb - (unsigned)nt * 256u);
  g_setup<C>(g, (unsigned)(size_t)lds, lane, wid, ldb);
  gemm_run<C>(g, klen / 64);
#pragma unroll
  for (int n = 0; n < 4; ++n) {
    float* dst = P + ((((size_t)kh * 64 + mb0) * 32 + (nt * 4 + n)) * 4) * 256 + lane * 4;
#pragma unroll
    for (int q = 0; q < 4; ++q) { f32x4 v = {g.acc[n][0][4 * q], g.acc[n][0][4 * q + 1], g.acc[n][0][4 * q + 2], g.acc[n][0][4 * q + 3]}; *(f32x4*)(dst + q * 256) = v; }
  }
}
DI float silu_mul(float g, float u) { return g * __builtin_amdgcn_rcpf(1.f + __builtin_amdgcn_exp2f(g * -1.4426950408889634f)) * u; }
using UCfg = GCfg<32, 8, 32, 14, true, 0, 4, 2, 1, 2>;
__global__ __launch_bounds__(512, 2) void k_gemm_gu(const u16* __restrict__ Af, const u16* __restrict__ Wg, const u16* __restrict__ Wu, u16* __restrict__ Act) {
  using C = UCfg;
  extern __shared__ __attribute__((aligned(16))) char lds[];
  constexpr int K = 1024, F = 3584;
  const unsigned tid = threadIdx.x, lane = tid & 63u, wid = rfl(tid >> 6);
  const int bid = blockIdx.x, xcd = bid & 7, j = bid >> 3;
  const int mt = (xcd & 1) * 4 + (j & 3), nt = (xcd >> 1) * 8 + (j >> 2);
  GState<C> g;
  const int mb0 = mt * 8 + wid;
  constexpr unsigned mbstride = (unsigned)K * 64u;
  g.srdA = mk_srd((const char*)Af + (size_t)mb0 * mbstride, mbstride);
  g.voffA[0] = lane * 16u;
  constexpr unsigned ldb = F * 2u;
  const bool up = wid >= 4;
  g.srdB = mk_srd((const char*)(up ? Wu : Wg) + (size_t)nt * 224, (unsigned)K * ldb - (unsigned)nt * 224u);
  g_setup<C>(g, (unsigned)(size_t)lds, lane, wid, ldb);
  gemm_run<C>(g, K / 64);
#pragma unroll
  for (int n = 0; n < 7; ++n) {
    float o[8];
#pragma unroll
    for (int e = 0; e < 8; ++e) o[e] = silu_mul(g.acc[n][0][e], g.acc[n][0][e + 8]);
    u32x4 w = {cvtpk(o[0], o[1]), cvtpk(o[2], o[3]), cvtpk(o[4], o[5]), cvtpk(o[6], o[7])};
    *(u32x4*)(Act + ((size_t)mb0 * (F >> 4) + (nt * 7 + n)) * 512 + lane * 8) = w;
  }
}

extern "C" void kernel_launch(void* const* d_in, const int* in_sizes, int n_in, void* d_out, int out_size, void* d_ws, size_t ws_size, hipStream_t stream) {
  const u16* x = (const u16*)d_in[0]; const u16* kc = (const u16*)d_in[1]; const u16* vc = (const u16*)d_in[2];
  const u16* wq = (const u16*)d_in[3]; const u16* wo = (const u16*)d_in[4];
  const u16* wg = (const u16*)d_in[6]; const u16* wu = (const u16*)d_in[7]; const u16* wd = (const u16*)d_in[8];
  const u16* c = (const u16*)d_in[9]; const u16* wmod = (const u16*)d_in[10];
  u16* out = (u16*)d_out;
  char* ws = (char*)d_ws;
  float* mod = (float*)ws;
  u16* hf = (u16*)(ws + (1 << 20));
  u16* x1f = (u16*)(ws + (8 << 20));
  u16* of = (u16*)(ws + (16 << 20));
  float* P = (float*)(ws + (32 << 20));
  u16* act = (u16*)(ws + (64 << 20));
  hipFuncSetAttribute((const void*)k_attn, hipFuncAttributeMaxDynamicSharedMemorySize, SHM_ATTN);
  hipFuncSetAttribute((const void*)k_gemm_sk<4096>, hipFuncAttributeMaxDynamicSharedMemorySize, SCfg::LDSB);
  hipFuncSetAttribute((const void*)k_gemm_sk<3584>, hipFuncAttributeMaxDynamicSharedMemorySize, SCfg::LDSB);
  hipFuncSetAttribute((const void*)k_gemm_gu, hipFuncAttributeMaxDynamicSharedMemorySize, UCfg::LDSB);
  k_mod<<<192, 256, 0, stream>>>(c, wmod, mod);
  k_ln<0><<<256, 256, 0, stream>>>(x, nullptr, mod, nullptr, hf);
  k_attn<<<256, 256, SHM_ATTN, stream>>>(hf, wq, kc, vc, of);
  k_gemm_sk<4096><<<256, 256, SCfg::LDSB, stream>>>(of, wo, P);
  k_ln<1><<<256, 256, 0, stream>>>(x, P, mod, x1f, hf);
  k_gemm_gu<<<256, 512, UCfg::LDSB, stream>>>(hf, wg, wu, act);
  k_gemm_sk<3584><<<256, 256, SCfg::LDSB, stream>>>(act, wd, P);
  k_fin<<<256, 256, 0, stream>>>(x1f, P, mod, out);
}
```

```cpp
#include <hip/hip_runtime.h>
#include <utility>
#include <type_traits>
typedef unsigned short u16;
typedef short bf16x8 __attribute__((ext_vector_type(8)));
typedef short s16x4 __attribute__((ext_vector_type(4)));
typedef float f32x4 __attribute__((ext_vector_type(4)));
typedef float f32x16 __attribute__((ext_vector_type(16)));
typedef unsigned u32x4 __attribute__((ext_vector_type(4)));
typedef unsigned u32x2 __attribute__((ext_vector_type(2)));
typedef float f32x2 __attribute__((ext_vector_type(2)));
typedef __bf16 bf16x2_t __attribute__((ext_vector_type(2)));
#define DI __device__ __forceinline__
#define LAS __attribute__((address_space(3)))
#define GAP() __builtin_amdgcn_sched_barrier(0)
template <int... Is> using iseq = std::integer_sequence<int, Is...>;
template <int N> using make_iseq = std::make_integer_sequence<int, N>;

#define KST_BEGIN(k)
#define KST_END(k)
DI unsigned rfl(unsigned x) { return __builtin_amdgcn_readfirstlane(x); }
DI float bf2f(u16 v) { return __uint_as_float(((unsigned)v) << 16); }
DI unsigned cvtpk(float lo, float hi) { f32x2 v = {lo, hi}; bf16x2_t b = __builtin_convertvector(v, bf16x2_t); return __builtin_bit_cast(unsigned, b); }
DI float rbf(float f) { return __uint_as_float(cvtpk(f, 0.f) << 16); }
DI __amdgpu_buffer_rsrc_t mk_srd(const void* p, unsigned bytes) { return __builtin_amdgcn_make_buffer_rsrc((void*)p, (short)0, (int)bytes, 0x00020000); }

#define W4_ACLOB "a0","a1","a2","a3","a4","a5","a6","a7","a8","a9","a10","a11","a12","a13","a14","a15","a16","a17","a18","a19","a20","a21","a22","a23","a24","a25","a26","a27","a28","a29","a30","a31","a32","a33","a34","a35","a36","a37","a38","a39","a40","a41","a42","a43","a44","a45","a46","a47","a48","a49","a50","a51","a52","a53","a54","a55","a56","a57","a58","a59","a60","a61","a62","a63","a64","a65","a66","a67","a68","a69","a70","a71","a72","a73","a74","a75","a76","a77","a78","a79","a80","a81","a82","a83","a84","a85","a86","a87","a88","a89","a90","a91","a92","a93","a94","a95","a96","a97","a98","a99","a100","a101","a102","a103","a104","a105","a106","a107","a108","a109","a110","a111","a112","a113","a114","a115","a116","a117","a118","a119","a120","a121","a122","a123","a124","a125","a126","a127"
#define W4_ACLOB_HI "a128","a129","a130","a131","a132","a133","a134","a135","a136","a137","a138","a139","a140","a141","a142","a143","a144","a145","a146","a147","a148","a149","a150","a151","a152","a153","a154","a155","a156","a157","a158","a159","a160","a161","a162","a163","a164","a165","a166","a167","a168","a169","a170","a171","a172","a173","a174","a175","a176","a177","a178","a179","a180","a181","a182","a183","a184","a185","a186","a187","a188","a189","a190","a191","a192","a193","a194","a195","a196","a197","a198","a199","a200","a201","a202","a203","a204","a205","a206","a207","a208","a209","a210","a211","a212","a213","a214","a215","a216","a217","a218","a219","a220","a221","a222","a223","a224","a225","a226","a227","a228","a229","a230","a231","a232","a233","a234","a235","a236","a237","a238","a239","a240","a241","a242","a243","a244","a245","a246","a247","a248","a249","a250","a251","a252","a253","a254","a255"
template <int SH_, int NW_, int WR_, int NB16_, bool GU_, int KORD_, int NS_, int PDT_, int FD_, int BARPOS_, bool ASMACC_ = false>
struct GCfg {
  static constexpr bool ASMACC = ASMACC_;
  static constexpr int SH = SH_, NW = NW_, WR = WR_, NB16 = NB16_, KORD = KORD_, NS = NS_, PDT = PDT_, FD = FD_, BARPOS = BARPOS_;
  static constexpr bool GU = GU_;
  static constexpr int STEPS = SH == 32 ? 4 : 2;
  static constexpr int MR = WR / SH;
  static constexpr int NB = SH == 32 ? NB16 / 2 : NB16;
  static constexpr int NR16 = GU ? NB16 / 2 : NB16;
  static constexpr int PIECES = NB16 * 2;
  static constexpr int PB = (PIECES + NW - 1) / NW;
  static constexpr int STAGE = PB * NW * 1024;
  static constexpr int REGION = STAGE / 2;
  static constexpr int LDSB = NS * STAGE;
  static constexpr int NOPS = STEPS * MR + PB;
  static constexpr int GS = NB * MR;
  static constexpr int G = STEPS * GS;
  static constexpr int BSTEP = STEPS - FD;
  static constexpr int GB = BSTEP * GS + (BARPOS + 1) * MR - 1;
  static constexpr int dma_lo(int ks) { return (PB * ks) / STEPS; }
  static constexpr int step_ops(int ks) { return MR + dma_lo(ks + 1) - dma_lo(ks); }
  static constexpr int step_first(int ks) { return ks * MR + dma_lo(ks); }
  static constexpr int op_step(int j) { int ks = 0; while (ks < STEPS - 1 && j >= step_first(ks + 1)) ++ks; return ks; }
  static constexpr int op_gap(int j) { const int ks = op_step(j), i = j - step_first(ks), n = step_ops(ks); return ks * GS + (i * GS) / n; }
  static constexpr int ops_before(int g) { int c = 0; for (int j = 0; j < NOPS; ++j) if (op_gap(j) < g) ++c; return c; }
  static constexpr int NA(int ks) {
    const int ts = (ks + 1) % STEPS, jt = step_first(ts) + MR - 1;
    const int full = (ks == STEPS - 1) ? PDT - 1 : PDT;
    return (NOPS - 1 - jt) + (full - 1) * NOPS + ops_before(ks * GS);
  }
  static constexpr int NBW() { return (NS - 3) * NOPS + ops_before(GB); }
  static_assert(NS >= 3 && PDT >= 2 && FD <= STEPS - 0 && BSTEP >= 0, "");
  using acc_t = std::conditional_t<SH == 32, f32x16, f32x4>;
};

template <class C>
struct GState {
  typename C::acc_t acc[C::NB][C::MR];
  bf16x8 a[C::PDT * C::STEPS][C::MR];
  s16x4 blo[C::FD][C::NB], bhi[C::FD][C::NB];
  __amdgpu_buffer_rsrc_t srdA, srdB;
  unsigned voffA[C::MR];
  unsigned voffB[C::GU ? C::PB : 1];
  unsigned sB[C::GU ? 1 : C::PB];
  unsigned m0base, vrd, ktileB;
  unsigned long long tch[3]; unsigned tch_off;
};
struct GIter { unsigned va_cur, va_nxt, sA, sA0, m0_fill, sK_fill; };

template <class C, int SL, int OFF, int MRI> DI void a_load1(GState<C>& g, unsigned sA) {
  asm volatile("buffer_load_dwordx4 %0, %1, %2, %3 offen offset:%4" : "=v"(g.a[SL][MRI]) : "v"(g.voffA[MRI]), "s"(g.srdA), "s"(sA), "i"(OFF) : "memory");
}
template <class C, int Q> DI void b_dma1(GState<C>& g, unsigned m0v, unsigned sK) {
  if constexpr (C::GU)
    asm volatile("s_mov_b32 m0, %0\n\ts_nop 0\n\tbuffer_load_dwordx4 %1, %2, %3 offen lds" :: "s"(m0v + 1024u * Q), "v"(g.voffB[Q]), "s"(g.srdB), "s"(sK) : "memory");
  else
    asm volatile("s_mov_b32 m0, %0\n\ts_nop 0\n\tbuffer_load_dwordx4 %1, %2, %3 offen lds" :: "s"(m0v + 1024u * Q), "v"(g.voffB[0]), "s"(g.srdB), "s"(sK + g.sB[Q]) : "memory");
}
template <class C> DI void g_dummy(GState<C>& g) {
  asm volatile("s_mov_b32 m0, %0\n\ts_nop 0\n\tbuffer_load_dwordx4 %1, %2, %3 offen lds" :: "s"(g.m0base + (unsigned)(C::NS - 1) * C::STAGE), "v"(g.voffA[0]), "s"(g.srdA), "s"(0x80000000u) : "memory");
}
template <class C, int TI, int J, bool VA, bool VB> DI void g_op(GState<C>& g, const GIter& it) {
  constexpr int ks = C::op_step(J), i = J - C::step_first(ks);
  if constexpr (i < C::MR) {
    constexpr int PD = C::PDT * C::STEPS, SL = (TI * C::STEPS + ks - 1 + PD) % PD;
    if constexpr (VA) { if constexpr (ks == 0) a_load1<C, SL, 0, i>(g, it.sA0); else a_load1<C, SL, (ks - 1) * 1024, i>(g, it.sA); } else g_dummy<C>(g);
  } else {
    if constexpr (VB) b_dma1<C, C::dma_lo(ks) + i - C::MR>(g, it.m0_fill, it.sK_fill); else g_dummy<C>(g);
  }
}
template <class C, int TI, int GI, int... Js> DI void g_ops_at(GState<C>& g, const GIter& it, iseq<Js...>) {
  ((C::op_gap(Js) == GI ? g_op<C, TI, Js, true, true>(g, it) : (void)0), ...);
}
template <class C> constexpr int b_off(int KS, int NBI, int t) {
  if (C::SH == 32) {
    return ((2 * KS + t) * C::NR16 + (C::GU ? NBI : 2 * NBI)) * 256;
  } else {
    const int kb = 4 * KS + (C::KORD == 0 ? 2 * t : t);
    return (kb * C::NR16 + (C::GU ? NBI / 2 : NBI)) * 256 + ((C::GU && (NBI & 1)) ? C::REGION : 0);
  }
}
template <class C, int BUF, int KS, int NBI> DI void b_read(GState<C>& g, unsigned va) {
  g.blo[BUF][NBI] = __builtin_amdgcn_ds_read_tr16_b64_v4i16((LAS s16x4*)(size_t)(va + b_off<C>(KS, NBI, 0)));
  g.bhi[BUF][NBI] = __builtin_amdgcn_ds_read_tr16_b64_v4i16((LAS s16x4*)(size_t)(va + b_off<C>(KS, NBI, 1)));
}
template <class C, int BUF, int KS, int... Ns> DI void b_read_all(GState<C>& g, unsigned va, iseq<Ns...>) { (b_read<C, BUF, KS, Ns>(g, va), ...); }

template <class C, int N, int SL> DI void a_wait(GState<C>& g) {
  static_assert(N >= 0 && N < 64, "vmcnt range");
  if constexpr (C::MR == 1) asm volatile("s_waitcnt vmcnt(%1)" : "+v"(g.a[SL][0]) : "i"(N) : "memory");
  else if constexpr (C::MR == 2) asm volatile("s_waitcnt vmcnt(%2)" : "+v"(g.a[SL][0]), "+v"(g.a[SL][1]) : "i"(N) : "memory");
  else asm volatile("s_waitcnt vmcnt(%4)" : "+v"(g.a[SL][0]), "+v"(g.a[SL][1]), "+v"(g.a[SL][2]), "+v"(g.a[SL][3]) : "i"(N) : "memory");
}

template <class C, int TI, int GI> DI void g_gap(GState<C>& g, const GIter& it) {
  constexpr int KS = GI / C::GS, r = GI % C::GS, NBI = r / C::MR, MRI = r % C::MR;
  constexpr int SL = TI * C::STEPS + KS, BUF = (C::FD == 1) ? 0 : (KS & 1), KN = (KS + C::FD) % C::STEPS;
  if constexpr (r == 0) { a_wait<C, C::NA(KS), SL>(g); GAP(); }
  {
    const bf16x8 bf = __builtin_shufflevector(g.blo[BUF][NBI], g.bhi[BUF][NBI], 0, 1, 2, 3, 4, 5, 6, 7);
    if constexpr (C::ASMACC) { constexpr int ao = 16 * (C::MR * NBI + MRI);
      asm volatile("v_mfma_f32_32x32x16_bf16 a[%c2:%c3], %0, %1, a[%c2:%c3]" :: "v"(bf), "v"(g.a[SL][MRI]), "i"(ao), "i"(ao + 15) : W4_ACLOB); }
    else if constexpr (C::SH == 32) g.acc[NBI][MRI] = __builtin_amdgcn_mfma_f32_32x32x16_bf16(bf, g.a[SL][MRI], g.acc[NBI][MRI], 0, 0, 0);
    else g.acc[NBI][MRI] = __builtin_amdgcn_mfma_f32_16x16x32_bf16(bf, g.a[SL][MRI], g.acc[NBI][MRI], 0, 0, 0);
  }
  if constexpr (GI == C::GB) {
    GAP();
    static_assert(C::NBW() < 64, "");
    asm volatile("s_waitcnt vmcnt(%0) lgkmcnt(0)\n\ts_barrier" :: "i"(C::NBW()) : "memory");
    GAP();
    b_read_all<C, BUF, KN>(g, it.va_nxt, make_iseq<C::BARPOS + 1>{});
  } else if constexpr (MRI == C::MR - 1) {
    if constexpr (KS == C::BSTEP) { if constexpr (NBI > C::BARPOS) b_read<C, BUF, KN, NBI>(g, it.va_nxt); }
    else if constexpr (KS > C::BSTEP) b_read<C, BUF, KN, NBI>(g, it.va_nxt);
    else b_read<C, BUF, KN, NBI>(g, it.va_cur);
  }
  g_ops_at<C, TI, GI>(g, it, make_iseq<C::NOPS>{});
  GAP();
}
template <class C, int TI, int... Gs> DI void g_gaps(GState<C>& g, const GIter& it, iseq<Gs...>) { (g_gap<C, TI, Gs>(g, it), ...); }
template <class C, int TI> DI void g_tile(GState<C>& g, int t, unsigned& stg, unsigned& pstg) {
  unsigned nstg = stg + C::STAGE; if (nstg == C::NS * C::STAGE) nstg = 0;
  GIter it;
  it.va_cur = g.vrd + stg; it.va_nxt = g.vrd + nstg;
  it.sA = (unsigned)(C::STEPS * 1024) * (unsigned)(t + C::PDT); it.sA0 = it.sA - 1024u;
  it.m0_fill = g.m0base + pstg; it.sK_fill = g.ktileB * (unsigned)(t - 1 + C::NS);
  g_gaps<C, TI>(g, it, make_iseq<C::G>{});
  pstg = stg; stg = nstg;
}
template <class C, int... TIs> DI void g_body(GState<C>& g, int t, unsigned& stg, unsigned& pstg, iseq<TIs...>) { (g_tile<C, TIs>(g, t + TIs, stg, pstg), ...); }

template <class C, int I> DI void g_touch(GState<C>& g) {
  asm volatile("s_mov_b32 m0, %0\n\ts_nop 0\n\tglobal_load_lds_dword %1, %2" :: "s"(g.m0base + (unsigned)(C::NS - 1) * C::STAGE), "v"(g.tch_off), "s"(g.tch[I]) : "memory");
}
template <class C, int V, int J> DI void g_virt_op(GState<C>& g, const GIter& it) {
  constexpr int ks = C::op_step(J);
  constexpr int TI = ((V % C::PDT) + C::PDT) % C::PDT;
  constexpr bool VA = (C::STEPS * (V + C::PDT) + ks - 1) >= 0, VB = (V - 1 + C::NS) >= 0;
  if constexpr (V == -1 && J == C::ops_before(C::GB)) asm volatile("s_waitcnt vmcnt(%0)\n\ts_barrier" :: "i"(C::NBW()) : "memory");
  if constexpr (C::ASMACC && V == -(C::NS - 1) && J < 2) { static_assert(!VA, ""); g_touch<C, J>(g); }
  else if constexpr (C::ASMACC && V == -(C::NS - 2) && J == 0) { static_assert(!VA, ""); g_touch<C, 2>(g); }
  else g_op<C, TI, J, VA, VB>(g, it);
}
template <class C, int V, int... Js> DI void g_virt(GState<C>& g, iseq<Js...>) {
  GIter it; constexpr int tb = V - 1 + C::NS;
  it.va_cur = it.va_nxt = 0; it.sA = (unsigned)(C::STEPS * 1024) * (unsigned)(V + C::PDT >= 0 ? V + C::PDT : 0); it.sA0 = it.sA - 1024u;
  it.m0_fill = g.m0base + (unsigned)(tb >= 0 ? tb : 0) * C::STAGE; it.sK_fill = g.ktileB * (unsigned)(tb >= 0 ? tb : 0);
  (g_virt_op<C, V, Js>(g, it), ...);
  if constexpr (V == -1 && C::ops_before(C::GB) == C::NOPS) asm volatile("s_waitcnt vmcnt(%0)\n\ts_barrier" :: "i"(C::NBW()) : "memory");
}
template <class C, int... Vs> DI void g_prologue(GState<C>& g, iseq<Vs...>) { constexpr int NV = sizeof...(Vs); (g_virt<C, Vs - NV>(g, make_iseq<C::NOPS>{}), ...); }

template <class C, bool PSWAP = false> DI void g_setup(GState<C>& g, unsigned ldsb, unsigned lane, unsigned wid, unsigned ldb) {
  g.m0base = rfl(ldsb + wid * (unsigned)C::PB * 1024u);
  g.ktileB = 64u * ldb;
  const unsigned q = (lane & 15u) >> 2, p0 = lane & 3u, p = PSWAP ? (((p0 & 1u) << 1) | (p0 >> 1)) : p0, g4 = lane >> 4;
  if constexpr (C::SH == 32) {
    const unsigned h = lane >> 5, blk = g4 & 1u;
    g.vrd = ldsb + blk * (C::GU ? (unsigned)C::REGION : 256u) + ((4u * h + q) ^ (4u * blk)) * 32u + 8u * p;
  } else {
    g.vrd = ldsb + (g4 >> 1) * (C::KORD == 0 ? 1u : 2u) * (unsigned)(C::NR16 * 256) + (4u * (g4 & 1u) + q) * 32u + 8u * p;
  }
  if constexpr (C::GU) {
    const unsigned wr = wid % (C::NW / 2), up = wid / (C::NW / 2);
#pragma unroll
    for (int qq = 0; qq < C::PB; ++qq) {
      const unsigned P = wr * C::PB + qq, s = 4u * P + g4 * 0u + (lane >> 4), kb = s / (unsigned)C::NR16, nb = s % (unsigned)C::NR16;
      const unsigned row = ((lane >> 1) & 7u) ^ ((C::SH == 32 && up) ? 4u : 0u);
      g.voffB[qq] = (P < (unsigned)(C::NR16 * 2)) ? (8u * kb + row) * ldb + 32u * nb + 16u * (lane & 1u) : 0x80000000u;
    }
  } else {
    static_assert(C::GU || (C::NR16 % 4 == 0), "");
    const unsigned sub = lane >> 4;
    const unsigned row = ((lane >> 1) & 7u) ^ ((C::SH == 32 && (sub & 1u)) ? 4u : 0u);
    g.voffB[0] = row * ldb + 32u * sub + 16u * (lane & 1u);
#pragma unroll
    for (int qq = 0; qq < C::PB; ++qq) { const unsigned P = wid * C::PB + qq, kb = P / (unsigned)(C::NR16 / 4), j = P % (unsigned)(C::NR16 / 4); g.sB[qq] = rfl(8u * kb * ldb + 128u * j); }
  }
}

template <int R> DI void acc_zero1() { asm volatile("v_accvgpr_write_b32 a[%c0], 0" :: "i"(R) : W4_ACLOB); }
template <int... Rs> DI void acc_zero(iseq<Rs...>) { (acc_zero1<Rs>(), ...); asm volatile("s_nop 1"); }
template <class C> DI void gemm_run(GState<C>& g, int NT) {
  constexpr int NV = C::PDT > C::NS - 1 ? C::PDT : C::NS - 1;
  if constexpr (!C::ASMACC) {
#pragma unroll
    for (int n = 0; n < C::NB; ++n)
#pragma unroll
      for (int m = 0; m < C::MR; ++m)
#pragma unroll
        for (int e = 0; e < (C::SH == 32 ? 16 : 4); ++e) g.acc[n][m][e] = 0.f;
  }
  g_prologue<C>(g, make_iseq<NV>{});
  if constexpr (C::ASMACC) acc_zero(make_iseq<C::NB * C::MR * 16>{});
  b_read_all<C, 0, 0>(g, g.vrd, make_iseq<C::NB>{});
  if constexpr (C::FD == 2) b_read_all<C, 1, 1>(g, g.vrd, make_iseq<C::NB>{});
  GAP();
  unsigned stg = 0, pstg = (C::NS - 1) * C::STAGE;
  for (int t = 0; t < NT; t += C::PDT) g_body<C>(g, t, stg, pstg, make_iseq<C::PDT>{});
  asm volatile("s_waitcnt vmcnt(0) lgkmcnt(0)" ::: "memory");
#pragma unroll
  for (int i = 0; i < C::PDT * C::STEPS; ++i)
#pragma unroll
    for (int m = 0; m < C::MR; ++m) asm volatile("" :: "v"(g.a[i][m]));
  GAP();
}
typedef __bf16 abf16x8 __attribute__((ext_vector_type(8)));
typedef __bf16 abf16x4 __attribute__((ext_vector_type(4)));
#define ATTN_THR 64.0f
constexpr unsigned KRS = 256u, KTILEB = 64u * KRS;
#define KRING 0x0000u
#define VRING 0x8000u
#define QGEMM_LDS 0x10000u
DI float max3f(float a, float b, float c) { float r; asm("v_max3_f32 %0, %1, %2, %3" : "=v"(r) : "v"(a), "v"(b), "v"(c)); return r; }
DI float max2f(float a, float b) { float r; asm("v_max_f32_e32 %0, %1, %2" : "=v"(r) : "v"(a), "v"(b)); return r; }
DI float fadd_s(float a, float b) { float r; asm("v_add_f32_e32 %0, %1, %2" : "=v"(r) : "v"(a), "v"(b)); return r; }
DI float fmul_s(float a, float b) { float r; asm("v_mul_f32_e32 %0, %1, %2" : "=v"(r) : "v"(a), "v"(b)); return r; }
DI float fsub_s(float a, float b) { float r; asm("v_sub_f32_e32 %0, %1, %2" : "=v"(r) : "v"(a), "v"(b)); return r; }
DI float w4_aone() { unsigned l; asm volatile("v_mbcnt_lo_u32_b32 %0, -1, 0\n\tv_mbcnt_hi_u32_b32 %0, -1, %0" : "=v"(l)); return l < 32u ? 1.f : 0.f; }
DI void negm_bcast(f32x16& negm, float aone, float neg_mhat) {
  asm volatile("s_nop 1\n\tv_mfma_f32_32x32x2_f32 %0, %1, %2, 0\n\ts_nop 7\n\ts_nop 7\n\ts_nop 3" : "+v"(negm) : "v"(aone), "v"(neg_mhat));
}
struct W4C {
  unsigned vkb[4], vvb, vksrc[2], vvsrc, m0k, m0v, sofk, sofv;
  __amdgpu_buffer_rsrc_t srdK, srdV;
  f32x16 S[2][2][2];
  f32x16 NEGM[2];
  abf16x8 Vf[4][4];
  abf16x8 Pk[2][4];
  abf16x4 vlo[4][4], vup[4][4];
  float mhat[2], lsum[2], fct[2]; bool pend;
  float rs0[2], rs1[2], pmx0[2], pmx1[2], rmax[2];
  unsigned pm0[8], psf[8];
  unsigned pf_lo, pf_hi, pf_off, pf_m0;
};
constexpr int AO_(int db, int qb) { return 16 * (2 * db + qb); }
constexpr int AQ_(int qb, int d0) { return 128 + 32 * qb + 4 * d0; }
constexpr int AK_(int kb, int d0) { return 192 + 32 * kb + 4 * d0; }

template <int I> DI void tr_read(W4C& c, unsigned vslot) {
  constexpr int ks = I >> 3, db = (I >> 1) & 3;
  if constexpr ((I & 1) == 0) asm volatile("ds_read_b64_tr_b16 %0, %1 offset:%2" : "=v"(c.vlo[db][ks]) : "v"(c.vvb + vslot), "i"(4096 * ks + 512 * db) : "memory");
  else                        asm volatile("ds_read_b64_tr_b16 %0, %1 offset:%2" : "=v"(c.vup[db][ks]) : "v"(c.vvb + vslot), "i"(4096 * ks + 2048 + 512 * db) : "memory");
}
template <int I> DI void k_read(W4C& c, unsigned slot) {
  constexpr int kb = I >> 3, d0 = I & 7;
  asm volatile("ds_read_b128 a[%c1:%c2], %0 offset:%c3" :: "v"(c.vkb[d0 & 3] + slot), "i"(AK_(kb, d0)), "i"(AK_(kb, d0) + 3), "i"(8192 * kb + 128 * (d0 >> 2)) : "memory", W4_ACLOB_HI);
}
template <int X, bool FIRST, int M> DI void qk_mfma(W4C& c) {
  constexpr int d0 = M >> 2, kb = (M >> 1) & 1, qb = M & 1, ak = AK_(kb, d0), aq = AQ_(qb, d0);
  if constexpr (d0 == 0) {
    if constexpr (FIRST) asm volatile("v_mfma_f32_32x32x16_bf16 %0, a[%c1:%c2], a[%c3:%c4], 0" : "=&v"(c.S[X][kb][qb]) : "i"(ak), "i"(ak + 3), "i"(aq), "i"(aq + 3) : W4_ACLOB, W4_ACLOB_HI);
    else                 asm volatile("v_mfma_f32_32x32x16_bf16 %0, a[%c2:%c3], a[%c4:%c5], %1" : "=&v"(c.S[X][kb][qb]) : "v"(c.NEGM[qb]), "i"(ak), "i"(ak + 3), "i"(aq), "i"(aq + 3) : W4_ACLOB, W4_ACLOB_HI);
  } else asm volatile("v_mfma_f32_32x32x16_bf16 %0, a[%c1:%c2], a[%c3:%c4], %0" : "+v"(c.S[X][kb][qb]) : "i"(ak), "i"(ak + 3), "i"(aq), "i"(aq + 3) : W4_ACLOB, W4_ACLOB_HI);
}
template <int M, bool ZERO> DI void pv_mfma(W4C& c) {
  constexpr int ks = M >> 3, db = (M >> 1) & 3, qb = M & 1, ao = AO_(db, qb);
  if constexpr (ZERO && ks == 0) asm volatile("v_mfma_f32_32x32x16_bf16 a[%c2:%c3], %0, %1, 0" :: "v"(c.Vf[db][ks]), "v"(c.Pk[qb][ks]), "i"(ao), "i"(ao + 15) : W4_ACLOB);
  else                         asm volatile("v_mfma_f32_32x32x16_bf16 a[%c2:%c3], %0, %1, a[%c2:%c3]" :: "v"(c.Vf[db][ks]), "v"(c.Pk[qb][ks]), "i"(ao), "i"(ao + 15) : W4_ACLOB);
}
template <int X, int QB, int KS, int P> DI void pack1(W4C& c) {
  u32x4 w = __builtin_bit_cast(u32x4, c.Pk[QB][KS]);
  w[P] = cvtpk(c.S[X][KS >> 1][QB][8 * (KS & 1) + 2 * P], c.S[X][KS >> 1][QB][8 * (KS & 1) + 2 * P + 1]);
  c.Pk[QB][KS] = __builtin_bit_cast(abf16x8, w);
}
template <int X, int J> DI void rowsum1(W4C& c) {
  constexpr int qb = J >> 4, ks = (J >> 2) & 3, p = J & 3;
  c.rs0[qb] = fadd_s(c.rs0[qb], c.S[X][ks >> 1][qb][8 * (ks & 1) + 2 * p]); c.rs1[qb] = fadd_s(c.rs1[qb], c.S[X][ks >> 1][qb][8 * (ks & 1) + 2 * p + 1]);
}
template <int X, bool FIRST, int M> DI void gapA(W4C& c, unsigned vslot, const int t) {
  constexpr int Xp = X ^ 1;
  qk_mfma<X, FIRST, M>(c);
  if constexpr (M >= 16 && M <= 31) {
    constexpr int q = (M - 16) >> 1;
    if constexpr (((M - 16) & 1) == 0) {
      if constexpr (q < 4) c.pm0[q] = c.m0k + (unsigned)((t + 2) & 1) * 0x4000u + 1024u * q; else c.pm0[q] = c.m0v + (unsigned)((t + 1) & 1) * 0x4000u + 1024u * (q - 4);
      asm volatile("" : "+s"(c.pm0[q]));
    } else {
      if constexpr (q < 4) c.psf[q] = KTILEB * (unsigned)(t + 2) + c.sofk + KRS * 4u * q; else c.psf[q] = KTILEB * (unsigned)(t + 1) + c.sofv + KRS * 8u * ((q - 4) >> 1) + 128u * ((q - 4) & 1);
      asm volatile("" : "+s"(c.psf[q]));
    }
  }
  if constexpr (M == 31) asm volatile("s_nop 4" : "+s"(c.pm0[0]), "+s"(c.pm0[1]), "+s"(c.pm0[2]), "+s"(c.pm0[3]), "+s"(c.pm0[4]), "+s"(c.pm0[5]), "+s"(c.pm0[6]), "+s"(c.pm0[7]),
                                                  "+s"(c.psf[0]), "+s"(c.psf[1]), "+s"(c.psf[2]), "+s"(c.psf[3]), "+s"(c.psf[4]), "+s"(c.psf[5]), "+s"(c.psf[6]), "+s"(c.psf[7]));
  if constexpr (!FIRST && M == 8) asm volatile("" :: "v"(c.NEGM[0]), "v"(c.NEGM[1]));
  if constexpr (!FIRST) {
    constexpr int base = M + (M >= 4) + (M >= 8) + (M >= 12) + (M >= 16) + (M >= 20) + (M >= 24);
    if constexpr (M <= 25) tr_read<base>(c, vslot);
    if constexpr (M == 3 || M == 7 || M == 11 || M == 15 || M == 19 || M == 23) tr_read<base + 1>(c, vslot);
    if constexpr (M < 16) {
      constexpr int e0 = 2 * M, e1 = 2 * M + 1;
      c.S[Xp][1][e0 >> 4][e0 & 15] = __builtin_amdgcn_exp2f(c.S[Xp][1][e0 >> 4][e0 & 15]);
      c.S[Xp][1][e1 >> 4][e1 & 15] = __builtin_amdgcn_exp2f(c.S[Xp][1][e1 >> 4][e1 & 15]);
      pack1<Xp, (M >> 3), ((M >> 2) & 1), (M & 3)>(c);
    } else {
      constexpr int cc = M - 16;
      pack1<Xp, (cc >> 3), 2 + ((cc >> 2) & 1), (cc & 3)>(c);
      rowsum1<Xp, 16 * (cc >> 3) + 8 + 4 * ((cc >> 2) & 1) + (cc & 3)>(c);
    }
  }
  GAP();
}
#define W4_VLO "+v"(c.vlo[0][0]), "+v"(c.vlo[0][1]), "+v"(c.vlo[0][2]), "+v"(c.vlo[0][3]), "+v"(c.vlo[1][0]), "+v"(c.vlo[1][1]), "+v"(c.vlo[1][2]), "+v"(c.vlo[1][3]), \
               "+v"(c.vlo[2][0]), "+v"(c.vlo[2][1]), "+v"(c.vlo[2][2]), "+v"(c.vlo[2][3]), "+v"(c.vlo[3][0]), "+v"(c.vlo[3][1]), "+v"(c.vlo[3][2]), "+v"(c.vlo[3][3])
#define W4_VUP "+v"(c.vup[0][0]), "+v"(c.vup[0][1]), "+v"(c.vup[0][2]), "+v"(c.vup[0][3]), "+v"(c.vup[1][0]), "+v"(c.vup[1][1]), "+v"(c.vup[1][2]), "+v"(c.vup[1][3]), \
               "+v"(c.vup[2][0]), "+v"(c.vup[2][1]), "+v"(c.vup[2][2]), "+v"(c.vup[2][3]), "+v"(c.vup[3][0]), "+v"(c.vup[3][1]), "+v"(c.vup[3][2]), "+v"(c.vup[3][3])
template <int X, bool FIRST, int... Ms> DI void phaseA_(W4C& c, const int t, iseq<Ms...>) {
  const unsigned vslot = (unsigned)((t - 1) & 1) * 0x4000u;
  (gapA<X, FIRST, Ms>(c, vslot, t), ...);
  if constexpr (!FIRST) {
#pragma unroll
    for (int qb = 0; qb < 2; ++qb) {
      const float rs = fadd_s(c.rs0[qb], c.rs1[qb]);
      auto pr = __builtin_amdgcn_permlane32_swap(__float_as_uint(rs), __float_as_uint(rs), false, false);
      c.lsum[qb] = fadd_s(c.lsum[qb], fadd_s(__uint_as_float(pr[0]), __uint_as_float(pr[1])));
    }
  }
  if constexpr (FIRST) asm volatile("s_waitcnt vmcnt(0) lgkmcnt(0)\n\ts_barrier" ::: "memory");
  else { asm volatile("s_waitcnt vmcnt(%c16) lgkmcnt(0)" : W4_VLO : "i"(X == 1 ? 1 : 0) : "memory"); asm volatile("s_barrier" : W4_VUP :: "memory"); }
  GAP();
  if constexpr (!FIRST) {
#pragma unroll
    for (int ks = 0; ks < 4; ++ks)
#pragma unroll
      for (int db = 0; db < 4; ++db) c.Vf[db][ks] = __builtin_shufflevector(c.vlo[db][ks], c.vup[db][ks], 0, 1, 2, 3, 4, 5, 6, 7);
  }
}
template <int X, bool FIRST> DI void phaseA(W4C& c, const int t) { phaseA_<X, FIRST>(c, t, make_iseq<32>{}); }

template <int X, bool PEEL, bool ZERO1, int M> DI void gapB(W4C& c, const unsigned kslot_rd, const int t) {
  if constexpr (!PEEL) pv_mfma<M, ZERO1>(c);
  if constexpr (M < 4)      asm volatile("s_mov_b32 m0, %0\n\ts_nop 0\n\tbuffer_load_dwordx4 %1, %2, %3 offen lds" :: "s"(c.pm0[M]), "v"(c.vksrc[M & 1]), "s"(c.srdK), "s"(c.psf[M]) : "memory");
  else if constexpr (M < 8) asm volatile("s_mov_b32 m0, %0\n\ts_nop 0\n\tbuffer_load_dwordx4 %1, %2, %3 offen lds" :: "s"(c.pm0[M]), "v"(c.vvsrc), "s"(c.srdV), "s"(c.psf[M]) : "memory");
  if constexpr (X == 0 && M == 20) {
    unsigned long long pb; unsigned plo, phi;
    asm volatile("v_readlane_b32 %0, %2, %4\n\tv_readlane_b32 %1, %3, %4" : "=s"(plo), "=s"(phi) : "v"(c.pf_lo), "v"(c.pf_hi), "s"((unsigned)(t >> 1)));
    pb = ((unsigned long long)phi << 32) | plo;
    asm volatile("s_mov_b32 m0, %2\n\ts_nop 4\n\tglobal_load_lds_dword %0, %1" :: "v"(c.pf_off), "s"(pb), "s"(c.pf_m0) : "memory");
  }
  if constexpr (M >= 1 && M <= 16) k_read<M - 1>(c, kslot_rd);
  if constexpr (M >= 8 && M < 16) {
    constexpr int i = M - 8, q = i >> 2, r = 4 * (i & 3);
    if constexpr ((i & 3) == 0) { c.pmx0[q] = max3f(c.S[X][0][q][0], c.S[X][0][q][1], c.S[X][1][q][0]); c.pmx1[q] = max3f(c.S[X][0][q][2], c.S[X][0][q][3], c.S[X][1][q][1]);
                                  c.pmx0[q] = max3f(c.pmx0[q], c.S[X][1][q][2], c.S[X][1][q][3]); }
    else                        { c.pmx0[q] = max3f(c.pmx0[q], c.S[X][0][q][r], c.S[X][0][q][r + 1]); c.pmx1[q] = max3f(c.pmx1[q], c.S[X][0][q][r + 2], c.S[X][0][q][r + 3]);
                                  c.pmx0[q] = max3f(c.pmx0[q], c.S[X][1][q][r], c.S[X][1][q][r + 1]); c.pmx1[q] = max3f(c.pmx1[q], c.S[X][1][q][r + 2], c.S[X][1][q][r + 3]); }
  }
  if constexpr (M == 16 || M == 17) {
    constexpr int q = M - 16; const float mm = max2f(c.pmx0[q], c.pmx1[q]);
    auto pr = __builtin_amdgcn_permlane32_swap(__float_as_uint(mm), __float_as_uint(mm), false, false);
    c.rmax[q] = max2f(__uint_as_float(pr[0]), __uint_as_float(pr[1]));
  }
  if constexpr (M == 18) {
    if constexpr (PEEL) {
#pragma unroll
      for (int q = 0; q < 2; ++q) { c.mhat[q] = c.rmax[q]; c.lsum[q] = 0.f; c.fct[q] = 1.f;
#pragma unroll
        for (int kb = 0; kb < 2; ++kb) { _Pragma("unroll") for (int e = 0; e < 16; ++e) c.S[X][kb][q][e] = fsub_s(c.S[X][kb][q][e], c.rmax[q]); }
        negm_bcast(c.NEGM[q], w4_aone(), -c.mhat[q]);
      }
    } else {
      const bool grew = (c.rmax[0] > ATTN_THR) | (c.rmax[1] > ATTN_THR);
      if (__builtin_expect(__any((int)grew), 0)) {
#pragma unroll
        for (int q = 0; q < 2; ++q) { const float dl = max2f(c.rmax[q], 0.f); c.mhat[q] = fadd_s(c.mhat[q], dl); c.fct[q] = __builtin_amdgcn_exp2f(-dl);
#pragma unroll
          for (int kb = 0; kb < 2; ++kb) { _Pragma("unroll") for (int e = 0; e < 16; ++e) c.S[X][kb][q][e] = fsub_s(c.S[X][kb][q][e], dl); }
          negm_bcast(c.NEGM[q], w4_aone(), -c.mhat[q]);
        }
        c.pend = true;
      }
    }
  }
  if constexpr (M == 18) { c.rs0[0] = c.rs0[1] = c.rs1[0] = c.rs1[1] = 0.f; }
  if constexpr (M >= 19) {
    constexpr int g = M - 19, e_lo = (32 * g) / 13, e_hi = (32 * (g + 1)) / 13;
    {
      constexpr int p_lo = g ? (32 * (g - 1)) / 13 : 0, p_hi = g ? (32 * g) / 13 : 0;
      if constexpr (p_lo + 0 < p_hi) { constexpr int e = p_lo + 0; if constexpr (e & 1) c.rs1[e >> 4] = fadd_s(c.rs1[e >> 4], c.S[X][0][e >> 4][e & 15]); else c.rs0[e >> 4] = fadd_s(c.rs0[e >> 4], c.S[X][0][e >> 4][e & 15]); }
      if constexpr (p_lo + 1 < p_hi) { constexpr int e = p_lo + 1; if constexpr (e & 1) c.rs1[e >> 4] = fadd_s(c.rs1[e >> 4], c.S[X][0][e >> 4][e & 15]); else c.rs0[e >> 4] = fadd_s(c.rs0[e >> 4], c.S[X][0][e >> 4][e & 15]); }
      if constexpr (p_lo + 2 < p_hi) { constexpr int e = p_lo + 2; if constexpr (e & 1) c.rs1[e >> 4] = fadd_s(c.rs1[e >> 4], c.S[X][0][e >> 4][e & 15]); else c.rs0[e >> 4] = fadd_s(c.rs0[e >> 4], c.S[X][0][e >> 4][e & 15]); }
    }
    if constexpr (e_lo + 0 < e_hi) c.S[X][0][(e_lo + 0) >> 4][(e_lo + 0) & 15] = __builtin_amdgcn_exp2f(c.S[X][0][(e_lo + 0) >> 4][(e_lo + 0) & 15]);
    if constexpr (e_lo + 1 < e_hi) c.S[X][0][(e_lo + 1) >> 4][(e_lo + 1) & 15] = __builtin_amdgcn_exp2f(c.S[X][0][(e_lo + 1) >> 4][(e_lo + 1) & 15]);
    if constexpr (e_lo + 2 < e_hi) c.S[X][0][(e_lo + 2) >> 4][(e_lo + 2) & 15] = __builtin_amdgcn_exp2f(c.S[X][0][(e_lo + 2) >> 4][(e_lo + 2) & 15]);
  }
  if constexpr (M == 31) asm volatile("" :: "v"(c.S[X][0][0]), "v"(c.S[X][0][1]), "v"(c.NEGM[0]), "v"(c.NEGM[1]), "v"(c.rs0[0]), "v"(c.rs0[1]), "v"(c.rs1[0]), "v"(c.rs1[1]));
  GAP();
}
template <int R> DI void resc4(float f) {
  float t0, t1, t2, t3;
  asm volatile("v_accvgpr_read_b32 %0, a[%c5]\n\tv_accvgpr_read_b32 %1, a[%c6]\n\tv_accvgpr_read_b32 %2, a[%c7]\n\tv_accvgpr_read_b32 %3, a[%c8]\n\t"
               "v_mul_f32 %0, %4, %0\n\tv_mul_f32 %1, %4, %1\n\tv_mul_f32 %2, %4, %2\n\tv_mul_f32 %3, %4, %3\n\t"
               "v_accvgpr_write_b32 a[%c5], %0\n\tv_accvgpr_write_b32 a[%c6], %1\n\tv_accvgpr_write_b32 a[%c7], %2\n\tv_accvgpr_write_b32 a[%c8], %3"
               : "=&v"(t0), "=&v"(t1), "=&v"(t2), "=&v"(t3) : "v"(f), "i"(R), "i"(R + 1), "i"(R + 2), "i"(R + 3) : W4_ACLOB);
}
template <int... Is> DI void resc_all(W4C& c, iseq<Is...>) { (resc4<4 * Is>(c.fct[(Is >> 2) & 1]), ...); }
template <int X, bool PEEL, bool ZERO1, int... Ms> DI void phaseB_(W4C& c, const int t, iseq<Ms...>) {
  const unsigned kslot_rd = (unsigned)((t + 1) & 1) * 0x4000u;
  if constexpr (!PEEL) asm volatile("s_nop 1" ::: "memory");
  GAP();
  (gapB<X, PEEL, ZERO1, Ms>(c, kslot_rd, t), ...);
  {
#pragma unroll
    for (int e = (32 * 12) / 13; e < 32; ++e) { if (e & 1) c.rs1[e >> 4] = fadd_s(c.rs1[e >> 4], c.S[X][0][e >> 4][e & 15]); else c.rs0[e >> 4] = fadd_s(c.rs0[e >> 4], c.S[X][0][e >> 4][e & 15]); }
  }
  if constexpr (!PEEL) {
    if (__builtin_expect(c.pend, 0)) {
      asm volatile("s_nop 7\n\ts_nop 7" ::: W4_ACLOB);
      resc_all(c, make_iseq<32>{});
      asm volatile("s_nop 1" ::: W4_ACLOB);
      c.lsum[0] = fmul_s(c.lsum[0], c.fct[0]); c.lsum[1] = fmul_s(c.lsum[1], c.fct[1]); c.pend = false;
    }
  }
  asm volatile("s_waitcnt lgkmcnt(0)" ::: "memory");
  GAP();
}
template <int X, bool PEEL, bool ZERO1> DI void phaseB(W4C& c, const int t) { phaseB_<X, PEEL, ZERO1>(c, t, make_iseq<32>{}); }
template <int... Is> DI void k_reads(W4C& c, unsigned slot, iseq<Is...>) { (k_read<Is>(c, slot), ...); }
template <int... Is> DI void tr_reads(W4C& c, unsigned vslot, iseq<Is...>) { (tr_read<Is>(c, vslot), ...); }
template <int X, int... Cs> DI void packs(W4C& c, iseq<Cs...>) { (pack1<X, ((Cs >> 3) & 1), 2 * (Cs >> 4) + ((Cs >> 2) & 1), (Cs & 3)>(c), ...); }
template <int X, int J> DI void rowsum1_padded(W4C& c) { rowsum1<X, J>(c); asm volatile("s_nop 1"); }
template <int X, int... Js> DI void rowsums(W4C& c, iseq<Js...>) { (rowsum1_padded<X, Js>(c), ...); }
template <int... Ms> DI void pv_all(W4C& c, iseq<Ms...>) { (pv_mfma<Ms, false>(c), ...); }
template <int R> DI u32x4 acc_pack8(float f) {
  float o[8];
  asm volatile("v_accvgpr_read_b32 %0, a[%c8]\n\tv_accvgpr_read_b32 %1, a[%c9]\n\tv_accvgpr_read_b32 %2, a[%c10]\n\tv_accvgpr_read_b32 %3, a[%c11]\n\t"
               "v_accvgpr_read_b32 %4, a[%c12]\n\tv_accvgpr_read_b32 %5, a[%c13]\n\tv_accvgpr_read_b32 %6, a[%c14]\n\tv_accvgpr_read_b32 %7, a[%c15]"
               : "=v"(o[0]), "=v"(o[1]), "=v"(o[2]), "=v"(o[3]), "=v"(o[4]), "=v"(o[5]), "=v"(o[6]), "=v"(o[7])
               : "i"(R), "i"(R + 1), "i"(R + 2), "i"(R + 3), "i"(R + 4), "i"(R + 5), "i"(R + 6), "i"(R + 7));
  u32x4 w = {cvtpk(o[0] * f, o[1] * f), cvtpk(o[2] * f, o[3] * f), cvtpk(o[4] * f, o[5] * f), cvtpk(o[6] * f, o[7] * f)};
  return w;
}
template <int I> DI void q_conv1(float C) {
  constexpr int qb = I >> 3, d0 = I & 7, nb = d0 >> 1, s = d0 & 1, aq = AQ_(qb, d0);
  const u32x4 w = acc_pack8<AO_(nb, qb) + 8 * s>(C);
  asm volatile("v_accvgpr_write_b32 a[%c4], %0\n\tv_accvgpr_write_b32 a[%c5], %1\n\tv_accvgpr_write_b32 a[%c6], %2\n\tv_accvgpr_write_b32 a[%c7], %3"
               :: "v"(w[0]), "v"(w[1]), "v"(w[2]), "v"(w[3]), "i"(aq), "i"(aq + 1), "i"(aq + 2), "i"(aq + 3) : W4_ACLOB_HI);
}
template <int... Is> DI void q_conv(float C, iseq<Is...>) { (q_conv1<Is>(C), ...); }
template <int I> DI void o_store1(char* base, float il0, float il1) {
  constexpr int qb = I >> 3, db = (I >> 1) & 3, s = I & 1;
  const u32x4 w = acc_pack8<AO_(db, qb) + 8 * s>(qb ? il1 : il0);
  *(u32x4*)(base + (size_t)qb * (256u * 1024u) + (2 * db + s) * 1024) = w;
}
template <int... Is> DI void o_store(char* base, float il0, float il1, iseq<Is...>) { (o_store1<Is>(base, il0, il1), ...); }

using QCfg = GCfg<32, 4, 64, 8, false, 0, 6, 2, 1, 1, true>;
constexpr int SHM_ATTN = 0x10000 + QCfg::LDSB;

#define ST(i)
__global__ __launch_bounds__(256, 1) void k_attn(const u16* __restrict__ H1f, const u16* __restrict__ Wq, const u16* __restrict__ Kc, const u16* __restrict__ Vc, u16* __restrict__ Of,
                                                        const u16* __restrict__ Wo, const u16* __restrict__ Wg, const u16* __restrict__ Wu, const u16* __restrict__ Wd, const u16* __restrict__ Wm) {
  ST(0); KST_BEGIN(2);
  extern __shared__ __attribute__((aligned(16))) char lds[];
  const unsigned tid = threadIdx.x, wid = rfl(tid >> 6);
  const int bid = blockIdx.x, xcd = bid & 7, j = bid >> 3;
  const int head = 4 * xcd + (j & 3), y = j >> 2, kvh = head >> 3;
  const unsigned ldsb = (unsigned)(size_t)lds;
  {
    const unsigned lane = tid & 63u;
    GState<QCfg> g;
    const int mb0 = y * 8 + wid * 2;
    constexpr unsigned mbstride = 1024u * 32u * 2u;
    g.srdA = mk_srd((const char*)H1f + (size_t)mb0 * mbstride, 2 * mbstride);
    g.voffA[0] = lane * 16u; g.voffA[1] = lane * 16u + mbstride;
    constexpr unsigned ldb = 4096u * 2u;
    g.srdB = mk_srd((const char*)Wq + (size_t)head * 256, 1024u * ldb - (unsigned)head * 256u);
    g_setup<QCfg, true>(g, ldsb + QGEMM_LDS, lane, wid, ldb);
    { const unsigned gw = blockIdx.x * 4u + wid;
      g.tch[0] = (unsigned long long)(size_t)((const char*)Wq + (size_t)gw * 4096); g.tch[1] = g.tch[0] + (4096u * 1024u);
      g.tch[2] = (unsigned long long)(size_t)((gw < 512u ? (const char*)Kc : (const char*)Vc) + (size_t)(gw & 511u) * 4096);
      g.tch_off = lane * 64u; }
    gemm_run<QCfg>(g, 16);
  }
  ST(1);
  unsigned lane; asm volatile("v_mbcnt_lo_u32_b32 %0, -1, 0\n\tv_mbcnt_hi_u32_b32 %0, -1, %0" : "=v"(lane));
  const unsigned l32 = lane & 31u, hi = lane >> 5;
  constexpr int T = 32;
  W4C c;
#pragma unroll
  for (int jj = 0; jj < 4; ++jj) c.vkb[jj] = ldsb + KRING + 256u * l32 + 16u * ((2u * jj + hi) ^ (l32 & 7u));
  c.vvb = ldsb + VRING + (((lane & 3u) << 3) | (((lane >> 2) & 3u) << 6) | (((lane >> 4) & 1u) << 5) | (hi << 8));
#pragma unroll
  for (int par = 0; par < 2; ++par) c.vksrc[par] = KRS * (lane >> 4) + 16u * ((lane & 15u) ^ (4u * par + (lane >> 4)));
  c.vvsrc = KRS * ((lane >> 2) & 7u) + 64u * hi + 16u * (lane & 3u);
  c.m0k = rfl(ldsb + KRING + 4096u * wid); c.m0v = rfl(ldsb + VRING + 4096u * wid);
  c.sofk = rfl(KRS * 16u * wid); c.sofv = rfl(KRS * 16u * wid);
  c.srdK = mk_srd((const char*)Kc + (size_t)kvh * (2048u * 256u), 2048u * 256u);
  c.srdV = mk_srd((const char*)Vc + (size_t)kvh * (2048u * 256u), 2048u * 256u);
  {
    const unsigned gw = blockIdx.x * 4u + wid;
    const char* p;
    const unsigned c2 = gw + 1024u, c2c = c2 < 1792u ? c2 : gw;
    if (lane == 0) p = (const char*)Wo + (size_t)gw * 4096; else if (lane == 1) p = (const char*)Wo + (size_t)c2 * 4096;
    else if (lane == 2) p = (const char*)Wg + (size_t)gw * 4096; else if (lane == 3) p = (const char*)Wg + (size_t)c2c * 4096;
    else if (lane == 4) p = (const char*)Wu + (size_t)gw * 4096; else if (lane == 5) p = (const char*)Wu + (size_t)c2c * 4096;
    else if (lane == 6) p = (const char*)Wd + (size_t)gw * 4096; else if (lane == 7) p = (const char*)Wd + (size_t)c2c * 4096;
    else if (lane < 11) p = (const char*)Wm + (size_t)(gw + 1024u * (lane - 8u)) * 4096;
    else p = (const char*)Wo + (size_t)gw * 4096;
    c.pf_lo = (unsigned)(size_t)p; c.pf_hi = (unsigned)((size_t)p >> 32);
    c.pf_off = lane * 64u; c.pf_m0 = rfl(ldsb + QGEMM_LDS + 256u * wid);
  }
  auto dma_k = [&](int tk) {
    const unsigned slot = (unsigned)(tk & 1) * 0x4000u, skv = KTILEB * (unsigned)tk;
#pragma unroll
    for (int q = 0; q < 4; ++q)
      asm volatile("s_nop 4\n\ts_mov_b32 m0, %0\n\ts_nop 0\n\tbuffer_load_dwordx4 %1, %2, %3 offen lds"
                   :: "s"(rfl(c.m0k + slot + 1024u * q)), "v"(c.vksrc[q & 1]), "s"(c.srdK), "s"(rfl(skv + c.sofk + KRS * 4u * q)) : "memory");
  };
  auto dma_v = [&](int tv) {
    const unsigned slot = (unsigned)(tv & 1) * 0x4000u, skv = KTILEB * (unsigned)tv;
#pragma unroll
    for (int q = 0; q < 4; ++q)
      asm volatile("s_nop 4\n\ts_mov_b32 m0, %0\n\ts_nop 0\n\tbuffer_load_dwordx4 %1, %2, %3 offen lds"
                   :: "s"(rfl(c.m0v + slot + 1024u * q)), "v"(c.vvsrc), "s"(c.srdV), "s"(rfl(skv + c.sofv + KRS * 8u * (q >> 1) + 128u * (q & 1))) : "memory");
  };
  dma_k(0); dma_v(0); dma_k(1);
  asm volatile("s_nop 7\n\ts_nop 7" ::: "memory");
  q_conv(0.12751743082f, make_iseq<16>{});
  asm volatile("s_waitcnt vmcnt(0)\n\ts_barrier" ::: "memory");
  k_reads(c, 0u, make_iseq<16>{});
  asm volatile("s_waitcnt lgkmcnt(0)\n\ts_nop 1" ::: "memory");
  c.mhat[0] = c.mhat[1] = 0.f; c.lsum[0] = c.lsum[1] = 0.f; c.fct[0] = c.fct[1] = 1.f; c.pend = false;
  ST(2);
  phaseA<0, true>(c, 0);  phaseB<0, true, false>(c, 0);
  phaseA<1, false>(c, 1); phaseB<1, false, true>(c, 1);
  for (int t = 2; t < T; t += 2) {
    phaseA<0, false>(c, t);     phaseB<0, false, false>(c, t);
    phaseA<1, false>(c, t + 1); phaseB<1, false, false>(c, t + 1);
  }
  ST(3);
  {
    const unsigned vslot = (unsigned)((T - 1) & 1) * 0x4000u;
    tr_reads(c, vslot, make_iseq<32>{});
#pragma unroll
    for (int e = 0; e < 32; ++e) c.S[1][1][e >> 4][e & 15] = __builtin_amdgcn_exp2f(c.S[1][1][e >> 4][e & 15]);
    packs<1>(c, make_iseq<32>{});
    GAP(); asm volatile("s_nop 1"); GAP();
    rowsums<1>(c, iseq<8, 9, 10, 11, 12, 13, 14, 15, 24, 25, 26, 27, 28, 29, 30, 31>{});
#pragma unroll
    for (int qb = 0; qb < 2; ++qb) { const float rs = fadd_s(c.rs0[qb], c.rs1[qb]);
      auto pr = __builtin_amdgcn_permlane32_swap(__float_as_uint(rs), __float_as_uint(rs), false, false);
      c.lsum[qb] = fadd_s(c.lsum[qb], fadd_s(__uint_as_float(pr[0]), __uint_as_float(pr[1]))); }
    asm volatile("s_waitcnt lgkmcnt(0)" : W4_VLO :: "memory"); asm volatile("" : W4_VUP :: "memory");
    GAP();
#pragma unroll
    for (int ks = 0; ks < 4; ++ks)
#pragma unroll
      for (int db = 0; db < 4; ++db) c.Vf[db][ks] = __builtin_shufflevector(c.vlo[db][ks], c.vup[db][ks], 0, 1, 2, 3, 4, 5, 6, 7);
    asm volatile("s_nop 1" ::: "memory");
    pv_all(c, make_iseq<32>{});
    asm volatile("s_nop 7\n\ts_nop 7" ::: "memory");
  }
  {
    unsigned lb; asm volatile("v_mbcnt_lo_u32_b32 %0, -1, 0\n\tv_mbcnt_hi_u32_b32 %0, -1, %0" : "=v"(lb));
    const float il0 = __builtin_amdgcn_rcpf(c.lsum[0]), il1 = __builtin_amdgcn_rcpf(c.lsum[1]);
    char* base = (char*)Of + ((size_t)(y * 8 + wid * 2) * 256 + head * 8) * 1024 + lb * 16u;
    o_store(base, il0, il1, make_iseq<16>{});
    asm volatile("s_waitcnt vmcnt(0)" ::: "memory");
  }
  ST(4); KST_END(2);
}
template <int N> DI void touch(const char* p, int stride) {
  unsigned d[N];
#pragma unroll
  for (int i = 0; i < N; ++i) d[i] = *(const unsigned*)(p + (size_t)i * stride);
#pragma unroll
  for (int i = 0; i < N; ++i) asm volatile("" :: "v"(d[i]));
}

template <int NT, int KL> DI void mod_body(const u16* __restrict__ c, const u16* __restrict__ wmod, float* __restrict__ dst, int cb, int kh, float* red  ) {
  const int tid = threadIdx.x, cg = tid & 7, kg = tid >> 3, wv = tid >> 6;
  constexpr int NG = NT / 8, NI = KL / NG;
  u32x4 w[NI]; u16 cv[NI];
#pragma unroll
  for (int i = 0; i < NI; ++i) { const int k = kh * KL + kg + NG * i; w[i] = *(const u32x4*)(wmod + (size_t)k * 6144 + cb * 64 + cg * 8); cv[i] = c[k]; }
  float acc[8] = {0, 0, 0, 0, 0, 0, 0, 0};
#pragma unroll
  for (int i = 0; i < NI; ++i) {
    const float xx = bf2f(cv[i]), s = rbf(xx / (1.f + __expf(-xx)));
#pragma unroll
    for (int jj = 0; jj < 4; ++jj) { acc[2 * jj] += s * __uint_as_float(w[i][jj] << 16); acc[2 * jj + 1] += s * __uint_as_float(w[i][jj] & 0xffff0000u); }
  }
#pragma unroll
  for (int jj = 0; jj < 8; ++jj) { float a = acc[jj]; a += __shfl_xor(a, 8); a += __shfl_xor(a, 16); a += __shfl_xor(a, 32); acc[jj] = a; }
  if ((tid & 63) < 8) {
#pragma unroll
    for (int jj = 0; jj < 8; ++jj) red[wv * 64 + cg * 8 + jj] = acc[jj];
  }
  __syncthreads();
  if (tid < 64) { float s = 0;
#pragma unroll
    for (int g = 0; g < NT / 64; ++g) s += red[g * 64 + tid];
    dst[tid] = s; }
}
__global__ __launch_bounds__(512) void k_pre(const u16* __restrict__ c, const u16* __restrict__ wmod, float* __restrict__ modA, const u16* __restrict__ x) {
  __shared__ float red[8 * 64];
  const int b = blockIdx.x;
  KST_BEGIN(0);
  unsigned dx = 0;
  if (threadIdx.x < 256) dx = *(const unsigned*)((const char*)x + (size_t)b * 16384 + threadIdx.x * 64);
  mod_body<512, 128>(c, wmod, modA + (b & 7) * 2048 + (b >> 3) * 64, b >> 3, b & 7, red);
  asm volatile("" :: "v"(dx));
  KST_END(0);
}
DI f32x4 modv(const float* __restrict__ modp, int n) { const f32x4 a = *(const f32x4*)(modp + n), b = *(const f32x4*)(modp + 6144 + n); f32x4 o; for (int e = 0; e < 4; ++e) o[e] = rbf(a[e] + b[e]); return o; }
DI void unpack8(const u32x4& w, float* v) {
#pragma unroll
  for (int j = 0; j < 4; ++j) { v[2 * j] = __uint_as_float(w[j] << 16); v[2 * j + 1] = __uint_as_float(w[j] & 0xffff0000u); }
}
DI u32x4 pack8(const float* o) { return (u32x4){cvtpk(o[0], o[1]), cvtpk(o[2], o[3]), cvtpk(o[4], o[5]), cvtpk(o[6], o[7])}; }

DI void rbf2(float& a, float& b) { const unsigned w = cvtpk(a, b); a = __uint_as_float(w << 16); b = __uint_as_float(w & 0xffff0000u); }
DI void rbf8(float* v) { rbf2(v[0], v[1]); rbf2(v[2], v[3]); rbf2(v[4], v[5]); rbf2(v[6], v[7]); }
DI void modv8(const float* __restrict__ modp, int n, float* o) {
  const f32x4 a0 = *(const f32x4*)(modp + n), b0 = *(const f32x4*)(modp + 6144 + n), a1 = *(const f32x4*)(modp + n + 8), b1 = *(const f32x4*)(modp + 6144 + n + 8);
#pragma unroll
  for (int e = 0; e < 4; ++e) { o[e] = a0[e] + b0[e]; o[4 + e] = a1[e] + b1[e]; }
  rbf8(o);
}
template <int MODE>
__global__ __launch_bounds__(1024) void k_ln(const u16* __restrict__ x, const u16* __restrict__ Pf, const float* __restrict__ modp, u16* __restrict__ xf, u16* __restrict__ hf) {
  __shared__ float red[2][16][8];
  __shared__ __attribute__((aligned(16))) float msum[MODE == 0 ? 2048 : 4];
  const int tid = threadIdx.x;
  KST_BEGIN(MODE == 0 ? 1 : 4);
  const int r = tid & 7, c = tid >> 3, h = c & 1, wid = tid >> 6;
  const int row = blockIdx.x * 8 + r, mb = row >> 5, ml = row & 31;
  const size_t fo = ((size_t)mb * 64 + (c >> 1)) * 512 + (h * 32 + ml) * 8;
  const int n = 16 * (c >> 1) + 4 * h;
  float v[8], sh[8], sc[8];
  if (MODE == 0) {
    const u32x4 xw = *(const u32x4*)(x + (size_t)row * 1024 + 8 * c);
    { f32x2 a = {0.f, 0.f};
#pragma unroll
      for (int ks = 0; ks < 8; ++ks) a += *(const f32x2*)(modp + ks * 2048 + 2 * tid);
      float a0 = a[0], a1 = a[1]; rbf2(a0, a1); *(f32x2*)(msum + 2 * tid) = (f32x2){a0, a1}; }
    const unsigned s0 = h ? xw[0] : xw[2], s1 = h ? xw[1] : xw[3];
    const unsigned g0 = __shfl_xor(s0, 8), g1 = __shfl_xor(s1, 8);
    const u32x4 f = h ? (u32x4){g0, g1, xw[2], xw[3]} : (u32x4){xw[0], xw[1], g0, g1};
    *(u32x4*)(xf + fo) = f;
    unpack8(f, v);
  } else {
    const u32x4 xw = *(const u32x4*)(xf + fo), p0 = *(const u32x4*)(Pf + fo), p1 = *(const u32x4*)(Pf + fo + (size_t)2048 * 1024);
    float ga[8], a[8], b[8];
    modv8(modp, 3072 + n, sh); modv8(modp, 4096 + n, sc); modv8(modp, 2048 + n, ga);
    unpack8(xw, v); unpack8(p0, a); unpack8(p1, b);
#pragma unroll
    for (int j = 0; j < 8; ++j) a[j] += b[j];
    rbf8(a);
#pragma unroll
    for (int j = 0; j < 8; ++j) a[j] *= ga[j];
    rbf8(a);
#pragma unroll
    for (int j = 0; j < 8; ++j) v[j] += a[j];
    const u32x4 w = pack8(v);
    *(u32x4*)(xf + fo) = w;
    unpack8(w, v);
  }
  float s = ((v[0] + v[1]) + (v[2] + v[3])) + ((v[4] + v[5]) + (v[6] + v[7]));
  s += __shfl_xor(s, 8); s += __shfl_xor(s, 16); s += __shfl_xor(s, 32);
  if ((tid & 63) < 8) red[0][wid][r] = s;
  __syncthreads();
  if (MODE == 0) {
    const f32x4 a0 = *(const f32x4*)(msum + n), a1 = *(const f32x4*)(msum + n + 8), b0 = *(const f32x4*)(msum + 1024 + n), b1 = *(const f32x4*)(msum + 1024 + n + 8);
#pragma unroll
    for (int e = 0; e < 4; ++e) { sh[e] = a0[e]; sh[4 + e] = a1[e]; sc[e] = b0[e]; sc[4 + e] = b1[e]; }
  }
  float mean = 0;
#pragma unroll
  for (int w = 0; w < 16; ++w) mean += red[0][w][r];
  mean *= (1.f / 1024.f);
  float qv = 0;
#pragma unroll
  for (int j = 0; j < 8; ++j) { const float d = v[j] - mean; qv += d * d; }
  qv += __shfl_xor(qv, 8); qv += __shfl_xor(qv, 16); qv += __shfl_xor(qv, 32);
  if ((tid & 63) < 8) red[1][wid][r] = qv;
  __syncthreads();
  float var = 0;
#pragma unroll
  for (int w = 0; w < 16; ++w) var += red[1][w][r];
  var = rbf(var * (1.f / 1024.f));
  const float mb16 = rbf(mean), rsd = 1.f / rbf(sqrtf(rbf(var + 1e-5f)));
#pragma unroll
  for (int j = 0; j < 8; ++j) { v[j] -= mb16; sc[j] += 1.f; }
  rbf8(v); rbf8(sc);
#pragma unroll
  for (int j = 0; j < 8; ++j) v[j] *= rsd;
  rbf8(v);
#pragma unroll
  for (int j = 0; j < 8; ++j) v[j] *= sc[j];
  rbf8(v);
#pragma unroll
  for (int j = 0; j < 8; ++j) v[j] += sh[j];
  *(u32x4*)(hf + fo) = pack8(v);
  KST_END(MODE == 0 ? 1 : 4);
}
__global__ __launch_bounds__(1024) void k_fin(const u16* __restrict__ x1f, const u16* __restrict__ Pf, const float* __restrict__ modp, u16* __restrict__ out) {
  KST_BEGIN(7);
  const int tid = threadIdx.x, r = tid & 7, c = tid >> 3, h = c & 1;
  const int row = blockIdx.x * 8 + r, mb = row >> 5, ml = row & 31;
  const size_t fo = ((size_t)mb * 64 + (c >> 1)) * 512 + (h * 32 + ml) * 8;
  const u32x4 xw = *(const u32x4*)(x1f + fo), p0 = *(const u32x4*)(Pf + fo), p1 = *(const u32x4*)(Pf + fo + (size_t)2048 * 1024);
  float gb[8], v[8], a[8], b[8];
  modv8(modp, 5120 + 16 * (c >> 1) + 4 * h, gb);
  unpack8(xw, v); unpack8(p0, a); unpack8(p1, b);
#pragma unroll
  for (int j = 0; j < 8; ++j) a[j] += b[j];
  rbf8(a);
#pragma unroll
  for (int j = 0; j < 8; ++j) a[j] *= gb[j];
  rbf8(a);
#pragma unroll
  for (int j = 0; j < 8; ++j) v[j] += a[j];
  const u32x4 w = pack8(v);
  const unsigned s0 = h ? w[0] : w[2], s1 = h ? w[1] : w[3];
  const unsigned g0 = __shfl_xor(s0, 8), g1 = __shfl_xor(s1, 8);
  const u32x4 f = h ? (u32x4){g0, g1, w[2], w[3]} : (u32x4){w[0], w[1], g0, g1};
  *(u32x4*)(out + (size_t)row * 1024 + 8 * c) = f;
  KST_END(7);
}

using SCfg = GCfg<32, 4, 32, 8, false, 0, 5, 4, 2, 1>;
template <int K>
__global__ __launch_bounds__(256, 1) void k_gemm_sk(const u16* __restrict__ Af, const u16* __restrict__ B, u16* __restrict__ Pf,
                                                           const u16* __restrict__ cvec, const u16* __restrict__ wmod, float* __restrict__ modp) {
  using C = SCfg;
  extern __shared__ __attribute__((aligned(16))) char lds[];
  KST_BEGIN(K == 4096 ? 3 : 6);
  if (K == 4096 && blockIdx.x >= 256) { const int b = blockIdx.x - 256; mod_body<256, 512>(cvec, wmod, modp + (b & 1) * 6144 + (32 + (b >> 1)) * 64, 32 + (b >> 1), b & 1, (float*)lds); KST_END(3); return; }
  constexpr int N = 1024, KSPLIT = 2;
  const unsigned tid = threadIdx.x, lane = tid & 63u, wid = rfl(tid >> 6);
  const int bid = blockIdx.x, xcd = bid & 7, j = bid >> 3;
  const int kh = xcd & 1, mg = xcd >> 1;
  const int mt = mg * 4 + (j & 3), nt = j >> 2;
  constexpr int klen = K / KSPLIT; const int k0 = kh * klen;
  GState<C> g;
  const int mb0 = mt * 4 + wid;
  constexpr unsigned mbstride = (unsigned)K * 64u;
  g.srdA = mk_srd((const char*)Af + (size_t)mb0 * mbstride + (size_t)k0 * 64, (unsigned)klen * 64u);
  g.voffA[0] = lane * 16u;
  constexpr unsigned ldb = (unsigned)N * 2u;
  g.srdB = mk_srd((const char*)B + (size_t)k0 * ldb + (size_t)nt * 256, (unsigned)klen * ldb - (unsigned)nt * 256u);
  g_setup<C>(g, (unsigned)(size_t)lds, lane, wid, ldb);
  gemm_run<C>(g, klen / 64);
#pragma unroll
  for (int n = 0; n < 4; ++n)
#pragma unroll
    for (int s = 0; s < 2; ++s) {
      u32x4 w = {cvtpk(g.acc[n][0][8 * s], g.acc[n][0][8 * s + 1]), cvtpk(g.acc[n][0][8 * s + 2], g.acc[n][0][8 * s + 3]), cvtpk(g.acc[n][0][8 * s + 4], g.acc[n][0][8 * s + 5]), cvtpk(g.acc[n][0][8 * s + 6], g.acc[n][0][8 * s + 7])};
      *(u32x4*)(Pf + (((size_t)kh * 64 + mb0) * 64 + (nt * 8 + 2 * n + s)) * 512 + lane * 8) = w;
    }
  KST_END(K == 4096 ? 3 : 6);
}
DI float silu_mul(float g, float u) { return g * __builtin_amdgcn_rcpf(1.f + __builtin_amdgcn_exp2f(g * -1.4426950408889634f)) * u; }
using UCfg = GCfg<32, 8, 32, 14, true, 0, 4, 2, 1, 2>;
__global__ __launch_bounds__(512, 2) void k_gemm_gu(const u16* __restrict__ Af, const u16* __restrict__ Wg, const u16* __restrict__ Wu, u16* __restrict__ Act) {
  using C = UCfg;
  extern __shared__ __attribute__((aligned(16))) char lds[];
  constexpr int K = 1024, F = 3584;
  KST_BEGIN(5);
  const unsigned tid = threadIdx.x, lane = tid & 63u, wid = rfl(tid >> 6);
  const int bid = blockIdx.x, xcd = bid & 7, j = bid >> 3;
  const int mt = (xcd & 1) * 4 + (j & 3), nt = (xcd >> 1) * 8 + (j >> 2);
  GState<C> g;
  const int mb0 = mt * 8 + wid;
  constexpr unsigned mbstride = (unsigned)K * 64u;
  g.srdA = mk_srd((const char*)Af + (size_t)mb0 * mbstride, mbstride);
  g.voffA[0] = lane * 16u;
  constexpr unsigned ldb = F * 2u;
  const bool up = wid >= 4;
  g.srdB = mk_srd((const char*)(up ? Wu : Wg) + (size_t)nt * 224, (unsigned)K * ldb - (unsigned)nt * 224u);
  g_setup<C>(g, (unsigned)(size_t)lds, lane, wid, ldb);
  gemm_run<C>(g, K / 64);
#pragma unroll
  for (int n = 0; n < 7; ++n) {
    float o[8];
#pragma unroll
    for (int e = 0; e < 8; ++e) o[e] = silu_mul(g.acc[n][0][e], g.acc[n][0][e + 8]);
    u32x4 w = {cvtpk(o[0], o[1]), cvtpk(o[2], o[3]), cvtpk(o[4], o[5]), cvtpk(o[6], o[7])};
    *(u32x4*)(Act + ((size_t)mb0 * (F >> 4) + (nt * 7 + n)) * 512 + lane * 8) = w;
  }
  KST_END(5);
}

extern "C" void kernel_launch(void* const* d_in, const int* in_sizes, int n_in, void* d_out, int out_size, void* d_ws, size_t ws_size, hipStream_t stream) {
  const u16* x = (const u16*)d_in[0]; const u16* kc = (const u16*)d_in[1]; const u16* vc = (const u16*)d_in[2];
  const u16* wq = (const u16*)d_in[3]; const u16* wo = (const u16*)d_in[4];
  const u16* wg = (const u16*)d_in[6]; const u16* wu = (const u16*)d_in[7]; const u16* wd = (const u16*)d_in[8];
  const u16* c = (const u16*)d_in[9]; const u16* wmod = (const u16*)d_in[10];
  u16* out = (u16*)d_out;
  char* ws = (char*)d_ws;
  float* mod = (float*)ws;
  u16* hf = (u16*)(ws + (1 << 20));
  u16* x1f = (u16*)(ws + (8 << 20));
  u16* of = (u16*)(ws + (16 << 20));
  u16* P = (u16*)(ws + (32 << 20));
  u16* act = (u16*)(ws + (64 << 20));
  hipFuncSetAttribute((const void*)k_attn, hipFuncAttributeMaxDynamicSharedMemorySize, SHM_ATTN);
  hipFuncSetAttribute((const void*)k_gemm_sk<4096>, hipFuncAttributeMaxDynamicSharedMemorySize, SCfg::LDSB);
  hipFuncSetAttribute((const void*)k_gemm_sk<3584>, hipFuncAttributeMaxDynamicSharedMemorySize, SCfg::LDSB);
  hipFuncSetAttribute((const void*)k_gemm_gu, hipFuncAttributeMaxDynamicSharedMemorySize, UCfg::LDSB);
  float* modA = (float*)(ws + (256 << 10));
  k_pre<<<256, 512, 0, stream>>>(c, wmod, modA, x);
  k_ln<0><<<256, 1024, 0, stream>>>(x, nullptr, modA, x1f, hf);
  k_attn<<<256, 256, SHM_ATTN, stream>>>(hf, wq, kc, vc, of, wo, wg, wu, wd, wmod);
  k_gemm_sk<4096><<<256 + 128, 256, SCfg::LDSB, stream>>>(of, wo, P, c, wmod, mod);
  k_ln<1><<<256, 1024, 0, stream>>>(x, P, mod, x1f, hf);
  k_gemm_gu<<<256, 512, UCfg::LDSB, stream>>>(hf, wg, wu, act);
  k_gemm_sk<3584><<<256, 256, SCfg::LDSB, stream>>>(act, wd, P, nullptr, nullptr, nullptr);
  k_fin<<<256, 1024, 0, stream>>>(x1f, P, mod, out);
}
```

```cpp
#include <hip/hip_runtime.h>
#include <utility>
#include <type_traits>
typedef unsigned short u16;
typedef short bf16x8 __attribute__((ext_vector_type(8)));
typedef short s16x4 __attribute__((ext_vector_type(4)));
typedef float f32x4 __attribute__((ext_vector_type(4)));
typedef float f32x16 __attribute__((ext_vector_type(16)));
typedef unsigned u32x4 __attribute__((ext_vector_type(4)));
typedef unsigned u32x2 __attribute__((ext_vector_type(2)));
typedef float f32x2 __attribute__((ext_vector_type(2)));
typedef __bf16 bf16x2_t __attribute__((ext_vector_type(2)));
#define DI __device__ __forceinline__
#define LAS __attribute__((address_space(3)))
#define GAP() __builtin_amdgcn_sched_barrier(0)
template <int... Is> using iseq = std::integer_sequence<int, Is...>;
template <int N> using make_iseq = std::make_integer_sequence<int, N>;

#define KST_BEGIN(k)
#define KST_END(k)
DI unsigned rfl(unsigned x) { return __builtin_amdgcn_readfirstlane(x); }
DI float bf2f(u16 v) { return __uint_as_float(((unsigned)v) << 16); }
DI unsigned cvtpk(float lo, float hi) { f32x2 v = {lo, hi}; bf16x2_t b = __builtin_convertvector(v, bf16x2_t); return __builtin_bit_cast(unsigned, b); }
DI float rbf(float f) { return __uint_as_float(cvtpk(f, 0.f) << 16); }
DI __amdgpu_buffer_rsrc_t mk_srd(const void* p, unsigned bytes) { return __builtin_amdgcn_make_buffer_rsrc((void*)p, (short)0, (int)bytes, 0x00020000); }

#define W4_ACLOB "a0","a1","a2","a3","a4","a5","a6","a7","a8","a9","a10","a11","a12","a13","a14","a15","a16","a17","a18","a19","a20","a21","a22","a23","a24","a25","a26","a27","a28","a29","a30","a31","a32","a33","a34","a35","a36","a37","a38","a39","a40","a41","a42","a43","a44","a45","a46","a47","a48","a49","a50","a51","a52","a53","a54","a55","a56","a57","a58","a59","a60","a61","a62","a63","a64","a65","a66","a67","a68","a69","a70","a71","a72","a73","a74","a75","a76","a77","a78","a79","a80","a81","a82","a83","a84","a85","a86","a87","a88","a89","a90","a91","a92","a93","a94","a95","a96","a97","a98","a99","a100","a101","a102","a103","a104","a105","a106","a107","a108","a109","a110","a111","a112","a113","a114","a115","a116","a117","a118","a119","a120","a121","a122","a123","a124","a125","a126","a127"
#define W4_ACLOB_HI "a128","a129","a130","a131","a132","a133","a134","a135","a136","a137","a138","a139","a140","a141","a142","a143","a144","a145","a146","a147","a148","a149","a150","a151","a152","a153","a154","a155","a156","a157","a158","a159","a160","a161","a162","a163","a164","a165","a166","a167","a168","a169","a170","a171","a172","a173","a174","a175","a176","a177","a178","a179","a180","a181","a182","a183","a184","a185","a186","a187","a188","a189","a190","a191","a192","a193","a194","a195","a196","a197","a198","a199","a200","a201","a202","a203","a204","a205","a206","a207","a208","a209","a210","a211","a212","a213","a214","a215","a216","a217","a218","a219","a220","a221","a222","a223","a224","a225","a226","a227","a228","a229","a230","a231","a232","a233","a234","a235","a236","a237","a238","a239","a240","a241","a242","a243","a244","a245","a246","a247","a248","a249","a250","a251","a252","a253","a254","a255"
template <int SH_, int NW_, int WR_, int NB16_, bool GU_, int KORD_, int NS_, int PDT_, int FD_, int BARPOS_, bool ASMACC_ = false>
struct GCfg {
  static constexpr bool ASMACC = ASMACC_;
  static constexpr int SH = SH_, NW = NW_, WR = WR_, NB16 = NB16_, KORD = KORD_, NS = NS_, PDT = PDT_, FD = FD_, BARPOS = BARPOS_;
  static constexpr bool GU = GU_;
  static constexpr int STEPS = SH == 32 ? 4 : 2;
  static constexpr int MR = WR / SH;
  static constexpr int NB = SH == 32 ? NB16 / 2 : NB16;
  static constexpr int NR16 = GU ? NB16 / 2 : NB16;
  static constexpr int PIECES = NB16 * 2;
  static constexpr int PB = (PIECES + NW - 1) / NW;
  static constexpr int STAGE = PB * NW * 1024;
  static constexpr int REGION = STAGE / 2;
  static constexpr int LDSB = NS * STAGE;
  static constexpr int NOPS = STEPS * MR + PB;
  static constexpr int GS = NB * MR;
  static constexpr int G = STEPS * GS;
  static constexpr int BSTEP = STEPS - FD;
  static constexpr int GB = BSTEP * GS + (BARPOS + 1) * MR - 1;
  static constexpr int dma_lo(int ks) { return (PB * ks) / STEPS; }
  static constexpr int step_ops(int ks) { return MR + dma_lo(ks + 1) - dma_lo(ks); }
  static constexpr int step_first(int ks) { return ks * MR + dma_lo(ks); }
  static constexpr int op_step(int j) { int ks = 0; while (ks < STEPS - 1 && j >= step_first(ks + 1)) ++ks; return ks; }
  static constexpr int op_gap(int j) { const int ks = op_step(j), i = j - step_first(ks), n = step_ops(ks); return ks * GS + (i * GS) / n; }
  static constexpr int ops_before(int g) { int c = 0; for (int j = 0; j < NOPS; ++j) if (op_gap(j) < g) ++c; return c; }
  static constexpr int NA(int ks) {
    const int ts = (ks + 1) % STEPS, jt = step_first(ts) + MR - 1;
    const int full = (ks == STEPS - 1) ? PDT - 1 : PDT;
    return (NOPS - 1 - jt) + (full - 1) * NOPS + ops_before(ks * GS);
  }
  static constexpr int NBW() { return (NS - 3) * NOPS + ops_before(GB); }
  static_assert(NS >= 3 && PDT >= 2 && FD <= STEPS - 0 && BSTEP >= 0, "");
  using acc_t = std::conditional_t<SH == 32, f32x16, f32x4>;
};

template <class C>
struct GState {
  typename C::acc_t acc[C::NB][C::MR];
  bf16x8 a[C::PDT * C::STEPS][C::MR];
  s16x4 blo[C::FD][C::NB], bhi[C::FD][C::NB];
  __amdgpu_buffer_rsrc_t srdA, srdB;
  unsigned voffA[C::MR];
  unsigned voffB[C::GU ? C::PB : 1];
  unsigned sB[C::GU ? 1 : C::PB];
  unsigned m0base, vrd, ktileB;
  unsigned long long tch[3]; unsigned tch_off;
};
struct GIter { unsigned va_cur, va_nxt, sA, sA0, m0_fill, sK_fill; };

template <class C, int SL, int OFF, int MRI> DI void a_load1(GState<C>& g, unsigned sA) {
  asm volatile("buffer_load_dwordx4 %0, %1, %2, %3 offen offset:%4" : "=v"(g.a[SL][MRI]) : "v"(g.voffA[MRI]), "s"(g.srdA), "s"(sA), "i"(OFF) : "memory");
}
template <class C, int Q> DI void b_dma1(GState<C>& g, unsigned m0v, unsigned sK) {
  if constexpr (C::GU)
    asm volatile("s_mov_b32 m0, %0\n\ts_nop 0\n\tbuffer_load_dwordx4 %1, %2, %3 offen lds" :: "s"(m0v + 1024u * Q), "v"(g.voffB[Q]), "s"(g.srdB), "s"(sK) : "memory");
  else
    asm volatile("s_mov_b32 m0, %0\n\ts_nop 0\n\tbuffer_load_dwordx4 %1, %2, %3 offen lds" :: "s"(m0v + 1024u * Q), "v"(g.voffB[0]), "s"(g.srdB), "s"(sK + g.sB[Q]) : "memory");
}
template <class C> DI void g_dummy(GState<C>& g) {
  asm volatile("s_mov_b32 m0, %0\n\ts_nop 0\n\tbuffer_load_dwordx4 %1, %2, %3 offen lds" :: "s"(g.m0base + (unsigned)(C::NS - 1) * C::STAGE), "v"(g.voffA[0]), "s"(g.srdA), "s"(0x80000000u) : "memory");
}
template <class C, int TI, int J, bool VA, bool VB> DI void g_op(GState<C>& g, const GIter& it) {
  constexpr int ks = C::op_step(J), i = J - C::step_first(ks);
  if constexpr (i < C::MR) {
    constexpr int PD = C::PDT * C::STEPS, SL = (TI * C::STEPS + ks - 1 + PD) % PD;
    if constexpr (VA) { if constexpr (ks == 0) a_load1<C, SL, 0, i>(g, it.sA0); else a_load1<C, SL, (ks - 1) * 1024, i>(g, it.sA); } else g_dummy<C>(g);
  } else {
    if constexpr (VB) b_dma1<C, C::dma_lo(ks) + i - C::MR>(g, it.m0_fill, it.sK_fill); else g_dummy<C>(g);
  }
}
template <class C, int TI, int GI, int... Js> DI void g_ops_at(GState<C>& g, const GIter& it, iseq<Js...>) {
  ((C::op_gap(Js) == GI ? g_op<C, TI, Js, true, true>(g, it) : (void)0), ...);
}
template <class C> constexpr int b_off(int KS, int NBI, int t) {
  if (C::SH == 32) {
    return ((2 * KS + t) * C::NR16 + (C::GU ? NBI : 2 * NBI)) * 256;
  } else {
    const int kb = 4 * KS + (C::KORD == 0 ? 2 * t : t);
    return (kb * C::NR16 + (C::GU ? NBI / 2 : NBI)) * 256 + ((C::GU && (NBI & 1)) ? C::REGION : 0);
  }
}
template <class C, int BUF, int KS, int NBI> DI void b_read(GState<C>& g, unsigned va) {
  g.blo[BUF][NBI] = __builtin_amdgcn_ds_read_tr16_b64_v4i16((LAS s16x4*)(size_t)(va + b_off<C>(KS, NBI, 0)));
  g.bhi[BUF][NBI] = __builtin_amdgcn_ds_read_tr16_b64_v4i16((LAS s16x4*)(size_t)(va + b_off<C>(KS, NBI, 1)));
}
template <class C, int BUF, int KS, int... Ns> DI void b_read_all(GState<C>& g, unsigned va, iseq<Ns...>) { (b_read<C, BUF, KS, Ns>(g, va), ...); }

template <class C, int N, int SL> DI void a_wait(GState<C>& g) {
  static_assert(N >= 0 && N < 64, "vmcnt range");
  if constexpr (C::MR == 1) asm volatile("s_waitcnt vmcnt(%1)" : "+v"(g.a[SL][0]) : "i"(N) : "memory");
  else if constexpr (C::MR == 2) asm volatile("s_waitcnt vmcnt(%2)" : "+v"(g.a[SL][0]), "+v"(g.a[SL][1]) : "i"(N) : "memory");
  else asm volatile("s_waitcnt vmcnt(%4)" : "+v"(g.a[SL][0]), "+v"(g.a[SL][1]), "+v"(g.a[SL][2]), "+v"(g.a[SL][3]) : "i"(N) : "memory");
}

template <class C, int TI, int GI> DI void g_gap(GState<C>& g, const GIter& it) {
  constexpr int KS = GI / C::GS, r = GI % C::GS, NBI = r / C::MR, MRI = r % C::MR;
  constexpr int SL = TI * C::STEPS + KS, BUF = (C::FD == 1) ? 0 : (KS & 1), KN = (KS + C::FD) % C::STEPS;
  if constexpr (r == 0) { a_wait<C, C::NA(KS), SL>(g); GAP(); }
  {
    const bf16x8 bf = __builtin_shufflevector(g.blo[BUF][NBI], g.bhi[BUF][NBI], 0, 1, 2, 3, 4, 5, 6, 7);
    if constexpr (C::ASMACC) { constexpr int ao = 16 * (C::MR * NBI + MRI);
      asm volatile("v_mfma_f32_32x32x16_bf16 a[%c2:%c3], %0, %1, a[%c2:%c3]" :: "v"(bf), "v"(g.a[SL][MRI]), "i"(ao), "i"(ao + 15)); }
    else if constexpr (C::SH == 32) g.acc[NBI][MRI] = __builtin_amdgcn_mfma_f32_32x32x16_bf16(bf, g.a[SL][MRI], g.acc[NBI][MRI], 0, 0, 0);
    else g.acc[NBI][MRI] = __builtin_amdgcn_mfma_f32_16x16x32_bf16(bf, g.a[SL][MRI], g.acc[NBI][MRI], 0, 0, 0);
  }
  if constexpr (GI == C::GB) {
    GAP();
    static_assert(C::NBW() < 64, "");
    asm volatile("s_waitcnt vmcnt(%0) lgkmcnt(0)\n\ts_barrier" :: "i"(C::NBW()) : "memory");
    GAP();
    b_read_all<C, BUF, KN>(g, it.va_nxt, make_iseq<C::BARPOS + 1>{});
  } else if constexpr (MRI == C::MR - 1) {
    if constexpr (KS == C::BSTEP) { if constexpr (NBI > C::BARPOS) b_read<C, BUF, KN, NBI>(g, it.va_nxt); }
    else if constexpr (KS > C::BSTEP) b_read<C, BUF, KN, NBI>(g, it.va_nxt);
    else b_read<C, BUF, KN, NBI>(g, it.va_cur);
  }
  g_ops_at<C, TI, GI>(g, it, make_iseq<C::NOPS>{});
  GAP();
}
template <class C, int TI, int... Gs> DI void g_gaps(GState<C>& g, const GIter& it, iseq<Gs...>) { (g_gap<C, TI, Gs>(g, it), ...); }
template <class C, int TI> DI void g_tile(GState<C>& g, int t, unsigned& stg, unsigned& pstg) {
  unsigned nstg = stg + C::STAGE; if (nstg == C::NS * C::STAGE) nstg = 0;
  GIter it;
  it.va_cur = g.vrd + stg; it.va_nxt = g.vrd + nstg;
  it.sA = (unsigned)(C::STEPS * 1024) * (unsigned)(t + C::PDT); it.sA0 = it.sA - 1024u;
  it.m0_fill = g.m0base + pstg; it.sK_fill = g.ktileB * (unsigned)(t - 1 + C::NS);
  g_gaps<C, TI>(g, it, make_iseq<C::G>{});
  pstg = stg; stg = nstg;
}
template <class C, int... TIs> DI void g_body(GState<C>& g, int t, unsigned& stg, unsigned& pstg, iseq<TIs...>) { (g_tile<C, TIs>(g, t + TIs, stg, pstg), ...); }

template <class C, int I> DI void g_touch(GState<C>& g) {
  asm volatile("s_mov_b32 m0, %0\n\ts_nop 0\n\tglobal_load_lds_dword %1, %2" :: "s"(g.m0base + (unsigned)(C::NS - 1) * C::STAGE), "v"(g.tch_off), "s"(g.tch[I]) : "memory");
}
template <class C, int V, int J> DI void g_virt_op(GState<C>& g, const GIter& it) {
  constexpr int ks = C::op_step(J);
  constexpr int TI = ((V % C::PDT) + C::PDT) % C::PDT;
  constexpr bool VA = (C::STEPS * (V + C::PDT) + ks - 1) >= 0, VB = (V - 1 + C::NS) >= 0;
  if constexpr (V == -1 && J == C::ops_before(C::GB)) asm volatile("s_waitcnt vmcnt(%0)\n\ts_barrier" :: "i"(C::NBW()) : "memory");
  if constexpr (C::ASMACC && V == -(C::NS - 1) && J < 2) { static_assert(!VA, ""); g_touch<C, J>(g); }
  else if constexpr (C::ASMACC && V == -(C::NS - 2) && J == 0) { static_assert(!VA, ""); g_touch<C, 2>(g); }
  else g_op<C, TI, J, VA, VB>(g, it);
}
template <class C, int V, int... Js> DI void g_virt(GState<C>& g, iseq<Js...>) {
  GIter it; constexpr int tb = V - 1 + C::NS;
  it.va_cur = it.va_nxt = 0; it.sA = (unsigned)(C::STEPS * 1024) * (unsigned)(V + C::PDT >= 0 ? V + C::PDT : 0); it.sA0 = it.sA - 1024u;
  it.m0_fill = g.m0base + (unsigned)(tb >= 0 ? tb : 0) * C::STAGE; it.sK_fill = g.ktileB * (unsigned)(tb >= 0 ? tb : 0);
  (g_virt_op<C, V, Js>(g, it), ...);
  if constexpr (V == -1 && C::ops_before(C::GB) == C::NOPS) asm volatile("s_waitcnt vmcnt(%0)\n\ts_barrier" :: "i"(C::NBW()) : "memory");
}
template <class C, int... Vs> DI void g_prologue(GState<C>& g, iseq<Vs...>) { constexpr int NV = sizeof...(Vs); (g_virt<C, Vs - NV>(g, make_iseq<C::NOPS>{}), ...); }

template <class C, bool PSWAP = false> DI void g_setup(GState<C>& g, unsigned ldsb, unsigned lane, unsigned wid, unsigned ldb) {
  g.m0base = rfl(ldsb + wid * (unsigned)C::PB * 1024u);
  g.ktileB = 64u * ldb;
  const unsigned q = (lane & 15u) >> 2, p0 = lane & 3u, p = PSWAP ? (((p0 & 1u) << 1) | (p0 >> 1)) : p0, g4 = lane >> 4;
  if constexpr (C::SH == 32) {
    const unsigned h = lane >> 5, blk = g4 & 1u;
    g.vrd = ldsb + blk * (C::GU ? (unsigned)C::REGION : 256u) + ((4u * h + q) ^ (4u * blk)) * 32u + 8u * p;
  } else {
    g.vrd = ldsb + (g4 >> 1) * (C::KORD == 0 ? 1u : 2u) * (unsigned)(C::NR16 * 256) + (4u * (g4 & 1u) + q) * 32u + 8u * p;
  }
  if constexpr (C::GU) {
    const unsigned wr = wid % (C::NW / 2), up = wid / (C::NW / 2);
#pragma unroll
    for (int qq = 0; qq < C::PB; ++qq) {
      const unsigned P = wr * C::PB + qq, s = 4u * P + g4 * 0u + (lane >> 4), kb = s / (unsigned)C::NR16, nb = s % (unsigned)C::NR16;
      const unsigned row = ((lane >> 1) & 7u) ^ ((C::SH == 32 && up) ? 4u : 0u);
      g.voffB[qq] = (P < (unsigned)(C::NR16 * 2)) ? (8u * kb + row) * ldb + 32u * nb + 16u * (lane & 1u) : 0x80000000u;
    }
  } else {
    static_assert(C::GU || (C::NR16 % 4 == 0), "");
    const unsigned sub = lane >> 4;
    const unsigned row = ((lane >> 1) & 7u) ^ ((C::SH == 32 && (sub & 1u)) ? 4u : 0u);
    g.voffB[0] = row * ldb + 32u * sub + 16u * (lane & 1u);
#pragma unroll
    for (int qq = 0; qq < C::PB; ++qq) { const unsigned P = wid * C::PB + qq, kb = P / (unsigned)(C::NR16 / 4), j = P % (unsigned)(C::NR16 / 4); g.sB[qq] = rfl(8u * kb * ldb + 128u * j); }
  }
}

template <int R> DI void acc_zero1() { asm volatile("v_accvgpr_write_b32 a[%c0], 0" :: "i"(R) : W4_ACLOB); }
template <int... Rs> DI void acc_zero(iseq<Rs...>) { (acc_zero1<Rs>(), ...); asm volatile("s_nop 1"); }
template <class C> DI void gemm_run(GState<C>& g, int NT) {
  constexpr int NV = C::PDT > C::NS - 1 ? C::PDT : C::NS - 1;
  if constexpr (!C::ASMACC) {
#pragma unroll
    for (int n = 0; n < C::NB; ++n)
#pragma unroll
      for (int m = 0; m < C::MR; ++m)
#pragma unroll
        for (int e = 0; e < (C::SH == 32 ? 16 : 4); ++e) g.acc[n][m][e] = 0.f;
  }
  g_prologue<C>(g, make_iseq<NV>{});
  if constexpr (C::ASMACC) acc_zero(make_iseq<C::NB * C::MR * 16>{});
  b_read_all<C, 0, 0>(g, g.vrd, make_iseq<C::NB>{});
  if constexpr (C::FD == 2) b_read_all<C, 1, 1>(g, g.vrd, make_iseq<C::NB>{});
  GAP();
  unsigned stg = 0, pstg = (C::NS - 1) * C::STAGE;
  for (int t = 0; t < NT; t += C::PDT) g_body<C>(g, t, stg, pstg, make_iseq<C::PDT>{});
  asm volatile("s_waitcnt vmcnt(0) lgkmcnt(0)" ::: "memory");
#pragma unroll
  for (int i = 0; i < C::PDT * C::STEPS; ++i)
#pragma unroll
    for (int m = 0; m < C::MR; ++m) asm volatile("" :: "v"(g.a[i][m]));
  GAP();
}
typedef __bf16 abf16x8 __attribute__((ext_vector_type(8)));
typedef __bf16 abf16x4 __attribute__((ext_vector_type(4)));
#define ATTN_THR 64.0f
constexpr unsigned KRS = 256u, KTILEB = 64u * KRS;
#define KRING 0x0000u
#define VRING 0x8000u
#define QGEMM_LDS 0x10000u
DI float max3f(float a, float b, float c) { float r; asm("v_max3_f32 %0, %1, %2, %3" : "=v"(r) : "v"(a), "v"(b), "v"(c)); return r; }
DI float max2f(float a, float b) { float r; asm("v_max_f32_e32 %0, %1, %2" : "=v"(r) : "v"(a), "v"(b)); return r; }
DI float fadd_s(float a, float b) { float r; asm("v_add_f32_e32 %0, %1, %2" : "=v"(r) : "v"(a), "v"(b)); return r; }
DI float fmul_s(float a, float b) { float r; asm("v_mul_f32_e32 %0, %1, %2" : "=v"(r) : "v"(a), "v"(b)); return r; }
DI float fsub_s(float a, float b) { float r; asm("v_sub_f32_e32 %0, %1, %2" : "=v"(r) : "v"(a), "v"(b)); return r; }
DI float w4_aone() { unsigned l; asm volatile("v_mbcnt_lo_u32_b32 %0, -1, 0\n\tv_mbcnt_hi_u32_b32 %0, -1, %0" : "=v"(l)); return l < 32u ? 1.f : 0.f; }
DI void negm_bcast(f32x16& negm, float aone, float neg_mhat) {
  asm volatile("s_nop 1\n\tv_mfma_f32_32x32x2_f32 %0, %1, %2, 0\n\ts_nop 7\n\ts_nop 7\n\ts_nop 3" : "+v"(negm) : "v"(aone), "v"(neg_mhat));
}
struct W4C {
  unsigned vkb[4], vvb, vksrc[2], vvsrc[4], m0k, m0v, sofk, sofv;
  __amdgpu_buffer_rsrc_t srdK, srdV;
  f32x16 S[2][2][2];
  f32x16 NEGM[2];
  abf16x8 Vf[4][4];
  unsigned pk[2][4][4];
  abf16x4 vlo[4][4], vup[4][4];
  float mhat[2], fct[2]; bool pend;
  float rs[2][2];
  float mA, mB;
  unsigned sk_m0, sk_so, sv_m0, sv_so;
  unsigned pf_lo, pf_hi, pf_off, pf_m0;
};
constexpr int AO_(int db, int qb) { return 16 * (2 * db + qb); }
constexpr int AQ_(int qb, int d0) { return 128 + 32 * qb + 4 * d0; }
constexpr int AK_(int kb, int d0) { return 192 + 32 * kb + 4 * d0; }

template <int I> DI void tr_read(W4C& c, unsigned va) {
  constexpr int ks = I >> 3, db = (I >> 1) & 3;
  if constexpr ((I & 1) == 0) asm volatile("ds_read_b64_tr_b16 %0, %1 offset:%2" : "=v"(c.vlo[db][ks]) : "v"(va), "i"(4096 * ks + 512 * db) : "memory");
  else                        asm volatile("ds_read_b64_tr_b16 %0, %1 offset:%2" : "=v"(c.vup[db][ks]) : "v"(va), "i"(4096 * ks + 2048 + 512 * db) : "memory");
}
template <int I, int SLOT> DI void k_read(W4C& c) {
  constexpr int kb = I >> 3, d0 = I & 7;
  asm volatile("ds_read_b128 a[%c1:%c2], %0 offset:%c3" :: "v"(c.vkb[d0 & 3]), "i"(AK_(kb, d0)), "i"(AK_(kb, d0) + 3), "i"(SLOT * 0x4000 + 8192 * kb + 128 * (d0 >> 2)) : "memory");
}
template <int X, bool FIRST, int M> DI void qk_mfma(W4C& c) {
  constexpr int d0 = M >> 2, kb = (M >> 1) & 1, qb = M & 1, ak = AK_(kb, d0), aq = AQ_(qb, d0);
  if constexpr (d0 == 0) {
    if constexpr (FIRST) asm volatile("v_mfma_f32_32x32x16_bf16 %0, a[%c1:%c2], a[%c3:%c4], 0" : "=&v"(c.S[X][kb][qb]) : "i"(ak), "i"(ak + 3), "i"(aq), "i"(aq + 3));
    else                 asm volatile("v_mfma_f32_32x32x16_bf16 %0, a[%c2:%c3], a[%c4:%c5], %1" : "=&v"(c.S[X][kb][qb]) : "v"(c.NEGM[qb]), "i"(ak), "i"(ak + 3), "i"(aq), "i"(aq + 3));
  } else asm volatile("v_mfma_f32_32x32x16_bf16 %0, a[%c1:%c2], a[%c3:%c4], %0" : "+v"(c.S[X][kb][qb]) : "i"(ak), "i"(ak + 3), "i"(aq), "i"(aq + 3));
}
template <int M, bool ZERO> DI void pv_mfma(W4C& c) {
  constexpr int ks = M >> 3, db = (M >> 1) & 3, qb = M & 1, ao = AO_(db, qb);
  const u32x4 pw = {c.pk[qb][ks][0], c.pk[qb][ks][1], c.pk[qb][ks][2], c.pk[qb][ks][3]};
  if constexpr (ZERO && ks == 0) asm volatile("v_mfma_f32_32x32x16_bf16 a[%c2:%c3], %0, %1, 0" :: "v"(c.Vf[db][ks]), "v"(pw), "i"(ao), "i"(ao + 15));
  else                         asm volatile("v_mfma_f32_32x32x16_bf16 a[%c2:%c3], %0, %1, a[%c2:%c3]" :: "v"(c.Vf[db][ks]), "v"(pw), "i"(ao), "i"(ao + 15));
}
template <int X, int KB, int E> DI void op_exp(W4C& c) { c.S[X][KB][E >> 4][E & 15] = __builtin_amdgcn_exp2f(c.S[X][KB][E >> 4][E & 15]); }
template <int X, int KB, int E> DI void op_add(W4C& c) { asm volatile("v_add_f32_e32 %0, %0, %1" : "+v"(c.rs[E >> 4][E & 1]) : "v"(c.S[X][KB][E >> 4][E & 15])); }
template <int X, int KB, int J> DI void op_cvt(W4C& c) {
  constexpr int qb = J >> 3, ks = 2 * KB + ((J >> 2) & 1), p = J & 3, r = 2 * (J & 7);
  c.pk[qb][ks][p] = cvtpk(c.S[X][KB][qb][r], c.S[X][KB][qb][r + 1]);
  if constexpr (KB == 1) asm volatile("" : "+v"(c.pk[qb][ks][p]));
}
template <int X, int I> DI float sval(W4C& c) { return c.S[X][I >> 5][(I >> 4) & 1][I & 15]; }
template <int X, int G> DI void op_max4(W4C& c) {
  if constexpr (G == 0) asm volatile("v_max3_f32 %0, %2, %3, %4\n\tv_max3_f32 %1, %5, %6, %7\n\tv_max3_f32 %0, %0, %8, %9" : "=&v"(c.mA), "=&v"(c.mB)
                                     : "v"(sval<X, 0>(c)), "v"(sval<X, 1>(c)), "v"(sval<X, 2>(c)), "v"(sval<X, 3>(c)), "v"(sval<X, 4>(c)), "v"(sval<X, 5>(c)), "v"(sval<X, 6>(c)), "v"(sval<X, 7>(c)));
  else asm volatile("v_max3_f32 %1, %1, %2, %3\n\tv_max3_f32 %0, %0, %4, %5\n\tv_max3_f32 %1, %1, %6, %7\n\tv_max3_f32 %0, %0, %8, %9" : "+v"(c.mA), "+v"(c.mB)
                    : "v"(sval<X, 8 * G>(c)), "v"(sval<X, 8 * G + 1>(c)), "v"(sval<X, 8 * G + 2>(c)), "v"(sval<X, 8 * G + 3>(c)), "v"(sval<X, 8 * G + 4>(c)), "v"(sval<X, 8 * G + 5>(c)), "v"(sval<X, 8 * G + 6>(c)), "v"(sval<X, 8 * G + 7>(c)));
}
constexpr int EB0 = 10;
constexpr int eb_lo(int g) { return g <= EB0 ? 0 : (32 * (g - EB0)) / (32 - EB0); }
template <int X, bool FIRST, int M> DI void gapA(W4C& c, unsigned va, const int t) {
  constexpr int Xp = X ^ 1;
  qk_mfma<X, FIRST, M>(c);
  GAP();
  if constexpr (!FIRST) {
    op_exp<Xp, 1, M>(c);
    constexpr int base = M + (M > 16) + (M > 18) + (M > 20) + (M > 22);
    if constexpr (M <= 27) tr_read<base>(c, va);
    if constexpr (M < 16) op_cvt<Xp, 0, M>(c);
    else if constexpr (M >= 17 && M <= 23 && (M & 1)) op_cvt<Xp, 1, (M - 17) / 2>(c);
    else if constexpr (M >= 25 && M <= 28) op_cvt<Xp, 1, 8 + M - 25>(c);
    if constexpr (M == 16 || M == 18 || M == 20 || M == 22) tr_read<base + 1>(c, va);
    if constexpr (M == 0) { op_add<Xp, 0, eb_lo(31)>(c); if constexpr (eb_lo(31) + 1 < 32) op_add<Xp, 0, eb_lo(31) + 1>(c); }
    else op_add<Xp, 1, M - 1>(c);
  }
  if constexpr (M == 29) { c.sk_m0 = c.m0k + (unsigned)X * 0x4000u; c.sk_so = KTILEB * (unsigned)(t + 2) + c.sofk; asm volatile("" : "+s"(c.sk_m0), "+s"(c.sk_so)); }
  if constexpr (M == 30) { c.sv_m0 = c.m0v + (unsigned)Xp * 0x4000u; c.sv_so = KTILEB * (unsigned)(t + 1) + c.sofv; asm volatile("" : "+s"(c.sv_m0), "+s"(c.sv_so)); }
  if constexpr (M == 31) asm volatile("s_mov_b32 m0, %0" :: "s"(c.sk_m0) : "memory");
  if constexpr (!FIRST && M == 8) asm volatile("" :: "v"(c.NEGM[0]), "v"(c.NEGM[1]));
  GAP();
}
#define W4_VLO "+v"(c.vlo[0][0]), "+v"(c.vlo[0][1]), "+v"(c.vlo[0][2]), "+v"(c.vlo[0][3]), "+v"(c.vlo[1][0]), "+v"(c.vlo[1][1]), "+v"(c.vlo[1][2]), "+v"(c.vlo[1][3]), \
               "+v"(c.vlo[2][0]), "+v"(c.vlo[2][1]), "+v"(c.vlo[2][2]), "+v"(c.vlo[2][3]), "+v"(c.vlo[3][0]), "+v"(c.vlo[3][1]), "+v"(c.vlo[3][2]), "+v"(c.vlo[3][3])
#define W4_VUP "+v"(c.vup[0][0]), "+v"(c.vup[0][1]), "+v"(c.vup[0][2]), "+v"(c.vup[0][3]), "+v"(c.vup[1][0]), "+v"(c.vup[1][1]), "+v"(c.vup[1][2]), "+v"(c.vup[1][3]), \
               "+v"(c.vup[2][0]), "+v"(c.vup[2][1]), "+v"(c.vup[2][2]), "+v"(c.vup[2][3]), "+v"(c.vup[3][0]), "+v"(c.vup[3][1]), "+v"(c.vup[3][2]), "+v"(c.vup[3][3])
template <int X, bool FIRST, int... Ms> DI void phaseA_(W4C& c, const int t, iseq<Ms...>) {
  const unsigned va = c.vvb + (unsigned)(X ^ 1) * 0x4000u;
  (gapA<X, FIRST, Ms>(c, va, t), ...);
  if constexpr (FIRST) asm volatile("s_waitcnt vmcnt(0) lgkmcnt(0)\n\ts_barrier\n\ts_nop 7" ::: "memory");
  else { asm volatile("s_waitcnt vmcnt(%c16) lgkmcnt(0)" : W4_VLO : "i"(X == 1 ? 1 : 0) : "memory"); asm volatile("s_barrier\n\ts_nop 1" : W4_VUP :: "memory"); }
  GAP();
  if constexpr (!FIRST) {
#pragma unroll
    for (int ks = 0; ks < 4; ++ks)
#pragma unroll
      for (int db = 0; db < 4; ++db) c.Vf[db][ks] = __builtin_shufflevector(c.vlo[db][ks], c.vup[db][ks], 0, 1, 2, 3, 4, 5, 6, 7);
  }
}
template <int X, bool FIRST> DI void phaseA(W4C& c, const int t) { phaseA_<X, FIRST>(c, t, make_iseq<32>{}); }

template <int X, bool INIT> DI void new_reference(W4C& c) {
#pragma unroll
  for (int q = 0; q < 2; ++q) {
    float m0 = max3f(c.S[X][0][q][0], c.S[X][0][q][1], c.S[X][1][q][0]), m1 = max2f(c.S[X][1][q][1], c.S[X][0][q][2]);
#pragma unroll
    for (int e = 2; e < 16; e += 2) { m0 = max3f(m0, c.S[X][0][q][e], c.S[X][0][q][e + 1]); m1 = max3f(m1, c.S[X][1][q][e], c.S[X][1][q][e + 1]); }
    const float mm = max2f(m0, m1);
    auto pr = __builtin_amdgcn_permlane32_swap(__float_as_uint(mm), __float_as_uint(mm), false, false);
    const float rmax = max2f(__uint_as_float(pr[0]), __uint_as_float(pr[1]));
    const float dl = INIT ? rmax : max2f(rmax, 0.f);
    if constexpr (INIT) { c.mhat[q] = dl; c.rs[q][0] = 0.f; c.rs[q][1] = 0.f; c.fct[q] = 1.f; }
    else { c.mhat[q] = fadd_s(c.mhat[q], dl); c.fct[q] = __builtin_amdgcn_exp2f(-dl); c.rs[q][0] = c.rs[q][0] * c.fct[q]; c.rs[q][1] = c.rs[q][1] * c.fct[q]; }
#pragma unroll
    for (int kb = 0; kb < 2; ++kb) { _Pragma("unroll") for (int e = 0; e < 16; ++e) c.S[X][kb][q][e] = fsub_s(c.S[X][kb][q][e], dl); }
    negm_bcast(c.NEGM[q], w4_aone(), -c.mhat[q]);
  }
  asm volatile("s_nop 1");
}
template <int X, bool PEEL, bool ZERO1, int M> DI void gapB(W4C& c, const int t) {
  constexpr int Xp = X ^ 1;
  if constexpr (!PEEL) { pv_mfma<M, ZERO1>(c); GAP(); }
  if constexpr (!PEEL && M == 0) op_add<Xp, 1, 31>(c);
  constexpr int kq = M == 0 ? 0 : M == 11 ? 1 : M == 13 ? 2 : M == 15 ? 3 : -1, vq = M == 18 ? 0 : M == 20 ? 1 : M == 22 ? 2 : M == 24 ? 3 : -1;
  if constexpr (kq >= 0) asm volatile("buffer_load_dwordx4 %0, %1, %2 offen offset:%c3 lds" :: "v"(c.vksrc[kq & 1]), "s"(c.srdK), "s"(c.sk_so), "i"(1024 * kq) : "memory");
  if constexpr (M == 16) asm volatile("s_mov_b32 m0, %0" :: "s"(c.sv_m0) : "memory");
  if constexpr (vq >= 0) asm volatile("buffer_load_dwordx4 %0, %1, %2 offen offset:%c3 lds" :: "v"(c.vvsrc[vq]), "s"(c.srdV), "s"(c.sv_so), "i"(1024 * vq) : "memory");
  if constexpr (M >= 1 && M <= 8) {
    if constexpr (!PEEL) { op_max4<X, M - 1>(c); op_cvt<Xp, 1, (M <= 4 ? 4 + M - 1 : 12 + M - 5)>(c); }
    k_read<M - 1, Xp>(c);
  }
  if constexpr (M == 9) {
    if constexpr (PEEL) new_reference<X, true>(c);
    else {
      const float mm = max2f(c.mA, c.mB);
      if (__builtin_expect(__any((int)(mm > ATTN_THR)), 0)) { new_reference<X, false>(c); c.pend = true; }
    }
  }
  if constexpr (M >= EB0) {
    constexpr int e0 = eb_lo(M), e1 = eb_lo(M + 1), p0 = eb_lo(M - 1);
    op_exp<X, 0, e0>(c);
    if constexpr (M > EB0) op_add<X, 0, p0>(c);
    if constexpr (e0 + 1 < e1) op_exp<X, 0, e0 + 1>(c);
    if constexpr (M > EB0 && p0 + 1 < e0) op_add<X, 0, p0 + 1>(c);
    constexpr int kr = M == 10 ? 8 : M == 12 ? 9 : M == 14 ? 10 : M == 17 ? 11 : M == 19 ? 12 : M == 21 ? 13 : M == 23 ? 14 : M == 25 ? 15 : -1;
    if constexpr (kr >= 0) k_read<kr, Xp>(c);
  }
  if constexpr (X == 0 && M == 26) {
    unsigned plo, phi;
    asm volatile("v_readlane_b32 %0, %2, %4\n\tv_readlane_b32 %1, %3, %4\n\ts_mov_b32 m0, %5" : "=s"(plo), "=s"(phi) : "v"(c.pf_lo), "v"(c.pf_hi), "s"((unsigned)(t >> 1)), "s"(c.pf_m0) : "memory");
    c.sk_m0 = plo; c.sk_so = phi;
  }
  if constexpr (X == 0 && M == 28) {
    const unsigned long long pb = ((unsigned long long)c.sk_so << 32) | c.sk_m0;
    asm volatile("global_load_lds_dword %0, %1" :: "v"(c.pf_off), "s"(pb) : "memory");
  }
  if constexpr (M == 31) asm volatile("" :: "v"(c.S[X][0][0]), "v"(c.S[X][0][1]));
  GAP();
}
template <int R> DI void resc4(float f) {
  float t0, t1, t2, t3;
  asm volatile("v_accvgpr_read_b32 %0, a[%c5]\n\tv_accvgpr_read_b32 %1, a[%c6]\n\tv_accvgpr_read_b32 %2, a[%c7]\n\tv_accvgpr_read_b32 %3, a[%c8]\n\t"
               "v_mul_f32 %0, %4, %0\n\tv_mul_f32 %1, %4, %1\n\tv_mul_f32 %2, %4, %2\n\tv_mul_f32 %3, %4, %3\n\t"
               "v_accvgpr_write_b32 a[%c5], %0\n\tv_accvgpr_write_b32 a[%c6], %1\n\tv_accvgpr_write_b32 a[%c7], %2\n\tv_accvgpr_write_b32 a[%c8], %3"
               : "=&v"(t0), "=&v"(t1), "=&v"(t2), "=&v"(t3) : "v"(f), "i"(R), "i"(R + 1), "i"(R + 2), "i"(R + 3) : W4_ACLOB);
}
template <int... Is> DI void resc_all(W4C& c, iseq<Is...>) { (resc4<4 * Is>(c.fct[(Is >> 2) & 1]), ...); }
template <int X, bool PEEL, bool ZERO1, int... Ms> DI void phaseB_(W4C& c, const int t, iseq<Ms...>) {
  (gapB<X, PEEL, ZERO1, Ms>(c, t), ...);
  if constexpr (!PEEL) {
    if (__builtin_expect(c.pend, 0)) {
      asm volatile("s_nop 7\n\ts_nop 7" ::: W4_ACLOB);
      resc_all(c, make_iseq<32>{});
      asm volatile("s_nop 1" ::: W4_ACLOB);
      c.pend = false;
    }
  }
  asm volatile("s_waitcnt lgkmcnt(0)" ::: "memory");
  GAP();
}
template <int X, bool PEEL, bool ZERO1> DI void phaseB(W4C& c, const int t) { phaseB_<X, PEEL, ZERO1>(c, t, make_iseq<32>{}); }
template <int... Is> DI void k_reads0(W4C& c, iseq<Is...>) { (k_read<Is, 0>(c), ...); }
template <int... Is> DI void tr_reads(W4C& c, unsigned va, iseq<Is...>) { (tr_read<Is>(c, va), ...); }
template <int... Is> DI void drain_exp(W4C& c, iseq<Is...>) { (op_exp<1, 1, Is>(c), ...); }
template <int... Is> DI void drain_cvt0(W4C& c, iseq<Is...>) { (op_cvt<1, 0, Is>(c), ...); }
template <int... Is> DI void drain_cvt1(W4C& c, iseq<Is...>) { (op_cvt<1, 1, Is>(c), ...); }
template <int... Is> DI void drain_add(W4C& c, iseq<Is...>) { (op_add<1, 1, Is>(c), ...); }
template <int... Ms> DI void pv_all(W4C& c, iseq<Ms...>) { (pv_mfma<Ms, false>(c), ...); }
template <int R> DI u32x4 acc_pack8(float f) {
  float o[8];
  asm volatile("v_accvgpr_read_b32 %0, a[%c8]\n\tv_accvgpr_read_b32 %1, a[%c9]\n\tv_accvgpr_read_b32 %2, a[%c10]\n\tv_accvgpr_read_b32 %3, a[%c11]\n\t"
               "v_accvgpr_read_b32 %4, a[%c12]\n\tv_accvgpr_read_b32 %5, a[%c13]\n\tv_accvgpr_read_b32 %6, a[%c14]\n\tv_accvgpr_read_b32 %7, a[%c15]"
               : "=v"(o[0]), "=v"(o[1]), "=v"(o[2]), "=v"(o[3]), "=v"(o[4]), "=v"(o[5]), "=v"(o[6]), "=v"(o[7])
               : "i"(R), "i"(R + 1), "i"(R + 2), "i"(R + 3), "i"(R + 4), "i"(R + 5), "i"(R + 6), "i"(R + 7));
  u32x4 w = {cvtpk(o[0] * f, o[1] * f), cvtpk(o[2] * f, o[3] * f), cvtpk(o[4] * f, o[5] * f), cvtpk(o[6] * f, o[7] * f)};
  return w;
}
template <int I> DI void q_conv1(float C) {
  constexpr int qb = I >> 3, d0 = I & 7, nb = d0 >> 1, s = d0 & 1, aq = AQ_(qb, d0);
  const u32x4 w = acc_pack8<AO_(nb, qb) + 8 * s>(C);
  asm volatile("v_accvgpr_write_b32 a[%c4], %0\n\tv_accvgpr_write_b32 a[%c5], %1\n\tv_accvgpr_write_b32 a[%c6], %2\n\tv_accvgpr_write_b32 a[%c7], %3"
               :: "v"(w[0]), "v"(w[1]), "v"(w[2]), "v"(w[3]), "i"(aq), "i"(aq + 1), "i"(aq + 2), "i"(aq + 3) : W4_ACLOB_HI);
}
template <int... Is> DI void q_conv(float C, iseq<Is...>) { (q_conv1<Is>(C), ...); }
template <int I> DI void o_store1(char* base, float il0, float il1) {
  constexpr int qb = I >> 3, db = (I >> 1) & 3, s = I & 1;
  const u32x4 w = acc_pack8<AO_(db, qb) + 8 * s>(qb ? il1 : il0);
  *(u32x4*)(base + (size_t)qb * (256u * 1024u) + (2 * db + s) * 1024) = w;
}
template <int... Is> DI void o_store(char* base, float il0, float il1, iseq<Is...>) { (o_store1<Is>(base, il0, il1), ...); }

using QCfg = GCfg<32, 4, 64, 8, false, 0, 6, 2, 1, 1, true>;
constexpr int SHM_ATTN = 0x10000 + QCfg::LDSB;

#define ST(i)
__global__ __launch_bounds__(256, 1) void k_attn(const u16* __restrict__ H1f, const u16* __restrict__ Wq, const u16* __restrict__ Kc, const u16* __restrict__ Vc, u16* __restrict__ Of,
                                                        const u16* __restrict__ Wo, const u16* __restrict__ Wg, const u16* __restrict__ Wu, const u16* __restrict__ Wd, const u16* __restrict__ Wm) {
  ST(0); KST_BEGIN(2);
  extern __shared__ __attribute__((aligned(16))) char lds[];
  const unsigned tid = threadIdx.x, wid = rfl(tid >> 6);
  const int bid = blockIdx.x, xcd = bid & 7, j = bid >> 3;
  const int head = 4 * xcd + (j & 3), y = j >> 2, kvh = head >> 3;
  const unsigned ldsb = (unsigned)(size_t)lds;
  {
    const unsigned lane = tid & 63u;
    GState<QCfg> g;
    const int mb0 = y * 8 + wid * 2;
    constexpr unsigned mbstride = 1024u * 32u * 2u;
    g.srdA = mk_srd((const char*)H1f + (size_t)mb0 * mbstride, 2 * mbstride);
    g.voffA[0] = lane * 16u; g.voffA[1] = lane * 16u + mbstride;
    constexpr unsigned ldb = 4096u * 2u;
    g.srdB = mk_srd((const char*)Wq + (size_t)head * 256, 1024u * ldb - (unsigned)head * 256u);
    g_setup<QCfg, true>(g, ldsb + QGEMM_LDS, lane, wid, ldb);
    { const unsigned gw = blockIdx.x * 4u + wid;
      g.tch[0] = (unsigned long long)(size_t)((const char*)Wq + (size_t)gw * 4096); g.tch[1] = g.tch[0] + (4096u * 1024u);
      g.tch[2] = (unsigned long long)(size_t)((gw < 512u ? (const char*)Kc : (const char*)Vc) + (size_t)(gw & 511u) * 4096);
      g.tch_off = lane * 64u; }
    gemm_run<QCfg>(g, 16);
  }
  ST(1);
  unsigned lane; asm volatile("v_mbcnt_lo_u32_b32 %0, -1, 0\n\tv_mbcnt_hi_u32_b32 %0, -1, %0" : "=v"(lane));
  const unsigned l32 = lane & 31u, hi = lane >> 5;
  constexpr int T = 32;
  W4C c;
#pragma unroll
  for (int jj = 0; jj < 4; ++jj) c.vkb[jj] = ldsb + KRING + 256u * l32 + 16u * ((2u * jj + hi) ^ (l32 & 7u));
  c.vvb = ldsb + VRING + (((lane & 3u) << 3) | (((lane >> 2) & 3u) << 6) | (((lane >> 4) & 1u) << 5) | (hi << 8));
#pragma unroll
  for (int par = 0; par < 2; ++par) c.vksrc[par] = KRS * (lane >> 4) + 16u * ((lane & 15u) ^ (4u * par + (lane >> 4)));
#pragma unroll
  for (int q = 0; q < 4; ++q) c.vvsrc[q] = KRS * ((lane >> 2) & 7u) + 64u * hi + 16u * (lane & 3u) + KRS * 8u * (q >> 1) + 128u * (q & 1) + (3072u - 1024u * q);
  c.m0k = rfl(ldsb + KRING + 4096u * wid); c.m0v = rfl(ldsb + VRING + 4096u * wid);
  c.sofk = rfl(KRS * 16u * wid); c.sofv = rfl(KRS * 16u * wid) - 3072u;
  c.srdK = mk_srd((const char*)Kc + (size_t)kvh * (2048u * 256u), 2048u * 256u);
  c.srdV = mk_srd((const char*)Vc + (size_t)kvh * (2048u * 256u), 2048u * 256u);
  {
    const unsigned gw = blockIdx.x * 4u + wid;
    const char* p;
    const unsigned c2 = gw + 1024u, c2c = c2 < 1792u ? c2 : gw;
    if (lane == 0) p = (const char*)Wo + (size_t)gw * 4096; else if (lane == 1) p = (const char*)Wo + (size_t)c2 * 4096;
    else if (lane == 2) p = (const char*)Wg + (size_t)gw * 4096; else if (lane == 3) p = (const char*)Wg + (size_t)c2c * 4096;
    else if (lane == 4) p = (const char*)Wu + (size_t)gw * 4096; else if (lane == 5) p = (const char*)Wu + (size_t)c2c * 4096;
    else if (lane == 6) p = (const char*)Wd + (size_t)gw * 4096; else if (lane == 7) p = (const char*)Wd + (size_t)c2c * 4096;
    else if (lane < 11) p = (const char*)Wm + (size_t)(gw + 1024u * (lane - 8u)) * 4096;
    else p = (const char*)Wo + (size_t)gw * 4096;
    c.pf_lo = (unsigned)(size_t)p; c.pf_hi = (unsigned)((size_t)p >> 32);
    c.pf_off = lane * 64u; c.pf_m0 = rfl(ldsb + QGEMM_LDS + 256u * wid);
  }
  {
    const unsigned v0 = KRS * ((lane >> 2) & 7u) + 64u * hi + 16u * (lane & 3u);
#pragma unroll
    for (int q = 0; q < 4; ++q)
      asm volatile("s_nop 4\n\ts_mov_b32 m0, %0\n\ts_nop 0\n\tbuffer_load_dwordx4 %1, %2, %3 offen offset:%c4 lds" :: "s"(c.m0k), "v"(c.vksrc[q & 1]), "s"(c.srdK), "s"(c.sofk), "i"(1024 * q) : "memory");
#pragma unroll
    for (int q = 0; q < 4; ++q)
      asm volatile("s_nop 4\n\ts_mov_b32 m0, %0\n\ts_nop 0\n\tbuffer_load_dwordx4 %1, %2, %3 offen lds" :: "s"(c.m0v + 1024u * q), "v"(v0), "s"(c.srdV), "s"(c.sofk + KRS * 8u * (q >> 1) + 128u * (q & 1)) : "memory");
#pragma unroll
    for (int q = 0; q < 4; ++q)
      asm volatile("s_nop 4\n\ts_mov_b32 m0, %0\n\ts_nop 0\n\tbuffer_load_dwordx4 %1, %2, %3 offen offset:%c4 lds" :: "s"(c.m0k + 0x4000u), "v"(c.vksrc[q & 1]), "s"(c.srdK), "s"(c.sofk + KTILEB), "i"(1024 * q) : "memory");
  }
  asm volatile("s_nop 7\n\ts_nop 7" ::: "memory");
  q_conv(0.12751743082f, make_iseq<16>{});
  asm volatile("s_waitcnt vmcnt(0)\n\ts_barrier" ::: "memory");
  k_reads0(c, make_iseq<16>{});
  asm volatile("s_waitcnt lgkmcnt(0)\n\ts_nop 1" ::: "memory");
  c.mhat[0] = c.mhat[1] = 0.f; c.fct[0] = c.fct[1] = 1.f; c.pend = false; c.rs[0][0] = c.rs[0][1] = c.rs[1][0] = c.rs[1][1] = 0.f;
  ST(2);
  phaseA<0, true>(c, 0);  phaseB<0, true, false>(c, 0);
  phaseA<1, false>(c, 1); phaseB<1, false, true>(c, 1);
  for (int t = 2; t < T; t += 2) {
    phaseA<0, false>(c, t);     phaseB<0, false, false>(c, t);
    phaseA<1, false>(c, t + 1); phaseB<1, false, false>(c, t + 1);
  }
  ST(3);
  float lsum[2];
  {
    tr_reads(c, c.vvb + 0x4000u, make_iseq<32>{});
    op_add<1, 0, eb_lo(31)>(c); if constexpr (eb_lo(31) + 1 < 32) op_add<1, 0, eb_lo(31) + 1>(c);
    drain_exp(c, make_iseq<32>{});
    drain_cvt0(c, make_iseq<16>{});
    drain_cvt1(c, make_iseq<16>{});
    GAP(); asm volatile("s_nop 1"); GAP();
    drain_add(c, make_iseq<32>{});
    asm volatile("s_waitcnt lgkmcnt(0)" : W4_VLO :: "memory"); asm volatile("" : W4_VUP :: "memory");
    GAP();
#pragma unroll
    for (int ks = 0; ks < 4; ++ks)
#pragma unroll
      for (int db = 0; db < 4; ++db) c.Vf[db][ks] = __builtin_shufflevector(c.vlo[db][ks], c.vup[db][ks], 0, 1, 2, 3, 4, 5, 6, 7);
    asm volatile("s_nop 1" ::: "memory");
    pv_all(c, make_iseq<32>{});
#pragma unroll
    for (int qb = 0; qb < 2; ++qb) { const float rs = c.rs[qb][0] + c.rs[qb][1];
      auto pr = __builtin_amdgcn_permlane32_swap(__float_as_uint(rs), __float_as_uint(rs), false, false);
      lsum[qb] = __uint_as_float(pr[0]) + __uint_as_float(pr[1]); }
    asm volatile("s_nop 7\n\ts_nop 7" ::: "memory");
  }
  {
    unsigned lb; asm volatile("v_mbcnt_lo_u32_b32 %0, -1, 0\n\tv_mbcnt_hi_u32_b32 %0, -1, %0" : "=v"(lb));
    const float il0 = __builtin_amdgcn_rcpf(lsum[0]), il1 = __builtin_amdgcn_rcpf(lsum[1]);
    char* base = (char*)Of + ((size_t)(y * 8 + wid * 2) * 256 + head * 8) * 1024 + lb * 16u;
    o_store(base, il0, il1, make_iseq<16>{});
    asm volatile("s_waitcnt vmcnt(0)" ::: "memory");
  }
  ST(4); KST_END(2);
}
template <int N> DI void touch(const char* p, int stride) {
  unsigned d[N];
#pragma unroll
  for (int i = 0; i < N; ++i) d[i] = *(const unsigned*)(p + (size_t)i * stride);
#pragma unroll
  for (int i = 0; i < N; ++i) asm volatile("" :: "v"(d[i]));
}

template <int NT, int KL> DI void mod_body(const u16* __restrict__ c, const u16* __restrict__ wmod, float* __restrict__ dst, int cb, int kh, float* red  ) {
  const int tid = threadIdx.x, cg = tid & 7, kg = tid >> 3, wv = tid >> 6;
  constexpr int NG = NT / 8, NI = KL / NG;
  u32x4 w[NI]; u16 cv[NI];
#pragma unroll
  for (int i = 0; i < NI; ++i) { const int k = kh * KL + kg + NG * i; w[i] = *(const u32x4*)(wmod + (size_t)k * 6144 + cb * 64 + cg * 8); cv[i] = c[k]; }
  float acc[8] = {0, 0, 0, 0, 0, 0, 0, 0};
#pragma unroll
  for (int i = 0; i < NI; ++i) {
    const float xx = bf2f(cv[i]), s = rbf(xx / (1.f + __expf(-xx)));
#pragma unroll
    for (int jj = 0; jj < 4; ++jj) { acc[2 * jj] += s * __uint_as_float(w[i][jj] << 16); acc[2 * jj + 1] += s * __uint_as_float(w[i][jj] & 0xffff0000u); }
  }
#pragma unroll
  for (int jj = 0; jj < 8; ++jj) { float a = acc[jj]; a += __shfl_xor(a, 8); a += __shfl_xor(a, 16); a += __shfl_xor(a, 32); acc[jj] = a; }
  if ((tid & 63) < 8) {
#pragma unroll
    for (int jj = 0; jj < 8; ++jj) red[wv * 64 + cg * 8 + jj] = acc[jj];
  }
  __syncthreads();
  if (tid < 64) { float s = 0;
#pragma unroll
    for (int g = 0; g < NT / 64; ++g) s += red[g * 64 + tid];
    dst[tid] = s; }
}
__global__ __launch_bounds__(512) void k_pre(const u16* __restrict__ c, const u16* __restrict__ wmod, float* __restrict__ modA, const u16* __restrict__ x, unsigned* __restrict__ flg) {
  __shared__ float red[8 * 64];
  const int b = blockIdx.x;
  KST_BEGIN(0);
  unsigned dx = 0;
  if (threadIdx.x == 0) flg[b * 64] = 0u;
  if (threadIdx.x < 256) dx = *(const unsigned*)((const char*)x + (size_t)b * 16384 + threadIdx.x * 64);
  mod_body<512, 128>(c, wmod, modA + (b & 7) * 2048 + (b >> 3) * 64, b >> 3, b & 7, red);
  asm volatile("" :: "v"(dx));
  KST_END(0);
}
DI f32x4 modv(const float* __restrict__ modp, int n) { const f32x4 a = *(const f32x4*)(modp + n), b = *(const f32x4*)(modp + 6144 + n); f32x4 o; for (int e = 0; e < 4; ++e) o[e] = rbf(a[e] + b[e]); return o; }
DI void unpack8(const u32x4& w, float* v) {
#pragma unroll
  for (int j = 0; j < 4; ++j) { v[2 * j] = __uint_as_float(w[j] << 16); v[2 * j + 1] = __uint_as_float(w[j] & 0xffff0000u); }
}
DI u32x4 pack8(const float* o) { return (u32x4){cvtpk(o[0], o[1]), cvtpk(o[2], o[3]), cvtpk(o[4], o[5]), cvtpk(o[6], o[7])}; }

DI void rbf2(float& a, float& b) { const unsigned w = cvtpk(a, b); a = __uint_as_float(w << 16); b = __uint_as_float(w & 0xffff0000u); }
DI void rbf8(float* v) { rbf2(v[0], v[1]); rbf2(v[2], v[3]); rbf2(v[4], v[5]); rbf2(v[6], v[7]); }
DI void modv8(const float* __restrict__ modp, int n, float* o) {
  const f32x4 a0 = *(const f32x4*)(modp + n), b0 = *(const f32x4*)(modp + 6144 + n), a1 = *(const f32x4*)(modp + n + 8), b1 = *(const f32x4*)(modp + 6144 + n + 8);
#pragma unroll
  for (int e = 0; e < 4; ++e) { o[e] = a0[e] + b0[e]; o[4 + e] = a1[e] + b1[e]; }
  rbf8(o);
}
template <int MODE>
__global__ __launch_bounds__(1024) void k_ln(const u16* __restrict__ x, const u16* __restrict__ Pf, const float* __restrict__ modp, u16* __restrict__ xf, u16* __restrict__ hf) {
  __shared__ float red[2][16][8];
  __shared__ __attribute__((aligned(16))) float msum[MODE == 0 ? 2048 : 4];
  const int tid = threadIdx.x;
  KST_BEGIN(MODE == 0 ? 1 : 4);
  const int r = tid & 7, c = tid >> 3, h = c & 1, wid = tid >> 6;
  const int row = blockIdx.x * 8 + r, mb = row >> 5, ml = row & 31;
  const size_t fo = ((size_t)mb * 64 + (c >> 1)) * 512 + (h * 32 + ml) * 8;
  const int n = 16 * (c >> 1) + 4 * h;
  float v[8], sh[8], sc[8];
  if (MODE == 0) {
    const u32x4 xw = *(const u32x4*)(x + (size_t)row * 1024 + 8 * c);
    { f32x2 a = {0.f, 0.f};
#pragma unroll
      for (int ks = 0; ks < 8; ++ks) a += *(const f32x2*)(modp + ks * 2048 + 2 * tid);
      float a0 = a[0], a1 = a[1]; rbf2(a0, a1); *(f32x2*)(msum + 2 * tid) = (f32x2){a0, a1}; }
    const unsigned s0 = h ? xw[0] : xw[2], s1 = h ? xw[1] : xw[3];
    const unsigned g0 = __shfl_xor(s0, 8), g1 = __shfl_xor(s1, 8);
    const u32x4 f = h ? (u32x4){g0, g1, xw[2], xw[3]} : (u32x4){xw[0], xw[1], g0, g1};
    *(u32x4*)(xf + fo) = f;
    unpack8(f, v);
  } else {
    const u32x4 xw = *(const u32x4*)(xf + fo), p0 = *(const u32x4*)(Pf + fo), p1 = *(const u32x4*)(Pf + fo + (size_t)2048 * 1024);
    float ga[8], a[8], b[8];
    modv8(modp, 3072 + n, sh); modv8(modp, 4096 + n, sc); modv8(modp, 2048 + n, ga);
    unpack8(xw, v); unpack8(p0, a); unpack8(p1, b);
#pragma unroll
    for (int j = 0; j < 8; ++j) a[j] += b[j];
    rbf8(a);
#pragma unroll
    for (int j = 0; j < 8; ++j) a[j] *= ga[j];
    rbf8(a);
#pragma unroll
    for (int j = 0; j < 8; ++j) v[j] += a[j];
    const u32x4 w = pack8(v);
    *(u32x4*)(xf + fo) = w;
    unpack8(w, v);
  }
  float s = ((v[0] + v[1]) + (v[2] + v[3])) + ((v[4] + v[5]) + (v[6] + v[7]));
  s += __shfl_xor(s, 8); s += __shfl_xor(s, 16); s += __shfl_xor(s, 32);
  if ((tid & 63) < 8) red[0][wid][r] = s;
  __syncthreads();
  if (MODE == 0) {
    const f32x4 a0 = *(const f32x4*)(msum + n), a1 = *(const f32x4*)(msum + n + 8), b0 = *(const f32x4*)(msum + 1024 + n), b1 = *(const f32x4*)(msum + 1024 + n + 8);
#pragma unroll
    for (int e = 0; e < 4; ++e) { sh[e] = a0[e]; sh[4 + e] = a1[e]; sc[e] = b0[e]; sc[4 + e] = b1[e]; }
  }
  float mean = 0;
#pragma unroll
  for (int w = 0; w < 16; ++w) mean += red[0][w][r];
  mean *= (1.f / 1024.f);
  float qv = 0;
#pragma unroll
  for (int j = 0; j < 8; ++j) { const float d = v[j] - mean; qv += d * d; }
  qv += __shfl_xor(qv, 8); qv += __shfl_xor(qv, 16); qv += __shfl_xor(qv, 32);
  if ((tid & 63) < 8) red[1][wid][r] = qv;
  __syncthreads();
  float var = 0;
#pragma unroll
  for (int w = 0; w < 16; ++w) var += red[1][w][r];
  var = rbf(var * (1.f / 1024.f));
  const float mb16 = rbf(mean), rsd = 1.f / rbf(sqrtf(rbf(var + 1e-5f)));
#pragma unroll
  for (int j = 0; j < 8; ++j) { v[j] -= mb16; sc[j] += 1.f; }
  rbf8(v); rbf8(sc);
#pragma unroll
  for (int j = 0; j < 8; ++j) v[j] *= rsd;
  rbf8(v);
#pragma unroll
  for (int j = 0; j < 8; ++j) v[j] *= sc[j];
  rbf8(v);
#pragma unroll
  for (int j = 0; j < 8; ++j) v[j] += sh[j];
  *(u32x4*)(hf + fo) = pack8(v);
  KST_END(MODE == 0 ? 1 : 4);
}
#define SK_PDT 2
using SCfg = GCfg<32, 4, 32, 8, false, 0, 5, SK_PDT, 2, 1>;
template <int K, bool FIN, int NT0>
__global__ __launch_bounds__(256, 1) void k_gemm_sk(const u16* __restrict__ Af, const u16* __restrict__ B, u16* __restrict__ Pf,
                                                    const u16* __restrict__ cvec, const u16* __restrict__ wmod, float* __restrict__ modp,
                                                    const u16* __restrict__ x1f, u16* __restrict__ out, unsigned* __restrict__ flg) {
  using C = SCfg;
  extern __shared__ __attribute__((aligned(16))) char lds[];
  KST_BEGIN(K == 4096 ? 3 : 6);
  if (K == 4096 && blockIdx.x >= 256) { const int b = blockIdx.x - 256; mod_body<256, 512>(cvec, wmod, modp + (b & 1) * 6144 + (32 + (b >> 1)) * 64, 32 + (b >> 1), b & 1, (float*)lds); KST_END(3); return; }
  constexpr int N = 1024;
  const unsigned tid = threadIdx.x, lane = tid & 63u, wid = rfl(tid >> 6);
  const int bid = blockIdx.x, xcd = bid & 7, j = bid >> 3;
  const int kh = xcd & 1, mg = xcd >> 1;
  const int mt = mg * 4 + (j & 3), nt = j >> 2;
  const int k0 = kh ? NT0 * 64 : 0, klen = kh ? K - NT0 * 64 : NT0 * 64;
  GState<C> g;
  const int mb0 = mt * 4 + wid;
  constexpr unsigned mbstride = (unsigned)K * 64u;
  g.srdA = mk_srd((const char*)Af + (size_t)mb0 * mbstride + (size_t)k0 * 64, (unsigned)klen * 64u);
  g.voffA[0] = lane * 16u;
  constexpr unsigned ldb = (unsigned)N * 2u;
  g.srdB = mk_srd((const char*)B + (size_t)k0 * ldb + (size_t)nt * 256, (unsigned)klen * ldb - (unsigned)nt * 256u);
  g_setup<C>(g, (unsigned)(size_t)lds, lane, wid, ldb);
  gemm_run<C>(g, klen / 64);
  const unsigned fo = (((unsigned)mb0 * 64u + (unsigned)nt * 8u) * 512u + lane * 8u) * 2u;
  if constexpr (!FIN) {
#pragma unroll
    for (int n = 0; n < 4; ++n)
#pragma unroll
      for (int s = 0; s < 2; ++s) {
        u32x4 w = {cvtpk(g.acc[n][0][8 * s], g.acc[n][0][8 * s + 1]), cvtpk(g.acc[n][0][8 * s + 2], g.acc[n][0][8 * s + 3]), cvtpk(g.acc[n][0][8 * s + 4], g.acc[n][0][8 * s + 5]), cvtpk(g.acc[n][0][8 * s + 6], g.acc[n][0][8 * s + 7])};
        *(u32x4*)((char*)Pf + (size_t)kh * (4u << 20) + fo + (2 * n + s) * 1024) = w;
      }
  } else {
    const __amdgpu_buffer_rsrc_t srdP = mk_srd(Pf, 4u << 20);
    unsigned* flag = flg + (128 + mt * 8 + nt) * 64;
    if (kh == 0) {
#pragma unroll
      for (int n = 0; n < 4; ++n)
#pragma unroll
        for (int s = 0; s < 2; ++s) {
          u32x4 w = {cvtpk(g.acc[n][0][8 * s], g.acc[n][0][8 * s + 1]), cvtpk(g.acc[n][0][8 * s + 2], g.acc[n][0][8 * s + 3]), cvtpk(g.acc[n][0][8 * s + 4], g.acc[n][0][8 * s + 5]), cvtpk(g.acc[n][0][8 * s + 6], g.acc[n][0][8 * s + 7])};
          __builtin_amdgcn_raw_buffer_store_b128(w, srdP, fo + (2 * n + s) * 1024, 0, 16);
        }
      asm volatile("s_waitcnt vmcnt(0)" ::: "memory");
      __syncthreads();
      if (tid == 0) __hip_atomic_store(flag, 1u, __ATOMIC_RELAXED, __HIP_MEMORY_SCOPE_AGENT);
    } else {
      const unsigned h = lane >> 5;
      u32x4 xw[8]; float gb[8][8];
#pragma unroll
      for (int f = 0; f < 8; ++f) { xw[f] = *(const u32x4*)((const char*)x1f + fo + f * 1024); modv8(modp, 5120 + nt * 128 + 16 * f + 4 * h, gb[f]); }
      if (wid == 0) {
        for (unsigned spins = 0; spins < (1u << 22); ++spins) {
          if ((unsigned)__builtin_amdgcn_readfirstlane(__hip_atomic_load(flag, __ATOMIC_RELAXED, __HIP_MEMORY_SCOPE_AGENT)) != 0u) break;
          __builtin_amdgcn_s_sleep(1);
        }
      }
      asm volatile("" ::: "memory"); __builtin_amdgcn_s_barrier(); asm volatile("" ::: "memory");
      u32x4 pw[8];
#pragma unroll
      for (int f = 0; f < 8; ++f) pw[f] = __builtin_amdgcn_raw_buffer_load_b128(srdP, fo + f * 1024, 0, 16);
      u16* orow = out + (size_t)(mb0 * 32 + (lane & 31u)) * 1024 + nt * 128 + 8 * h;
#pragma unroll
      for (int f = 0; f < 8; ++f) {
        float v[8], a[8];
        unpack8(xw[f], v); unpack8(pw[f], a);
#pragma unroll
        for (int jj = 0; jj < 8; ++jj) a[jj] += g.acc[f >> 1][0][8 * (f & 1) + jj];
        rbf8(a);
#pragma unroll
        for (int jj = 0; jj < 8; ++jj) a[jj] *= gb[f][jj];
        rbf8(a);
#pragma unroll
        for (int jj = 0; jj < 8; ++jj) v[jj] += a[jj];
        const u32x4 w = pack8(v);
        auto r0 = __builtin_amdgcn_permlane32_swap(w[0], w[2], false, false); auto r1 = __builtin_amdgcn_permlane32_swap(w[1], w[3], false, false);
        *(u32x4*)(orow + 16 * f) = (u32x4){r0[0], r1[0], r0[1], r1[1]};
      }
    }
  }
  KST_END(K == 4096 ? 3 : 6);
}
#define SK2_NT0 28
DI float silu_mul(float g, float u) { return g * __builtin_amdgcn_rcpf(1.f + __builtin_amdgcn_exp2f(g * -1.4426950408889634f)) * u; }
using UCfg = GCfg<32, 8, 32, 14, true, 0, 4, 2, 1, 2>;
__global__ __launch_bounds__(512, 2) void k_gemm_gu(const u16* __restrict__ Af, const u16* __restrict__ Wg, const u16* __restrict__ Wu, u16* __restrict__ Act) {
  using C = UCfg;
  extern __shared__ __attribute__((aligned(16))) char lds[];
  constexpr int K = 1024, F = 3584;
  KST_BEGIN(5);
  const unsigned tid = threadIdx.x, lane = tid & 63u, wid = rfl(tid >> 6);
  const int bid = blockIdx.x, xcd = bid & 7, j = bid >> 3;
  const int mt = (xcd & 1) * 4 + (j & 3), nt = (xcd >> 1) * 8 + (j >> 2);
  GState<C> g;
  const int mb0 = mt * 8 + wid;
  constexpr unsigned mbstride = (unsigned)K * 64u;
  g.srdA = mk_srd((const char*)Af + (size_t)mb0 * mbstride, mbstride);
  g.voffA[0] = lane * 16u;
  constexpr unsigned ldb = F * 2u;
  const bool up = wid >= 4;
  g.srdB = mk_srd((const char*)(up ? Wu : Wg) + (size_t)nt * 224, (unsigned)K * ldb - (unsigned)nt * 224u);
  g_setup<C>(g, (unsigned)(size_t)lds, lane, wid, ldb);
  gemm_run<C>(g, K / 64);
#pragma unroll
  for (int n = 0; n < 7; ++n) {
    float o[8];
#pragma unroll
    for (int e = 0; e < 8; ++e) o[e] = silu_mul(g.acc[n][0][e], g.acc[n][0][e + 8]);
    u32x4 w = {cvtpk(o[0], o[1]), cvtpk(o[2], o[3]), cvtpk(o[4], o[5]), cvtpk(o[6], o[7])};
    *(u32x4*)(Act + ((size_t)mb0 * (F >> 4) + (nt * 7 + n)) * 512 + lane * 8) = w;
  }
  KST_END(5);
}

extern "C" void kernel_launch(void* const* d_in, const int* in_sizes, int n_in, void* d_out, int out_size, void* d_ws, size_t ws_size, hipStream_t stream) {
  const u16* x = (const u16*)d_in[0]; const u16* kc = (const u16*)d_in[1]; const u16* vc = (const u16*)d_in[2];
  const u16* wq = (const u16*)d_in[3]; const u16* wo = (const u16*)d_in[4];
  const u16* wg = (const u16*)d_in[6]; const u16* wu = (const u16*)d_in[7]; const u16* wd = (const u16*)d_in[8];
  const u16* c = (const u16*)d_in[9]; const u16* wmod = (const u16*)d_in[10];
  u16* out = (u16*)d_out;
  char* ws = (char*)d_ws;
  float* mod = (float*)ws;
  u16* hf = (u16*)(ws + (1 << 20));
  u16* x1f = (u16*)(ws + (8 << 20));
  u16* of = (u16*)(ws + (16 << 20));
  u16* P = (u16*)(ws + (32 << 20));
  u16* act = (u16*)(ws + (64 << 20));
  hipFuncSetAttribute((const void*)k_attn, hipFuncAttributeMaxDynamicSharedMemorySize, SHM_ATTN);
  hipFuncSetAttribute((const void*)k_gemm_sk<4096, false, 32>, hipFuncAttributeMaxDynamicSharedMemorySize, SCfg::LDSB);
  hipFuncSetAttribute((const void*)k_gemm_sk<3584, true, SK2_NT0>, hipFuncAttributeMaxDynamicSharedMemorySize, SCfg::LDSB);
  hipFuncSetAttribute((const void*)k_gemm_gu, hipFuncAttributeMaxDynamicSharedMemorySize, UCfg::LDSB);
  float* modA = (float*)(ws + (256 << 10));
  unsigned* flg = (unsigned*)(ws + (512 << 10));
  u16* P2 = (u16*)(ws + (48 << 20));
  k_pre<<<256, 512, 0, stream>>>(c, wmod, modA, x, flg);
  k_ln<0><<<256, 1024, 0, stream>>>(x, nullptr, modA, x1f, hf);
  k_attn<<<256, 256, SHM_ATTN, stream>>>(hf, wq, kc, vc, of, wo, wg, wu, wd, wmod);
  k_gemm_sk<4096, false, 32><<<256 + 128, 256, SCfg::LDSB, stream>>>(of, wo, P, c, wmod, mod, nullptr, nullptr, nullptr);
  k_ln<1><<<256, 1024, 0, stream>>>(x, P, mod, x1f, hf);
  k_gemm_gu<<<256, 512, UCfg::LDSB, stream>>>(hf, wg, wu, act);
  k_gemm_sk<3584, true, SK2_NT0><<<256, 256, SCfg::LDSB, stream>>>(act, wd, P2, nullptr, nullptr, mod, x1f, out, flg);
}
```

```cpp
#include <hip/hip_runtime.h>
#include <utility>
#include <type_traits>
typedef unsigned short u16;
typedef short bf16x8 __attribute__((ext_vector_type(8)));
typedef short s16x4 __attribute__((ext_vector_type(4)));
typedef float f32x4 __attribute__((ext_vector_type(4)));
typedef float f32x16 __attribute__((ext_vector_type(16)));
typedef unsigned u32x4 __attribute__((ext_vector_type(4)));
typedef unsigned u32x2 __attribute__((ext_vector_type(2)));
typedef float f32x2 __attribute__((ext_vector_type(2)));
typedef __bf16 bf16x2_t __attribute__((ext_vector_type(2)));
#define DI __device__ __forceinline__
#define LAS __attribute__((address_space(3)))
#define GAP() __builtin_amdgcn_sched_barrier(0)
template <int... Is> using iseq = std::integer_sequence<int, Is...>;
template <int N> using make_iseq = std::make_integer_sequence<int, N>;

#define KST_BEGIN(k)
#define KST_END(k)
DI unsigned rfl(unsigned x) { return __builtin_amdgcn_readfirstlane(x); }
DI float bf2f(u16 v) { return __uint_as_float(((unsigned)v) << 16); }
DI unsigned cvtpk(float lo, float hi) { f32x2 v = {lo, hi}; bf16x2_t b = __builtin_convertvector(v, bf16x2_t); return __builtin_bit_cast(unsigned, b); }
DI float rbf(float f);
DI float rbf(float f) { return __uint_as_float(cvtpk(f, 0.f) << 16); }
#define WT 17
DI __amdgpu_buffer_rsrc_t mk_srd(const void* p, unsigned bytes) { return __builtin_amdgcn_make_buffer_rsrc((void*)p, (short)0, (int)bytes, 0x00020000); }

#define W4_ACLOB "a0","a1","a2","a3","a4","a5","a6","a7","a8","a9","a10","a11","a12","a13","a14","a15","a16","a17","a18","a19","a20","a21","a22","a23","a24","a25","a26","a27","a28","a29","a30","a31","a32","a33","a34","a35","a36","a37","a38","a39","a40","a41","a42","a43","a44","a45","a46","a47","a48","a49","a50","a51","a52","a53","a54","a55","a56","a57","a58","a59","a60","a61","a62","a63","a64","a65","a66","a67","a68","a69","a70","a71","a72","a73","a74","a75","a76","a77","a78","a79","a80","a81","a82","a83","a84","a85","a86","a87","a88","a89","a90","a91","a92","a93","a94","a95","a96","a97","a98","a99","a100","a101","a102","a103","a104","a105","a106","a107","a108","a109","a110","a111","a112","a113","a114","a115","a116","a117","a118","a119","a120","a121","a122","a123","a124","a125","a126","a127"
#define W4_ACLOB_HI "a128","a129","a130","a131","a132","a133","a134","a135","a136","a137","a138","a139","a140","a141","a142","a143","a144","a145","a146","a147","a148","a149","a150","a151","a152","a153","a154","a155","a156","a157","a158","a159","a160","a161","a162","a163","a164","a165","a166","a167","a168","a169","a170","a171","a172","a173","a174","a175","a176","a177","a178","a179","a180","a181","a182","a183","a184","a185","a186","a187","a188","a189","a190","a191","a192","a193","a194","a195","a196","a197","a198","a199","a200","a201","a202","a203","a204","a205","a206","a207","a208","a209","a210","a211","a212","a213","a214","a215","a216","a217","a218","a219","a220","a221","a222","a223","a224","a225","a226","a227","a228","a229","a230","a231","a232","a233","a234","a235","a236","a237","a238","a239","a240","a241","a242","a243","a244","a245","a246","a247","a248","a249","a250","a251","a252","a253","a254","a255"
template <int SH_, int NW_, int WR_, int NB16_, bool GU_, int KORD_, int NS_, int PDT_, int FD_, int BARPOS_, bool ASMACC_ = false>
struct GCfg {
  static constexpr bool ASMACC = ASMACC_;
  static constexpr int SH = SH_, NW = NW_, WR = WR_, NB16 = NB16_, KORD = KORD_, NS = NS_, PDT = PDT_, FD = FD_, BARPOS = BARPOS_;
  static constexpr bool GU = GU_;
  static constexpr int STEPS = SH == 32 ? 4 : 2;
  static constexpr int MR = WR / SH;
  static constexpr int NB = SH == 32 ? NB16 / 2 : NB16;
  static constexpr int NR16 = GU ? NB16 / 2 : NB16;
  static constexpr int PIECES = NB16 * 2;
  static constexpr int PB = (PIECES + NW - 1) / NW;
  static constexpr int STAGE = PB * NW * 1024;
  static constexpr int REGION = STAGE / 2;
  static constexpr int LDSB = NS * STAGE;
  static constexpr int NOPS = STEPS * MR + PB;
  static constexpr int GS = NB * MR;
  static constexpr int G = STEPS * GS;
  static constexpr int BSTEP = STEPS - FD;
  static constexpr int GB = BSTEP * GS + (BARPOS + 1) * MR - 1;
  static constexpr int dma_lo(int ks) { return (PB * ks) / STEPS; }
  static constexpr int step_ops(int ks) { return MR + dma_lo(ks + 1) - dma_lo(ks); }
  static constexpr int step_first(int ks) { return ks * MR + dma_lo(ks); }
  static constexpr int op_step(int j) { int ks = 0; while (ks < STEPS - 1 && j >= step_first(ks + 1)) ++ks; return ks; }
  static constexpr int op_gap(int j) { const int ks = op_step(j), i = j - step_first(ks), n = step_ops(ks); return ks * GS + (i * GS) / n; }
  static constexpr int ops_before(int g) { int c = 0; for (int j = 0; j < NOPS; ++j) if (op_gap(j) < g) ++c; return c; }
  static constexpr int NA(int ks) {
    const int ts = (ks + 1) % STEPS, jt = step_first(ts) + MR - 1;
    const int full = (ks == STEPS - 1) ? PDT - 1 : PDT;
    return (NOPS - 1 - jt) + (full - 1) * NOPS + ops_before(ks * GS);
  }
  static constexpr int NBW() { return (NS - 3) * NOPS + ops_before(GB); }
  static_assert(NS >= 3 && PDT >= 2 && FD <= STEPS - 0 && BSTEP >= 0, "");
  using acc_t = std::conditional_t<SH == 32, f32x16, f32x4>;
};

template <class C>
struct GState {
  typename C::acc_t acc[C::NB][C::MR];
  bf16x8 a[C::PDT * C::STEPS][C::MR];
  s16x4 blo[C::FD][C::NB], bhi[C::FD][C::NB];
  __amdgpu_buffer_rsrc_t srdA, srdB;
  unsigned voffA[C::MR];
  unsigned voffB[C::GU ? C::PB : 1];
  unsigned sB[C::GU ? 1 : C::PB];
  unsigned m0base, vrd, ktileB;
  unsigned long long tch[3]; unsigned tch_off;
};
struct GIter { unsigned va_cur, va_nxt, sA, sA0, m0_fill, sK_fill; };

template <class C, int SL, int OFF, int MRI> DI void a_load1(GState<C>& g, unsigned sA) {
  asm volatile("buffer_load_dwordx4 %0, %1, %2, %3 offen offset:%4" : "=v"(g.a[SL][MRI]) : "v"(g.voffA[MRI]), "s"(g.srdA), "s"(sA), "i"(OFF) : "memory");
}
template <class C, int Q> DI void b_dma1(GState<C>& g, unsigned m0v, unsigned sK) {
  if constexpr (C::GU)
    asm volatile("s_mov_b32 m0, %0\n\ts_nop 0\n\tbuffer_load_dwordx4 %1, %2, %3 offen lds" :: "s"(m0v + 1024u * Q), "v"(g.voffB[Q]), "s"(g.srdB), "s"(sK) : "memory");
  else
    asm volatile("s_mov_b32 m0, %0\n\ts_nop 0\n\tbuffer_load_dwordx4 %1, %2, %3 offen lds" :: "s"(m0v + 1024u * Q), "v"(g.voffB[0]), "s"(g.srdB), "s"(sK + g.sB[Q]) : "memory");
}
template <class C> DI void g_dummy(GState<C>& g) {
  asm volatile("s_mov_b32 m0, %0\n\ts_nop 0\n\tbuffer_load_dwordx4 %1, %2, %3 offen lds" :: "s"(g.m0base + (unsigned)(C::NS - 1) * C::STAGE), "v"(g.voffA[0]), "s"(g.srdA), "s"(0x80000000u) : "memory");
}
template <class C, int TI, int J, bool VA, bool VB> DI void g_op(GState<C>& g, const GIter& it) {
  constexpr int ks = C::op_step(J), i = J - C::step_first(ks);
  if constexpr (i < C::MR) {
    constexpr int PD = C::PDT * C::STEPS, SL = (TI * C::STEPS + ks - 1 + PD) % PD;
    if constexpr (VA) { if constexpr (ks == 0) a_load1<C, SL, 0, i>(g, it.sA0); else a_load1<C, SL, (ks - 1) * 1024, i>(g, it.sA); } else g_dummy<C>(g);
  } else {
    if constexpr (VB) b_dma1<C, C::dma_lo(ks) + i - C::MR>(g, it.m0_fill, it.sK_fill); else g_dummy<C>(g);
  }
}
template <class C, int TI, int GI, int... Js> DI void g_ops_at(GState<C>& g, const GIter& it, iseq<Js...>) {
  ((C::op_gap(Js) == GI ? g_op<C, TI, Js, true, true>(g, it) : (void)0), ...);
}
template <class C> constexpr int b_off(int KS, int NBI, int t) {
  if (C::SH == 32) {
    return ((2 * KS + t) * C::NR16 + (C::GU ? NBI : 2 * NBI)) * 256;
  } else {
    const int kb = 4 * KS + (C::KORD == 0 ? 2 * t : t);
    return (kb * C::NR16 + (C::GU ? NBI / 2 : NBI)) * 256 + ((C::GU && (NBI & 1)) ? C::REGION : 0);
  }
}
template <class C, int BUF, int KS, int NBI> DI void b_read(GState<C>& g, unsigned va) {
  g.blo[BUF][NBI] = __builtin_amdgcn_ds_read_tr16_b64_v4i16((LAS s16x4*)(size_t)(va + b_off<C>(KS, NBI, 0)));
  g.bhi[BUF][NBI] = __builtin_amdgcn_ds_read_tr16_b64_v4i16((LAS s16x4*)(size_t)(va + b_off<C>(KS, NBI, 1)));
}
template <class C, int BUF, int KS, int... Ns> DI void b_read_all(GState<C>& g, unsigned va, iseq<Ns...>) { (b_read<C, BUF, KS, Ns>(g, va), ...); }

template <class C, int N, int SL> DI void a_wait(GState<C>& g) {
  static_assert(N >= 0 && N < 64, "vmcnt range");
  if constexpr (C::MR == 1) asm volatile("s_waitcnt vmcnt(%1)" : "+v"(g.a[SL][0]) : "i"(N) : "memory");
  else if constexpr (C::MR == 2) asm volatile("s_waitcnt vmcnt(%2)" : "+v"(g.a[SL][0]), "+v"(g.a[SL][1]) : "i"(N) : "memory");
  else asm volatile("s_waitcnt vmcnt(%4)" : "+v"(g.a[SL][0]), "+v"(g.a[SL][1]), "+v"(g.a[SL][2]), "+v"(g.a[SL][3]) : "i"(N) : "memory");
}

template <class C, int TI, int GI> DI void g_gap(GState<C>& g, const GIter& it) {
  constexpr int KS = GI / C::GS, r = GI % C::GS, NBI = r / C::MR, MRI = r % C::MR;
  constexpr int SL = TI * C::STEPS + KS, BUF = (C::FD == 1) ? 0 : (KS & 1), KN = (KS + C::FD) % C::STEPS;
  if constexpr (r == 0) { a_wait<C, C::NA(KS), SL>(g); GAP(); }
  {
    const bf16x8 bf = __builtin_shufflevector(g.blo[BUF][NBI], g.bhi[BUF][NBI], 0, 1, 2, 3, 4, 5, 6, 7);
    if constexpr (C::ASMACC) { constexpr int ao = 16 * (C::MR * NBI + MRI);
      asm volatile("v_mfma_f32_32x32x16_bf16 a[%c2:%c3], %0, %1, a[%c2:%c3]" :: "v"(bf), "v"(g.a[SL][MRI]), "i"(ao), "i"(ao + 15)); }
    else if constexpr (C::SH == 32) g.acc[NBI][MRI] = __builtin_amdgcn_mfma_f32_32x32x16_bf16(bf, g.a[SL][MRI], g.acc[NBI][MRI], 0, 0, 0);
    else g.acc[NBI][MRI] = __builtin_amdgcn_mfma_f32_16x16x32_bf16(bf, g.a[SL][MRI], g.acc[NBI][MRI], 0, 0, 0);
  }
  if constexpr (GI == C::GB) {
    GAP();
    static_assert(C::NBW() < 64, "");
    asm volatile("s_waitcnt vmcnt(%0) lgkmcnt(0)\n\ts_barrier" :: "i"(C::NBW()) : "memory");
    GAP();
    b_read_all<C, BUF, KN>(g, it.va_nxt, make_iseq<C::BARPOS + 1>{});
  } else if constexpr (MRI == C::MR - 1) {
    if constexpr (KS == C::BSTEP) { if constexpr (NBI > C::BARPOS) b_read<C, BUF, KN, NBI>(g, it.va_nxt); }
    else if constexpr (KS > C::BSTEP) b_read<C, BUF, KN, NBI>(g, it.va_nxt);
    else b_read<C, BUF, KN, NBI>(g, it.va_cur);
  }
  g_ops_at<C, TI, GI>(g, it, make_iseq<C::NOPS>{});
  GAP();
}
template <class C, int TI, int... Gs> DI void g_gaps(GState<C>& g, const GIter& it, iseq<Gs...>) { (g_gap<C, TI, Gs>(g, it), ...); }
template <class C, int TI> DI void g_tile(GState<C>& g, int t, unsigned& stg, unsigned& pstg) {
  unsigned nstg = stg + C::STAGE; if (nstg == C::NS * C::STAGE) nstg = 0;
  GIter it;
  it.va_cur = g.vrd + stg; it.va_nxt = g.vrd + nstg;
  it.sA = (unsigned)(C::STEPS * 1024) * (unsigned)(t + C::PDT); it.sA0 = it.sA - 1024u;
  it.m0_fill = g.m0base + pstg; it.sK_fill = g.ktileB * (unsigned)(t - 1 + C::NS);
  g_gaps<C, TI>(g, it, make_iseq<C::G>{});
  pstg = stg; stg = nstg;
}
template <class C, int... TIs> DI void g_body(GState<C>& g, int t, unsigned& stg, unsigned& pstg, iseq<TIs...>) { (g_tile<C, TIs>(g, t + TIs, stg, pstg), ...); }

template <class C, int I> DI void g_touch(GState<C>& g) {
  asm volatile("s_mov_b32 m0, %0\n\ts_nop 0\n\tglobal_load_lds_dword %1, %2" :: "s"(g.m0base + (unsigned)(C::NS - 1) * C::STAGE), "v"(g.tch_off), "s"(g.tch[I]) : "memory");
}
template <class C, int V, int J> DI void g_virt_op(GState<C>& g, const GIter& it) {
  constexpr int ks = C::op_step(J);
  constexpr int TI = ((V % C::PDT) + C::PDT) % C::PDT;
  constexpr bool VA = (C::STEPS * (V + C::PDT) + ks - 1) >= 0, VB = (V - 1 + C::NS) >= 0;
  if constexpr (V == -1 && J == C::ops_before(C::GB)) asm volatile("s_waitcnt vmcnt(%0)\n\ts_barrier" :: "i"(C::NBW()) : "memory");
  if constexpr (C::ASMACC && V == -(C::NS - 1) && J < 2) { static_assert(!VA, ""); g_touch<C, J>(g); }
  else if constexpr (C::ASMACC && V == -(C::NS - 2) && J == 0) { static_assert(!VA, ""); g_touch<C, 2>(g); }
  else g_op<C, TI, J, VA, VB>(g, it);
}
template <class C, int V, int... Js> DI void g_virt(GState<C>& g, iseq<Js...>) {
  GIter it; constexpr int tb = V - 1 + C::NS;
  it.va_cur = it.va_nxt = 0; it.sA = (unsigned)(C::STEPS * 1024) * (unsigned)(V + C::PDT >= 0 ? V + C::PDT : 0); it.sA0 = it.sA - 1024u;
  it.m0_fill = g.m0base + (unsigned)(tb >= 0 ? tb : 0) * C::STAGE; it.sK_fill = g.ktileB * (unsigned)(tb >= 0 ? tb : 0);
  (g_virt_op<C, V, Js>(g, it), ...);
  if constexpr (V == -1 && C::ops_before(C::GB) == C::NOPS) asm volatile("s_waitcnt vmcnt(%0)\n\ts_barrier" :: "i"(C::NBW()) : "memory");
}
template <class C, int... Vs> DI void g_prologue(GState<C>& g, iseq<Vs...>) { constexpr int NV = sizeof...(Vs); (g_virt<C, Vs - NV>(g, make_iseq<C::NOPS>{}), ...); }

template <class C, bool PSWAP = false> DI void g_setup(GState<C>& g, unsigned ldsb, unsigned lane, unsigned wid, unsigned ldb) {
  g.m0base = rfl(ldsb + wid * (unsigned)C::PB * 1024u);
  g.ktileB = 64u * ldb;
  const unsigned q = (lane & 15u) >> 2, p0 = lane & 3u, p = PSWAP ? (((p0 & 1u) << 1) | (p0 >> 1)) : p0, g4 = lane >> 4;
  if constexpr (C::SH == 32) {
    const unsigned h = lane >> 5, blk = g4 & 1u;
    g.vrd = ldsb + blk * (C::GU ? (unsigned)C::REGION : 256u) + ((4u * h + q) ^ (4u * blk)) * 32u + 8u * p;
  } else {
    g.vrd = ldsb + (g4 >> 1) * (C::KORD == 0 ? 1u : 2u) * (unsigned)(C::NR16 * 256) + (4u * (g4 & 1u) + q) * 32u + 8u * p;
  }
  if constexpr (C::GU) {
    const unsigned wr = wid % (C::NW / 2), up = wid / (C::NW / 2);
#pragma unroll
    for (int qq = 0; qq < C::PB; ++qq) {
      const unsigned P = wr * C::PB + qq, s = 4u * P + g4 * 0u + (lane >> 4), kb = s / (unsigned)C::NR16, nb = s % (unsigned)C::NR16;
      const unsigned row = ((lane >> 1) & 7u) ^ ((C::SH == 32 && up) ? 4u : 0u);
      g.voffB[qq] = (P < (unsigned)(C::NR16 * 2)) ? (8u * kb + row) * ldb + 32u * nb + 16u * (lane & 1u) : 0x80000000u;
    }
  } else {
    static_assert(C::GU || (C::NR16 % 4 == 0), "");
    const unsigned sub = lane >> 4;
    const unsigned row = ((lane >> 1) & 7u) ^ ((C::SH == 32 && (sub & 1u)) ? 4u : 0u);
    g.voffB[0] = row * ldb + 32u * sub + 16u * (lane & 1u);
#pragma unroll
    for (int qq = 0; qq < C::PB; ++qq) { const unsigned P = wid * C::PB + qq, kb = P / (unsigned)(C::NR16 / 4), j = P % (unsigned)(C::NR16 / 4); g.sB[qq] = rfl(8u * kb * ldb + 128u * j); }
  }
}

template <int R> DI void acc_zero1() { asm volatile("v_accvgpr_write_b32 a[%c0], 0" :: "i"(R) : W4_ACLOB); }
template <int... Rs> DI void acc_zero(iseq<Rs...>) { (acc_zero1<Rs>(), ...); asm volatile("s_nop 1"); }
template <class C> DI void gemm_run(GState<C>& g, int NT) {
  constexpr int NV = C::PDT > C::NS - 1 ? C::PDT : C::NS - 1;
  if constexpr (!C::ASMACC) {
#pragma unroll
    for (int n = 0; n < C::NB; ++n)
#pragma unroll
      for (int m = 0; m < C::MR; ++m)
#pragma unroll
        for (int e = 0; e < (C::SH == 32 ? 16 : 4); ++e) g.acc[n][m][e] = 0.f;
  }
  g_prologue<C>(g, make_iseq<NV>{});
  if constexpr (C::ASMACC) acc_zero(make_iseq<C::NB * C::MR * 16>{});
  b_read_all<C, 0, 0>(g, g.vrd, make_iseq<C::NB>{});
  if constexpr (C::FD == 2) b_read_all<C, 1, 1>(g, g.vrd, make_iseq<C::NB>{});
  GAP();
  unsigned stg = 0, pstg = (C::NS - 1) * C::STAGE;
  for (int t = 0; t < NT; t += C::PDT) g_body<C>(g, t, stg, pstg, make_iseq<C::PDT>{});
  asm volatile("s_waitcnt vmcnt(0) lgkmcnt(0)" ::: "memory");
#pragma unroll
  for (int i = 0; i < C::PDT * C::STEPS; ++i)
#pragma unroll
    for (int m = 0; m < C::MR; ++m) asm volatile("" :: "v"(g.a[i][m]));
  GAP();
}
typedef __bf16 abf16x8 __attribute__((ext_vector_type(8)));
typedef __bf16 abf16x4 __attribute__((ext_vector_type(4)));
#define ATTN_THR 64.0f
constexpr unsigned KRS = 256u, KTILEB = 64u * KRS;
#define KRING 0x0000u
#define VRING 0x8000u
#define QGEMM_LDS 0x10000u
DI float max3f(float a, float b, float c) { float r; asm("v_max3_f32 %0, %1, %2, %3" : "=v"(r) : "v"(a), "v"(b), "v"(c)); return r; }
DI float max2f(float a, float b) { float r; asm("v_max_f32_e32 %0, %1, %2" : "=v"(r) : "v"(a), "v"(b)); return r; }
DI float fadd_s(float a, float b) { float r; asm("v_add_f32_e32 %0, %1, %2" : "=v"(r) : "v"(a), "v"(b)); return r; }
DI float fmul_s(float a, float b) { float r; asm("v_mul_f32_e32 %0, %1, %2" : "=v"(r) : "v"(a), "v"(b)); return r; }
DI float fsub_s(float a, float b) { float r; asm("v_sub_f32_e32 %0, %1, %2" : "=v"(r) : "v"(a), "v"(b)); return r; }
DI float w4_aone() { unsigned l; asm volatile("v_mbcnt_lo_u32_b32 %0, -1, 0\n\tv_mbcnt_hi_u32_b32 %0, -1, %0" : "=v"(l)); return l < 32u ? 1.f : 0.f; }
DI void negm_bcast(f32x16& negm, float aone, float neg_mhat) {
  asm volatile("s_nop 1\n\tv_mfma_f32_32x32x2_f32 %0, %1, %2, 0\n\ts_nop 7\n\ts_nop 7\n\ts_nop 3" : "+v"(negm) : "v"(aone), "v"(neg_mhat));
}
struct W4C {
  unsigned vkb[4], vvb, vksrc[2], vvsrc[4], m0k, m0v, sofk, sofv;
  __amdgpu_buffer_rsrc_t srdK, srdV;
  f32x16 S[2][2][2];
  f32x16 NEGM[2];
  abf16x8 Vf[4][4];
  unsigned pk[2][4][4];
  abf16x4 vlo[4][4], vup[4][4];
  float mhat[2], fct[2]; bool pend;
  float rs[2][2];
  float mA, mB;
  unsigned sk_m0, sk_so, sv_m0, sv_so;
  unsigned pf_lo, pf_hi, pf_off, pf_m0;
};
constexpr int AO_(int db, int qb) { return 16 * (2 * db + qb); }
constexpr int AQ_(int qb, int d0) { return 128 + 32 * qb + 4 * d0; }
constexpr int AK_(int kb, int d0) { return 192 + 32 * kb + 4 * d0; }

template <int I> DI void tr_read(W4C& c, unsigned va) {
  constexpr int ks = I >> 3, db = (I >> 1) & 3;
  if constexpr ((I & 1) == 0) asm volatile("ds_read_b64_tr_b16 %0, %1 offset:%2" : "=v"(c.vlo[db][ks]) : "v"(va), "i"(4096 * ks + 512 * db) : "memory");
  else                        asm volatile("ds_read_b64_tr_b16 %0, %1 offset:%2" : "=v"(c.vup[db][ks]) : "v"(va), "i"(4096 * ks + 2048 + 512 * db) : "memory");
}
template <int I, int SLOT> DI void k_read(W4C& c) {
  constexpr int kb = I >> 3, d0 = I & 7;
  asm volatile("ds_read_b128 a[%c1:%c2], %0 offset:%c3" :: "v"(c.vkb[d0 & 3]), "i"(AK_(kb, d0)), "i"(AK_(kb, d0) + 3), "i"(SLOT * 0x4000 + 8192 * kb + 128 * (d0 >> 2)) : "memory");
}
template <int X, bool FIRST, int M> DI void qk_mfma(W4C& c) {
  constexpr int d0 = M >> 2, kb = (M >> 1) & 1, qb = M & 1, ak = AK_(kb, d0), aq = AQ_(qb, d0);
  if constexpr (d0 == 0) {
    if constexpr (FIRST) asm volatile("v_mfma_f32_32x32x16_bf16 %0, a[%c1:%c2], a[%c3:%c4], 0" : "=&v"(c.S[X][kb][qb]) : "i"(ak), "i"(ak + 3), "i"(aq), "i"(aq + 3));
    else                 asm volatile("v_mfma_f32_32x32x16_bf16 %0, a[%c2:%c3], a[%c4:%c5], %1" : "=&v"(c.S[X][kb][qb]) : "v"(c.NEGM[qb]), "i"(ak), "i"(ak + 3), "i"(aq), "i"(aq + 3));
  } else asm volatile("v_mfma_f32_32x32x16_bf16 %0, a[%c1:%c2], a[%c3:%c4], %0" : "+v"(c.S[X][kb][qb]) : "i"(ak), "i"(ak + 3), "i"(aq), "i"(aq + 3));
}
template <int M, bool ZERO> DI void pv_mfma(W4C& c) {
  constexpr int ks = M >> 3, db = (M >> 1) & 3, qb = M & 1, ao = AO_(db, qb);
  const u32x4 pw = {c.pk[qb][ks][0], c.pk[qb][ks][1], c.pk[qb][ks][2], c.pk[qb][ks][3]};
  if constexpr (ZERO && ks == 0) asm volatile("v_mfma_f32_32x32x16_bf16 a[%c2:%c3], %0, %1, 0" :: "v"(c.Vf[db][ks]), "v"(pw), "i"(ao), "i"(ao + 15));
  else                         asm volatile("v_mfma_f32_32x32x16_bf16 a[%c2:%c3], %0, %1, a[%c2:%c3]" :: "v"(c.Vf[db][ks]), "v"(pw), "i"(ao), "i"(ao + 15));
}
template <int X, int KB, int E> DI void op_exp(W4C& c) { c.S[X][KB][E >> 4][E & 15] = __builtin_amdgcn_exp2f(c.S[X][KB][E >> 4][E & 15]); }
template <int X, int KB, int E> DI void op_add(W4C& c) { asm volatile("v_add_f32_e32 %0, %0, %1" : "+v"(c.rs[E >> 4][E & 1]) : "v"(c.S[X][KB][E >> 4][E & 15])); }
template <int X, int KB, int J> DI void op_cvt(W4C& c) {
  constexpr int qb = J >> 3, ks = 2 * KB + ((J >> 2) & 1), p = J & 3, r = 2 * (J & 7);
  c.pk[qb][ks][p] = cvtpk(c.S[X][KB][qb][r], c.S[X][KB][qb][r + 1]);
  if constexpr (KB == 1) asm volatile("" : "+v"(c.pk[qb][ks][p]));
}
template <int X, int I> DI float sval(W4C& c) { return c.S[X][I >> 5][(I >> 4) & 1][I & 15]; }
template <int X, int G> DI void op_max4(W4C& c) {
  if constexpr (G == 0) asm volatile("v_max3_f32 %0, %2, %3, %4\n\tv_max3_f32 %1, %5, %6, %7\n\tv_max3_f32 %0, %0, %8, %9" : "=&v"(c.mA), "=&v"(c.mB)
                                     : "v"(sval<X, 0>(c)), "v"(sval<X, 1>(c)), "v"(sval<X, 2>(c)), "v"(sval<X, 3>(c)), "v"(sval<X, 4>(c)), "v"(sval<X, 5>(c)), "v"(sval<X, 6>(c)), "v"(sval<X, 7>(c)));
  else asm volatile("v_max3_f32 %1, %1, %2, %3\n\tv_max3_f32 %0, %0, %4, %5\n\tv_max3_f32 %1, %1, %6, %7\n\tv_max3_f32 %0, %0, %8, %9" : "+v"(c.mA), "+v"(c.mB)
                    : "v"(sval<X, 8 * G>(c)), "v"(sval<X, 8 * G + 1>(c)), "v"(sval<X, 8 * G + 2>(c)), "v"(sval<X, 8 * G + 3>(c)), "v"(sval<X, 8 * G + 4>(c)), "v"(sval<X, 8 * G + 5>(c)), "v"(sval<X, 8 * G + 6>(c)), "v"(sval<X, 8 * G + 7>(c)));
}
constexpr int EB0 = 10;
constexpr int eb_lo(int g) { return g <= EB0 ? 0 : (32 * (g - EB0)) / (32 - EB0); }
template <int X, bool FIRST, int M> DI void gapA(W4C& c, unsigned va, const int t) {
  constexpr int Xp = X ^ 1;
  qk_mfma<X, FIRST, M>(c);
  GAP();
  if constexpr (!FIRST) {
    op_exp<Xp, 1, M>(c);
    constexpr int base = M + (M > 16) + (M > 18) + (M > 20) + (M > 22);
    if constexpr (M <= 27) tr_read<base>(c, va);
    if constexpr (M < 16) op_cvt<Xp, 0, M>(c);
    else if constexpr (M >= 17 && M <= 23 && (M & 1)) op_cvt<Xp, 1, (M - 17) / 2>(c);
    else if constexpr (M >= 25 && M <= 28) op_cvt<Xp, 1, 8 + M - 25>(c);
    if constexpr (M == 16 || M == 18 || M == 20 || M == 22) tr_read<base + 1>(c, va);
    if constexpr (M == 0) { op_add<Xp, 0, eb_lo(31)>(c); if constexpr (eb_lo(31) + 1 < 32) op_add<Xp, 0, eb_lo(31) + 1>(c); }
    else op_add<Xp, 1, M - 1>(c);
  }
  if constexpr (M == 29) { c.sk_m0 = c.m0k + (unsigned)X * 0x4000u; c.sk_so = KTILEB * (unsigned)(t + 2) + c.sofk; asm volatile("" : "+s"(c.sk_m0), "+s"(c.sk_so)); }
  if constexpr (M == 30) { c.sv_m0 = c.m0v + (unsigned)Xp * 0x4000u; c.sv_so = KTILEB * (unsigned)(t + 1) + c.sofv; asm volatile("" : "+s"(c.sv_m0), "+s"(c.sv_so)); }
  if constexpr (M == 31) asm volatile("s_mov_b32 m0, %0" :: "s"(c.sk_m0) : "memory");
  if constexpr (!FIRST && M == 8) asm volatile("" :: "v"(c.NEGM[0]), "v"(c.NEGM[1]));
  GAP();
}
#define W4_VLO "+v"(c.vlo[0][0]), "+v"(c.vlo[0][1]), "+v"(c.vlo[0][2]), "+v"(c.vlo[0][3]), "+v"(c.vlo[1][0]), "+v"(c.vlo[1][1]), "+v"(c.vlo[1][2]), "+v"(c.vlo[1][3]), \
               "+v"(c.vlo[2][0]), "+v"(c.vlo[2][1]), "+v"(c.vlo[2][2]), "+v"(c.vlo[2][3]), "+v"(c.vlo[3][0]), "+v"(c.vlo[3][1]), "+v"(c.vlo[3][2]), "+v"(c.vlo[3][3])
#define W4_VUP "+v"(c.vup[0][0]), "+v"(c.vup[0][1]), "+v"(c.vup[0][2]), "+v"(c.vup[0][3]), "+v"(c.vup[1][0]), "+v"(c.vup[1][1]), "+v"(c.vup[1][2]), "+v"(c.vup[1][3]), \
               "+v"(c.vup[2][0]), "+v"(c.vup[2][1]), "+v"(c.vup[2][2]), "+v"(c.vup[2][3]), "+v"(c.vup[3][0]), "+v"(c.vup[3][1]), "+v"(c.vup[3][2]), "+v"(c.vup[3][3])
template <int X, bool FIRST, int... Ms> DI void phaseA_(W4C& c, const int t, iseq<Ms...>) {
  const unsigned va = c.vvb + (unsigned)(X ^ 1) * 0x4000u;
  (gapA<X, FIRST, Ms>(c, va, t), ...);
  if constexpr (FIRST) asm volatile("s_waitcnt vmcnt(0) lgkmcnt(0)\n\ts_barrier\n\ts_nop 7" ::: "memory");
  else { asm volatile("s_waitcnt vmcnt(%c16) lgkmcnt(0)" : W4_VLO : "i"(X == 1 ? 1 : 0) : "memory"); asm volatile("s_barrier\n\ts_nop 1" : W4_VUP :: "memory"); }
  GAP();
  if constexpr (!FIRST) {
#pragma unroll
    for (int ks = 0; ks < 4; ++ks)
#pragma unroll
      for (int db = 0; db < 4; ++db) c.Vf[db][ks] = __builtin_shufflevector(c.vlo[db][ks], c.vup[db][ks], 0, 1, 2, 3, 4, 5, 6, 7);
  }
}
template <int X, bool FIRST> DI void phaseA(W4C& c, const int t) { phaseA_<X, FIRST>(c, t, make_iseq<32>{}); }

template <int X, bool INIT> DI void new_reference(W4C& c) {
#pragma unroll
  for (int q = 0; q < 2; ++q) {
    float m0 = max3f(c.S[X][0][q][0], c.S[X][0][q][1], c.S[X][1][q][0]), m1 = max2f(c.S[X][1][q][1], c.S[X][0][q][2]);
#pragma unroll
    for (int e = 2; e < 16; e += 2) { m0 = max3f(m0, c.S[X][0][q][e], c.S[X][0][q][e + 1]); m1 = max3f(m1, c.S[X][1][q][e], c.S[X][1][q][e + 1]); }
    const float mm = max2f(m0, m1);
    auto pr = __builtin_amdgcn_permlane32_swap(__float_as_uint(mm), __float_as_uint(mm), false, false);
    const float rmax = max2f(__uint_as_float(pr[0]), __uint_as_float(pr[1]));
    const float dl = INIT ? rmax : max2f(rmax, 0.f);
    if constexpr (INIT) { c.mhat[q] = dl; c.rs[q][0] = 0.f; c.rs[q][1] = 0.f; c.fct[q] = 1.f; }
    else { c.mhat[q] = fadd_s(c.mhat[q], dl); c.fct[q] = __builtin_amdgcn_exp2f(-dl); c.rs[q][0] = c.rs[q][0] * c.fct[q]; c.rs[q][1] = c.rs[q][1] * c.fct[q]; }
#pragma unroll
    for (int kb = 0; kb < 2; ++kb) { _Pragma("unroll") for (int e = 0; e < 16; ++e) c.S[X][kb][q][e] = fsub_s(c.S[X][kb][q][e], dl); }
    negm_bcast(c.NEGM[q], w4_aone(), -c.mhat[q]);
  }
  asm volatile("s_nop 1");
}
template <int X, bool PEEL, bool ZERO1, int M> DI void gapB(W4C& c, const int t) {
  constexpr int Xp = X ^ 1;
  if constexpr (!PEEL) { pv_mfma<M, ZERO1>(c); GAP(); }
  if constexpr (!PEEL && M == 0) op_add<Xp, 1, 31>(c);
  constexpr int kq = M == 0 ? 0 : M == 11 ? 1 : M == 13 ? 2 : M == 15 ? 3 : -1, vq = M == 18 ? 0 : M == 20 ? 1 : M == 22 ? 2 : M == 24 ? 3 : -1;
  if constexpr (kq >= 0) asm volatile("buffer_load_dwordx4 %0, %1, %2 offen offset:%c3 lds" :: "v"(c.vksrc[kq & 1]), "s"(c.srdK), "s"(c.sk_so), "i"(1024 * kq) : "memory");
  if constexpr (M == 16) asm volatile("s_mov_b32 m0, %0" :: "s"(c.sv_m0) : "memory");
  if constexpr (vq >= 0) asm volatile("buffer_load_dwordx4 %0, %1, %2 offen offset:%c3 lds" :: "v"(c.vvsrc[vq]), "s"(c.srdV), "s"(c.sv_so), "i"(1024 * vq) : "memory");
  if constexpr (M >= 1 && M <= 8) {
    if constexpr (!PEEL) { op_max4<X, M - 1>(c); op_cvt<Xp, 1, (M <= 4 ? 4 + M - 1 : 12 + M - 5)>(c); }
    k_read<M - 1, Xp>(c);
  }
  if constexpr (M == 9) {
    if constexpr (PEEL) new_reference<X, true>(c);
    else {
      const float mm = max2f(c.mA, c.mB);
      if (__builtin_expect(__any((int)(mm > ATTN_THR)), 0)) { new_reference<X, false>(c); c.pend = true; }
    }
  }
  if constexpr (M >= EB0) {
    constexpr int e0 = eb_lo(M), e1 = eb_lo(M + 1), p0 = eb_lo(M - 1);
    op_exp<X, 0, e0>(c);
    if constexpr (M > EB0) op_add<X, 0, p0>(c);
    if constexpr (e0 + 1 < e1) op_exp<X, 0, e0 + 1>(c);
    if constexpr (M > EB0 && p0 + 1 < e0) op_add<X, 0, p0 + 1>(c);
    constexpr int kr = M == 10 ? 8 : M == 12 ? 9 : M == 14 ? 10 : M == 17 ? 11 : M == 19 ? 12 : M == 21 ? 13 : M == 23 ? 14 : M == 25 ? 15 : -1;
    if constexpr (kr >= 0) k_read<kr, Xp>(c);
  }
  if constexpr (X == 0 && M == 26) {
    unsigned plo, phi;
    asm volatile("v_readlane_b32 %0, %2, %4\n\tv_readlane_b32 %1, %3, %4\n\ts_mov_b32 m0, %5" : "=s"(plo), "=s"(phi) : "v"(c.pf_lo), "v"(c.pf_hi), "s"((unsigned)(t >> 1)), "s"(c.pf_m0) : "memory");
    c.sk_m0 = plo; c.sk_so = phi;
  }
  if constexpr (X == 0 && M == 28) {
    const unsigned long long pb = ((unsigned long long)c.sk_so << 32) | c.sk_m0;
    asm volatile("global_load_lds_dword %0, %1" :: "v"(c.pf_off), "s"(pb) : "memory");
  }
  if constexpr (M == 31) asm volatile("" :: "v"(c.S[X][0][0]), "v"(c.S[X][0][1]));
  GAP();
}
template <int R> DI void resc4(float f) {
  float t0, t1, t2, t3;
  asm volatile("v_accvgpr_read_b32 %0, a[%c5]\n\tv_accvgpr_read_b32 %1, a[%c6]\n\tv_accvgpr_read_b32 %2, a[%c7]\n\tv_accvgpr_read_b32 %3, a[%c8]\n\t"
               "v_mul_f32 %0, %4, %0\n\tv_mul_f32 %1, %4, %1\n\tv_mul_f32 %2, %4, %2\n\tv_mul_f32 %3, %4, %3\n\t"
               "v_accvgpr_write_b32 a[%c5], %0\n\tv_accvgpr_write_b32 a[%c6], %1\n\tv_accvgpr_write_b32 a[%c7], %2\n\tv_accvgpr_write_b32 a[%c8], %3"
               : "=&v"(t0), "=&v"(t1), "=&v"(t2), "=&v"(t3) : "v"(f), "i"(R), "i"(R + 1), "i"(R + 2), "i"(R + 3) : W4_ACLOB);
}
template <int... Is> DI void resc_all(W4C& c, iseq<Is...>) { (resc4<4 * Is>(c.fct[(Is >> 2) & 1]), ...); }
template <int X, bool PEEL, bool ZERO1, int... Ms> DI void phaseB_(W4C& c, const int t, iseq<Ms...>) {
  (gapB<X, PEEL, ZERO1, Ms>(c, t), ...);
  if constexpr (!PEEL) {
    if (__builtin_expect(c.pend, 0)) {
      asm volatile("s_nop 7\n\ts_nop 7" ::: W4_ACLOB);
      resc_all(c, make_iseq<32>{});
      asm volatile("s_nop 1" ::: W4_ACLOB);
      c.pend = false;
    }
  }
  asm volatile("s_waitcnt lgkmcnt(0)" ::: "memory");
  GAP();
}
template <int X, bool PEEL, bool ZERO1> DI void phaseB(W4C& c, const int t) { phaseB_<X, PEEL, ZERO1>(c, t, make_iseq<32>{}); }
template <int... Is> DI void k_reads0(W4C& c, iseq<Is...>) { (k_read<Is, 0>(c), ...); }
template <int... Is> DI void tr_reads(W4C& c, unsigned va, iseq<Is...>) { (tr_read<Is>(c, va), ...); }
template <int... Is> DI void drain_exp(W4C& c, iseq<Is...>) { (op_exp<1, 1, Is>(c), ...); }
template <int... Is> DI void drain_cvt0(W4C& c, iseq<Is...>) { (op_cvt<1, 0, Is>(c), ...); }
template <int... Is> DI void drain_cvt1(W4C& c, iseq<Is...>) { (op_cvt<1, 1, Is>(c), ...); }
template <int... Is> DI void drain_add(W4C& c, iseq<Is...>) { (op_add<1, 1, Is>(c), ...); }
template <int... Ms> DI void pv_all(W4C& c, iseq<Ms...>) { (pv_mfma<Ms, false>(c), ...); }
template <int R> DI u32x4 acc_pack8(float f) {
  float o[8];
  asm volatile("v_accvgpr_read_b32 %0, a[%c8]\n\tv_accvgpr_read_b32 %1, a[%c9]\n\tv_accvgpr_read_b32 %2, a[%c10]\n\tv_accvgpr_read_b32 %3, a[%c11]\n\t"
               "v_accvgpr_read_b32 %4, a[%c12]\n\tv_accvgpr_read_b32 %5, a[%c13]\n\tv_accvgpr_read_b32 %6, a[%c14]\n\tv_accvgpr_read_b32 %7, a[%c15]"
               : "=v"(o[0]), "=v"(o[1]), "=v"(o[2]), "=v"(o[3]), "=v"(o[4]), "=v"(o[5]), "=v"(o[6]), "=v"(o[7])
               : "i"(R), "i"(R + 1), "i"(R + 2), "i"(R + 3), "i"(R + 4), "i"(R + 5), "i"(R + 6), "i"(R + 7));
  u32x4 w = {cvtpk(o[0] * f, o[1] * f), cvtpk(o[2] * f, o[3] * f), cvtpk(o[4] * f, o[5] * f), cvtpk(o[6] * f, o[7] * f)};
  return w;
}
template <int I> DI void q_conv1(float C) {
  constexpr int qb = I >> 3, d0 = I & 7, nb = d0 >> 1, s = d0 & 1, aq = AQ_(qb, d0);
  const u32x4 w = acc_pack8<AO_(nb, qb) + 8 * s>(C);
  asm volatile("v_accvgpr_write_b32 a[%c4], %0\n\tv_accvgpr_write_b32 a[%c5], %1\n\tv_accvgpr_write_b32 a[%c6], %2\n\tv_accvgpr_write_b32 a[%c7], %3"
               :: "v"(w[0]), "v"(w[1]), "v"(w[2]), "v"(w[3]), "i"(aq), "i"(aq + 1), "i"(aq + 2), "i"(aq + 3) : W4_ACLOB_HI);
}
template <int... Is> DI void q_conv(float C, iseq<Is...>) { (q_conv1<Is>(C), ...); }
template <int I> DI void o_store1(__amdgpu_buffer_rsrc_t srd, unsigned vo, float il0, float il1) {
  constexpr int qb = I >> 3, db = (I >> 1) & 3, s = I & 1;
  const u32x4 w = acc_pack8<AO_(db, qb) + 8 * s>(qb ? il1 : il0);
  __builtin_amdgcn_raw_buffer_store_b128(w, srd, vo + qb * (256u * 1024u) + (2 * db + s) * 1024, 0, WT);
}
template <int... Is> DI void o_store(__amdgpu_buffer_rsrc_t srd, unsigned vo, float il0, float il1, iseq<Is...>) { (o_store1<Is>(srd, vo, il0, il1), ...); }

using QCfg = GCfg<32, 4, 64, 8, false, 0, 6, 2, 1, 1, true>;
constexpr int SHM_ATTN = 0x10000 + QCfg::LDSB;

#define ST(i)
__global__ __launch_bounds__(256, 1) void k_attn(const u16* __restrict__ H1f, const u16* __restrict__ Wq, const u16* __restrict__ Kc, const u16* __restrict__ Vc, u16* __restrict__ Of,
                                                        const u16* __restrict__ Wo, const u16* __restrict__ Wg, const u16* __restrict__ Wu, const u16* __restrict__ Wd, const u16* __restrict__ Wm) {
  ST(0); KST_BEGIN(2);
  extern __shared__ __attribute__((aligned(16))) char lds[];
  const unsigned tid = threadIdx.x, wid = rfl(tid >> 6);
  const int bid = blockIdx.x, xcd = bid & 7, j = bid >> 3;
  const int head = 4 * xcd + (j & 3), y = j >> 2, kvh = head >> 3;
  const unsigned ldsb = (unsigned)(size_t)lds;
  {
    const unsigned lane = tid & 63u;
    GState<QCfg> g;
    const int mb0 = y * 8 + wid * 2;
    constexpr unsigned mbstride = 1024u * 32u * 2u;
    g.srdA = mk_srd((const char*)H1f + (size_t)mb0 * mbstride, 2 * mbstride);
    g.voffA[0] = lane * 16u; g.voffA[1] = lane * 16u + mbstride;
    constexpr unsigned ldb = 4096u * 2u;
    g.srdB = mk_srd((const char*)Wq + (size_t)head * 256, 1024u * ldb - (unsigned)head * 256u);
    g_setup<QCfg, true>(g, ldsb + QGEMM_LDS, lane, wid, ldb);
    { const unsigned gw = blockIdx.x * 4u + wid;
      g.tch[0] = (unsigned long long)(size_t)((const char*)Wq + (size_t)gw * 4096); g.tch[1] = g.tch[0] + (4096u * 1024u);
      g.tch[2] = (unsigned long long)(size_t)((gw < 512u ? (const char*)Kc : (const char*)Vc) + (size_t)(gw & 511u) * 4096);
      g.tch_off = lane * 64u; }
    gemm_run<QCfg>(g, 16);
  }
  ST(1);
  unsigned lane; asm volatile("v_mbcnt_lo_u32_b32 %0, -1, 0\n\tv_mbcnt_hi_u32_b32 %0, -1, %0" : "=v"(lane));
  const unsigned l32 = lane & 31u, hi = lane >> 5;
  constexpr int T = 32;
  W4C c;
#pragma unroll
  for (int jj = 0; jj < 4; ++jj) c.vkb[jj] = ldsb + KRING + 256u * l32 + 16u * ((2u * jj + hi) ^ (l32 & 7u));
  c.vvb = ldsb + VRING + (((lane & 3u) << 3) | (((lane >> 2) & 3u) << 6) | (((lane >> 4) & 1u) << 5) | (hi << 8));
#pragma unroll
  for (int par = 0; par < 2; ++par) c.vksrc[par] = KRS * (lane >> 4) + 16u * ((lane & 15u) ^ (4u * par + (lane >> 4)));
#pragma unroll
  for (int q = 0; q < 4; ++q) c.vvsrc[q] = KRS * ((lane >> 2) & 7u) + 64u * hi + 16u * (lane & 3u) + KRS * 8u * (q >> 1) + 128u * (q & 1) + (3072u - 1024u * q);
  c.m0k = rfl(ldsb + KRING + 4096u * wid); c.m0v = rfl(ldsb + VRING + 4096u * wid);
  c.sofk = rfl(KRS * 16u * wid); c.sofv = rfl(KRS * 16u * wid) - 3072u;
  c.srdK = mk_srd((const char*)Kc + (size_t)kvh * (2048u * 256u), 2048u * 256u);
  c.srdV = mk_srd((const char*)Vc + (size_t)kvh * (2048u * 256u), 2048u * 256u);
  {
    const unsigned gw = blockIdx.x * 4u + wid;
    const char* p;
    const unsigned c2 = gw + 1024u, c2c = c2 < 1792u ? c2 : gw;
    if (lane == 0) p = (const char*)Wo + (size_t)gw * 4096; else if (lane == 1) p = (const char*)Wo + (size_t)c2 * 4096;
    else if (lane == 2) p = (const char*)Wg + (size_t)gw * 4096; else if (lane == 3) p = (const char*)Wg + (size_t)c2c * 4096;
    else if (lane == 4) p = (const char*)Wu + (size_t)gw * 4096; else if (lane == 5) p = (const char*)Wu + (size_t)c2c * 4096;
    else if (lane == 6) p = (const char*)Wd + (size_t)gw * 4096; else if (lane == 7) p = (const char*)Wd + (size_t)c2c * 4096;
    else if (lane < 11) p = (const char*)Wm + (size_t)(gw + 1024u * (lane - 8u)) * 4096;
    else p = (const char*)Wo + (size_t)gw * 4096;
    c.pf_lo = (unsigned)(size_t)p; c.pf_hi = (unsigned)((size_t)p >> 32);
    c.pf_off = lane * 64u; c.pf_m0 = rfl(ldsb + QGEMM_LDS + 256u * wid);
  }
  {
    const unsigned v0 = KRS * ((lane >> 2) & 7u) + 64u * hi + 16u * (lane & 3u);
#pragma unroll
    for (int q = 0; q < 4; ++q)
      asm volatile("s_nop 4\n\ts_mov_b32 m0, %0\n\ts_nop 0\n\tbuffer_load_dwordx4 %1, %2, %3 offen offset:%c4 lds" :: "s"(c.m0k), "v"(c.vksrc[q & 1]), "s"(c.srdK), "s"(c.sofk), "i"(1024 * q) : "memory");
#pragma unroll
    for (int q = 0; q < 4; ++q)
      asm volatile("s_nop 4\n\ts_mov_b32 m0, %0\n\ts_nop 0\n\tbuffer_load_dwordx4 %1, %2, %3 offen lds" :: "s"(c.m0v + 1024u * q), "v"(v0), "s"(c.srdV), "s"(c.sofk + KRS * 8u * (q >> 1) + 128u * (q & 1)) : "memory");
#pragma unroll
    for (int q = 0; q < 4; ++q)
      asm volatile("s_nop 4\n\ts_mov_b32 m0, %0\n\ts_nop 0\n\tbuffer_load_dwordx4 %1, %2, %3 offen offset:%c4 lds" :: "s"(c.m0k + 0x4000u), "v"(c.vksrc[q & 1]), "s"(c.srdK), "s"(c.sofk + KTILEB), "i"(1024 * q) : "memory");
  }
  asm volatile("s_nop 7\n\ts_nop 7" ::: "memory");
  q_conv(0.12751743082f, make_iseq<16>{});
  asm volatile("s_waitcnt vmcnt(0)\n\ts_barrier" ::: "memory");
  k_reads0(c, make_iseq<16>{});
  asm volatile("s_waitcnt lgkmcnt(0)\n\ts_nop 1" ::: "memory");
  c.mhat[0] = c.mhat[1] = 0.f; c.fct[0] = c.fct[1] = 1.f; c.pend = false; c.rs[0][0] = c.rs[0][1] = c.rs[1][0] = c.rs[1][1] = 0.f;
  ST(2);
  phaseA<0, true>(c, 0);  phaseB<0, true, false>(c, 0);
  phaseA<1, false>(c, 1); phaseB<1, false, true>(c, 1);
  for (int t = 2; t < T; t += 2) {
    phaseA<0, false>(c, t);     phaseB<0, false, false>(c, t);
    phaseA<1, false>(c, t + 1); phaseB<1, false, false>(c, t + 1);
  }
  ST(3);
  float lsum[2];
  {
    tr_reads(c, c.vvb + 0x4000u, make_iseq<32>{});
    op_add<1, 0, eb_lo(31)>(c); if constexpr (eb_lo(31) + 1 < 32) op_add<1, 0, eb_lo(31) + 1>(c);
    drain_exp(c, make_iseq<32>{});
    drain_cvt0(c, make_iseq<16>{});
    drain_cvt1(c, make_iseq<16>{});
    GAP(); asm volatile("s_nop 1"); GAP();
    drain_add(c, make_iseq<32>{});
    asm volatile("s_waitcnt lgkmcnt(0)" : W4_VLO :: "memory"); asm volatile("" : W4_VUP :: "memory");
    GAP();
#pragma unroll
    for (int ks = 0; ks < 4; ++ks)
#pragma unroll
      for (int db = 0; db < 4; ++db) c.Vf[db][ks] = __builtin_shufflevector(c.vlo[db][ks], c.vup[db][ks], 0, 1, 2, 3, 4, 5, 6, 7);
    asm volatile("s_nop 1" ::: "memory");
    pv_all(c, make_iseq<32>{});
#pragma unroll
    for (int qb = 0; qb < 2; ++qb) { const float rs = c.rs[qb][0] + c.rs[qb][1];
      auto pr = __builtin_amdgcn_permlane32_swap(__float_as_uint(rs), __float_as_uint(rs), false, false);
      lsum[qb] = __uint_as_float(pr[0]) + __uint_as_float(pr[1]); }
    asm volatile("s_nop 7\n\ts_nop 7" ::: "memory");
  }
  {
    unsigned lb; asm volatile("v_mbcnt_lo_u32_b32 %0, -1, 0\n\tv_mbcnt_hi_u32_b32 %0, -1, %0" : "=v"(lb));
    const float il0 = __builtin_amdgcn_rcpf(lsum[0]), il1 = __builtin_amdgcn_rcpf(lsum[1]);
    o_store(mk_srd(Of, 16u << 20), ((unsigned)(y * 8 + wid * 2) * 256u + (unsigned)head * 8u) * 1024u + lb * 16u, il0, il1, make_iseq<16>{});
  }
  ST(4); KST_END(2);
}
template <int N> DI void touch(const char* p, int stride) {
  unsigned d[N];
#pragma unroll
  for (int i = 0; i < N; ++i) d[i] = *(const unsigned*)(p + (size_t)i * stride);
#pragma unroll
  for (int i = 0; i < N; ++i) asm volatile("" :: "v"(d[i]));
}

template <int NT, int KL> DI void mod_body(const u16* __restrict__ c, const u16* __restrict__ wmod, float* __restrict__ dst, int cb, int kh, float* red  ) {
  const int tid = threadIdx.x, cg = tid & 7, kg = tid >> 3, wv = tid >> 6;
  constexpr int NG = NT / 8, NI = KL / NG;
  u32x4 w[NI]; u16 cv[NI];
#pragma unroll
  for (int i = 0; i < NI; ++i) { const int k = kh * KL + kg + NG * i; w[i] = *(const u32x4*)(wmod + (size_t)k * 6144 + cb * 64 + cg * 8); cv[i] = c[k]; }
  float acc[8] = {0, 0, 0, 0, 0, 0, 0, 0};
#pragma unroll
  for (int i = 0; i < NI; ++i) {
    const float xx = bf2f(cv[i]), s = rbf(xx / (1.f + __expf(-xx)));
#pragma unroll
    for (int jj = 0; jj < 4; ++jj) { acc[2 * jj] += s * __uint_as_float(w[i][jj] << 16); acc[2 * jj + 1] += s * __uint_as_float(w[i][jj] & 0xffff0000u); }
  }
#pragma unroll
  for (int jj = 0; jj < 8; ++jj) { float a = acc[jj]; a += __shfl_xor(a, 8); a += __shfl_xor(a, 16); a += __shfl_xor(a, 32); acc[jj] = a; }
  if ((tid & 63) < 8) {
#pragma unroll
    for (int jj = 0; jj < 8; ++jj) red[wv * 64 + cg * 8 + jj] = acc[jj];
  }
  __syncthreads();
  if (tid < 64) { float s = 0;
#pragma unroll
    for (int g = 0; g < NT / 64; ++g) s += red[g * 64 + tid];
    dst[tid] = s; }
}
__global__ __launch_bounds__(512) void k_pre(const u16* __restrict__ c, const u16* __restrict__ wmod, float* __restrict__ modA, const u16* __restrict__ x, unsigned* __restrict__ flg) {
  __shared__ float red[8 * 64];
  const int b = blockIdx.x;
  KST_BEGIN(0);
  unsigned dx = 0;
  if (threadIdx.x == 0) flg[b * 64] = 0u;
  if (threadIdx.x < 256) dx = *(const unsigned*)((const char*)x + (size_t)b * 16384 + threadIdx.x * 64);
  mod_body<512, 128>(c, wmod, modA + (b & 7) * 2048 + (b >> 3) * 64, b >> 3, b & 7, red);
  asm volatile("" :: "v"(dx));
  KST_END(0);
}
DI f32x4 modv(const float* __restrict__ modp, int n) { const f32x4 a = *(const f32x4*)(modp + n), b = *(const f32x4*)(modp + 6144 + n); f32x4 o; for (int e = 0; e < 4; ++e) o[e] = rbf(a[e] + b[e]); return o; }
DI void unpack8(const u32x4& w, float* v) {
#pragma unroll
  for (int j = 0; j < 4; ++j) { v[2 * j] = __uint_as_float(w[j] << 16); v[2 * j + 1] = __uint_as_float(w[j] & 0xffff0000u); }
}
DI u32x4 pack8(const float* o) { return (u32x4){cvtpk(o[0], o[1]), cvtpk(o[2], o[3]), cvtpk(o[4], o[5]), cvtpk(o[6], o[7])}; }

DI void rbf2(float& a, float& b) { const unsigned w = cvtpk(a, b); a = __uint_as_float(w << 16); b = __uint_as_float(w & 0xffff0000u); }
DI void rbf8(float* v) { rbf2(v[0], v[1]); rbf2(v[2], v[3]); rbf2(v[4], v[5]); rbf2(v[6], v[7]); }
DI void modv8(const float* __restrict__ modp, int n, float* o) {
  const f32x4 a0 = *(const f32x4*)(modp + n), b0 = *(const f32x4*)(modp + 6144 + n), a1 = *(const f32x4*)(modp + n + 8), b1 = *(const f32x4*)(modp + 6144 + n + 8);
#pragma unroll
  for (int e = 0; e < 4; ++e) { o[e] = a0[e] + b0[e]; o[4 + e] = a1[e] + b1[e]; }
  rbf8(o);
}
template <int MODE>
__global__ __launch_bounds__(1024) void k_ln(const u16* __restrict__ x, const u16* __restrict__ Pf, const float* __restrict__ modp, u16* __restrict__ xf, u16* __restrict__ hf) {
  __shared__ float red[2][16][8];
  __shared__ __attribute__((aligned(16))) float msum[MODE == 0 ? 2048 : 4];
  const int tid = threadIdx.x;
  KST_BEGIN(MODE == 0 ? 1 : 4);
  const int r = tid & 7, c = tid >> 3, h = c & 1, wid = tid >> 6;
  const int row = blockIdx.x * 8 + r, mb = row >> 5, ml = row & 31;
  const size_t fo = ((size_t)mb * 64 + (c >> 1)) * 512 + (h * 32 + ml) * 8;
  const int n = 16 * (c >> 1) + 4 * h;
  float v[8], sh[8], sc[8];
  if (MODE == 0) {
    const u32x4 xw = *(const u32x4*)(x + (size_t)row * 1024 + 8 * c);
    { f32x2 a = {0.f, 0.f};
#pragma unroll
      for (int ks = 0; ks < 8; ++ks) a += *(const f32x2*)(modp + ks * 2048 + 2 * tid);
      float a0 = a[0], a1 = a[1]; rbf2(a0, a1); *(f32x2*)(msum + 2 * tid) = (f32x2){a0, a1}; }
    const unsigned s0 = h ? xw[0] : xw[2], s1 = h ? xw[1] : xw[3];
    const unsigned g0 = __shfl_xor(s0, 8), g1 = __shfl_xor(s1, 8);
    const u32x4 f = h ? (u32x4){g0, g1, xw[2], xw[3]} : (u32x4){xw[0], xw[1], g0, g1};
    __builtin_amdgcn_raw_buffer_store_b128(f, mk_srd(xf, 4u << 20), (unsigned)fo * 2u, 0, WT);
    unpack8(f, v);
  } else {
    const u32x4 xw = *(const u32x4*)(xf + fo), p0 = *(const u32x4*)(Pf + fo), p1 = *(const u32x4*)(Pf + fo + (size_t)2048 * 1024);
    float ga[8], a[8], b[8];
    modv8(modp, 3072 + n, sh); modv8(modp, 4096 + n, sc); modv8(modp, 2048 + n, ga);
    unpack8(xw, v); unpack8(p0, a); unpack8(p1, b);
#pragma unroll
    for (int j = 0; j < 8; ++j) a[j] += b[j];
    rbf8(a);
#pragma unroll
    for (int j = 0; j < 8; ++j) a[j] *= ga[j];
    rbf8(a);
#pragma unroll
    for (int j = 0; j < 8; ++j) v[j] += a[j];
    const u32x4 w = pack8(v);
    __builtin_amdgcn_raw_buffer_store_b128(w, mk_srd(xf, 4u << 20), (unsigned)fo * 2u, 0, WT);
    unpack8(w, v);
  }
  float s = ((v[0] + v[1]) + (v[2] + v[3])) + ((v[4] + v[5]) + (v[6] + v[7]));
  s += __shfl_xor(s, 8); s += __shfl_xor(s, 16); s += __shfl_xor(s, 32);
  if ((tid & 63) < 8) red[0][wid][r] = s;
  __syncthreads();
  if (MODE == 0) {
    const f32x4 a0 = *(const f32x4*)(msum + n), a1 = *(const f32x4*)(msum + n + 8), b0 = *(const f32x4*)(msum + 1024 + n), b1 = *(const f32x4*)(msum + 1024 + n + 8);
#pragma unroll
    for (int e = 0; e < 4; ++e) { sh[e] = a0[e]; sh[4 + e] = a1[e]; sc[e] = b0[e]; sc[4 + e] = b1[e]; }
  }
  float mean = 0;
#pragma unroll
  for (int w = 0; w < 16; ++w) mean += red[0][w][r];
  mean *= (1.f / 1024.f);
  float qv = 0;
#pragma unroll
  for (int j = 0; j < 8; ++j) { const float d = v[j] - mean; qv += d * d; }
  qv += __shfl_xor(qv, 8); qv += __shfl_xor(qv, 16); qv += __shfl_xor(qv, 32);
  if ((tid & 63) < 8) red[1][wid][r] = qv;
  __syncthreads();
  float var = 0;
#pragma unroll
  for (int w = 0; w < 16; ++w) var += red[1][w][r];
  var = rbf(var * (1.f / 1024.f));
  const float mb16 = rbf(mean), rsd = 1.f / rbf(sqrtf(rbf(var + 1e-5f)));
#pragma unroll
  for (int j = 0; j < 8; ++j) { v[j] -= mb16; sc[j] += 1.f; }
  rbf8(v); rbf8(sc);
#pragma unroll
  for (int j = 0; j < 8; ++j) v[j] *= rsd;
  rbf8(v);
#pragma unroll
  for (int j = 0; j < 8; ++j) v[j] *= sc[j];
  rbf8(v);
#pragma unroll
  for (int j = 0; j < 8; ++j) v[j] += sh[j];
  __builtin_amdgcn_raw_buffer_store_b128(pack8(v), mk_srd(hf, 4u << 20), (unsigned)fo * 2u, 0, WT);
  KST_END(MODE == 0 ? 1 : 4);
}
#define SK_PDT 4
using SCfg = GCfg<32, 4, 32, 8, false, 0, 5, SK_PDT, 2, 1>;
template <int K, bool FIN, int NT0>
__global__ __launch_bounds__(256, 1) void k_gemm_sk(const u16* __restrict__ Af, const u16* __restrict__ B, u16* __restrict__ Pf,
                                                    const u16* __restrict__ cvec, const u16* __restrict__ wmod, float* __restrict__ modp,
                                                    const u16* __restrict__ x1f, u16* __restrict__ out, unsigned* __restrict__ flg) {
  using C = SCfg;
  extern __shared__ __attribute__((aligned(16))) char lds[];
  KST_BEGIN(K == 4096 ? 3 : 6);
  if (K == 4096 && blockIdx.x >= 256) { const int b = blockIdx.x - 256; mod_body<256, 512>(cvec, wmod, modp + (b & 1) * 6144 + (32 + (b >> 1)) * 64, 32 + (b >> 1), b & 1, (float*)lds); KST_END(3); return; }
  constexpr int N = 1024;
  const unsigned tid = threadIdx.x, lane = tid & 63u, wid = rfl(tid >> 6);
  const int bid = blockIdx.x, xcd = bid & 7, j = bid >> 3;
  const int kh = xcd & 1, mg = xcd >> 1;
  const int mt = mg * 4 + (j & 3), nt = j >> 2;
  const int k0 = kh ? NT0 * 64 : 0, klen = kh ? K - NT0 * 64 : NT0 * 64;
  GState<C> g;
  const int mb0 = mt * 4 + wid;
  constexpr unsigned mbstride = (unsigned)K * 64u;
  g.srdA = mk_srd((const char*)Af + (size_t)mb0 * mbstride + (size_t)k0 * 64, (unsigned)klen * 64u);
  g.voffA[0] = lane * 16u;
  constexpr unsigned ldb = (unsigned)N * 2u;
  g.srdB = mk_srd((const char*)B + (size_t)k0 * ldb + (size_t)nt * 256, (unsigned)klen * ldb - (unsigned)nt * 256u);
  g_setup<C>(g, (unsigned)(size_t)lds, lane, wid, ldb);
  gemm_run<C>(g, klen / 64);
  const unsigned fo = (((unsigned)mb0 * 64u + (unsigned)nt * 8u) * 512u + lane * 8u) * 2u;
  if constexpr (!FIN) {
#pragma unroll
    for (int n = 0; n < 4; ++n)
#pragma unroll
      for (int s = 0; s < 2; ++s) {
        u32x4 w = {cvtpk(g.acc[n][0][8 * s], g.acc[n][0][8 * s + 1]), cvtpk(g.acc[n][0][8 * s + 2], g.acc[n][0][8 * s + 3]), cvtpk(g.acc[n][0][8 * s + 4], g.acc[n][0][8 * s + 5]), cvtpk(g.acc[n][0][8 * s + 6], g.acc[n][0][8 * s + 7])};
        __builtin_amdgcn_raw_buffer_store_b128(w, mk_srd(Pf, 8u << 20), (unsigned)kh * (4u << 20) + fo + (2 * n + s) * 1024, 0, WT);
      }
  } else {
    const __amdgpu_buffer_rsrc_t srdP = mk_srd(Pf, 4u << 20);
    unsigned* flag = flg + (128 + mt * 8 + nt) * 64;
    if (kh == 0) {
#pragma unroll
      for (int n = 0; n < 4; ++n)
#pragma unroll
        for (int s = 0; s < 2; ++s) {
          u32x4 w = {cvtpk(g.acc[n][0][8 * s], g.acc[n][0][8 * s + 1]), cvtpk(g.acc[n][0][8 * s + 2], g.acc[n][0][8 * s + 3]), cvtpk(g.acc[n][0][8 * s + 4], g.acc[n][0][8 * s + 5]), cvtpk(g.acc[n][0][8 * s + 6], g.acc[n][0][8 * s + 7])};
          __builtin_amdgcn_raw_buffer_store_b128(w, srdP, fo + (2 * n + s) * 1024, 0, 16);
        }
      asm volatile("s_waitcnt vmcnt(0)" ::: "memory");
      __syncthreads();
      if (tid == 0) __hip_atomic_store(flag, 1u, __ATOMIC_RELAXED, __HIP_MEMORY_SCOPE_AGENT);
    } else {
      const unsigned h = lane >> 5;
      u32x4 xw[8]; float gb[8][8];
#pragma unroll
      for (int f = 0; f < 8; ++f) { xw[f] = *(const u32x4*)((const char*)x1f + fo + f * 1024); modv8(modp, 5120 + nt * 128 + 16 * f + 4 * h, gb[f]); }
      if (wid == 0) {
        for (unsigned spins = 0; spins < (1u << 22); ++spins) {
          if ((unsigned)__builtin_amdgcn_readfirstlane(__hip_atomic_load(flag, __ATOMIC_RELAXED, __HIP_MEMORY_SCOPE_AGENT)) != 0u) break;
          __builtin_amdgcn_s_sleep(1);
        }
      }
      asm volatile("" ::: "memory"); __builtin_amdgcn_s_barrier(); asm volatile("" ::: "memory");
      u32x4 pw[8];
#pragma unroll
      for (int f = 0; f < 8; ++f) pw[f] = __builtin_amdgcn_raw_buffer_load_b128(srdP, fo + f * 1024, 0, 16);
      u16* orow = out + (size_t)(mb0 * 32 + (lane & 31u)) * 1024 + nt * 128 + 8 * h;
#pragma unroll
      for (int f = 0; f < 8; ++f) {
        float v[8], a[8];
        unpack8(xw[f], v); unpack8(pw[f], a);
#pragma unroll
        for (int jj = 0; jj < 8; ++jj) a[jj] += g.acc[f >> 1][0][8 * (f & 1) + jj];
        rbf8(a);
#pragma unroll
        for (int jj = 0; jj < 8; ++jj) a[jj] *= gb[f][jj];
        rbf8(a);
#pragma unroll
        for (int jj = 0; jj < 8; ++jj) v[jj] += a[jj];
        const u32x4 w = pack8(v);
        auto r0 = __builtin_amdgcn_permlane32_swap(w[0], w[2], false, false); auto r1 = __builtin_amdgcn_permlane32_swap(w[1], w[3], false, false);
        *(u32x4*)(orow + 16 * f) = (u32x4){r0[0], r1[0], r0[1], r1[1]};
      }
    }
  }
  KST_END(K == 4096 ? 3 : 6);
}
#define SK2_NT0 28
DI float silu_mul(float g, float u) { return g * __builtin_amdgcn_rcpf(1.f + __builtin_amdgcn_exp2f(g * -1.4426950408889634f)) * u; }
using UCfg = GCfg<32, 8, 32, 14, true, 0, 4, 2, 1, 2>;
__global__ __launch_bounds__(512, 2) void k_gemm_gu(const u16* __restrict__ Af, const u16* __restrict__ Wg, const u16* __restrict__ Wu, u16* __restrict__ Act) {
  using C = UCfg;
  extern __shared__ __attribute__((aligned(16))) char lds[];
  constexpr int K = 1024, F = 3584;
  KST_BEGIN(5);
  const unsigned tid = threadIdx.x, lane = tid & 63u, wid = rfl(tid >> 6);
  const int bid = blockIdx.x, xcd = bid & 7, j = bid >> 3;
  const int mt = (xcd & 1) * 4 + (j & 3), nt = (xcd >> 1) * 8 + (j >> 2);
  GState<C> g;
  const int mb0 = mt * 8 + wid;
  constexpr unsigned mbstride = (unsigned)K * 64u;
  g.srdA = mk_srd((const char*)Af + (size_t)mb0 * mbstride, mbstride);
  g.voffA[0] = lane * 16u;
  constexpr unsigned ldb = F * 2u;
  const bool up = wid >= 4;
  g.srdB = mk_srd((const char*)(up ? Wu : Wg) + (size_t)nt * 224, (unsigned)K * ldb - (unsigned)nt * 224u);
  g_setup<C>(g, (unsigned)(size_t)lds, lane, wid, ldb);
  gemm_run<C>(g, K / 64);
  const __amdgpu_buffer_rsrc_t srdO = mk_srd(Act, 2048u * F * 2u);
#pragma unroll
  for (int n = 0; n < 7; ++n) {
    float o[8];
#pragma unroll
    for (int e = 0; e < 8; ++e) o[e] = silu_mul(g.acc[n][0][e], g.acc[n][0][e + 8]);
    u32x4 w = {cvtpk(o[0], o[1]), cvtpk(o[2], o[3]), cvtpk(o[4], o[5]), cvtpk(o[6], o[7])};
    __builtin_amdgcn_raw_buffer_store_b128(w, srdO, (((unsigned)mb0 * (F >> 4) + (unsigned)(nt * 7 + n)) * 512u + lane * 8u) * 2u, 0, WT);
  }
  KST_END(5);
}

extern "C" void kernel_launch(void* const* d_in, const int* in_sizes, int n_in, void* d_out, int out_size, void* d_ws, size_t ws_size, hipStream_t stream) {
  const u16* x = (const u16*)d_in[0]; const u16* kc = (const u16*)d_in[1]; const u16* vc = (const u16*)d_in[2];
  const u16* wq = (const u16*)d_in[3]; const u16* wo = (const u16*)d_in[4];
  const u16* wg = (const u16*)d_in[6]; const u16* wu = (const u16*)d_in[7]; const u16* wd = (const u16*)d_in[8];
  const u16* c = (const u16*)d_in[9]; const u16* wmod = (const u16*)d_in[10];
  u16* out = (u16*)d_out;
  char* ws = (char*)d_ws;
  float* mod = (float*)ws;
  u16* hf = (u16*)(ws + (1 << 20));
  u16* x1f = (u16*)(ws + (8 << 20));
  u16* of = (u16*)(ws + (16 << 20));
  u16* P = (u16*)(ws + (32 << 20));
  u16* act = (u16*)(ws + (64 << 20));
  hipFuncSetAttribute((const void*)k_attn, hipFuncAttributeMaxDynamicSharedMemorySize, SHM_ATTN);
  hipFuncSetAttribute((const void*)k_gemm_sk<4096, false, 32>, hipFuncAttributeMaxDynamicSharedMemorySize, SCfg::LDSB);
  hipFuncSetAttribute((const void*)k_gemm_sk<3584, true, SK2_NT0>, hipFuncAttributeMaxDynamicSharedMemorySize, SCfg::LDSB);
  hipFuncSetAttribute((const void*)k_gemm_gu, hipFuncAttributeMaxDynamicSharedMemorySize, UCfg::LDSB);
  float* modA = (float*)(ws + (256 << 10));
  unsigned* flg = (unsigned*)(ws + (512 << 10));
  u16* P2 = (u16*)(ws + (48 << 20));
  k_pre<<<256, 512, 0, stream>>>(c, wmod, modA, x, flg);
  k_ln<0><<<256, 1024, 0, stream>>>(x, nullptr, modA, x1f, hf);
  k_attn<<<256, 256, SHM_ATTN, stream>>>(hf, wq, kc, vc, of, wo, wg, wu, wd, wmod);
  k_gemm_sk<4096, false, 32><<<256 + 128, 256, SCfg::LDSB, stream>>>(of, wo, P, c, wmod, mod, nullptr, nullptr, nullptr);
  k_ln<1><<<256, 1024, 0, stream>>>(x, P, mod, x1f, hf);
  k_gemm_gu<<<256, 512, UCfg::LDSB, stream>>>(hf, wg, wu, act);
  k_gemm_sk<3584, true, SK2_NT0><<<256, 256, SCfg::LDSB, stream>>>(act, wd, P2, nullptr, nullptr, mod, x1f, out, flg);
}
```

```cpp
#include <hip/hip_runtime.h>
#include <utility>
#include <type_traits>
typedef unsigned short u16;
typedef short bf16x8 __attribute__((ext_vector_type(8)));
typedef short s16x4 __attribute__((ext_vector_type(4)));
typedef float f32x4 __attribute__((ext_vector_type(4)));
typedef float f32x16 __attribute__((ext_vector_type(16)));
typedef unsigned u32x4 __attribute__((ext_vector_type(4)));
typedef unsigned u32x2 __attribute__((ext_vector_type(2)));
typedef float f32x2 __attribute__((ext_vector_type(2)));
typedef __bf16 bf16x2_t __attribute__((ext_vector_type(2)));
#define DI __device__ __forceinline__
#define LAS __attribute__((address_space(3)))
#define GAP() __builtin_amdgcn_sched_barrier(0)
template <int... Is> using iseq = std::integer_sequence<int, Is...>;
template <int N> using make_iseq = std::make_integer_sequence<int, N>;

#define KST_BEGIN(k)
#define KST_END(k)
DI unsigned rfl(unsigned x) { return __builtin_amdgcn_readfirstlane(x); }
DI float bf2f(u16 v) { return __uint_as_float(((unsigned)v) << 16); }
DI unsigned cvtpk(float lo, float hi) { f32x2 v = {lo, hi}; bf16x2_t b = __builtin_convertvector(v, bf16x2_t); return __builtin_bit_cast(unsigned, b); }
DI float rbf(float f);
DI float rbf(float f) { return __uint_as_float(cvtpk(f, 0.f) << 16); }
#define KV_FIRST 12
#define NTOUCH 0
#define WT 16
DI __amdgpu_buffer_rsrc_t mk_srd(const void* p, unsigned bytes) { return __builtin_amdgcn_make_buffer_rsrc((void*)p, (short)0, (int)bytes, 0x00020000); }

#define W4_ACLOB "a0","a1","a2","a3","a4","a5","a6","a7","a8","a9","a10","a11","a12","a13","a14","a15","a16","a17","a18","a19","a20","a21","a22","a23","a24","a25","a26","a27","a28","a29","a30","a31","a32","a33","a34","a35","a36","a37","a38","a39","a40","a41","a42","a43","a44","a45","a46","a47","a48","a49","a50","a51","a52","a53","a54","a55","a56","a57","a58","a59","a60","a61","a62","a63","a64","a65","a66","a67","a68","a69","a70","a71","a72","a73","a74","a75","a76","a77","a78","a79","a80","a81","a82","a83","a84","a85","a86","a87","a88","a89","a90","a91","a92","a93","a94","a95","a96","a97","a98","a99","a100","a101","a102","a103","a104","a105","a106","a107","a108","a109","a110","a111","a112","a113","a114","a115","a116","a117","a118","a119","a120","a121","a122","a123","a124","a125","a126","a127"
#define W4_ACLOB_HI "a128","a129","a130","a131","a132","a133","a134","a135","a136","a137","a138","a139","a140","a141","a142","a143","a144","a145","a146","a147","a148","a149","a150","a151","a152","a153","a154","a155","a156","a157","a158","a159","a160","a161","a162","a163","a164","a165","a166","a167","a168","a169","a170","a171","a172","a173","a174","a175","a176","a177","a178","a179","a180","a181","a182","a183","a184","a185","a186","a187","a188","a189","a190","a191","a192","a193","a194","a195","a196","a197","a198","a199","a200","a201","a202","a203","a204","a205","a206","a207","a208","a209","a210","a211","a212","a213","a214","a215","a216","a217","a218","a219","a220","a221","a222","a223","a224","a225","a226","a227","a228","a229","a230","a231","a232","a233","a234","a235","a236","a237","a238","a239","a240","a241","a242","a243","a244","a245","a246","a247","a248","a249","a250","a251","a252","a253","a254","a255"
template <int SH_, int NW_, int WR_, int NB16_, bool GU_, int KORD_, int NS_, int PDT_, int FD_, int BARPOS_, bool ASMACC_ = false>
struct GCfg {
  static constexpr bool ASMACC = ASMACC_;
  static constexpr int SH = SH_, NW = NW_, WR = WR_, NB16 = NB16_, KORD = KORD_, NS = NS_, PDT = PDT_, FD = FD_, BARPOS = BARPOS_;
  static constexpr bool GU = GU_;
  static constexpr int STEPS = SH == 32 ? 4 : 2;
  static constexpr int MR = WR / SH;
  static constexpr int NB = SH == 32 ? NB16 / 2 : NB16;
  static constexpr int NR16 = GU ? NB16 / 2 : NB16;
  static constexpr int PIECES = NB16 * 2;
  static constexpr int PB = (PIECES + NW - 1) / NW;
  static constexpr int STAGE = PB * NW * 1024;
  static constexpr int REGION = STAGE / 2;
  static constexpr int LDSB = NS * STAGE;
  static constexpr int NOPS = STEPS * MR + PB;
  static constexpr int GS = NB * MR;
  static constexpr int G = STEPS * GS;
  static constexpr int BSTEP = STEPS - FD;
  static constexpr int GB = BSTEP * GS + (BARPOS + 1) * MR - 1;
  static constexpr int dma_lo(int ks) { return (PB * ks) / STEPS; }
  static constexpr int step_ops(int ks) { return MR + dma_lo(ks + 1) - dma_lo(ks); }
  static constexpr int step_first(int ks) { return ks * MR + dma_lo(ks); }
  static constexpr int op_step(int j) { int ks = 0; while (ks < STEPS - 1 && j >= step_first(ks + 1)) ++ks; return ks; }
  static constexpr int op_gap(int j) { const int ks = op_step(j), i = j - step_first(ks), n = step_ops(ks); return ks * GS + (i * GS) / n; }
  static constexpr int ops_before(int g) { int c = 0; for (int j = 0; j < NOPS; ++j) if (op_gap(j) < g) ++c; return c; }
  static constexpr int NA(int ks) {
    const int ts = (ks + 1) % STEPS, jt = step_first(ts) + MR - 1;
    const int full = (ks == STEPS - 1) ? PDT - 1 : PDT;
    return (NOPS - 1 - jt) + (full - 1) * NOPS + ops_before(ks * GS);
  }
  static constexpr int NBW() { return (NS - 3) * NOPS + ops_before(GB); }
  static_assert(NS >= 3 && PDT >= 2 && FD <= STEPS - 0 && BSTEP >= 0, "");
  using acc_t = std::conditional_t<SH == 32, f32x16, f32x4>;
};

template <class C>
struct GState {
  typename C::acc_t acc[C::NB][C::MR];
  bf16x8 a[C::PDT * C::STEPS][C::MR];
  s16x4 blo[C::FD][C::NB], bhi[C::FD][C::NB];
  __amdgpu_buffer_rsrc_t srdA, srdB;
  unsigned voffA[C::MR];
  unsigned voffB[C::GU ? C::PB : 1];
  unsigned sB[C::GU ? 1 : C::PB];
  unsigned m0base, vrd, ktileB;
  unsigned long long tch[3]; unsigned tch_off;
  __amdgpu_buffer_rsrc_t srdK, srdV; unsigned kvm0, kvso, vk[2], vv;
};
struct GIter { unsigned va_cur, va_nxt, sA, sA0, m0_fill, sK_fill; };

template <class C, int SL, int OFF, int MRI> DI void a_load1(GState<C>& g, unsigned sA) {
  asm volatile("buffer_load_dwordx4 %0, %1, %2, %3 offen offset:%4" : "=v"(g.a[SL][MRI]) : "v"(g.voffA[MRI]), "s"(g.srdA), "s"(sA), "i"(OFF) : "memory");
}
template <class C, int Q> DI void b_dma1(GState<C>& g, unsigned m0v, unsigned sK) {
  if constexpr (C::GU)
    asm volatile("s_mov_b32 m0, %0\n\ts_nop 0\n\tbuffer_load_dwordx4 %1, %2, %3 offen lds" :: "s"(m0v + 1024u * Q), "v"(g.voffB[Q]), "s"(g.srdB), "s"(sK) : "memory");
  else
    asm volatile("s_mov_b32 m0, %0\n\ts_nop 0\n\tbuffer_load_dwordx4 %1, %2, %3 offen lds" :: "s"(m0v + 1024u * Q), "v"(g.voffB[0]), "s"(g.srdB), "s"(sK + g.sB[Q]) : "memory");
}
template <class C> DI void g_dummy(GState<C>& g) {
  asm volatile("s_mov_b32 m0, %0\n\ts_nop 0\n\tbuffer_load_dwordx4 %1, %2, %3 offen lds" :: "s"(g.m0base + (unsigned)(C::NS - 1) * C::STAGE), "v"(g.voffA[0]), "s"(g.srdA), "s"(0x80000000u) : "memory");
}
template <class C, int TI, int J, bool VA, bool VB> DI void g_op(GState<C>& g, const GIter& it) {
  constexpr int ks = C::op_step(J), i = J - C::step_first(ks);
  if constexpr (i < C::MR) {
    constexpr int PD = C::PDT * C::STEPS, SL = (TI * C::STEPS + ks - 1 + PD) % PD;
    if constexpr (VA) { if constexpr (ks == 0) a_load1<C, SL, 0, i>(g, it.sA0); else a_load1<C, SL, (ks - 1) * 1024, i>(g, it.sA); } else g_dummy<C>(g);
  } else {
    if constexpr (VB) b_dma1<C, C::dma_lo(ks) + i - C::MR>(g, it.m0_fill, it.sK_fill); else g_dummy<C>(g);
  }
}
template <class C, int TI, int GI, int... Js> DI void g_ops_at(GState<C>& g, const GIter& it, iseq<Js...>) {
  ((C::op_gap(Js) == GI ? g_op<C, TI, Js, true, true>(g, it) : (void)0), ...);
}
template <class C> constexpr int b_off(int KS, int NBI, int t) {
  if (C::SH == 32) {
    return ((2 * KS + t) * C::NR16 + (C::GU ? NBI : 2 * NBI)) * 256;
  } else {
    const int kb = 4 * KS + (C::KORD == 0 ? 2 * t : t);
    return (kb * C::NR16 + (C::GU ? NBI / 2 : NBI)) * 256 + ((C::GU && (NBI & 1)) ? C::REGION : 0);
  }
}
template <class C, int BUF, int KS, int NBI> DI void b_read(GState<C>& g, unsigned va) {
  g.blo[BUF][NBI] = __builtin_amdgcn_ds_read_tr16_b64_v4i16((LAS s16x4*)(size_t)(va + b_off<C>(KS, NBI, 0)));
  g.bhi[BUF][NBI] = __builtin_amdgcn_ds_read_tr16_b64_v4i16((LAS s16x4*)(size_t)(va + b_off<C>(KS, NBI, 1)));
}
template <class C, int BUF, int KS, int... Ns> DI void b_read_all(GState<C>& g, unsigned va, iseq<Ns...>) { (b_read<C, BUF, KS, Ns>(g, va), ...); }

template <class C, int N, int SL> DI void a_wait(GState<C>& g) {
  static_assert(N >= 0 && N < 64, "vmcnt range");
  if constexpr (C::MR == 1) asm volatile("s_waitcnt vmcnt(%1)" : "+v"(g.a[SL][0]) : "i"(N) : "memory");
  else if constexpr (C::MR == 2) asm volatile("s_waitcnt vmcnt(%2)" : "+v"(g.a[SL][0]), "+v"(g.a[SL][1]) : "i"(N) : "memory");
  else asm volatile("s_waitcnt vmcnt(%4)" : "+v"(g.a[SL][0]), "+v"(g.a[SL][1]), "+v"(g.a[SL][2]), "+v"(g.a[SL][3]) : "i"(N) : "memory");
}

template <class C, int TI, int GI> DI void g_gap(GState<C>& g, const GIter& it) {
  constexpr int KS = GI / C::GS, r = GI % C::GS, NBI = r / C::MR, MRI = r % C::MR;
  constexpr int SL = TI * C::STEPS + KS, BUF = (C::FD == 1) ? 0 : (KS & 1), KN = (KS + C::FD) % C::STEPS;
  if constexpr (r == 0) { a_wait<C, C::NA(KS), SL>(g); GAP(); }
  {
    const bf16x8 bf = __builtin_shufflevector(g.blo[BUF][NBI], g.bhi[BUF][NBI], 0, 1, 2, 3, 4, 5, 6, 7);
    if constexpr (C::ASMACC) { constexpr int ao = 16 * (C::MR * NBI + MRI);
      asm volatile("v_mfma_f32_32x32x16_bf16 a[%c2:%c3], %0, %1, a[%c2:%c3]" :: "v"(bf), "v"(g.a[SL][MRI]), "i"(ao), "i"(ao + 15)); }
    else if constexpr (C::SH == 32) g.acc[NBI][MRI] = __builtin_amdgcn_mfma_f32_32x32x16_bf16(bf, g.a[SL][MRI], g.acc[NBI][MRI], 0, 0, 0);
    else g.acc[NBI][MRI] = __builtin_amdgcn_mfma_f32_16x16x32_bf16(bf, g.a[SL][MRI], g.acc[NBI][MRI], 0, 0, 0);
  }
  if constexpr (GI == C::GB) {
    GAP();
    static_assert(C::NBW() < 64, "");
    asm volatile("s_waitcnt vmcnt(%0) lgkmcnt(0)\n\ts_barrier" :: "i"(C::NBW()) : "memory");
    GAP();
    b_read_all<C, BUF, KN>(g, it.va_nxt, make_iseq<C::BARPOS + 1>{});
  } else if constexpr (MRI == C::MR - 1) {
    if constexpr (KS == C::BSTEP) { if constexpr (NBI > C::BARPOS) b_read<C, BUF, KN, NBI>(g, it.va_nxt); }
    else if constexpr (KS > C::BSTEP) b_read<C, BUF, KN, NBI>(g, it.va_nxt);
    else b_read<C, BUF, KN, NBI>(g, it.va_cur);
  }
  g_ops_at<C, TI, GI>(g, it, make_iseq<C::NOPS>{});
  GAP();
}
template <class C, int TI, int... Gs> DI void g_gaps(GState<C>& g, const GIter& it, iseq<Gs...>) { (g_gap<C, TI, Gs>(g, it), ...); }
template <class C, int TI> DI void g_tile(GState<C>& g, int t, unsigned& stg, unsigned& pstg) {
  unsigned nstg = stg + C::STAGE; if (nstg == C::NS * C::STAGE) nstg = 0;
  GIter it;
  it.va_cur = g.vrd + stg; it.va_nxt = g.vrd + nstg;
  it.sA = (unsigned)(C::STEPS * 1024) * (unsigned)(t + C::PDT); it.sA0 = it.sA - 1024u;
  it.m0_fill = g.m0base + pstg; it.sK_fill = g.ktileB * (unsigned)(t - 1 + C::NS);
  g_gaps<C, TI>(g, it, make_iseq<C::G>{});
  pstg = stg; stg = nstg;
}
template <class C, int... TIs> DI void g_body(GState<C>& g, int t, unsigned& stg, unsigned& pstg, iseq<TIs...>) { (g_tile<C, TIs>(g, t + TIs, stg, pstg), ...); }

template <class C, int I> DI void g_touch(GState<C>& g) {
  asm volatile("s_mov_b32 m0, %0\n\ts_nop 0\n\tglobal_load_lds_dword %1, %2" :: "s"(g.m0base + (unsigned)(C::NS - 1) * C::STAGE), "v"(g.tch_off), "s"(g.tch[I]) : "memory");
}
template <class C, int I> DI void g_kv(GState<C>& g) {
  constexpr int q = I & 3;
  if constexpr ((I >> 2) == 1) asm volatile("s_mov_b32 m0, %0\n\ts_nop 0\n\tbuffer_load_dwordx4 %1, %2, %3 offen lds" :: "s"(g.kvm0 + 0x8000u + 1024u * q), "v"(g.vv), "s"(g.srdV), "s"(g.kvso + 2048u * (q >> 1) + 128u * (q & 1)) : "memory");
  else asm volatile("s_mov_b32 m0, %0\n\ts_nop 0\n\tbuffer_load_dwordx4 %1, %2, %3 offen offset:%c4 lds" :: "s"(g.kvm0 + (I >> 3) * 0x4000u), "v"(g.vk[q & 1]), "s"(g.srdK), "s"(g.kvso + (I >> 3) * 16384u), "i"(1024 * q) : "memory");
}
template <class C> constexpr int dummy_index(int V, int J) {
  const int ks = C::op_step(J), i = J - C::step_first(ks);
  if (i >= C::MR || (C::STEPS * (V + C::PDT) + ks - 1) >= 0) return -1;
  return (V + (C::NS - 1)) * C::STEPS * C::MR + ks * C::MR + i;
}
template <class C, int V, int J> DI void g_virt_op(GState<C>& g, const GIter& it) {
  constexpr int ks = C::op_step(J);
  constexpr int TI = ((V % C::PDT) + C::PDT) % C::PDT;
  constexpr bool VA = (C::STEPS * (V + C::PDT) + ks - 1) >= 0, VB = (V - 1 + C::NS) >= 0;
  if constexpr (V == -1 && J == C::ops_before(C::GB)) asm volatile("s_waitcnt vmcnt(%0)\n\ts_barrier" :: "i"(C::NBW()) : "memory");
  constexpr int di = C::ASMACC ? dummy_index<C>(V, J) : -1;
  if constexpr (di >= 0 && di < NTOUCH) g_touch<C, di>(g);
  else if constexpr (di >= KV_FIRST && di < KV_FIRST + 12) g_kv<C, di - KV_FIRST>(g);
  else g_op<C, TI, J, VA, VB>(g, it);
}
template <class C, int V, int... Js> DI void g_virt(GState<C>& g, iseq<Js...>) {
  GIter it; constexpr int tb = V - 1 + C::NS;
  it.va_cur = it.va_nxt = 0; it.sA = (unsigned)(C::STEPS * 1024) * (unsigned)(V + C::PDT >= 0 ? V + C::PDT : 0); it.sA0 = it.sA - 1024u;
  it.m0_fill = g.m0base + (unsigned)(tb >= 0 ? tb : 0) * C::STAGE; it.sK_fill = g.ktileB * (unsigned)(tb >= 0 ? tb : 0);
  (g_virt_op<C, V, Js>(g, it), ...);
  if constexpr (V == -1 && C::ops_before(C::GB) == C::NOPS) asm volatile("s_waitcnt vmcnt(%0)\n\ts_barrier" :: "i"(C::NBW()) : "memory");
}
template <class C, int... Vs> DI void g_prologue(GState<C>& g, iseq<Vs...>) { constexpr int NV = sizeof...(Vs); (g_virt<C, Vs - NV>(g, make_iseq<C::NOPS>{}), ...); }

template <class C, bool PSWAP = false> DI void g_setup(GState<C>& g, unsigned ldsb, unsigned lane, unsigned wid, unsigned ldb) {
  g.m0base = rfl(ldsb + wid * (unsigned)C::PB * 1024u);
  g.ktileB = 64u * ldb;
  const unsigned q = (lane & 15u) >> 2, p0 = lane & 3u, p = PSWAP ? (((p0 & 1u) << 1) | (p0 >> 1)) : p0, g4 = lane >> 4;
  if constexpr (C::SH == 32) {
    const unsigned h = lane >> 5, blk = g4 & 1u;
    g.vrd = ldsb + blk * (C::GU ? (unsigned)C::REGION : 256u) + ((4u * h + q) ^ (4u * blk)) * 32u + 8u * p;
  } else {
    g.vrd = ldsb + (g4 >> 1) * (C::KORD == 0 ? 1u : 2u) * (unsigned)(C::NR16 * 256) + (4u * (g4 & 1u) + q) * 32u + 8u * p;
  }
  if constexpr (C::GU) {
    const unsigned wr = wid % (C::NW / 2), up = wid / (C::NW / 2);
#pragma unroll
    for (int qq = 0; qq < C::PB; ++qq) {
      const unsigned P = wr * C::PB + qq, s = 4u * P + g4 * 0u + (lane >> 4), kb = s / (unsigned)C::NR16, nb = s % (unsigned)C::NR16;
      const unsigned row = ((lane >> 1) & 7u) ^ ((C::SH == 32 && up) ? 4u : 0u);
      g.voffB[qq] = (P < (unsigned)(C::NR16 * 2)) ? (8u * kb + row) * ldb + 32u * nb + 16u * (lane & 1u) : 0x80000000u;
    }
  } else {
    static_assert(C::GU || (C::NR16 % 4 == 0), "");
    const unsigned sub = lane >> 4;
    const unsigned row = ((lane >> 1) & 7u) ^ ((C::SH == 32 && (sub & 1u)) ? 4u : 0u);
    g.voffB[0] = row * ldb + 32u * sub + 16u * (lane & 1u);
#pragma unroll
    for (int qq = 0; qq < C::PB; ++qq) { const unsigned P = wid * C::PB + qq, kb = P / (unsigned)(C::NR16 / 4), j = P % (unsigned)(C::NR16 / 4); g.sB[qq] = rfl(8u * kb * ldb + 128u * j); }
  }
}

template <int R> DI void acc_zero1() { asm volatile("v_accvgpr_write_b32 a[%c0], 0" :: "i"(R) : W4_ACLOB); }
template <int... Rs> DI void acc_zero(iseq<Rs...>) { (acc_zero1<Rs>(), ...); asm volatile("s_nop 1"); }
template <class C> DI void gemm_run(GState<C>& g, int NT) {
  constexpr int NV = C::PDT > C::NS - 1 ? C::PDT : C::NS - 1;
  if constexpr (!C::ASMACC) {
#pragma unroll
    for (int n = 0; n < C::NB; ++n)
#pragma unroll
      for (int m = 0; m < C::MR; ++m)
#pragma unroll
        for (int e = 0; e < (C::SH == 32 ? 16 : 4); ++e) g.acc[n][m][e] = 0.f;
  }
  g_prologue<C>(g, make_iseq<NV>{});
  if constexpr (C::ASMACC) acc_zero(make_iseq<C::NB * C::MR * 16>{});
  b_read_all<C, 0, 0>(g, g.vrd, make_iseq<C::NB>{});
  if constexpr (C::FD == 2) b_read_all<C, 1, 1>(g, g.vrd, make_iseq<C::NB>{});
  GAP();
  unsigned stg = 0, pstg = (C::NS - 1) * C::STAGE;
  for (int t = 0; t < NT; t += C::PDT) g_body<C>(g, t, stg, pstg, make_iseq<C::PDT>{});
  asm volatile("s_waitcnt vmcnt(0) lgkmcnt(0)" ::: "memory");
#pragma unroll
  for (int i = 0; i < C::PDT * C::STEPS; ++i)
#pragma unroll
    for (int m = 0; m < C::MR; ++m) asm volatile("" :: "v"(g.a[i][m]));
  GAP();
}
typedef __bf16 abf16x8 __attribute__((ext_vector_type(8)));
typedef __bf16 abf16x4 __attribute__((ext_vector_type(4)));
#define ATTN_THR 64.0f
constexpr unsigned KRS = 256u, KTILEB = 64u * KRS;
#define KRING 0x0000u
#define VRING 0x8000u
#define QGEMM_LDS 0x10000u
DI float max3f(float a, float b, float c) { float r; asm("v_max3_f32 %0, %1, %2, %3" : "=v"(r) : "v"(a), "v"(b), "v"(c)); return r; }
DI float max2f(float a, float b) { float r; asm("v_max_f32_e32 %0, %1, %2" : "=v"(r) : "v"(a), "v"(b)); return r; }
DI float fadd_s(float a, float b) { float r; asm("v_add_f32_e32 %0, %1, %2" : "=v"(r) : "v"(a), "v"(b)); return r; }
DI float fmul_s(float a, float b) { float r; asm("v_mul_f32_e32 %0, %1, %2" : "=v"(r) : "v"(a), "v"(b)); return r; }
DI float fsub_s(float a, float b) { float r; asm("v_sub_f32_e32 %0, %1, %2" : "=v"(r) : "v"(a), "v"(b)); return r; }
DI float w4_aone() { unsigned l; asm volatile("v_mbcnt_lo_u32_b32 %0, -1, 0\n\tv_mbcnt_hi_u32_b32 %0, -1, %0" : "=v"(l)); return l < 32u ? 1.f : 0.f; }
DI void negm_bcast(f32x16& negm, float aone, float neg_mhat) {
  asm volatile("s_nop 1\n\tv_mfma_f32_32x32x2_f32 %0, %1, %2, 0\n\ts_nop 7\n\ts_nop 7\n\ts_nop 3" : "+v"(negm) : "v"(aone), "v"(neg_mhat));
}
struct W4C {
  unsigned vkb[4], vvb, vksrc[2], vvsrc[4], m0k, m0v, sofk, sofv;
  __amdgpu_buffer_rsrc_t srdK, srdV;
  f32x16 S[2][2][2];
  f32x16 NEGM[2];
  abf16x8 Vf[4][4];
  unsigned pk[2][4][4];
  abf16x4 vlo[4][4], vup[4][4];
  float mhat[2], fct[2]; bool pend;
  float rs[2][2];
  float mA, mB;
  unsigned sk_m0, sk_so, sv_m0, sv_so;
  unsigned pf_lo, pf_hi, pf_off, pf_m0;
};
constexpr int AO_(int db, int qb) { return 16 * (2 * db + qb); }
constexpr int AQ_(int qb, int d0) { return 128 + 32 * qb + 4 * d0; }
constexpr int AK_(int kb, int d0) { return 192 + 32 * kb + 4 * d0; }

template <int I> DI void tr_read(W4C& c, unsigned va) {
  constexpr int ks = I >> 3, db = (I >> 1) & 3;
  if constexpr ((I & 1) == 0) asm volatile("ds_read_b64_tr_b16 %0, %1 offset:%2" : "=v"(c.vlo[db][ks]) : "v"(va), "i"(4096 * ks + 512 * db) : "memory");
  else                        asm volatile("ds_read_b64_tr_b16 %0, %1 offset:%2" : "=v"(c.vup[db][ks]) : "v"(va), "i"(4096 * ks + 2048 + 512 * db) : "memory");
}
template <int I, int SLOT> DI void k_read(W4C& c) {
  constexpr int kb = I >> 3, d0 = I & 7;
  asm volatile("ds_read_b128 a[%c1:%c2], %0 offset:%c3" :: "v"(c.vkb[d0 & 3]), "i"(AK_(kb, d0)), "i"(AK_(kb, d0) + 3), "i"(SLOT * 0x4000 + 8192 * kb + 128 * (d0 >> 2)) : "memory");
}
template <int X, bool FIRST, int M> DI void qk_mfma(W4C& c) {
  constexpr int d0 = M >> 2, kb = (M >> 1) & 1, qb = M & 1, ak = AK_(kb, d0), aq = AQ_(qb, d0);
  if constexpr (d0 == 0) {
    if constexpr (FIRST) asm volatile("v_mfma_f32_32x32x16_bf16 %0, a[%c1:%c2], a[%c3:%c4], 0" : "=&v"(c.S[X][kb][qb]) : "i"(ak), "i"(ak + 3), "i"(aq), "i"(aq + 3));
    else                 asm volatile("v_mfma_f32_32x32x16_bf16 %0, a[%c2:%c3], a[%c4:%c5], %1" : "=&v"(c.S[X][kb][qb]) : "v"(c.NEGM[qb]), "i"(ak), "i"(ak + 3), "i"(aq), "i"(aq + 3));
  } else asm volatile("v_mfma_f32_32x32x16_bf16 %0, a[%c1:%c2], a[%c3:%c4], %0" : "+v"(c.S[X][kb][qb]) : "i"(ak), "i"(ak + 3), "i"(aq), "i"(aq + 3));
}
template <int M, bool ZERO> DI void pv_mfma(W4C& c) {
  constexpr int ks = M >> 3, db = (M >> 1) & 3, qb = M & 1, ao = AO_(db, qb);
  const u32x4 pw = {c.pk[qb][ks][0], c.pk[qb][ks][1], c.pk[qb][ks][2], c.pk[qb][ks][3]};
  if constexpr (ZERO && ks == 0) asm volatile("v_mfma_f32_32x32x16_bf16 a[%c2:%c3], %0, %1, 0" :: "v"(c.Vf[db][ks]), "v"(pw), "i"(ao), "i"(ao + 15));
  else                         asm volatile("v_mfma_f32_32x32x16_bf16 a[%c2:%c3], %0, %1, a[%c2:%c3]" :: "v"(c.Vf[db][ks]), "v"(pw), "i"(ao), "i"(ao + 15));
}
template <int X, int KB, int E> DI void op_exp(W4C& c) { c.S[X][KB][E >> 4][E & 15] = __builtin_amdgcn_exp2f(c.S[X][KB][E >> 4][E & 15]); }
template <int X, int KB, int E> DI void op_add(W4C& c) { asm volatile("v_add_f32_e32 %0, %0, %1" : "+v"(c.rs[E >> 4][E & 1]) : "v"(c.S[X][KB][E >> 4][E & 15])); }
template <int X, int KB, int J> DI void op_cvt(W4C& c) {
  constexpr int qb = J >> 3, ks = 2 * KB + ((J >> 2) & 1), p = J & 3, r = 2 * (J & 7);
  c.pk[qb][ks][p] = cvtpk(c.S[X][KB][qb][r], c.S[X][KB][qb][r + 1]);
  if constexpr (KB == 1) asm volatile("" : "+v"(c.pk[qb][ks][p]));
}
template <int X, int I> DI float sval(W4C& c) { return c.S[X][I >> 5][(I >> 4) & 1][I & 15]; }
template <int X, int G> DI void op_max4(W4C& c) {
  if constexpr (G == 0) asm volatile("v_max3_f32 %0, %2, %3, %4\n\tv_max3_f32 %1, %5, %6, %7\n\tv_max3_f32 %0, %0, %8, %9" : "=&v"(c.mA), "=&v"(c.mB)
                                     : "v"(sval<X, 0>(c)), "v"(sval<X, 1>(c)), "v"(sval<X, 2>(c)), "v"(sval<X, 3>(c)), "v"(sval<X, 4>(c)), "v"(sval<X, 5>(c)), "v"(sval<X, 6>(c)), "v"(sval<X, 7>(c)));
  else asm volatile("v_max3_f32 %1, %1, %2, %3\n\tv_max3_f32 %0, %0, %4, %5\n\tv_max3_f32 %1, %1, %6, %7\n\tv_max3_f32 %0, %0, %8, %9" : "+v"(c.mA), "+v"(c.mB)
                    : "v"(sval<X, 8 * G>(c)), "v"(sval<X, 8 * G + 1>(c)), "v"(sval<X, 8 * G + 2>(c)), "v"(sval<X, 8 * G + 3>(c)), "v"(sval<X, 8 * G + 4>(c)), "v"(sval<X, 8 * G + 5>(c)), "v"(sval<X, 8 * G + 6>(c)), "v"(sval<X, 8 * G + 7>(c)));
}
constexpr int EB0 = 10;
constexpr int eb_lo(int g) { return g <= EB0 ? 0 : (32 * (g - EB0)) / (32 - EB0); }
template <int X, bool FIRST, int M> DI void gapA(W4C& c, unsigned va, const int t) {
  constexpr int Xp = X ^ 1;
  qk_mfma<X, FIRST, M>(c);
  GAP();
  if constexpr (!FIRST) {
    op_exp<Xp, 1, M>(c);
    constexpr int base = M + (M > 16) + (M > 18) + (M > 20) + (M > 22);
    if constexpr (M <= 27) tr_read<base>(c, va);
    if constexpr (M < 16) op_cvt<Xp, 0, M>(c);
    else if constexpr (M >= 17 && M <= 23 && (M & 1)) op_cvt<Xp, 1, (M - 17) / 2>(c);
    else if constexpr (M >= 25 && M <= 28) op_cvt<Xp, 1, 8 + M - 25>(c);
    if constexpr (M == 16 || M == 18 || M == 20 || M == 22) tr_read<base + 1>(c, va);
    if constexpr (M == 0) { op_add<Xp, 0, eb_lo(31)>(c); if constexpr (eb_lo(31) + 1 < 32) op_add<Xp, 0, eb_lo(31) + 1>(c); }
    else op_add<Xp, 1, M - 1>(c);
  }
  if constexpr (M == 29) { c.sk_m0 = c.m0k + (unsigned)X * 0x4000u; c.sk_so = KTILEB * (unsigned)(t + 2) + c.sofk; asm volatile("" : "+s"(c.sk_m0), "+s"(c.sk_so)); }
  if constexpr (M == 30) { c.sv_m0 = c.m0v + (unsigned)Xp * 0x4000u; c.sv_so = KTILEB * (unsigned)(t + 1) + c.sofv; asm volatile("" : "+s"(c.sv_m0), "+s"(c.sv_so)); }
  if constexpr (M == 31) asm volatile("s_mov_b32 m0, %0" :: "s"(c.sk_m0) : "memory");
  if constexpr (!FIRST && M == 8) asm volatile("" :: "v"(c.NEGM[0]), "v"(c.NEGM[1]));
  GAP();
}
#define W4_VLO "+v"(c.vlo[0][0]), "+v"(c.vlo[0][1]), "+v"(c.vlo[0][2]), "+v"(c.vlo[0][3]), "+v"(c.vlo[1][0]), "+v"(c.vlo[1][1]), "+v"(c.vlo[1][2]), "+v"(c.vlo[1][3]), \
               "+v"(c.vlo[2][0]), "+v"(c.vlo[2][1]), "+v"(c.vlo[2][2]), "+v"(c.vlo[2][3]), "+v"(c.vlo[3][0]), "+v"(c.vlo[3][1]), "+v"(c.vlo[3][2]), "+v"(c.vlo[3][3])
#define W4_VUP "+v"(c.vup[0][0]), "+v"(c.vup[0][1]), "+v"(c.vup[0][2]), "+v"(c.vup[0][3]), "+v"(c.vup[1][0]), "+v"(c.vup[1][1]), "+v"(c.vup[1][2]), "+v"(c.vup[1][3]), \
               "+v"(c.vup[2][0]), "+v"(c.vup[2][1]), "+v"(c.vup[2][2]), "+v"(c.vup[2][3]), "+v"(c.vup[3][0]), "+v"(c.vup[3][1]), "+v"(c.vup[3][2]), "+v"(c.vup[3][3])
template <int X, bool FIRST, int... Ms> DI void phaseA_(W4C& c, const int t, iseq<Ms...>) {
  const unsigned va = c.vvb + (unsigned)(X ^ 1) * 0x4000u;
  (gapA<X, FIRST, Ms>(c, va, t), ...);
  if constexpr (FIRST) asm volatile("s_waitcnt vmcnt(0) lgkmcnt(0)\n\ts_barrier\n\ts_nop 7" ::: "memory");
  else { asm volatile("s_waitcnt vmcnt(%c16) lgkmcnt(0)" : W4_VLO : "i"(X == 1 ? 1 : 0) : "memory"); asm volatile("s_barrier\n\ts_nop 1" : W4_VUP :: "memory"); }
  GAP();
  if constexpr (!FIRST) {
#pragma unroll
    for (int ks = 0; ks < 4; ++ks)
#pragma unroll
      for (int db = 0; db < 4; ++db) c.Vf[db][ks] = __builtin_shufflevector(c.vlo[db][ks], c.vup[db][ks], 0, 1, 2, 3, 4, 5, 6, 7);
  }
}
template <int X, bool FIRST> DI void phaseA(W4C& c, const int t) { phaseA_<X, FIRST>(c, t, make_iseq<32>{}); }

template <int X, bool INIT> DI void new_reference(W4C& c) {
#pragma unroll
  for (int q = 0; q < 2; ++q) {
    float m0 = max3f(c.S[X][0][q][0], c.S[X][0][q][1], c.S[X][1][q][0]), m1 = max2f(c.S[X][1][q][1], c.S[X][0][q][2]);
#pragma unroll
    for (int e = 2; e < 16; e += 2) { m0 = max3f(m0, c.S[X][0][q][e], c.S[X][0][q][e + 1]); m1 = max3f(m1, c.S[X][1][q][e], c.S[X][1][q][e + 1]); }
    const float mm = max2f(m0, m1);
    auto pr = __builtin_amdgcn_permlane32_swap(__float_as_uint(mm), __float_as_uint(mm), false, false);
    const float rmax = max2f(__uint_as_float(pr[0]), __uint_as_float(pr[1]));
    const float dl = INIT ? rmax : max2f(rmax, 0.f);
    if constexpr (INIT) { c.mhat[q] = dl; c.rs[q][0] = 0.f; c.rs[q][1] = 0.f; c.fct[q] = 1.f; }
    else { c.mhat[q] = fadd_s(c.mhat[q], dl); c.fct[q] = __builtin_amdgcn_exp2f(-dl); c.rs[q][0] = c.rs[q][0] * c.fct[q]; c.rs[q][1] = c.rs[q][1] * c.fct[q]; }
#pragma unroll
    for (int kb = 0; kb < 2; ++kb) { _Pragma("unroll") for (int e = 0; e < 16; ++e) c.S[X][kb][q][e] = fsub_s(c.S[X][kb][q][e], dl); }
    negm_bcast(c.NEGM[q], w4_aone(), -c.mhat[q]);
  }
  asm volatile("s_nop 1");
}
template <int X, bool PEEL, bool ZERO1, int M> DI void gapB(W4C& c, const int t) {
  constexpr int Xp = X ^ 1;
  if constexpr (!PEEL) { pv_mfma<M, ZERO1>(c); GAP(); }
  if constexpr (!PEEL && M == 0) op_add<Xp, 1, 31>(c);
  constexpr int kq = M == 0 ? 0 : M == 11 ? 1 : M == 13 ? 2 : M == 15 ? 3 : -1, vq = M == 18 ? 0 : M == 20 ? 1 : M == 22 ? 2 : M == 24 ? 3 : -1;
  if constexpr (kq >= 0) asm volatile("buffer_load_dwordx4 %0, %1, %2 offen offset:%c3 lds" :: "v"(c.vksrc[kq & 1]), "s"(c.srdK), "s"(c.sk_so), "i"(1024 * kq) : "memory");
  if constexpr (M == 16) asm volatile("s_mov_b32 m0, %0" :: "s"(c.sv_m0) : "memory");
  if constexpr (vq >= 0) asm volatile("buffer_load_dwordx4 %0, %1, %2 offen offset:%c3 lds" :: "v"(c.vvsrc[vq]), "s"(c.srdV), "s"(c.sv_so), "i"(1024 * vq) : "memory");
  if constexpr (M >= 1 && M <= 8) {
    if constexpr (!PEEL) { op_max4<X, M - 1>(c); op_cvt<Xp, 1, (M <= 4 ? 4 + M - 1 : 12 + M - 5)>(c); }
    k_read<M - 1, Xp>(c);
  }
  if constexpr (M == 9) {
    if constexpr (PEEL) new_reference<X, true>(c);
    else {
      const float mm = max2f(c.mA, c.mB);
      if (__builtin_expect(__any((int)(mm > ATTN_THR)), 0)) { new_reference<X, false>(c); c.pend = true; }
    }
  }
  if constexpr (M >= EB0) {
    constexpr int e0 = eb_lo(M), e1 = eb_lo(M + 1), p0 = eb_lo(M - 1);
    op_exp<X, 0, e0>(c);
    if constexpr (M > EB0) op_add<X, 0, p0>(c);
    if constexpr (e0 + 1 < e1) op_exp<X, 0, e0 + 1>(c);
    if constexpr (M > EB0 && p0 + 1 < e0) op_add<X, 0, p0 + 1>(c);
    constexpr int kr = M == 10 ? 8 : M == 12 ? 9 : M == 14 ? 10 : M == 17 ? 11 : M == 19 ? 12 : M == 21 ? 13 : M == 23 ? 14 : M == 25 ? 15 : -1;
    if constexpr (kr >= 0) k_read<kr, Xp>(c);
  }
  if constexpr (X == 0 && M == 26) {
    unsigned plo, phi;
    asm volatile("v_readlane_b32 %0, %2, %4\n\tv_readlane_b32 %1, %3, %4\n\ts_mov_b32 m0, %5" : "=s"(plo), "=s"(phi) : "v"(c.pf_lo), "v"(c.pf_hi), "s"((unsigned)(t >> 1)), "s"(c.pf_m0) : "memory");
    c.sk_m0 = plo; c.sk_so = phi;
  }
  if constexpr (X == 0 && M == 28) {
    const unsigned long long pb = ((unsigned long long)c.sk_so << 32) | c.sk_m0;
    asm volatile("global_load_lds_dword %0, %1" :: "v"(c.pf_off), "s"(pb) : "memory");
  }
  if constexpr (M == 31) asm volatile("" :: "v"(c.S[X][0][0]), "v"(c.S[X][0][1]));
  GAP();
}
template <int R> DI void resc4(float f) {
  float t0, t1, t2, t3;
  asm volatile("v_accvgpr_read_b32 %0, a[%c5]\n\tv_accvgpr_read_b32 %1, a[%c6]\n\tv_accvgpr_read_b32 %2, a[%c7]\n\tv_accvgpr_read_b32 %3, a[%c8]\n\t"
               "v_mul_f32 %0, %4, %0\n\tv_mul_f32 %1, %4, %1\n\tv_mul_f32 %2, %4, %2\n\tv_mul_f32 %3, %4, %3\n\t"
               "v_accvgpr_write_b32 a[%c5], %0\n\tv_accvgpr_write_b32 a[%c6], %1\n\tv_accvgpr_write_b32 a[%c7], %2\n\tv_accvgpr_write_b32 a[%c8], %3"
               : "=&v"(t0), "=&v"(t1), "=&v"(t2), "=&v"(t3) : "v"(f), "i"(R), "i"(R + 1), "i"(R + 2), "i"(R + 3) : W4_ACLOB);
}
template <int... Is> DI void resc_all(W4C& c, iseq<Is...>) { (resc4<4 * Is>(c.fct[(Is >> 2) & 1]), ...); }
template <int X, bool PEEL, bool ZERO1, int... Ms> DI void phaseB_(W4C& c, const int t, iseq<Ms...>) {
  (gapB<X, PEEL, ZERO1, Ms>(c, t), ...);
  if constexpr (!PEEL) {
    if (__builtin_expect(c.pend, 0)) {
      asm volatile("s_nop 7\n\ts_nop 7" ::: W4_ACLOB);
      resc_all(c, make_iseq<32>{});
      asm volatile("s_nop 1" ::: W4_ACLOB);
      c.pend = false;
    }
  }
  asm volatile("s_waitcnt lgkmcnt(0)" ::: "memory");
  GAP();
}
template <int X, bool PEEL, bool ZERO1> DI void phaseB(W4C& c, const int t) { phaseB_<X, PEEL, ZERO1>(c, t, make_iseq<32>{}); }
template <int... Is> DI void k_reads0(W4C& c, iseq<Is...>) { (k_read<Is, 0>(c), ...); }
template <int... Is> DI void tr_reads(W4C& c, unsigned va, iseq<Is...>) { (tr_read<Is>(c, va), ...); }
template <int... Is> DI void drain_exp(W4C& c, iseq<Is...>) { (op_exp<1, 1, Is>(c), ...); }
template <int... Is> DI void drain_cvt0(W4C& c, iseq<Is...>) { (op_cvt<1, 0, Is>(c), ...); }
template <int... Is> DI void drain_cvt1(W4C& c, iseq<Is...>) { (op_cvt<1, 1, Is>(c), ...); }
template <int... Is> DI void drain_add(W4C& c, iseq<Is...>) { (op_add<1, 1, Is>(c), ...); }
template <int... Ms> DI void pv_all(W4C& c, iseq<Ms...>) { (pv_mfma<Ms, false>(c), ...); }
template <int R> DI u32x4 acc_pack8(float f) {
  float o[8];
  asm volatile("v_accvgpr_read_b32 %0, a[%c8]\n\tv_accvgpr_read_b32 %1, a[%c9]\n\tv_accvgpr_read_b32 %2, a[%c10]\n\tv_accvgpr_read_b32 %3, a[%c11]\n\t"
               "v_accvgpr_read_b32 %4, a[%c12]\n\tv_accvgpr_read_b32 %5, a[%c13]\n\tv_accvgpr_read_b32 %6, a[%c14]\n\tv_accvgpr_read_b32 %7, a[%c15]"
               : "=v"(o[0]), "=v"(o[1]), "=v"(o[2]), "=v"(o[3]), "=v"(o[4]), "=v"(o[5]), "=v"(o[6]), "=v"(o[7])
               : "i"(R), "i"(R + 1), "i"(R + 2), "i"(R + 3), "i"(R + 4), "i"(R + 5), "i"(R + 6), "i"(R + 7));
  u32x4 w = {cvtpk(o[0] * f, o[1] * f), cvtpk(o[2] * f, o[3] * f), cvtpk(o[4] * f, o[5] * f), cvtpk(o[6] * f, o[7] * f)};
  return w;
}
template <int I> DI void q_conv1(float C) {
  constexpr int qb = I >> 3, d0 = I & 7, nb = d0 >> 1, s = d0 & 1, aq = AQ_(qb, d0);
  const u32x4 w = acc_pack8<AO_(nb, qb) + 8 * s>(C);
  asm volatile("v_accvgpr_write_b32 a[%c4], %0\n\tv_accvgpr_write_b32 a[%c5], %1\n\tv_accvgpr_write_b32 a[%c6], %2\n\tv_accvgpr_write_b32 a[%c7], %3"
               :: "v"(w[0]), "v"(w[1]), "v"(w[2]), "v"(w[3]), "i"(aq), "i"(aq + 1), "i"(aq + 2), "i"(aq + 3) : W4_ACLOB_HI);
}
template <int... Is> DI void q_conv(float C, iseq<Is...>) { (q_conv1<Is>(C), ...); }
template <int I> DI void o_store1(__amdgpu_buffer_rsrc_t srd, unsigned vo, float il0, float il1) {
  constexpr int qb = I >> 3, db = (I >> 1) & 3, s = I & 1;
  const u32x4 w = acc_pack8<AO_(db, qb) + 8 * s>(qb ? il1 : il0);
  __builtin_amdgcn_raw_buffer_store_b128(w, srd, vo + qb * (256u * 1024u) + (2 * db + s) * 1024, 0, WT);
}
template <int... Is> DI void o_store(__amdgpu_buffer_rsrc_t srd, unsigned vo, float il0, float il1, iseq<Is...>) { (o_store1<Is>(srd, vo, il0, il1), ...); }

#define Q_NS 6
#define Q_PDT 2
#define Q_FD 1
#define Q_BP 1
using QCfg = GCfg<32, 4, 64, 8, false, 0, Q_NS, Q_PDT, Q_FD, Q_BP, true>;
constexpr int SHM_ATTN = 0x10000 + 6 * 16384;

#define ST(i)
__global__ __launch_bounds__(256, 1) void k_attn(const u16* __restrict__ H1f, const u16* __restrict__ Wq, const u16* __restrict__ Kc, const u16* __restrict__ Vc, u16* __restrict__ Of,
                                                        const u16* __restrict__ Wo, const u16* __restrict__ Wg, const u16* __restrict__ Wu, const u16* __restrict__ Wd, const u16* __restrict__ Wm) {
  ST(0); KST_BEGIN(2);
  extern __shared__ __attribute__((aligned(16))) char lds[];
  const unsigned tid = threadIdx.x, wid = rfl(tid >> 6);
  const int bid = blockIdx.x, xcd = bid & 7, j = bid >> 3;
  const int head = 4 * xcd + (j & 3), y = j >> 2, kvh = head >> 3;
  const unsigned ldsb = (unsigned)(size_t)lds;
  {
    const unsigned lane = tid & 63u;
    GState<QCfg> g;
    const int mb0 = y * 8 + wid * 2;
    constexpr unsigned mbstride = 1024u * 32u * 2u;
    g.srdA = mk_srd((const char*)H1f + (size_t)mb0 * mbstride, 2 * mbstride);
    g.voffA[0] = lane * 16u; g.voffA[1] = lane * 16u + mbstride;
    constexpr unsigned ldb = 4096u * 2u;
    g.srdB = mk_srd((const char*)Wq + (size_t)head * 256, 1024u * ldb - (unsigned)head * 256u);
    g_setup<QCfg, true>(g, ldsb + QGEMM_LDS, lane, wid, ldb);
    { const unsigned gw = blockIdx.x * 4u + wid;
      g.tch[0] = (unsigned long long)(size_t)((const char*)Wq + (size_t)gw * 4096); g.tch[1] = g.tch[0] + (4096u * 1024u);
      g.tch[2] = (unsigned long long)(size_t)((gw < 512u ? (const char*)Kc : (const char*)Vc) + (size_t)(gw & 511u) * 4096);
      g.tch_off = lane * 64u; }
    g.srdK = mk_srd((const char*)Kc + (size_t)kvh * (2048u * 256u), 2048u * 256u);
    g.srdV = mk_srd((const char*)Vc + (size_t)kvh * (2048u * 256u), 2048u * 256u);
    g.kvm0 = rfl(ldsb + KRING + 4096u * wid); g.kvso = rfl(KRS * 16u * wid);
#pragma unroll
    for (int par = 0; par < 2; ++par) g.vk[par] = KRS * (lane >> 4) + 16u * ((lane & 15u) ^ (4u * par + (lane >> 4)));
    g.vv = KRS * ((lane >> 2) & 7u) + 64u * (lane >> 5) + 16u * (lane & 3u);
    gemm_run<QCfg>(g, 16);
  }
  ST(1);
  unsigned lane; asm volatile("v_mbcnt_lo_u32_b32 %0, -1, 0\n\tv_mbcnt_hi_u32_b32 %0, -1, %0" : "=v"(lane));
  const unsigned l32 = lane & 31u, hi = lane >> 5;
  constexpr int T = 32;
  W4C c;
#pragma unroll
  for (int jj = 0; jj < 4; ++jj) c.vkb[jj] = ldsb + KRING + 256u * l32 + 16u * ((2u * jj + hi) ^ (l32 & 7u));
  c.vvb = ldsb + VRING + (((lane & 3u) << 3) | (((lane >> 2) & 3u) << 6) | (((lane >> 4) & 1u) << 5) | (hi << 8));
#pragma unroll
  for (int par = 0; par < 2; ++par) c.vksrc[par] = KRS * (lane >> 4) + 16u * ((lane & 15u) ^ (4u * par + (lane >> 4)));
#pragma unroll
  for (int q = 0; q < 4; ++q) c.vvsrc[q] = KRS * ((lane >> 2) & 7u) + 64u * hi + 16u * (lane & 3u) + KRS * 8u * (q >> 1) + 128u * (q & 1) + (3072u - 1024u * q);
  c.m0k = rfl(ldsb + KRING + 4096u * wid); c.m0v = rfl(ldsb + VRING + 4096u * wid);
  c.sofk = rfl(KRS * 16u * wid); c.sofv = rfl(KRS * 16u * wid) - 3072u;
  c.srdK = mk_srd((const char*)Kc + (size_t)kvh * (2048u * 256u), 2048u * 256u);
  c.srdV = mk_srd((const char*)Vc + (size_t)kvh * (2048u * 256u), 2048u * 256u);
  {
    const unsigned gw = blockIdx.x * 4u + wid;
    const char* p;
    const unsigned c2 = gw + 1024u, c2c = c2 < 1792u ? c2 : gw;
    if (lane == 0) p = (const char*)Wo + (size_t)gw * 4096; else if (lane == 1) p = (const char*)Wo + (size_t)c2 * 4096;
    else if (lane == 2) p = (const char*)Wg + (size_t)gw * 4096; else if (lane == 3) p = (const char*)Wg + (size_t)c2c * 4096;
    else if (lane == 4) p = (const char*)Wu + (size_t)gw * 4096; else if (lane == 5) p = (const char*)Wu + (size_t)c2c * 4096;
    else if (lane == 6) p = (const char*)Wd + (size_t)gw * 4096; else if (lane == 7) p = (const char*)Wd + (size_t)c2c * 4096;
    else if (lane < 11) p = (const char*)Wm + (size_t)(gw + 1024u * (lane - 8u)) * 4096;
    else p = (const char*)Wo + (size_t)gw * 4096;
    c.pf_lo = (unsigned)(size_t)p; c.pf_hi = (unsigned)((size_t)p >> 32);
    c.pf_off = lane * 64u; c.pf_m0 = rfl(ldsb + QGEMM_LDS + 256u * wid);
  }
  asm volatile("s_barrier" ::: "memory");
  k_reads0(c, make_iseq<16>{});
  q_conv(0.12751743082f, make_iseq<16>{});
  asm volatile("s_waitcnt lgkmcnt(0)\n\ts_nop 1" ::: "memory");
  c.mhat[0] = c.mhat[1] = 0.f; c.fct[0] = c.fct[1] = 1.f; c.pend = false; c.rs[0][0] = c.rs[0][1] = c.rs[1][0] = c.rs[1][1] = 0.f;
  ST(2);
  phaseA<0, true>(c, 0);  phaseB<0, true, false>(c, 0);
  phaseA<1, false>(c, 1); phaseB<1, false, true>(c, 1);
  for (int t = 2; t < T; t += 2) {
    phaseA<0, false>(c, t);     phaseB<0, false, false>(c, t);
    phaseA<1, false>(c, t + 1); phaseB<1, false, false>(c, t + 1);
  }
  ST(3);
  float lsum[2];
  {
    tr_reads(c, c.vvb + 0x4000u, make_iseq<32>{});
    op_add<1, 0, eb_lo(31)>(c); if constexpr (eb_lo(31) + 1 < 32) op_add<1, 0, eb_lo(31) + 1>(c);
    drain_exp(c, make_iseq<32>{});
    drain_cvt0(c, make_iseq<16>{});
    drain_cvt1(c, make_iseq<16>{});
    GAP(); asm volatile("s_nop 1"); GAP();
    drain_add(c, make_iseq<32>{});
    asm volatile("s_waitcnt lgkmcnt(0)" : W4_VLO :: "memory"); asm volatile("" : W4_VUP :: "memory");
    GAP();
#pragma unroll
    for (int ks = 0; ks < 4; ++ks)
#pragma unroll
      for (int db = 0; db < 4; ++db) c.Vf[db][ks] = __builtin_shufflevector(c.vlo[db][ks], c.vup[db][ks], 0, 1, 2, 3, 4, 5, 6, 7);
    asm volatile("s_nop 1" ::: "memory");
    pv_all(c, make_iseq<32>{});
#pragma unroll
    for (int qb = 0; qb < 2; ++qb) { const float rs = c.rs[qb][0] + c.rs[qb][1];
      auto pr = __builtin_amdgcn_permlane32_swap(__float_as_uint(rs), __float_as_uint(rs), false, false);
      lsum[qb] = __uint_as_float(pr[0]) + __uint_as_float(pr[1]); }
    asm volatile("s_nop 7\n\ts_nop 7" ::: "memory");
  }
  {
    unsigned lb; asm volatile("v_mbcnt_lo_u32_b32 %0, -1, 0\n\tv_mbcnt_hi_u32_b32 %0, -1, %0" : "=v"(lb));
    const float il0 = __builtin_amdgcn_rcpf(lsum[0]), il1 = __builtin_amdgcn_rcpf(lsum[1]);
    o_store(mk_srd(Of, 16u << 20), ((unsigned)(y * 8 + wid * 2) * 256u + (unsigned)head * 8u) * 1024u + lb * 16u, il0, il1, make_iseq<16>{});
  }
  ST(4); KST_END(2);
}
template <int N> DI void touch(const char* p, int stride) {
  unsigned d[N];
#pragma unroll
  for (int i = 0; i < N; ++i) d[i] = *(const unsigned*)(p + (size_t)i * stride);
#pragma unroll
  for (int i = 0; i < N; ++i) asm volatile("" :: "v"(d[i]));
}

template <int NT, int KL> DI void mod_body(const u16* __restrict__ c, const u16* __restrict__ wmod, float* __restrict__ dst, int cb, int kh, float* red  ) {
  const int tid = threadIdx.x, cg = tid & 7, kg = tid >> 3, wv = tid >> 6;
  constexpr int NG = NT / 8, NI = KL / NG;
  u32x4 w[NI]; u16 cv[NI];
#pragma unroll
  for (int i = 0; i < NI; ++i) { const int k = kh * KL + kg + NG * i; w[i] = *(const u32x4*)(wmod + (size_t)k * 6144 + cb * 64 + cg * 8); cv[i] = c[k]; }
  float acc[8] = {0, 0, 0, 0, 0, 0, 0, 0};
#pragma unroll
  for (int i = 0; i < NI; ++i) {
    const float xx = bf2f(cv[i]), s = rbf(xx / (1.f + __expf(-xx)));
#pragma unroll
    for (int jj = 0; jj < 4; ++jj) { acc[2 * jj] += s * __uint_as_float(w[i][jj] << 16); acc[2 * jj + 1] += s * __uint_as_float(w[i][jj] & 0xffff0000u); }
  }
#pragma unroll
  for (int jj = 0; jj < 8; ++jj) { float a = acc[jj]; a += __shfl_xor(a, 8); a += __shfl_xor(a, 16); a += __shfl_xor(a, 32); acc[jj] = a; }
  if ((tid & 63) < 8) {
#pragma unroll
    for (int jj = 0; jj < 8; ++jj) red[wv * 64 + cg * 8 + jj] = acc[jj];
  }
  __syncthreads();
  if (tid < 64) { float s = 0;
#pragma unroll
    for (int g = 0; g < NT / 64; ++g) s += red[g * 64 + tid];
    dst[tid] = s; }
}
__global__ __launch_bounds__(512) void k_pre(const u16* __restrict__ c, const u16* __restrict__ wmod, float* __restrict__ modA, const u16* __restrict__ x, unsigned* __restrict__ flg) {
  __shared__ float red[8 * 64];
  const int b = blockIdx.x;
  KST_BEGIN(0);
  unsigned dx = 0;
  if (threadIdx.x == 0) flg[b * 64] = 0u;
  if (threadIdx.x < 256) dx = *(const unsigned*)((const char*)x + (size_t)b * 16384 + threadIdx.x * 64);
  mod_body<512, 128>(c, wmod, modA + (b & 7) * 2048 + (b >> 3) * 64, b >> 3, b & 7, red);
  asm volatile("" :: "v"(dx));
  KST_END(0);
}
DI f32x4 modv(const float* __restrict__ modp, int n) { const f32x4 a = *(const f32x4*)(modp + n), b = *(const f32x4*)(modp + 6144 + n); f32x4 o; for (int e = 0; e < 4; ++e) o[e] = rbf(a[e] + b[e]); return o; }
DI void unpack8(const u32x4& w, float* v) {
#pragma unroll
  for (int j = 0; j < 4; ++j) { v[2 * j] = __uint_as_float(w[j] << 16); v[2 * j + 1] = __uint_as_float(w[j] & 0xffff0000u); }
}
DI u32x4 pack8(const float* o) { return (u32x4){cvtpk(o[0], o[1]), cvtpk(o[2], o[3]), cvtpk(o[4], o[5]), cvtpk(o[6], o[7])}; }

DI void rbf2(float& a, float& b) { const unsigned w = cvtpk(a, b); a = __uint_as_float(w << 16); b = __uint_as_float(w & 0xffff0000u); }
DI void rbf8(float* v) { rbf2(v[0], v[1]); rbf2(v[2], v[3]); rbf2(v[4], v[5]); rbf2(v[6], v[7]); }
DI void modv8(const float* __restrict__ modp, int n, float* o) {
  const f32x4 a0 = *(const f32x4*)(modp + n), b0 = *(const f32x4*)(modp + 6144 + n), a1 = *(const f32x4*)(modp + n + 8), b1 = *(const f32x4*)(modp + 6144 + n + 8);
#pragma unroll
  for (int e = 0; e < 4; ++e) { o[e] = a0[e] + b0[e]; o[4 + e] = a1[e] + b1[e]; }
  rbf8(o);
}
template <int MODE>
__global__ __launch_bounds__(1024) void k_ln(const u16* __restrict__ x, const u16* __restrict__ Pf, const float* __restrict__ modp, u16* __restrict__ xf, u16* __restrict__ hf,
                                             const u16* __restrict__ w0, const u16* __restrict__ w1, const u16* __restrict__ w2) {
  __shared__ float red[2][16][8];
  __shared__ __attribute__((aligned(16))) float msum[MODE == 0 ? 2048 : 4];
  const int tid = threadIdx.x;
  KST_BEGIN(MODE == 0 ? 1 : 4);
  const int r = tid & 7, c = tid >> 3, h = c & 1, wid = tid >> 6;
  const int row = blockIdx.x * 8 + r, mb = row >> 5, ml = row & 31;
  const size_t fo = ((size_t)mb * 64 + (c >> 1)) * 512 + (h * 32 + ml) * 8;
  const int n = 16 * (c >> 1) + 4 * h;
  float v[8], sh[8], sc[8];
  if (MODE == 0) {
    const u32x4 xw = *(const u32x4*)(x + (size_t)row * 1024 + 8 * c);
    { f32x2 a = {0.f, 0.f};
#pragma unroll
      for (int ks = 0; ks < 8; ++ks) a += *(const f32x2*)(modp + ks * 2048 + 2 * tid);
      float a0 = a[0], a1 = a[1]; rbf2(a0, a1); *(f32x2*)(msum + 2 * tid) = (f32x2){a0, a1}; }
    const unsigned s0 = h ? xw[0] : xw[2], s1 = h ? xw[1] : xw[3];
    const unsigned g0 = __shfl_xor(s0, 8), g1 = __shfl_xor(s1, 8);
    const u32x4 f = h ? (u32x4){g0, g1, xw[2], xw[3]} : (u32x4){xw[0], xw[1], g0, g1};
    __builtin_amdgcn_raw_buffer_store_b128(f, mk_srd(xf, 4u << 20), (unsigned)fo * 2u, 0, WT);
    unpack8(f, v);
  } else {
    const u32x4 xw = *(const u32x4*)(xf + fo), p0 = *(const u32x4*)(Pf + fo), p1 = *(const u32x4*)(Pf + fo + (size_t)2048 * 1024);
    float ga[8], a[8], b[8];
    modv8(modp, 3072 + n, sh); modv8(modp, 4096 + n, sc); modv8(modp, 2048 + n, ga);
    unpack8(xw, v); unpack8(p0, a); unpack8(p1, b);
#pragma unroll
    for (int j = 0; j < 8; ++j) a[j] += b[j];
    rbf8(a);
#pragma unroll
    for (int j = 0; j < 8; ++j) a[j] *= ga[j];
    rbf8(a);
#pragma unroll
    for (int j = 0; j < 8; ++j) v[j] += a[j];
    const u32x4 w = pack8(v);
    __builtin_amdgcn_raw_buffer_store_b128(w, mk_srd(xf, 4u << 20), (unsigned)fo * 2u, 0, WT);
    unpack8(w, v);
  }
  float s = ((v[0] + v[1]) + (v[2] + v[3])) + ((v[4] + v[5]) + (v[6] + v[7]));
  s += __shfl_xor(s, 8); s += __shfl_xor(s, 16); s += __shfl_xor(s, 32);
  if ((tid & 63) < 8) red[0][wid][r] = s;
  __syncthreads();
  if (MODE == 0) {
    const f32x4 a0 = *(const f32x4*)(msum + n), a1 = *(const f32x4*)(msum + n + 8), b0 = *(const f32x4*)(msum + 1024 + n), b1 = *(const f32x4*)(msum + 1024 + n + 8);
#pragma unroll
    for (int e = 0; e < 4; ++e) { sh[e] = a0[e]; sh[4 + e] = a1[e]; sc[e] = b0[e]; sc[4 + e] = b1[e]; }
  }
  float mean = 0;
#pragma unroll
  for (int w = 0; w < 16; ++w) mean += red[0][w][r];
  mean *= (1.f / 1024.f);
  float qv = 0;
#pragma unroll
  for (int j = 0; j < 8; ++j) { const float d = v[j] - mean; qv += d * d; }
  qv += __shfl_xor(qv, 8); qv += __shfl_xor(qv, 16); qv += __shfl_xor(qv, 32);
  if ((tid & 63) < 8) red[1][wid][r] = qv;
  __syncthreads();
  float var = 0;
#pragma unroll
  for (int w = 0; w < 16; ++w) var += red[1][w][r];
  var = rbf(var * (1.f / 1024.f));
  const float mb16 = rbf(mean), rsd = 1.f / rbf(sqrtf(rbf(var + 1e-5f)));
#pragma unroll
  for (int j = 0; j < 8; ++j) { v[j] -= mb16; sc[j] += 1.f; }
  rbf8(v); rbf8(sc);
#pragma unroll
  for (int j = 0; j < 8; ++j) v[j] *= rsd;
  rbf8(v);
#pragma unroll
  for (int j = 0; j < 8; ++j) v[j] *= sc[j];
  rbf8(v);
#pragma unroll
  for (int j = 0; j < 8; ++j) v[j] += sh[j];
  __builtin_amdgcn_raw_buffer_store_b128(pack8(v), mk_srd(hf, 4u << 20), (unsigned)fo * 2u, 0, WT);
  if (MODE == 0) {
    const unsigned gw = blockIdx.x * 16u + (unsigned)__builtin_amdgcn_readfirstlane(wid);
    if (gw < 3072u) {
      const char* p = gw < 2048u ? (const char*)w0 + (size_t)gw * 4096 : (gw < 2560u ? (const char*)w1 + (size_t)(gw - 2048u) * 4096 : (const char*)w2 + (size_t)(gw - 2560u) * 4096);
      const unsigned long long pb = ((unsigned long long)rfl((unsigned)((size_t)p >> 32)) << 32) | rfl((unsigned)(size_t)p);
      asm volatile("s_nop 4\n\ts_mov_b32 m0, %0\n\ts_nop 0\n\tglobal_load_lds_dword %1, %2" :: "s"(rfl((unsigned)(size_t)msum)), "v"((unsigned)(tid & 63) * 64u), "s"(pb) : "memory");
    }
  }
  KST_END(MODE == 0 ? 1 : 4);
}
#define WTO 0
#define SK_PDT 4
#define SK_NS 5
#define SK_FD 2
#define SK_BP 1
using SCfg = GCfg<32, 4, 32, 8, false, 0, SK_NS, SK_PDT, SK_FD, SK_BP>;
template <int K, bool FIN, int NT0>
__global__ __launch_bounds__(256, 1) void k_gemm_sk(const u16* __restrict__ Af, const u16* __restrict__ B, u16* __restrict__ Pf,
                                                    const u16* __restrict__ cvec, const u16* __restrict__ wmod, float* __restrict__ modp,
                                                    const u16* __restrict__ x1f, u16* __restrict__ out, unsigned* __restrict__ flg) {
  using C = SCfg;
  extern __shared__ __attribute__((aligned(16))) char lds[];
  KST_BEGIN(K == 4096 ? 3 : 6);
  if (K == 4096 && blockIdx.x >= 256) { const int b = blockIdx.x - 256; mod_body<256, 512>(cvec, wmod, modp + (b & 1) * 6144 + (32 + (b >> 1)) * 64, 32 + (b >> 1), b & 1, (float*)lds); KST_END(3); return; }
  constexpr int N = 1024;
  const unsigned tid = threadIdx.x, lane = tid & 63u, wid = rfl(tid >> 6);
  const int bid = blockIdx.x, xcd = bid & 7, j = bid >> 3;
  const int kh = xcd & 1, mg = xcd >> 1;
  const int mt = mg * 4 + (j & 3), nt = j >> 2;
  const int k0 = kh ? NT0 * 64 : 0, klen = kh ? K - NT0 * 64 : NT0 * 64;
  GState<C> g;
  const int mb0 = mt * 4 + wid;
  constexpr unsigned mbstride = (unsigned)K * 64u;
  g.srdA = mk_srd((const char*)Af + (size_t)mb0 * mbstride + (size_t)k0 * 64, (unsigned)klen * 64u);
  g.voffA[0] = lane * 16u;
  constexpr unsigned ldb = (unsigned)N * 2u;
  g.srdB = mk_srd((const char*)B + (size_t)k0 * ldb + (size_t)nt * 256, (unsigned)klen * ldb - (unsigned)nt * 256u);
  g_setup<C>(g, (unsigned)(size_t)lds, lane, wid, ldb);
  const unsigned fo = (((unsigned)mb0 * 64u + (unsigned)nt * 8u) * 512u + lane * 8u) * 2u;
  u32x4 xw[8]; unsigned gbp[8][4];
  if constexpr (FIN) {
    if (kh == 1) {
#pragma unroll
      for (int f = 0; f < 8; ++f) { xw[f] = *(const u32x4*)((const char*)x1f + fo + f * 1024); float gb[8]; modv8(modp, 5120 + nt * 128 + 16 * f + 4 * (lane >> 5), gb);
        const u32x4 w = pack8(gb); gbp[f][0] = w[0]; gbp[f][1] = w[1]; gbp[f][2] = w[2]; gbp[f][3] = w[3]; }
    }
  }
  gemm_run<C>(g, klen / 64);
  if constexpr (!FIN) {
#pragma unroll
    for (int n = 0; n < 4; ++n)
#pragma unroll
      for (int s = 0; s < 2; ++s) {
        u32x4 w = {cvtpk(g.acc[n][0][8 * s], g.acc[n][0][8 * s + 1]), cvtpk(g.acc[n][0][8 * s + 2], g.acc[n][0][8 * s + 3]), cvtpk(g.acc[n][0][8 * s + 4], g.acc[n][0][8 * s + 5]), cvtpk(g.acc[n][0][8 * s + 6], g.acc[n][0][8 * s + 7])};
        __builtin_amdgcn_raw_buffer_store_b128(w, mk_srd(Pf, 8u << 20), (unsigned)kh * (4u << 20) + fo + (2 * n + s) * 1024, 0, WT);
      }
  } else {
    const __amdgpu_buffer_rsrc_t srdP = mk_srd(Pf, 4u << 20);
    unsigned* flag = flg + (128 + mt * 8 + nt) * 64;
    if (kh == 0) {
#pragma unroll
      for (int n = 0; n < 4; ++n)
#pragma unroll
        for (int s = 0; s < 2; ++s) {
          u32x4 w = {cvtpk(g.acc[n][0][8 * s], g.acc[n][0][8 * s + 1]), cvtpk(g.acc[n][0][8 * s + 2], g.acc[n][0][8 * s + 3]), cvtpk(g.acc[n][0][8 * s + 4], g.acc[n][0][8 * s + 5]), cvtpk(g.acc[n][0][8 * s + 6], g.acc[n][0][8 * s + 7])};
          __builtin_amdgcn_raw_buffer_store_b128(w, srdP, fo + (2 * n + s) * 1024, 0, 16);
        }
      asm volatile("s_waitcnt vmcnt(0)" ::: "memory");
      __syncthreads();
      if (tid == 0) __hip_atomic_store(flag, 1u, __ATOMIC_RELAXED, __HIP_MEMORY_SCOPE_AGENT);
    } else {
      const unsigned h = lane >> 5;
      if (wid == 0) {
        for (unsigned spins = 0; spins < (1u << 22); ++spins) {
          if ((unsigned)__builtin_amdgcn_readfirstlane(__hip_atomic_load(flag, __ATOMIC_RELAXED, __HIP_MEMORY_SCOPE_AGENT)) != 0u) break;
          __builtin_amdgcn_s_sleep(1);
        }
      }
      asm volatile("" ::: "memory"); __builtin_amdgcn_s_barrier(); asm volatile("" ::: "memory");
      u32x4 pw[8];
#pragma unroll
      for (int f = 0; f < 8; ++f) pw[f] = __builtin_amdgcn_raw_buffer_load_b128(srdP, fo + f * 1024, 0, 16);
      const unsigned oo = (((unsigned)mb0 * 32u + (lane & 31u)) * 1024u + (unsigned)nt * 128u + 8u * h) * 2u;
      const __amdgpu_buffer_rsrc_t srdOut = mk_srd(out, 4u << 20);
#pragma unroll
      for (int f = 0; f < 8; ++f) {
        float v[8], a[8], gb[8];
        unpack8(xw[f], v); unpack8(pw[f], a); unpack8((u32x4){gbp[f][0], gbp[f][1], gbp[f][2], gbp[f][3]}, gb);
#pragma unroll
        for (int jj = 0; jj < 8; ++jj) a[jj] += g.acc[f >> 1][0][8 * (f & 1) + jj];
        rbf8(a);
#pragma unroll
        for (int jj = 0; jj < 8; ++jj) a[jj] *= gb[jj];
        rbf8(a);
#pragma unroll
        for (int jj = 0; jj < 8; ++jj) v[jj] += a[jj];
        const u32x4 w = pack8(v);
        auto r0 = __builtin_amdgcn_permlane32_swap(w[0], w[2], false, false); auto r1 = __builtin_amdgcn_permlane32_swap(w[1], w[3], false, false);
        __builtin_amdgcn_raw_buffer_store_b128((u32x4){r0[0], r1[0], r0[1], r1[1]}, srdOut, oo + 32u * f, 0, WTO);
      }
    }
  }
  KST_END(K == 4096 ? 3 : 6);
}
#define SK2_NT0 28
DI float silu_mul(float g, float u) { return g * __builtin_amdgcn_rcpf(1.f + __builtin_amdgcn_exp2f(g * -1.4426950408889634f)) * u; }
#define U_NS 3
#define U_PDT 2
#define U_FD 1
#define U_BP 2
using UCfg = GCfg<32, 8, 32, 14, true, 0, U_NS, U_PDT, U_FD, U_BP>;
__global__ __launch_bounds__(512, 2) void k_gemm_gu(const u16* __restrict__ Af, const u16* __restrict__ Wg, const u16* __restrict__ Wu, u16* __restrict__ Act) {
  using C = UCfg;
  extern __shared__ __attribute__((aligned(16))) char lds[];
  constexpr int K = 1024, F = 3584;
  KST_BEGIN(5);
  const unsigned tid = threadIdx.x, lane = tid & 63u, wid = rfl(tid >> 6);
  const int bid = blockIdx.x, xcd = bid & 7, j = bid >> 3;
  const int mt = (xcd & 1) * 4 + (j & 3), nt = (xcd >> 1) * 8 + (j >> 2);
  GState<C> g;
  const int mb0 = mt * 8 + wid;
  constexpr unsigned mbstride = (unsigned)K * 64u;
  g.srdA = mk_srd((const char*)Af + (size_t)mb0 * mbstride, mbstride);
  g.voffA[0] = lane * 16u;
  constexpr unsigned ldb = F * 2u;
  const bool up = wid >= 4;
  g.srdB = mk_srd((const char*)(up ? Wu : Wg) + (size_t)nt * 224, (unsigned)K * ldb - (unsigned)nt * 224u);
  g_setup<C>(g, (unsigned)(size_t)lds, lane, wid, ldb);
  gemm_run<C>(g, K / 64);
  const __amdgpu_buffer_rsrc_t srdO = mk_srd(Act, 2048u * F * 2u);
#pragma unroll
  for (int n = 0; n < 7; ++n) {
    float o[8];
#pragma unroll
    for (int e = 0; e < 8; ++e) o[e] = silu_mul(g.acc[n][0][e], g.acc[n][0][e + 8]);
    u32x4 w = {cvtpk(o[0], o[1]), cvtpk(o[2], o[3]), cvtpk(o[4], o[5]), cvtpk(o[6], o[7])};
    __builtin_amdgcn_raw_buffer_store_b128(w, srdO, (((unsigned)mb0 * (F >> 4) + (unsigned)(nt * 7 + n)) * 512u + lane * 8u) * 2u, 0, WT);
  }
  KST_END(5);
}

extern "C" void kernel_launch(void* const* d_in, const int* in_sizes, int n_in, void* d_out, int out_size, void* d_ws, size_t ws_size, hipStream_t stream) {
  const u16* x = (const u16*)d_in[0]; const u16* kc = (const u16*)d_in[1]; const u16* vc = (const u16*)d_in[2];
  const u16* wq = (const u16*)d_in[3]; const u16* wo = (const u16*)d_in[4];
  const u16* wg = (const u16*)d_in[6]; const u16* wu = (const u16*)d_in[7]; const u16* wd = (const u16*)d_in[8];
  const u16* c = (const u16*)d_in[9]; const u16* wmod = (const u16*)d_in[10];
  u16* out = (u16*)d_out;
  char* ws = (char*)d_ws;
  float* mod = (float*)ws;
  u16* hf = (u16*)(ws + (1 << 20));
  u16* x1f = (u16*)(ws + (8 << 20));
  u16* of = (u16*)(ws + (16 << 20));
  u16* P = (u16*)(ws + (32 << 20));
  u16* act = (u16*)(ws + (64 << 20));
  hipFuncSetAttribute((const void*)k_attn, hipFuncAttributeMaxDynamicSharedMemorySize, SHM_ATTN);
  hipFuncSetAttribute((const void*)k_gemm_sk<4096, false, 32>, hipFuncAttributeMaxDynamicSharedMemorySize, SCfg::LDSB);
  hipFuncSetAttribute((const void*)k_gemm_sk<3584, true, SK2_NT0>, hipFuncAttributeMaxDynamicSharedMemorySize, SCfg::LDSB);
  hipFuncSetAttribute((const void*)k_gemm_gu, hipFuncAttributeMaxDynamicSharedMemorySize, UCfg::LDSB);
  float* modA = (float*)(ws + (256 << 10));
  unsigned* flg = (unsigned*)(ws + (512 << 10));
  u16* P2 = (u16*)(ws + (48 << 20));
  k_pre<<<256, 512, 0, stream>>>(c, wmod, modA, x, flg);
  k_ln<0><<<256, 1024, 0, stream>>>(x, nullptr, modA, x1f, hf, wq, kc, vc);
  k_attn<<<256, 256, SHM_ATTN, stream>>>(hf, wq, kc, vc, of, wo, wg, wu, wd, wmod);
  k_gemm_sk<4096, false, 32><<<256 + 128, 256, SCfg::LDSB, stream>>>(of, wo, P, c, wmod, mod, nullptr, nullptr, nullptr);
  k_ln<1><<<256, 1024, 0, stream>>>(x, P, mod, x1f, hf, nullptr, nullptr, nullptr);
  k_gemm_gu<<<256, 512, UCfg::LDSB, stream>>>(hf, wg, wu, act);
  k_gemm_sk<3584, true, SK2_NT0><<<256, 256, SCfg::LDSB, stream>>>(act, wd, P2, nullptr, nullptr, mod, x1f, out, flg);
}
```

```cpp
#include <hip/hip_runtime.h>
#include <utility>
#include <type_traits>
typedef unsigned short u16;
typedef short bf16x8 __attribute__((ext_vector_type(8)));
typedef short s16x4 __attribute__((ext_vector_type(4)));
typedef float f32x4 __attribute__((ext_vector_type(4)));
typedef float f32x16 __attribute__((ext_vector_type(16)));
typedef unsigned u32x4 __attribute__((ext_vector_type(4)));
typedef unsigned u32x2 __attribute__((ext_vector_type(2)));
typedef float f32x2 __attribute__((ext_vector_type(2)));
typedef __bf16 bf16x2_t __attribute__((ext_vector_type(2)));
#define DI __device__ __forceinline__
#define LAS __attribute__((address_space(3)))
#define GAP() __builtin_amdgcn_sched_barrier(0)
template <int... Is> using iseq = std::integer_sequence<int, Is...>;
template <int N> using make_iseq = std::make_integer_sequence<int, N>;

#define KST_BEGIN(k)
#define KST_END(k)
DI unsigned rfl(unsigned x) { return __builtin_amdgcn_readfirstlane(x); }
DI float bf2f(u16 v) { return __uint_as_float(((unsigned)v) << 16); }
DI unsigned cvtpk(float lo, float hi) { f32x2 v = {lo, hi}; bf16x2_t b = __builtin_convertvector(v, bf16x2_t); return __builtin_bit_cast(unsigned, b); }
DI float rbf(float f);
DI float rbf(float f) { return __uint_as_float(cvtpk(f, 0.f) << 16); }
#define KV_FIRST 12
#define WT 16
DI __amdgpu_buffer_rsrc_t mk_srd(const void* p, unsigned bytes) { return __builtin_amdgcn_make_buffer_rsrc((void*)p, (short)0, (int)bytes, 0x00020000); }

#define W4_ACLOB "a0","a1","a2","a3","a4","a5","a6","a7","a8","a9","a10","a11","a12","a13","a14","a15","a16","a17","a18","a19","a20","a21","a22","a23","a24","a25","a26","a27","a28","a29","a30","a31","a32","a33","a34","a35","a36","a37","a38","a39","a40","a41","a42","a43","a44","a45","a46","a47","a48","a49","a50","a51","a52","a53","a54","a55","a56","a57","a58","a59","a60","a61","a62","a63","a64","a65","a66","a67","a68","a69","a70","a71","a72","a73","a74","a75","a76","a77","a78","a79","a80","a81","a82","a83","a84","a85","a86","a87","a88","a89","a90","a91","a92","a93","a94","a95","a96","a97","a98","a99","a100","a101","a102","a103","a104","a105","a106","a107","a108","a109","a110","a111","a112","a113","a114","a115","a116","a117","a118","a119","a120","a121","a122","a123","a124","a125","a126","a127"
#define W4_ACLOB_HI "a128","a129","a130","a131","a132","a133","a134","a135","a136","a137","a138","a139","a140","a141","a142","a143","a144","a145","a146","a147","a148","a149","a150","a151","a152","a153","a154","a155","a156","a157","a158","a159","a160","a161","a162","a163","a164","a165","a166","a167","a168","a169","a170","a171","a172","a173","a174","a175","a176","a177","a178","a179","a180","a181","a182","a183","a184","a185","a186","a187","a188","a189","a190","a191","a192","a193","a194","a195","a196","a197","a198","a199","a200","a201","a202","a203","a204","a205","a206","a207","a208","a209","a210","a211","a212","a213","a214","a215","a216","a217","a218","a219","a220","a221","a222","a223","a224","a225","a226","a227","a228","a229","a230","a231","a232","a233","a234","a235","a236","a237","a238","a239","a240","a241","a242","a243","a244","a245","a246","a247","a248","a249","a250","a251","a252","a253","a254","a255"
template <int SH_, int NW_, int WR_, int NB16_, bool GU_, int KORD_, int NS_, int PDT_, int FD_, int BARPOS_, bool ASMACC_ = false>
struct GCfg {
  static constexpr bool ASMACC = ASMACC_;
  static constexpr int SH = SH_, NW = NW_, WR = WR_, NB16 = NB16_, KORD = KORD_, NS = NS_, PDT = PDT_, FD = FD_, BARPOS = BARPOS_;
  static constexpr bool GU = GU_;
  static constexpr int STEPS = SH == 32 ? 4 : 2;
  static constexpr int MR = WR / SH;
  static constexpr int NB = SH == 32 ? NB16 / 2 : NB16;
  static constexpr int NR16 = GU ? NB16 / 2 : NB16;
  static constexpr int PIECES = NB16 * 2;
  static constexpr int PB = (PIECES + NW - 1) / NW;
  static constexpr int STAGE = PB * NW * 1024;
  static constexpr int REGION = STAGE / 2;
  static constexpr int LDSB = NS * STAGE;
  static constexpr int NOPS = STEPS * MR + PB;
  static constexpr int GS = NB * MR;
  static constexpr int G = STEPS * GS;
  static constexpr int BSTEP = STEPS - FD;
  static constexpr int GB = BSTEP * GS + (BARPOS + 1) * MR - 1;
  static constexpr int dma_lo(int ks) { return (PB * ks) / STEPS; }
  static constexpr int step_ops(int ks) { return MR + dma_lo(ks + 1) - dma_lo(ks); }
  static constexpr int step_first(int ks) { return ks * MR + dma_lo(ks); }
  static constexpr int op_step(int j) { int ks = 0; while (ks < STEPS - 1 && j >= step_first(ks + 1)) ++ks; return ks; }
  static constexpr int op_gap(int j) { const int ks = op_step(j), i = j - step_first(ks), n = step_ops(ks); return ks * GS + (i * GS) / n; }
  static constexpr int ops_before(int g) { int c = 0; for (int j = 0; j < NOPS; ++j) if (op_gap(j) < g) ++c; return c; }
  static constexpr int NA(int ks) {
    const int ts = (ks + 1) % STEPS, jt = step_first(ts) + MR - 1;
    const int full = (ks == STEPS - 1) ? PDT - 1 : PDT;
    return (NOPS - 1 - jt) + (full - 1) * NOPS + ops_before(ks * GS);
  }
  static constexpr int NBW() { return (NS - 3) * NOPS + ops_before(GB); }
  static_assert(NS >= 3 && PDT >= 2 && FD <= STEPS - 0 && BSTEP >= 0, "");
  using acc_t = std::conditional_t<SH == 32, f32x16, f32x4>;
};

template <class C>
struct GState {
  typename C::acc_t acc[C::NB][C::MR];
  bf16x8 a[C::PDT * C::STEPS][C::MR];
  s16x4 blo[C::FD][C::NB], bhi[C::FD][C::NB];
  __amdgpu_buffer_rsrc_t srdA, srdB;
  unsigned voffA[C::MR];
  unsigned voffB[C::GU ? C::PB : 1];
  unsigned sB[C::GU ? 1 : C::PB];
  unsigned m0base, vrd, ktileB;
  unsigned long long tch[3]; unsigned tch_off;
  __amdgpu_buffer_rsrc_t srdK, srdV; unsigned kvm0, kvso, vk[2], vv;
};
struct GIter { unsigned va_cur, va_nxt, sA, sA0, m0_fill, sK_fill; };

template <class C, int SL, int OFF, int MRI> DI void a_load1(GState<C>& g, unsigned sA) {
  asm volatile("buffer_load_dwordx4 %0, %1, %2, %3 offen offset:%4" : "=v"(g.a[SL][MRI]) : "v"(g.voffA[MRI]), "s"(g.srdA), "s"(sA), "i"(OFF) : "memory");
}
template <class C, int Q> DI void b_dma1(GState<C>& g, unsigned m0v, unsigned sK) {
  if constexpr (C::GU)
    asm volatile("s_mov_b32 m0, %0\n\ts_nop 0\n\tbuffer_load_dwordx4 %1, %2, %3 offen lds" :: "s"(m0v + 1024u * Q), "v"(g.voffB[Q]), "s"(g.srdB), "s"(sK) : "memory");
  else
    asm volatile("s_mov_b32 m0, %0\n\ts_nop 0\n\tbuffer_load_dwordx4 %1, %2, %3 offen lds" :: "s"(m0v + 1024u * Q), "v"(g.voffB[0]), "s"(g.srdB), "s"(sK + g.sB[Q]) : "memory");
}
template <class C> DI void g_dummy(GState<C>& g) {
  asm volatile("s_mov_b32 m0, %0\n\ts_nop 0\n\tbuffer_load_dwordx4 %1, %2, %3 offen lds" :: "s"(g.m0base + (unsigned)(C::NS - 1) * C::STAGE), "v"(g.voffA[0]), "s"(g.srdA), "s"(0x80000000u) : "memory");
}
template <class C, int TI, int J, bool VA, bool VB> DI void g_op(GState<C>& g, const GIter& it) {
  constexpr int ks = C::op_step(J), i = J - C::step_first(ks);
  if constexpr (i < C::MR) {
    constexpr int PD = C::PDT * C::STEPS, SL = (TI * C::STEPS + ks - 1 + PD) % PD;
    if constexpr (VA) { if constexpr (ks == 0) a_load1<C, SL, 0, i>(g, it.sA0); else a_load1<C, SL, (ks - 1) * 1024, i>(g, it.sA); } else g_dummy<C>(g);
  } else {
    if constexpr (VB) b_dma1<C, C::dma_lo(ks) + i - C::MR>(g, it.m0_fill, it.sK_fill); else g_dummy<C>(g);
  }
}
template <class C, int TI, int GI, int... Js> DI void g_ops_at(GState<C>& g, const GIter& it, iseq<Js...>) {
  ((C::op_gap(Js) == GI ? g_op<C, TI, Js, true, true>(g, it) : (void)0), ...);
}
template <class C> constexpr int b_off(int KS, int NBI, int t) {
  if (C::SH == 32) {
    return ((2 * KS + t) * C::NR16 + (C::GU ? NBI : 2 * NBI)) * 256;
  } else {
    const int kb = 4 * KS + (C::KORD == 0 ? 2 * t : t);
    return (kb * C::NR16 + (C::GU ? NBI / 2 : NBI)) * 256 + ((C::GU && (NBI & 1)) ? C::REGION : 0);
  }
}
template <class C, int BUF, int KS, int NBI> DI void b_read(GState<C>& g, unsigned va) {
  g.blo[BUF][NBI] = __builtin_amdgcn_ds_read_tr16_b64_v4i16((LAS s16x4*)(size_t)(va + b_off<C>(KS, NBI, 0)));
  g.bhi[BUF][NBI] = __builtin_amdgcn_ds_read_tr16_b64_v4i16((LAS s16x4*)(size_t)(va + b_off<C>(KS, NBI, 1)));
}
template <class C, int BUF, int KS, int... Ns> DI void b_read_all(GState<C>& g, unsigned va, iseq<Ns...>) { (b_read<C, BUF, KS, Ns>(g, va), ...); }

template <class C, int N, int SL> DI void a_wait(GState<C>& g) {
  static_assert(N >= 0 && N < 64, "vmcnt range");
  if constexpr (C::MR == 1) asm volatile("s_waitcnt vmcnt(%1)" : "+v"(g.a[SL][0]) : "i"(N) : "memory");
  else if constexpr (C::MR == 2) asm volatile("s_waitcnt vmcnt(%2)" : "+v"(g.a[SL][0]), "+v"(g.a[SL][1]) : "i"(N) : "memory");
  else asm volatile("s_waitcnt vmcnt(%4)" : "+v"(g.a[SL][0]), "+v"(g.a[SL][1]), "+v"(g.a[SL][2]), "+v"(g.a[SL][3]) : "i"(N) : "memory");
}

template <class C, int TI, int GI> DI void g_gap(GState<C>& g, const GIter& it) {
  constexpr int KS = GI / C::GS, r = GI % C::GS, NBI = r / C::MR, MRI = r % C::MR;
  constexpr int SL = TI * C::STEPS + KS, BUF = (C::FD == 1) ? 0 : (KS & 1), KN = (KS + C::FD) % C::STEPS;
  if constexpr (r == 0) { a_wait<C, C::NA(KS), SL>(g); GAP(); }
  {
    const bf16x8 bf = __builtin_shufflevector(g.blo[BUF][NBI], g.bhi[BUF][NBI], 0, 1, 2, 3, 4, 5, 6, 7);
    if constexpr (C::ASMACC) { constexpr int ao = 16 * (C::MR * NBI + MRI);
      asm volatile("v_mfma_f32_32x32x16_bf16 a[%c2:%c3], %0, %1, a[%c2:%c3]" :: "v"(bf), "v"(g.a[SL][MRI]), "i"(ao), "i"(ao + 15)); }
    else if constexpr (C::SH == 32) g.acc[NBI][MRI] = __builtin_amdgcn_mfma_f32_32x32x16_bf16(bf, g.a[SL][MRI], g.acc[NBI][MRI], 0, 0, 0);
    else if constexpr (C::GU) asm volatile("v_mfma_f32_16x16x32_bf16 %0, %1, %2, %0" : "+v"(g.acc[NBI][MRI]) : "v"(bf), "v"(g.a[SL][MRI]));
    else g.acc[NBI][MRI] = __builtin_amdgcn_mfma_f32_16x16x32_bf16(bf, g.a[SL][MRI], g.acc[NBI][MRI], 0, 0, 0);
  }
  if constexpr (GI == C::GB) {
    GAP();
    static_assert(C::NBW() < 64, "");
    asm volatile("s_waitcnt vmcnt(%0) lgkmcnt(0)\n\ts_barrier" :: "i"(C::NBW()) : "memory");
    GAP();
    b_read_all<C, BUF, KN>(g, it.va_nxt, make_iseq<C::BARPOS + 1>{});
  } else if constexpr (MRI == C::MR - 1) {
    if constexpr (KS == C::BSTEP) { if constexpr (NBI > C::BARPOS) b_read<C, BUF, KN, NBI>(g, it.va_nxt); }
    else if constexpr (KS > C::BSTEP) b_read<C, BUF, KN, NBI>(g, it.va_nxt);
    else b_read<C, BUF, KN, NBI>(g, it.va_cur);
  }
  g_ops_at<C, TI, GI>(g, it, make_iseq<C::NOPS>{});
  GAP();
}
template <class C, int TI, int... Gs> DI void g_gaps(GState<C>& g, const GIter& it, iseq<Gs...>) { (g_gap<C, TI, Gs>(g, it), ...); }
template <class C, int TI> DI void g_tile(GState<C>& g, int t, unsigned& stg, unsigned& pstg) {
  unsigned nstg = stg + C::STAGE; if (nstg == C::NS * C::STAGE) nstg = 0;
  GIter it;
  it.va_cur = g.vrd + stg; it.va_nxt = g.vrd + nstg;
  it.sA = (unsigned)(C::STEPS * 1024) * (unsigned)(t + C::PDT); it.sA0 = it.sA - 1024u;
  it.m0_fill = g.m0base + pstg; it.sK_fill = g.ktileB * (unsigned)(t - 1 + C::NS);
  g_gaps<C, TI>(g, it, make_iseq<C::G>{});
  pstg = stg; stg = nstg;
}
template <class C, int... TIs> DI void g_body(GState<C>& g, int t, unsigned& stg, unsigned& pstg, iseq<TIs...>) { (g_tile<C, TIs>(g, t + TIs, stg, pstg), ...); }

template <class C, int I> DI void g_touch(GState<C>& g) {
  asm volatile("s_mov_b32 m0, %0\n\ts_nop 0\n\tglobal_load_lds_dword %1, %2" :: "s"(g.m0base + (unsigned)(C::NS - 1) * C::STAGE), "v"(g.tch_off), "s"(g.tch[I]) : "memory");
}
template <class C, int I> DI void g_kv(GState<C>& g) {
  constexpr int q = I & 3;
  if constexpr ((I >> 2) == 1) asm volatile("s_mov_b32 m0, %0\n\ts_nop 0\n\tbuffer_load_dwordx4 %1, %2, %3 offen lds" :: "s"(g.kvm0 + 0x8000u + 1024u * q), "v"(g.vv), "s"(g.srdV), "s"(g.kvso + 2048u * (q >> 1) + 128u * (q & 1)) : "memory");
  else asm volatile("s_mov_b32 m0, %0\n\ts_nop 0\n\tbuffer_load_dwordx4 %1, %2, %3 offen offset:%c4 lds" :: "s"(g.kvm0 + (I >> 3) * 0x4000u), "v"(g.vk[q & 1]), "s"(g.srdK), "s"(g.kvso + (I >> 3) * 16384u), "i"(1024 * q) : "memory");
}
template <class C> constexpr int dummy_index(int V, int J) {
  const int ks = C::op_step(J), i = J - C::step_first(ks);
  if (i >= C::MR || (C::STEPS * (V + C::PDT) + ks - 1) >= 0) return -1;
  return (V + (C::NS - 1)) * C::STEPS * C::MR + ks * C::MR + i;
}
template <class C, int V, int J> DI void g_virt_op(GState<C>& g, const GIter& it) {
  constexpr int ks = C::op_step(J);
  constexpr int TI = ((V % C::PDT) + C::PDT) % C::PDT;
  constexpr bool VA = (C::STEPS * (V + C::PDT) + ks - 1) >= 0, VB = (V - 1 + C::NS) >= 0;
  if constexpr (V == -1 && J == C::ops_before(C::GB)) asm volatile("s_waitcnt vmcnt(%0)\n\ts_barrier" :: "i"(C::NBW()) : "memory");
  constexpr int di = C::ASMACC ? dummy_index<C>(V, J) : -1;
  if constexpr (di >= 0 && di < 3) g_touch<C, di>(g);
  else if constexpr (di >= KV_FIRST && di < KV_FIRST + 12) g_kv<C, di - KV_FIRST>(g);
  else g_op<C, TI, J, VA, VB>(g, it);
}
template <class C, int V, int... Js> DI void g_virt(GState<C>& g, iseq<Js...>) {
  GIter it; constexpr int tb = V - 1 + C::NS;
  it.va_cur = it.va_nxt = 0; it.sA = (unsigned)(C::STEPS * 1024) * (unsigned)(V + C::PDT >= 0 ? V + C::PDT : 0); it.sA0 = it.sA - 1024u;
  it.m0_fill = g.m0base + (unsigned)(tb >= 0 ? tb : 0) * C::STAGE; it.sK_fill = g.ktileB * (unsigned)(tb >= 0 ? tb : 0);
  (g_virt_op<C, V, Js>(g, it), ...);
  if constexpr (V == -1 && C::ops_before(C::GB) == C::NOPS) asm volatile("s_waitcnt vmcnt(%0)\n\ts_barrier" :: "i"(C::NBW()) : "memory");
}
template <class C, int... Vs> DI void g_prologue(GState<C>& g, iseq<Vs...>) { constexpr int NV = sizeof...(Vs); (g_virt<C, Vs - NV>(g, make_iseq<C::NOPS>{}), ...); }

template <class C, bool PSWAP = false> DI void g_setup(GState<C>& g, unsigned ldsb, unsigned lane, unsigned wid, unsigned ldb) {
  g.m0base = rfl(ldsb + wid * (unsigned)C::PB * 1024u);
  g.ktileB = 64u * ldb;
  const unsigned q = (lane & 15u) >> 2, p0 = lane & 3u, p = PSWAP ? (((p0 & 1u) << 1) | (p0 >> 1)) : p0, g4 = lane >> 4;
  if constexpr (C::SH == 32) {
    const unsigned h = lane >> 5, blk = g4 & 1u;
    g.vrd = ldsb + blk * (C::GU ? (unsigned)C::REGION : 256u) + ((4u * h + q) ^ (4u * blk)) * 32u + 8u * p;
  } else {
    g.vrd = ldsb + (g4 >> 1) * (C::KORD == 0 ? 1u : 2u) * (unsigned)(C::NR16 * 256) + (4u * (g4 & 1u) + q) * 32u + 8u * p;
  }
  if constexpr (C::GU) {
    const unsigned wr = wid % (C::NW / 2), up = wid / (C::NW / 2);
#pragma unroll
    for (int qq = 0; qq < C::PB; ++qq) {
      const unsigned P = wr * C::PB + qq, s = 4u * P + g4 * 0u + (lane >> 4), kb = s / (unsigned)C::NR16, nb = s % (unsigned)C::NR16;
      const unsigned row = ((lane >> 1) & 7u) ^ ((C::SH == 32 && up) ? 4u : 0u);
      g.voffB[qq] = (P < (unsigned)(C::NR16 * 2)) ? (8u * kb + row) * ldb + 32u * nb + 16u * (lane & 1u) : 0x80000000u;
    }
  } else {
    static_assert(C::GU || (C::NR16 % 4 == 0), "");
    const unsigned sub = lane >> 4;
    const unsigned row = ((lane >> 1) & 7u) ^ ((C::SH == 32 && (sub & 1u)) ? 4u : 0u);
    g.voffB[0] = row * ldb + 32u * sub + 16u * (lane & 1u);
#pragma unroll
    for (int qq = 0; qq < C::PB; ++qq) { const unsigned P = wid * C::PB + qq, kb = P / (unsigned)(C::NR16 / 4), j = P % (unsigned)(C::NR16 / 4); g.sB[qq] = rfl(8u * kb * ldb + 128u * j); }
  }
}

template <int R> DI void acc_zero1() { asm volatile("v_accvgpr_write_b32 a[%c0], 0" :: "i"(R) : W4_ACLOB); }
template <int... Rs> DI void acc_zero(iseq<Rs...>) { (acc_zero1<Rs>(), ...); asm volatile("s_nop 1"); }
template <class C> DI void gemm_run(GState<C>& g, int NT) {
  constexpr int NV = C::PDT > C::NS - 1 ? C::PDT : C::NS - 1;
  if constexpr (!C::ASMACC) {
#pragma unroll
    for (int n = 0; n < C::NB; ++n)
#pragma unroll
      for (int m = 0; m < C::MR; ++m)
#pragma unroll
        for (int e = 0; e < (C::SH == 32 ? 16 : 4); ++e) g.acc[n][m][e] = 0.f;
  }
  g_prologue<C>(g, make_iseq<NV>{});
  if constexpr (C::ASMACC) acc_zero(make_iseq<C::NB * C::MR * 16>{});
  b_read_all<C, 0, 0>(g, g.vrd, make_iseq<C::NB>{});
  if constexpr (C::FD == 2) b_read_all<C, 1, 1>(g, g.vrd, make_iseq<C::NB>{});
  asm volatile("s_nop 1");
  GAP();
  unsigned stg = 0, pstg = (C::NS - 1) * C::STAGE;
  for (int t = 0; t < NT; t += C::PDT) g_body<C>(g, t, stg, pstg, make_iseq<C::PDT>{});
  asm volatile("s_waitcnt vmcnt(0) lgkmcnt(0)\n\ts_nop 7\n\ts_nop 7" ::: "memory");
#pragma unroll
  for (int i = 0; i < C::PDT * C::STEPS; ++i)
#pragma unroll
    for (int m = 0; m < C::MR; ++m) asm volatile("" :: "v"(g.a[i][m]));
  GAP();
}
typedef __bf16 abf16x8 __attribute__((ext_vector_type(8)));
typedef __bf16 abf16x4 __attribute__((ext_vector_type(4)));
#define ATTN_THR 64.0f
constexpr unsigned KRS = 256u, KTILEB = 64u * KRS;
#define KRING 0x0000u
#define VRING 0x8000u
#define QGEMM_LDS 0x10000u
DI float max3f(float a, float b, float c) { float r; asm("v_max3_f32 %0, %1, %2, %3" : "=v"(r) : "v"(a), "v"(b), "v"(c)); return r; }
DI float max2f(float a, float b) { float r; asm("v_max_f32_e32 %0, %1, %2" : "=v"(r) : "v"(a), "v"(b)); return r; }
DI float fadd_s(float a, float b) { float r; asm("v_add_f32_e32 %0, %1, %2" : "=v"(r) : "v"(a), "v"(b)); return r; }
DI float fmul_s(float a, float b) { float r; asm("v_mul_f32_e32 %0, %1, %2" : "=v"(r) : "v"(a), "v"(b)); return r; }
DI float fsub_s(float a, float b) { float r; asm("v_sub_f32_e32 %0, %1, %2" : "=v"(r) : "v"(a), "v"(b)); return r; }
DI float w4_aone() { unsigned l; asm volatile("v_mbcnt_lo_u32_b32 %0, -1, 0\n\tv_mbcnt_hi_u32_b32 %0, -1, %0" : "=v"(l)); return l < 32u ? 1.f : 0.f; }
DI void negm_bcast(f32x16& negm, float aone, float neg_mhat) {
  asm volatile("s_nop 1\n\tv_mfma_f32_32x32x2_f32 %0, %1, %2, 0\n\ts_nop 7\n\ts_nop 7\n\ts_nop 3" : "+v"(negm) : "v"(aone), "v"(neg_mhat));
}
struct W4C {
  unsigned vkb[4], vvb, vksrc[2], vvsrc[4], m0k, m0v, sofk, sofv;
  __amdgpu_buffer_rsrc_t srdK, srdV;
  f32x16 S[2][2][2];
  f32x16 NEGM[2];
  abf16x8 Vf[4][4];
  unsigned pk[2][4][4];
  abf16x4 vlo[4][4], vup[4][4];
  float mhat[2], fct[2]; bool pend;
  float rs[2][2];
  float mA, mB;
  unsigned sk_m0, sk_so, sv_m0, sv_so;
  unsigned pf_lo, pf_hi, pf_off, pf_m0;
};
constexpr int AO_(int db, int qb) { return 16 * (2 * db + qb); }
constexpr int AQ_(int qb, int d0) { return 128 + 32 * qb + 4 * d0; }
constexpr int AK_(int kb, int d0) { return 192 + 32 * kb + 4 * d0; }

template <int I> DI void tr_read(W4C& c, unsigned va) {
  constexpr int ks = I >> 3, db = (I >> 1) & 3;
  if constexpr ((I & 1) == 0) asm volatile("ds_read_b64_tr_b16 %0, %1 offset:%2" : "=v"(c.vlo[db][ks]) : "v"(va), "i"(4096 * ks + 512 * db) : "memory");
  else                        asm volatile("ds_read_b64_tr_b16 %0, %1 offset:%2" : "=v"(c.vup[db][ks]) : "v"(va), "i"(4096 * ks + 2048 + 512 * db) : "memory");
}
template <int I, int SLOT> DI void k_read(W4C& c) {
  constexpr int kb = I >> 3, d0 = I & 7;
  asm volatile("ds_read_b128 a[%c1:%c2], %0 offset:%c3" :: "v"(c.vkb[d0 & 3]), "i"(AK_(kb, d0)), "i"(AK_(kb, d0) + 3), "i"(SLOT * 0x4000 + 8192 * kb + 128 * (d0 >> 2)) : "memory");
}
template <int X, bool FIRST, int M> DI void qk_mfma(W4C& c) {
  constexpr int d0 = M >> 2, kb = (M >> 1) & 1, qb = M & 1, ak = AK_(kb, d0), aq = AQ_(qb, d0);
  if constexpr (d0 == 0) {
    if constexpr (FIRST) asm volatile("v_mfma_f32_32x32x16_bf16 %0, a[%c1:%c2], a[%c3:%c4], 0" : "=&v"(c.S[X][kb][qb]) : "i"(ak), "i"(ak + 3), "i"(aq), "i"(aq + 3));
    else                 asm volatile("v_mfma_f32_32x32x16_bf16 %0, a[%c2:%c3], a[%c4:%c5], %1" : "=&v"(c.S[X][kb][qb]) : "v"(c.NEGM[qb]), "i"(ak), "i"(ak + 3), "i"(aq), "i"(aq + 3));
  } else asm volatile("v_mfma_f32_32x32x16_bf16 %0, a[%c1:%c2], a[%c3:%c4], %0" : "+v"(c.S[X][kb][qb]) : "i"(ak), "i"(ak + 3), "i"(aq), "i"(aq + 3));
}
template <int M, bool ZERO> DI void pv_mfma(W4C& c) {
  constexpr int ks = M >> 3, db = (M >> 1) & 3, qb = M & 1, ao = AO_(db, qb);
  const u32x4 pw = {c.pk[qb][ks][0], c.pk[qb][ks][1], c.pk[qb][ks][2], c.pk[qb][ks][3]};
  if constexpr (ZERO && ks == 0) asm volatile("v_mfma_f32_32x32x16_bf16 a[%c2:%c3], %0, %1, 0" :: "v"(c.Vf[db][ks]), "v"(pw), "i"(ao), "i"(ao + 15));
  else                         asm volatile("v_mfma_f32_32x32x16_bf16 a[%c2:%c3], %0, %1, a[%c2:%c3]" :: "v"(c.Vf[db][ks]), "v"(pw), "i"(ao), "i"(ao + 15));
}
template <int X, int KB, int E> DI void op_exp(W4C& c) { c.S[X][KB][E >> 4][E & 15] = __builtin_amdgcn_exp2f(c.S[X][KB][E >> 4][E & 15]); }
template <int X, int KB, int E> DI void op_add(W4C& c) { asm volatile("v_add_f32_e32 %0, %0, %1" : "+v"(c.rs[E >> 4][E & 1]) : "v"(c.S[X][KB][E >> 4][E & 15])); }
template <int X, int KB, int J> DI void op_cvt(W4C& c) {
  constexpr int qb = J >> 3, ks = 2 * KB + ((J >> 2) & 1), p = J & 3, r = 2 * (J & 7);
  c.pk[qb][ks][p] = cvtpk(c.S[X][KB][qb][r], c.S[X][KB][qb][r + 1]);
  if constexpr (KB == 1) asm volatile("" : "+v"(c.pk[qb][ks][p]));
}
template <int X, int I> DI float sval(W4C& c) { return c.S[X][I >> 5][(I >> 4) & 1][I & 15]; }
template <int X, int G> DI void op_max4(W4C& c) {
  if constexpr (G == 0) asm volatile("v_max3_f32 %0, %2, %3, %4\n\tv_max3_f32 %1, %5, %6, %7\n\tv_max3_f32 %0, %0, %8, %9" : "=&v"(c.mA), "=&v"(c.mB)
                                     : "v"(sval<X, 0>(c)), "v"(sval<X, 1>(c)), "v"(sval<X, 2>(c)), "v"(sval<X, 3>(c)), "v"(sval<X, 4>(c)), "v"(sval<X, 5>(c)), "v"(sval<X, 6>(c)), "v"(sval<X, 7>(c)));
  else asm volatile("v_max3_f32 %1, %1, %2, %3\n\tv_max3_f32 %0, %0, %4, %5\n\tv_max3_f32 %1, %1, %6, %7\n\tv_max3_f32 %0, %0, %8, %9" : "+v"(c.mA), "+v"(c.mB)
                    : "v"(sval<X, 8 * G>(c)), "v"(sval<X, 8 * G + 1>(c)), "v"(sval<X, 8 * G + 2>(c)), "v"(sval<X, 8 * G + 3>(c)), "v"(sval<X, 8 * G + 4>(c)), "v"(sval<X, 8 * G + 5>(c)), "v"(sval<X, 8 * G + 6>(c)), "v"(sval<X, 8 * G + 7>(c)));
}
constexpr int EB0 = 10;
constexpr int eb_lo(int g) { return g <= EB0 ? 0 : (32 * (g - EB0)) / (32 - EB0); }
template <int X, bool FIRST, int M> DI void gapA(W4C& c, unsigned va, const int t) {
  constexpr int Xp = X ^ 1;
  qk_mfma<X, FIRST, M>(c);
  GAP();
  if constexpr (!FIRST) {
    op_exp<Xp, 1, M>(c);
    constexpr int base = M + (M > 16) + (M > 18) + (M > 20) + (M > 22);
    if constexpr (M <= 27) tr_read<base>(c, va);
    if constexpr (M < 16) op_cvt<Xp, 0, M>(c);
    else if constexpr (M >= 17 && M <= 23 && (M & 1)) op_cvt<Xp, 1, (M - 17) / 2>(c);
    else if constexpr (M >= 25 && M <= 28) op_cvt<Xp, 1, 8 + M - 25>(c);
    if constexpr (M == 16 || M == 18 || M == 20 || M == 22) tr_read<base + 1>(c, va);
    if constexpr (M == 0) { op_add<Xp, 0, eb_lo(31)>(c); if constexpr (eb_lo(31) + 1 < 32) op_add<Xp, 0, eb_lo(31) + 1>(c); }
    else op_add<Xp, 1, M - 1>(c);
  }
  if constexpr (M == 29) { c.sk_m0 = c.m0k + (unsigned)X * 0x4000u; c.sk_so = KTILEB * (unsigned)(t + 2) + c.sofk; asm volatile("" : "+s"(c.sk_m0), "+s"(c.sk_so)); }
  if constexpr (M == 30) { c.sv_m0 = c.m0v + (unsigned)Xp * 0x4000u; c.sv_so = KTILEB * (unsigned)(t + 1) + c.sofv; asm volatile("" : "+s"(c.sv_m0), "+s"(c.sv_so)); }
  if constexpr (M == 31) asm volatile("s_mov_b32 m0, %0" :: "s"(c.sk_m0) : "memory");
  if constexpr (!FIRST && M == 8) asm volatile("" :: "v"(c.NEGM[0]), "v"(c.NEGM[1]));
  GAP();
}
#define W4_VLO "+v"(c.vlo[0][0]), "+v"(c.vlo[0][1]), "+v"(c.vlo[0][2]), "+v"(c.vlo[0][3]), "+v"(c.vlo[1][0]), "+v"(c.vlo[1][1]), "+v"(c.vlo[1][2]), "+v"(c.vlo[1][3]), \
               "+v"(c.vlo[2][0]), "+v"(c.vlo[2][1]), "+v"(c.vlo[2][2]), "+v"(c.vlo[2][3]), "+v"(c.vlo[3][0]), "+v"(c.vlo[3][1]), "+v"(c.vlo[3][2]), "+v"(c.vlo[3][3])
#define W4_VUP "+v"(c.vup[0][0]), "+v"(c.vup[0][1]), "+v"(c.vup[0][2]), "+v"(c.vup[0][3]), "+v"(c.vup[1][0]), "+v"(c.vup[1][1]), "+v"(c.vup[1][2]), "+v"(c.vup[1][3]), \
               "+v"(c.vup[2][0]), "+v"(c.vup[2][1]), "+v"(c.vup[2][2]), "+v"(c.vup[2][3]), "+v"(c.vup[3][0]), "+v"(c.vup[3][1]), "+v"(c.vup[3][2]), "+v"(c.vup[3][3])
template <int X, bool FIRST, int... Ms> DI void phaseA_(W4C& c, const int t, iseq<Ms...>) {
  const unsigned va = c.vvb + (unsigned)(X ^ 1) * 0x4000u;
  (gapA<X, FIRST, Ms>(c, va, t), ...);
  if constexpr (FIRST) asm volatile("s_waitcnt vmcnt(0) lgkmcnt(0)\n\ts_barrier\n\ts_nop 7" ::: "memory");
  else { asm volatile("s_waitcnt vmcnt(%c16) lgkmcnt(0)" : W4_VLO : "i"(X == 1 ? 1 : 0) : "memory"); asm volatile("s_barrier\n\ts_nop 1" : W4_VUP :: "memory"); }
  GAP();
  if constexpr (!FIRST) {
#pragma unroll
    for (int ks = 0; ks < 4; ++ks)
#pragma unroll
      for (int db = 0; db < 4; ++db) c.Vf[db][ks] = __builtin_shufflevector(c.vlo[db][ks], c.vup[db][ks], 0, 1, 2, 3, 4, 5, 6, 7);
  }
}
template <int X, bool FIRST> DI void phaseA(W4C& c, const int t) { phaseA_<X, FIRST>(c, t, make_iseq<32>{}); }

template <int X, bool INIT> DI void new_reference(W4C& c) {
#pragma unroll
  for (int q = 0; q < 2; ++q) {
    float m0 = max3f(c.S[X][0][q][0], c.S[X][0][q][1], c.S[X][1][q][0]), m1 = max2f(c.S[X][1][q][1], c.S[X][0][q][2]);
#pragma unroll
    for (int e = 2; e < 16; e += 2) { m0 = max3f(m0, c.S[X][0][q][e], c.S[X][0][q][e + 1]); m1 = max3f(m1, c.S[X][1][q][e], c.S[X][1][q][e + 1]); }
    const float mm = max2f(m0, m1);
    auto pr = __builtin_amdgcn_permlane32_swap(__float_as_uint(mm), __float_as_uint(mm), false, false);
    const float rmax = max2f(__uint_as_float(pr[0]), __uint_as_float(pr[1]));
    const float dl = INIT ? rmax : max2f(rmax, 0.f);
    if constexpr (INIT) { c.mhat[q] = dl; c.rs[q][0] = 0.f; c.rs[q][1] = 0.f; c.fct[q] = 1.f; }
    else { c.mhat[q] = fadd_s(c.mhat[q], dl); c.fct[q] = __builtin_amdgcn_exp2f(-dl); c.rs[q][0] = c.rs[q][0] * c.fct[q]; c.rs[q][1] = c.rs[q][1] * c.fct[q]; }
#pragma unroll
    for (int kb = 0; kb < 2; ++kb) { _Pragma("unroll") for (int e = 0; e < 16; ++e) c.S[X][kb][q][e] = fsub_s(c.S[X][kb][q][e], dl); }
    negm_bcast(c.NEGM[q], w4_aone(), -c.mhat[q]);
  }
  asm volatile("s_nop 1");
}
template <int X, bool PEEL, bool ZERO1, int M> DI void gapB(W4C& c, const int t) {
  constexpr int Xp = X ^ 1;
  if constexpr (!PEEL) { pv_mfma<M, ZERO1>(c); GAP(); }
  if constexpr (!PEEL && M == 0) op_add<Xp, 1, 31>(c);
  constexpr int kq = M == 0 ? 0 : M == 11 ? 1 : M == 13 ? 2 : M == 15 ? 3 : -1, vq = M == 18 ? 0 : M == 20 ? 1 : M == 22 ? 2 : M == 24 ? 3 : -1;
  if constexpr (kq >= 0) asm volatile("buffer_load_dwordx4 %0, %1, %2 offen offset:%c3 lds" :: "v"(c.vksrc[kq & 1]), "s"(c.srdK), "s"(c.sk_so), "i"(1024 * kq) : "memory");
  if constexpr (M == 16) asm volatile("s_mov_b32 m0, %0" :: "s"(c.sv_m0) : "memory");
  if constexpr (vq >= 0) asm volatile("buffer_load_dwordx4 %0, %1, %2 offen offset:%c3 lds" :: "v"(c.vvsrc[vq]), "s"(c.srdV), "s"(c.sv_so), "i"(1024 * vq) : "memory");
  if constexpr (M >= 1 && M <= 8) {
    if constexpr (!PEEL) { op_max4<X, M - 1>(c); op_cvt<Xp, 1, (M <= 4 ? 4 + M - 1 : 12 + M - 5)>(c); }
    k_read<M - 1, Xp>(c);
  }
  if constexpr (M == 9) {
    if constexpr (PEEL) new_reference<X, true>(c);
    else {
      const float mm = max2f(c.mA, c.mB);
      if (__builtin_expect(__any((int)(mm > ATTN_THR)), 0)) { new_reference<X, false>(c); c.pend = true; }
    }
  }
  if constexpr (M >= EB0) {
    constexpr int e0 = eb_lo(M), e1 = eb_lo(M + 1), p0 = eb_lo(M - 1);
    op_exp<X, 0, e0>(c);
    if constexpr (M > EB0) op_add<X, 0, p0>(c);
    if constexpr (e0 + 1 < e1) op_exp<X, 0, e0 + 1>(c);
    if constexpr (M > EB0 && p0 + 1 < e0) op_add<X, 0, p0 + 1>(c);
    constexpr int kr = M == 10 ? 8 : M == 12 ? 9 : M == 14 ? 10 : M == 17 ? 11 : M == 19 ? 12 : M == 21 ? 13 : M == 23 ? 14 : M == 25 ? 15 : -1;
    if constexpr (kr >= 0) k_read<kr, Xp>(c);
  }
  if constexpr (X == 0 && M == 26) {
    unsigned plo, phi;
    asm volatile("v_readlane_b32 %0, %2, %4\n\tv_readlane_b32 %1, %3, %4\n\ts_mov_b32 m0, %5" : "=s"(plo), "=s"(phi) : "v"(c.pf_lo), "v"(c.pf_hi), "s"((unsigned)(t >> 1)), "s"(c.pf_m0) : "memory");
    c.sk_m0 = plo; c.sk_so = phi;
  }
  if constexpr (X == 0 && M == 28) {
    const unsigned long long pb = ((unsigned long long)c.sk_so << 32) | c.sk_m0;
    asm volatile("global_load_lds_dword %0, %1" :: "v"(c.pf_off), "s"(pb) : "memory");
  }
  if constexpr (M == 31) asm volatile("" :: "v"(c.S[X][0][0]), "v"(c.S[X][0][1]));
  GAP();
}
template <int R> DI void resc4(float f) {
  float t0, t1, t2, t3;
  asm volatile("v_accvgpr_read_b32 %0, a[%c5]\n\tv_accvgpr_read_b32 %1, a[%c6]\n\tv_accvgpr_read_b32 %2, a[%c7]\n\tv_accvgpr_read_b32 %3, a[%c8]\n\t"
               "v_mul_f32 %0, %4, %0\n\tv_mul_f32 %1, %4, %1\n\tv_mul_f32 %2, %4, %2\n\tv_mul_f32 %3, %4, %3\n\t"
               "v_accvgpr_write_b32 a[%c5], %0\n\tv_accvgpr_write_b32 a[%c6], %1\n\tv_accvgpr_write_b32 a[%c7], %2\n\tv_accvgpr_write_b32 a[%c8], %3"
               : "=&v"(t0), "=&v"(t1), "=&v"(t2), "=&v"(t3) : "v"(f), "i"(R), "i"(R + 1), "i"(R + 2), "i"(R + 3) : W4_ACLOB);
}
template <int... Is> DI void resc_all(W4C& c, iseq<Is...>) { (resc4<4 * Is>(c.fct[(Is >> 2) & 1]), ...); }
template <int X, bool PEEL, bool ZERO1, int... Ms> DI void phaseB_(W4C& c, const int t, iseq<Ms...>) {
  (gapB<X, PEEL, ZERO1, Ms>(c, t), ...);
  if constexpr (!PEEL) {
    if (__builtin_expect(c.pend, 0)) {
      asm volatile("s_nop 7\n\ts_nop 7" ::: W4_ACLOB);
      resc_all(c, make_iseq<32>{});
      asm volatile("s_nop 1" ::: W4_ACLOB);
      c.pend = false;
    }
  }
  asm volatile("s_waitcnt lgkmcnt(0)" ::: "memory");
  GAP();
}
template <int X, bool PEEL, bool ZERO1> DI void phaseB(W4C& c, const int t) { phaseB_<X, PEEL, ZERO1>(c, t, make_iseq<32>{}); }
template <int... Is> DI void k_reads0(W4C& c, iseq<Is...>) { (k_read<Is, 0>(c), ...); }
template <int... Is> DI void tr_reads(W4C& c, unsigned va, iseq<Is...>) { (tr_read<Is>(c, va), ...); }
template <int... Is> DI void drain_exp(W4C& c, iseq<Is...>) { (op_exp<1, 1, Is>(c), ...); }
template <int... Is> DI void drain_cvt0(W4C& c, iseq<Is...>) { (op_cvt<1, 0, Is>(c), ...); }
template <int... Is> DI void drain_cvt1(W4C& c, iseq<Is...>) { (op_cvt<1, 1, Is>(c), ...); }
template <int... Is> DI void drain_add(W4C& c, iseq<Is...>) { (op_add<1, 1, Is>(c), ...); }
template <int... Ms> DI void pv_all(W4C& c, iseq<Ms...>) { (pv_mfma<Ms, false>(c), ...); }
template <int R> DI u32x4 acc_pack8(float f) {
  float o[8];
  asm volatile("v_accvgpr_read_b32 %0, a[%c8]\n\tv_accvgpr_read_b32 %1, a[%c9]\n\tv_accvgpr_read_b32 %2, a[%c10]\n\tv_accvgpr_read_b32 %3, a[%c11]\n\t"
               "v_accvgpr_read_b32 %4, a[%c12]\n\tv_accvgpr_read_b32 %5, a[%c13]\n\tv_accvgpr_read_b32 %6, a[%c14]\n\tv_accvgpr_read_b32 %7, a[%c15]"
               : "=v"(o[0]), "=v"(o[1]), "=v"(o[2]), "=v"(o[3]), "=v"(o[4]), "=v"(o[5]), "=v"(o[6]), "=v"(o[7])
               : "i"(R), "i"(R + 1), "i"(R + 2), "i"(R + 3), "i"(R + 4), "i"(R + 5), "i"(R + 6), "i"(R + 7));
  u32x4 w = {cvtpk(o[0] * f, o[1] * f), cvtpk(o[2] * f, o[3] * f), cvtpk(o[4] * f, o[5] * f), cvtpk(o[6] * f, o[7] * f)};
  return w;
}
template <int I> DI void q_conv1(float C) {
  constexpr int qb = I >> 3, d0 = I & 7, nb = d0 >> 1, s = d0 & 1, aq = AQ_(qb, d0);
  const u32x4 w = acc_pack8<AO_(nb, qb) + 8 * s>(C);
  asm volatile("v_accvgpr_write_b32 a[%c4], %0\n\tv_accvgpr_write_b32 a[%c5], %1\n\tv_accvgpr_write_b32 a[%c6], %2\n\tv_accvgpr_write_b32 a[%c7], %3"
               :: "v"(w[0]), "v"(w[1]), "v"(w[2]), "v"(w[3]), "i"(aq), "i"(aq + 1), "i"(aq + 2), "i"(aq + 3) : W4_ACLOB_HI);
}
template <int... Is> DI void q_conv(float C, iseq<Is...>) { (q_conv1<Is>(C), ...); }
template <int I> DI void o_store1(__amdgpu_buffer_rsrc_t srd, unsigned vo, float il0, float il1) {
  constexpr int qb = I >> 3, db = (I >> 1) & 3, s = I & 1;
  const u32x4 w = acc_pack8<AO_(db, qb) + 8 * s>(qb ? il1 : il0);
  __builtin_amdgcn_raw_buffer_store_b128(w, srd, vo + qb * (256u * 1024u) + db * 1024 + s * 512, 0, WT);
}
template <int... Is> DI void o_store(__amdgpu_buffer_rsrc_t srd, unsigned vo, float il0, float il1, iseq<Is...>) { (o_store1<Is>(srd, vo, il0, il1), ...); }

#define Q_NS 6
#define Q_PDT 2
#define Q_FD 1
#define Q_BP 1
using QCfg = GCfg<32, 4, 64, 8, false, 0, Q_NS, Q_PDT, Q_FD, Q_BP, true>;
constexpr int SHM_ATTN = 0x10000 + 6 * 16384;

#define ST(i)
__global__ __launch_bounds__(256, 1) void k_attn(const u16* __restrict__ H1f, const u16* __restrict__ Wq, const u16* __restrict__ Kc, const u16* __restrict__ Vc, u16* __restrict__ Of,
                                                        const u16* __restrict__ Wo, const u16* __restrict__ Wg, const u16* __restrict__ Wu, const u16* __restrict__ Wd, const u16* __restrict__ Wm) {
  ST(0); KST_BEGIN(2);
  extern __shared__ __attribute__((aligned(16))) char lds[];
  const unsigned tid = threadIdx.x, wid = rfl(tid >> 6);
  const int bid = blockIdx.x, xcd = bid & 7, j = bid >> 3;
  const int head = 4 * xcd + (j & 3), y = j >> 2, kvh = head >> 3;
  const unsigned ldsb = (unsigned)(size_t)lds;
  {
    const unsigned lane = tid & 63u;
    GState<QCfg> g;
    const int mb0 = y * 8 + wid * 2;
    constexpr unsigned mbstride = 1024u * 32u * 2u;
    g.srdA = mk_srd((const char*)H1f + (size_t)mb0 * mbstride, 2 * mbstride);
    g.voffA[0] = lane * 16u; g.voffA[1] = lane * 16u + mbstride;
    constexpr unsigned ldb = 4096u * 2u;
    g.srdB = mk_srd((const char*)Wq + (size_t)head * 256, 1024u * ldb - (unsigned)head * 256u);
    g_setup<QCfg, true>(g, ldsb + QGEMM_LDS, lane, wid, ldb);
    { const unsigned gw = blockIdx.x * 4u + wid;
      g.tch[0] = (unsigned long long)(size_t)((const char*)Wq + (size_t)gw * 4096); g.tch[1] = g.tch[0] + (4096u * 1024u);
      g.tch[2] = (unsigned long long)(size_t)((gw < 512u ? (const char*)Kc : (const char*)Vc) + (size_t)(gw & 511u) * 4096);
      g.tch_off = lane * 64u; }
    g.srdK = mk_srd((const char*)Kc + (size_t)kvh * (2048u * 256u), 2048u * 256u);
    g.srdV = mk_srd((const char*)Vc + (size_t)kvh * (2048u * 256u), 2048u * 256u);
    g.kvm0 = rfl(ldsb + KRING + 4096u * wid); g.kvso = rfl(KRS * 16u * wid);
#pragma unroll
    for (int par = 0; par < 2; ++par) g.vk[par] = KRS * (lane >> 4) + 16u * ((lane & 15u) ^ (4u * par + (lane >> 4)));
    g.vv = KRS * ((lane >> 2) & 7u) + 64u * (lane >> 5) + 16u * (lane & 3u);
    gemm_run<QCfg>(g, 16);
  }
  ST(1);
  unsigned lane; asm volatile("v_mbcnt_lo_u32_b32 %0, -1, 0\n\tv_mbcnt_hi_u32_b32 %0, -1, %0" : "=v"(lane));
  const unsigned l32 = lane & 31u, hi = lane >> 5;
  constexpr int T = 32;
  W4C c;
#pragma unroll
  for (int jj = 0; jj < 4; ++jj) c.vkb[jj] = ldsb + KRING + 256u * l32 + 16u * ((2u * jj + hi) ^ (l32 & 7u));
  c.vvb = ldsb + VRING + (((lane & 3u) << 3) | (((lane >> 2) & 3u) << 6) | (((lane >> 4) & 1u) << 5) | (hi << 8));
#pragma unroll
  for (int par = 0; par < 2; ++par) c.vksrc[par] = KRS * (lane >> 4) + 16u * ((lane & 15u) ^ (4u * par + (lane >> 4)));
#pragma unroll
  for (int q = 0; q < 4; ++q) c.vvsrc[q] = KRS * ((lane >> 2) & 7u) + 64u * hi + 16u * (lane & 3u) + KRS * 8u * (q >> 1) + 128u * (q & 1) + (3072u - 1024u * q);
  c.m0k = rfl(ldsb + KRING + 4096u * wid); c.m0v = rfl(ldsb + VRING + 4096u * wid);
  c.sofk = rfl(KRS * 16u * wid); c.sofv = rfl(KRS * 16u * wid) - 3072u;
  c.srdK = mk_srd((const char*)Kc + (size_t)kvh * (2048u * 256u), 2048u * 256u);
  c.srdV = mk_srd((const char*)Vc + (size_t)kvh * (2048u * 256u), 2048u * 256u);
  {
    const unsigned gw = blockIdx.x * 4u + wid;
    const char* p;
    const unsigned c2 = gw + 1024u, c2c = c2 < 1792u ? c2 : gw;
    if (lane == 0) p = (const char*)Wo + (size_t)gw * 4096; else if (lane == 1) p = (const char*)Wo + (size_t)c2 * 4096;
    else if (lane == 2) p = (const char*)Wg + (size_t)gw * 4096; else if (lane == 3) p = (const char*)Wg + (size_t)c2c * 4096;
    else if (lane == 4) p = (const char*)Wu + (size_t)gw * 4096; else if (lane == 5) p = (const char*)Wu + (size_t)c2c * 4096;
    else if (lane == 6) p = (const char*)Wd + (size_t)gw * 4096; else if (lane == 7) p = (const char*)Wd + (size_t)c2c * 4096;
    else if (lane < 11) p = (const char*)Wm + (size_t)(gw + 1024u * (lane - 8u)) * 4096;
    else p = (const char*)Wo + (size_t)gw * 4096;
    c.pf_lo = (unsigned)(size_t)p; c.pf_hi = (unsigned)((size_t)p >> 32);
    c.pf_off = lane * 64u; c.pf_m0 = rfl(ldsb + QGEMM_LDS + 256u * wid);
  }
  asm volatile("s_barrier" ::: "memory");
  k_reads0(c, make_iseq<16>{});
  q_conv(0.12751743082f, make_iseq<16>{});
  asm volatile("s_waitcnt lgkmcnt(0)\n\ts_nop 1" ::: "memory");
  c.mhat[0] = c.mhat[1] = 0.f; c.fct[0] = c.fct[1] = 1.f; c.pend = false; c.rs[0][0] = c.rs[0][1] = c.rs[1][0] = c.rs[1][1] = 0.f;
  ST(2);
  phaseA<0, true>(c, 0);  phaseB<0, true, false>(c, 0);
  phaseA<1, false>(c, 1); phaseB<1, false, true>(c, 1);
  for (int t = 2; t < T; t += 2) {
    phaseA<0, false>(c, t);     phaseB<0, false, false>(c, t);
    phaseA<1, false>(c, t + 1); phaseB<1, false, false>(c, t + 1);
  }
  ST(3);
  float lsum[2];
  {
    tr_reads(c, c.vvb + 0x4000u, make_iseq<32>{});
    op_add<1, 0, eb_lo(31)>(c); if constexpr (eb_lo(31) + 1 < 32) op_add<1, 0, eb_lo(31) + 1>(c);
    drain_exp(c, make_iseq<32>{});
    drain_cvt0(c, make_iseq<16>{});
    drain_cvt1(c, make_iseq<16>{});
    GAP(); asm volatile("s_nop 1"); GAP();
    drain_add(c, make_iseq<32>{});
    asm volatile("s_waitcnt lgkmcnt(0)" : W4_VLO :: "memory"); asm volatile("" : W4_VUP :: "memory");
    GAP();
#pragma unroll
    for (int ks = 0; ks < 4; ++ks)
#pragma unroll
      for (int db = 0; db < 4; ++db) c.Vf[db][ks] = __builtin_shufflevector(c.vlo[db][ks], c.vup[db][ks], 0, 1, 2, 3, 4, 5, 6, 7);
    asm volatile("s_nop 1" ::: "memory");
    pv_all(c, make_iseq<32>{});
#pragma unroll
    for (int qb = 0; qb < 2; ++qb) { const float rs = c.rs[qb][0] + c.rs[qb][1];
      auto pr = __builtin_amdgcn_permlane32_swap(__float_as_uint(rs), __float_as_uint(rs), false, false);
      lsum[qb] = __uint_as_float(pr[0]) + __uint_as_float(pr[1]); }
    asm volatile("s_nop 7\n\ts_nop 7" ::: "memory");
  }
  {
    unsigned lb; asm volatile("v_mbcnt_lo_u32_b32 %0, -1, 0\n\tv_mbcnt_hi_u32_b32 %0, -1, %0" : "=v"(lb));
    const float il0 = __builtin_amdgcn_rcpf(lsum[0]), il1 = __builtin_amdgcn_rcpf(lsum[1]);
    o_store(mk_srd(Of, 16u << 20), (((unsigned)(y * 16 + wid * 4) + ((lb >> 4) & 1u)) * 128u + (unsigned)head * 4u) * 1024u + (lb >> 5) * 256u + (lb & 15u) * 16u, il0, il1, make_iseq<16>{});
  }
  ST(4); KST_END(2);
}
template <int N> DI void touch(const char* p, int stride) {
  unsigned d[N];
#pragma unroll
  for (int i = 0; i < N; ++i) d[i] = *(const unsigned*)(p + (size_t)i * stride);
#pragma unroll
  for (int i = 0; i < N; ++i) asm volatile("" :: "v"(d[i]));
}

template <int NT, int KL> DI void mod_body(const u16* __restrict__ c, const u16* __restrict__ wmod, float* __restrict__ dst, int cb, int kh, float* red  ) {
  const int tid = threadIdx.x, cg = tid & 7, kg = tid >> 3, wv = tid >> 6;
  constexpr int NG = NT / 8, NI = KL / NG;
  u32x4 w[NI]; u16 cv[NI];
#pragma unroll
  for (int i = 0; i < NI; ++i) { const int k = kh * KL + kg + NG * i; w[i] = *(const u32x4*)(wmod + (size_t)k * 6144 + cb * 64 + cg * 8); cv[i] = c[k]; }
  float acc[8] = {0, 0, 0, 0, 0, 0, 0, 0};
#pragma unroll
  for (int i = 0; i < NI; ++i) {
    const float xx = bf2f(cv[i]), s = rbf(xx / (1.f + __expf(-xx)));
#pragma unroll
    for (int jj = 0; jj < 4; ++jj) { acc[2 * jj] += s * __uint_as_float(w[i][jj] << 16); acc[2 * jj + 1] += s * __uint_as_float(w[i][jj] & 0xffff0000u); }
  }
#pragma unroll
  for (int jj = 0; jj < 8; ++jj) { float a = acc[jj]; a += __shfl_xor(a, 8); a += __shfl_xor(a, 16); a += __shfl_xor(a, 32); acc[jj] = a; }
  if ((tid & 63) < 8) {
#pragma unroll
    for (int jj = 0; jj < 8; ++jj) red[wv * 64 + cg * 8 + jj] = acc[jj];
  }
  __syncthreads();
  if (tid < 64) { float s = 0;
#pragma unroll
    for (int g = 0; g < NT / 64; ++g) s += red[g * 64 + tid];
    dst[tid] = s; }
}
__global__ __launch_bounds__(512) void k_pre(const u16* __restrict__ c, const u16* __restrict__ wmod, float* __restrict__ modA, const u16* __restrict__ x, unsigned* __restrict__ flg) {
  __shared__ float red[8 * 64];
  const int b = blockIdx.x;
  KST_BEGIN(0);
  unsigned dx = 0;
  if (threadIdx.x == 0) flg[b * 64] = 0u;
  if (threadIdx.x < 256) dx = *(const unsigned*)((const char*)x + (size_t)b * 16384 + threadIdx.x * 64);
  mod_body<512, 128>(c, wmod, modA + (b & 7) * 2048 + (b >> 3) * 64, b >> 3, b & 7, red);
  asm volatile("" :: "v"(dx));
  KST_END(0);
}
DI f32x4 modv(const float* __restrict__ modp, int n) { const f32x4 a = *(const f32x4*)(modp + n), b = *(const f32x4*)(modp + 6144 + n); f32x4 o; for (int e = 0; e < 4; ++e) o[e] = rbf(a[e] + b[e]); return o; }
DI void unpack8(const u32x4& w, float* v) {
#pragma unroll
  for (int j = 0; j < 4; ++j) { v[2 * j] = __uint_as_float(w[j] << 16); v[2 * j + 1] = __uint_as_float(w[j] & 0xffff0000u); }
}
DI u32x4 pack8(const float* o) { return (u32x4){cvtpk(o[0], o[1]), cvtpk(o[2], o[3]), cvtpk(o[4], o[5]), cvtpk(o[6], o[7])}; }

DI void rbf2(float& a, float& b) { const unsigned w = cvtpk(a, b); a = __uint_as_float(w << 16); b = __uint_as_float(w & 0xffff0000u); }
DI void rbf8(float* v) { rbf2(v[0], v[1]); rbf2(v[2], v[3]); rbf2(v[4], v[5]); rbf2(v[6], v[7]); }
DI void modv8(const float* __restrict__ modp, int n, float* o) {
  const f32x4 a0 = *(const f32x4*)(modp + n), b0 = *(const f32x4*)(modp + 6144 + n), a1 = *(const f32x4*)(modp + n + 8), b1 = *(const f32x4*)(modp + 6144 + n + 8);
#pragma unroll
  for (int e = 0; e < 4; ++e) { o[e] = a0[e] + b0[e]; o[4 + e] = a1[e] + b1[e]; }
  rbf8(o);
}
template <int MODE>
__global__ __launch_bounds__(1024) void k_ln(const u16* __restrict__ x, const u16* __restrict__ Pf, const float* __restrict__ modp, u16* __restrict__ xf, u16* __restrict__ hf) {
  __shared__ float red[2][16][8];
  __shared__ __attribute__((aligned(16))) float msum[MODE == 0 ? 2048 : 4];
  const int tid = threadIdx.x;
  KST_BEGIN(MODE == 0 ? 1 : 4);
  const int r = tid & 7, c = tid >> 3, h = c & 1, wid = tid >> 6;
  const int row = blockIdx.x * 8 + r, mb = row >> 5, ml = row & 31;
  const size_t fo = ((size_t)mb * 64 + (c >> 1)) * 512 + (h * 32 + ml) * 8;
  const int n = 16 * (c >> 1) + 4 * h;
  float v[8], sh[8], sc[8];
  if (MODE == 0) {
    const u32x4 xw = *(const u32x4*)(x + (size_t)row * 1024 + 8 * c);
    { f32x2 a = {0.f, 0.f};
#pragma unroll
      for (int ks = 0; ks < 8; ++ks) a += *(const f32x2*)(modp + ks * 2048 + 2 * tid);
      float a0 = a[0], a1 = a[1]; rbf2(a0, a1); *(f32x2*)(msum + 2 * tid) = (f32x2){a0, a1}; }
    const unsigned s0 = h ? xw[0] : xw[2], s1 = h ? xw[1] : xw[3];
    const unsigned g0 = __shfl_xor(s0, 8), g1 = __shfl_xor(s1, 8);
    const u32x4 f = h ? (u32x4){g0, g1, xw[2], xw[3]} : (u32x4){xw[0], xw[1], g0, g1};
    __builtin_amdgcn_raw_buffer_store_b128(f, mk_srd(xf, 4u << 20), (unsigned)fo * 2u, 0, WT);
    unpack8(f, v);
  } else {
    const u32x4 xw = *(const u32x4*)(xf + fo), p0 = *(const u32x4*)(Pf + fo), p1 = *(const u32x4*)(Pf + fo + (size_t)2048 * 1024);
    float ga[8], a[8], b[8];
    modv8(modp, 3072 + n, sh); modv8(modp, 4096 + n, sc); modv8(modp, 2048 + n, ga);
    unpack8(xw, v); unpack8(p0, a); unpack8(p1, b);
#pragma unroll
    for (int j = 0; j < 8; ++j) a[j] += b[j];
    rbf8(a);
#pragma unroll
    for (int j = 0; j < 8; ++j) a[j] *= ga[j];
    rbf8(a);
#pragma unroll
    for (int j = 0; j < 8; ++j) v[j] += a[j];
    const u32x4 w = pack8(v);
    __builtin_amdgcn_raw_buffer_store_b128(w, mk_srd(xf, 4u << 20), (unsigned)fo * 2u, 0, WT);
    unpack8(w, v);
  }
  float s = ((v[0] + v[1]) + (v[2] + v[3])) + ((v[4] + v[5]) + (v[6] + v[7]));
  s += __shfl_xor(s, 8); s += __shfl_xor(s, 16); s += __shfl_xor(s, 32);
  if ((tid & 63) < 8) red[0][wid][r] = s;
  __syncthreads();
  if (MODE == 0) {
    const f32x4 a0 = *(const f32x4*)(msum + n), a1 = *(const f32x4*)(msum + n + 8), b0 = *(const f32x4*)(msum + 1024 + n), b1 = *(const f32x4*)(msum + 1024 + n + 8);
#pragma unroll
    for (int e = 0; e < 4; ++e) { sh[e] = a0[e]; sh[4 + e] = a1[e]; sc[e] = b0[e]; sc[4 + e] = b1[e]; }
  }
  float mean = 0;
#pragma unroll
  for (int w = 0; w < 16; ++w) mean += red[0][w][r];
  mean *= (1.f / 1024.f);
  float qv = 0;
#pragma unroll
  for (int j = 0; j < 8; ++j) { const float d = v[j] - mean; qv += d * d; }
  qv += __shfl_xor(qv, 8); qv += __shfl_xor(qv, 16); qv += __shfl_xor(qv, 32);
  if ((tid & 63) < 8) red[1][wid][r] = qv;
  __syncthreads();
  float var = 0;
#pragma unroll
  for (int w = 0; w < 16; ++w) var += red[1][w][r];
  var = rbf(var * (1.f / 1024.f));
  const float mb16 = rbf(mean), rsd = 1.f / rbf(sqrtf(rbf(var + 1e-5f)));
#pragma unroll
  for (int j = 0; j < 8; ++j) { v[j] -= mb16; sc[j] += 1.f; }
  rbf8(v); rbf8(sc);
#pragma unroll
  for (int j = 0; j < 8; ++j) v[j] *= rsd;
  rbf8(v);
#pragma unroll
  for (int j = 0; j < 8; ++j) v[j] *= sc[j];
  rbf8(v);
#pragma unroll
  for (int j = 0; j < 8; ++j) v[j] += sh[j];
  const unsigned ho = MODE == 0 ? (unsigned)fo * 2u : (((unsigned)(row >> 4) * 32u + (unsigned)(c >> 2)) * 1024u + (unsigned)(c & 3) * 256u + (unsigned)(row & 15) * 16u);
  __builtin_amdgcn_raw_buffer_store_b128(pack8(v), mk_srd(hf, 4u << 20), ho, 0, WT);
  KST_END(MODE == 0 ? 1 : 4);
}
using SCfg = GCfg<16, 4, 32, 8, false, 1, 4, 4, 1, 1>;
template <int K, bool FIN, int NT0>
__global__ __launch_bounds__(256, 2) void k_gemm_sk(const u16* __restrict__ Af, const u16* __restrict__ B, u16* __restrict__ Pf,
                                                    const u16* __restrict__ cvec, const u16* __restrict__ wmod, float* __restrict__ modp,
                                                    const u16* __restrict__ x1f, u16* __restrict__ out, unsigned* __restrict__ flg) {
  using C = SCfg;
  extern __shared__ __attribute__((aligned(16))) char lds[];
  KST_BEGIN(K == 4096 ? 3 : 6);
  if (K == 4096 && blockIdx.x >= 256) { const int b = blockIdx.x - 256; mod_body<256, 512>(cvec, wmod, modp + (b & 1) * 6144 + (32 + (b >> 1)) * 64, 32 + (b >> 1), b & 1, (float*)lds); KST_END(3); return; }
  constexpr int N = 1024;
  const unsigned tid = threadIdx.x, lane = tid & 63u, wid = rfl(tid >> 6);
  const int bid = blockIdx.x, xcd = bid & 7, j = bid >> 3;
  const int kh = xcd & 1, mg = xcd >> 1;
  const int mt = mg * 4 + (j & 3), nt = j >> 2;
  const int k0 = kh ? NT0 * 64 : 0, klen = kh ? K - NT0 * 64 : NT0 * 64;
  GState<C> g;
  const int mb0 = mt * 4 + wid;
  constexpr unsigned mbstride = (unsigned)K * 32u;
  g.srdA = mk_srd((const char*)Af + (size_t)(2 * mb0) * mbstride + (size_t)k0 * 32, mbstride + (unsigned)klen * 32u);
  g.voffA[0] = lane * 16u; g.voffA[1] = lane * 16u + mbstride;
  constexpr unsigned ldb = (unsigned)N * 2u;
  g.srdB = mk_srd((const char*)B + (size_t)k0 * ldb + (size_t)nt * 256, (unsigned)klen * ldb - (unsigned)nt * 256u);
  g_setup<C>(g, (unsigned)(size_t)lds, lane, wid, ldb);
  gemm_run<C>(g, klen / 64);
  const unsigned h = (lane >> 4) & 1u, hi = lane >> 5;
  const unsigned fo = (((unsigned)mb0 * 64u + (unsigned)nt * 8u + hi) * 512u + (h * 32u + (lane & 15u)) * 8u) * 2u;
  float acc[4][2][8];
#pragma unroll
  for (int i = 0; i < 4; ++i)
#pragma unroll
    for (int m = 0; m < 2; ++m)
#pragma unroll
      for (int e = 0; e < 4; ++e) {
        auto sw = __builtin_amdgcn_permlane32_swap(__float_as_uint(g.acc[2 * i][m][e]), __float_as_uint(g.acc[2 * i + 1][m][e]), false, false);
        acc[i][m][e] = __uint_as_float(sw[0]); acc[i][m][4 + e] = __uint_as_float(sw[1]);
      }
  if constexpr (!FIN) {
    const __amdgpu_buffer_rsrc_t srdP = mk_srd(Pf, 8u << 20);
#pragma unroll
    for (int i = 0; i < 4; ++i)
#pragma unroll
      for (int m = 0; m < 2; ++m) __builtin_amdgcn_raw_buffer_store_b128(pack8(acc[i][m]), srdP, (unsigned)kh * (4u << 20) + fo + i * 2048 + m * 256, 0, WT);
  } else {
    const __amdgpu_buffer_rsrc_t srdP = mk_srd(Pf, 4u << 20);
    unsigned* flag = flg + (128 + mt * 8 + nt) * 64;
    if (kh == 0) {
#pragma unroll
      for (int i = 0; i < 4; ++i)
#pragma unroll
        for (int m = 0; m < 2; ++m) __builtin_amdgcn_raw_buffer_store_b128(pack8(acc[i][m]), srdP, fo + i * 2048 + m * 256, 0, 16);
      asm volatile("s_waitcnt vmcnt(0)" ::: "memory");
      __syncthreads();
      if (tid == 0) __hip_atomic_store(flag, 1u, __ATOMIC_RELAXED, __HIP_MEMORY_SCOPE_AGENT);
    } else {
      u32x4 xw[4][2]; float gb[4][8];
#pragma unroll
      for (int i = 0; i < 4; ++i) { xw[i][0] = *(const u32x4*)((const char*)x1f + fo + i * 2048); xw[i][1] = *(const u32x4*)((const char*)x1f + fo + i * 2048 + 256);
        modv8(modp, 5120 + nt * 128 + 16 * (2 * i + hi) + 4 * h, gb[i]); }
      if (wid == 0) {
        for (unsigned spins = 0; spins < (1u << 22); ++spins) {
          if ((unsigned)__builtin_amdgcn_readfirstlane(__hip_atomic_load(flag, __ATOMIC_RELAXED, __HIP_MEMORY_SCOPE_AGENT)) != 0u) break;
          __builtin_amdgcn_s_sleep(1);
        }
      }
      asm volatile("" ::: "memory"); __builtin_amdgcn_s_barrier(); asm volatile("" ::: "memory");
      u32x4 pw[4][2];
#pragma unroll
      for (int i = 0; i < 4; ++i)
#pragma unroll
        for (int m = 0; m < 2; ++m) pw[i][m] = __builtin_amdgcn_raw_buffer_load_b128(srdP, fo + i * 2048 + m * 256, 0, 16);
      const unsigned oo = (((unsigned)mb0 * 32u + (lane & 15u)) * 1024u + (unsigned)nt * 128u + 16u * hi + 8u * h) * 2u;
      const __amdgpu_buffer_rsrc_t srdOut = mk_srd(out, 4u << 20);
#pragma unroll
      for (int i = 0; i < 4; ++i)
#pragma unroll
        for (int m = 0; m < 2; ++m) {
          float v[8], a[8];
          unpack8(xw[i][m], v); unpack8(pw[i][m], a);
#pragma unroll
          for (int jj = 0; jj < 8; ++jj) a[jj] += acc[i][m][jj];
          rbf8(a);
#pragma unroll
          for (int jj = 0; jj < 8; ++jj) a[jj] *= gb[i][jj];
          rbf8(a);
#pragma unroll
          for (int jj = 0; jj < 8; ++jj) v[jj] += a[jj];
          const u32x4 w = pack8(v);
          const unsigned s0 = h ? w[0] : w[2], s1 = h ? w[1] : w[3];
          const unsigned g0 = __shfl_xor(s0, 16), g1 = __shfl_xor(s1, 16);
          const u32x4 f = h ? (u32x4){g0, g1, w[2], w[3]} : (u32x4){w[0], w[1], g0, g1};
          __builtin_amdgcn_raw_buffer_store_b128(f, srdOut, oo + 64u * i + 32768u * m, 0, WT);
        }
    }
  }
  KST_END(K == 4096 ? 3 : 6);
}
#define SK2_NT0 28
DI float silu_mul(float g, float u) { return g * __builtin_amdgcn_rcpf(1.f + __builtin_amdgcn_exp2f(g * -1.4426950408889634f)) * u; }
#define U_NS 3
#define U_BP 3
using UCfg = GCfg<16, 8, 32, 14, true, 1, U_NS, 2, 1, U_BP>;
__global__ __launch_bounds__(512, 2) void k_gemm_gu(const u16* __restrict__ Af, const u16* __restrict__ Wg, const u16* __restrict__ Wu, u16* __restrict__ Act) {
  using C = UCfg;
  extern __shared__ __attribute__((aligned(16))) char lds[];
  constexpr int K = 1024, F = 3584;
  KST_BEGIN(5);
  const unsigned tid = threadIdx.x, wid = rfl(tid >> 6);
  const int bid = blockIdx.x, xcd = bid & 7, j = bid >> 3;
  const int mt = (xcd & 1) * 4 + (j & 3), nt = (xcd >> 1) * 8 + (j >> 2);
  GState<C> g;
  const int mb0 = mt * 8 + wid;
  {
    const unsigned lane = tid & 63u;
    constexpr unsigned mbstride = (unsigned)K * 32u;
    g.srdA = mk_srd((const char*)Af + (size_t)(2 * mb0) * mbstride, 2 * mbstride);
    g.voffA[0] = lane * 16u; g.voffA[1] = lane * 16u + mbstride;
    constexpr unsigned ldb = F * 2u;
    const bool up = wid >= 4;
    g.srdB = mk_srd((const char*)(up ? Wu : Wg) + (size_t)nt * 224, (unsigned)K * ldb - (unsigned)nt * 224u);
    g_setup<C>(g, (unsigned)(size_t)lds, lane, wid, ldb);
  }
  gemm_run<C>(g, K / 64);
  unsigned lane; asm volatile("v_mbcnt_lo_u32_b32 %0, -1, 0\n\tv_mbcnt_hi_u32_b32 %0, -1, %0" : "=v"(lane));
  const __amdgpu_buffer_rsrc_t srdO = mk_srd(Act, 2048u * F * 2u);
  const unsigned vo = ((unsigned)mb0 * 2u + (lane >> 5)) * (unsigned)((F >> 5) * 1024) + (lane & 31u) * 16u;
#pragma unroll
  for (int n = 0; n < 7; ++n) {
    unsigned q[2][2];
#pragma unroll
    for (int m = 0; m < 2; ++m) {
      float o[4];
#pragma unroll
      for (int e = 0; e < 4; ++e) o[e] = silu_mul(g.acc[2 * n][m][e], g.acc[2 * n + 1][m][e]);
      q[m][0] = cvtpk(o[0], o[1]); q[m][1] = cvtpk(o[2], o[3]);
    }
    auto s0 = __builtin_amdgcn_permlane32_swap(q[0][0], q[1][0], false, false); auto s1 = __builtin_amdgcn_permlane32_swap(q[0][1], q[1][1], false, false);
    const unsigned c16 = (unsigned)(nt * 7 + n);
    __builtin_amdgcn_raw_buffer_store_b128((u32x4){s0[0], s1[0], s0[1], s1[1]}, srdO, vo, (c16 >> 1) * 1024u + (c16 & 1u) * 512u, WT);
  }
  KST_END(5);
}

extern "C" void kernel_launch(void* const* d_in, const int* in_sizes, int n_in, void* d_out, int out_size, void* d_ws, size_t ws_size, hipStream_t stream) {
  const u16* x = (const u16*)d_in[0]; const u16* kc = (const u16*)d_in[1]; const u16* vc = (const u16*)d_in[2];
  const u16* wq = (const u16*)d_in[3]; const u16* wo = (const u16*)d_in[4];
  const u16* wg = (const u16*)d_in[6]; const u16* wu = (const u16*)d_in[7]; const u16* wd = (const u16*)d_in[8];
  const u16* c = (const u16*)d_in[9]; const u16* wmod = (const u16*)d_in[10];
  u16* out = (u16*)d_out;
  char* ws = (char*)d_ws;
  float* mod = (float*)ws;
  u16* hf = (u16*)(ws + (1 << 20));
  u16* x1f = (u16*)(ws + (8 << 20));
  u16* of = (u16*)(ws + (16 << 20));
  u16* P = (u16*)(ws + (32 << 20));
  u16* act = (u16*)(ws + (64 << 20));
  hipFuncSetAttribute((const void*)k_attn, hipFuncAttributeMaxDynamicSharedMemorySize, SHM_ATTN);
  hipFuncSetAttribute((const void*)k_gemm_sk<4096, false, 32>, hipFuncAttributeMaxDynamicSharedMemorySize, SCfg::LDSB);
  hipFuncSetAttribute((const void*)k_gemm_sk<3584, true, SK2_NT0>, hipFuncAttributeMaxDynamicSharedMemorySize, SCfg::LDSB);
  hipFuncSetAttribute((const void*)k_gemm_gu, hipFuncAttributeMaxDynamicSharedMemorySize, UCfg::LDSB);
  float* modA = (float*)(ws + (256 << 10));
  unsigned* flg = (unsigned*)(ws + (512 << 10));
  u16* P2 = (u16*)(ws + (48 << 20));
  k_pre<<<256, 512, 0, stream>>>(c, wmod, modA, x, flg);
  k_ln<0><<<256, 1024, 0, stream>>>(x, nullptr, modA, x1f, hf);
  k_attn<<<256, 256, SHM_ATTN, stream>>>(hf, wq, kc, vc, of, wo, wg, wu, wd, wmod);
  k_gemm_sk<4096, false, 32><<<256 + 128, 256, SCfg::LDSB, stream>>>(of, wo, P, c, wmod, mod, nullptr, nullptr, nullptr);
  k_ln<1><<<256, 1024, 0, stream>>>(x, P, mod, x1f, hf);
  k_gemm_gu<<<256, 512, UCfg::LDSB, stream>>>(hf, wg, wu, act);
  k_gemm_sk<3584, true, SK2_NT0><<<256, 256, SCfg::LDSB, stream>>>(act, wd, P2, nullptr, nullptr, mod, x1f, out, flg);
}
```

```cpp
#include <hip/hip_runtime.h>
#include <utility>
#include <type_traits>
typedef unsigned short u16;
typedef short bf16x8 __attribute__((ext_vector_type(8)));
typedef short s16x4 __attribute__((ext_vector_type(4)));
typedef float f32x4 __attribute__((ext_vector_type(4)));
typedef float f32x16 __attribute__((ext_vector_type(16)));
typedef unsigned u32x4 __attribute__((ext_vector_type(4)));
typedef unsigned u32x2 __attribute__((ext_vector_type(2)));
typedef float f32x2 __attribute__((ext_vector_type(2)));
typedef __bf16 bf16x2_t __attribute__((ext_vector_type(2)));
#define DI __device__ __forceinline__
#define LAS __attribute__((address_space(3)))
#define GAP() __builtin_amdgcn_sched_barrier(0)
template <int... Is> using iseq = std::integer_sequence<int, Is...>;
template <int N> using make_iseq = std::make_integer_sequence<int, N>;

#define KST_BEGIN(k)
#define KST_END(k)
DI unsigned rfl(unsigned x) { return __builtin_amdgcn_readfirstlane(x); }
DI float bf2f(u16 v) { return __uint_as_float(((unsigned)v) << 16); }
DI unsigned cvtpk(float lo, float hi) { f32x2 v = {lo, hi}; bf16x2_t b = __builtin_convertvector(v, bf16x2_t); return __builtin_bit_cast(unsigned, b); }
DI float rbf(float f);
DI float rbf(float f) { return __uint_as_float(cvtpk(f, 0.f) << 16); }
#define KV_FIRST 12
#define WT 16
DI __amdgpu_buffer_rsrc_t mk_srd(const void* p, unsigned bytes) { return __builtin_amdgcn_make_buffer_rsrc((void*)p, (short)0, (int)bytes, 0x00020000); }

#define W4_ACLOB "a0","a1","a2","a3","a4","a5","a6","a7","a8","a9","a10","a11","a12","a13","a14","a15","a16","a17","a18","a19","a20","a21","a22","a23","a24","a25","a26","a27","a28","a29","a30","a31","a32","a33","a34","a35","a36","a37","a38","a39","a40","a41","a42","a43","a44","a45","a46","a47","a48","a49","a50","a51","a52","a53","a54","a55","a56","a57","a58","a59","a60","a61","a62","a63","a64","a65","a66","a67","a68","a69","a70","a71","a72","a73","a74","a75","a76","a77","a78","a79","a80","a81","a82","a83","a84","a85","a86","a87","a88","a89","a90","a91","a92","a93","a94","a95","a96","a97","a98","a99","a100","a101","a102","a103","a104","a105","a106","a107","a108","a109","a110","a111","a112","a113","a114","a115","a116","a117","a118","a119","a120","a121","a122","a123","a124","a125","a126","a127"
#define W4_ACLOB_HI "a128","a129","a130","a131","a132","a133","a134","a135","a136","a137","a138","a139","a140","a141","a142","a143","a144","a145","a146","a147","a148","a149","a150","a151","a152","a153","a154","a155","a156","a157","a158","a159","a160","a161","a162","a163","a164","a165","a166","a167","a168","a169","a170","a171","a172","a173","a174","a175","a176","a177","a178","a179","a180","a181","a182","a183","a184","a185","a186","a187","a188","a189","a190","a191","a192","a193","a194","a195","a196","a197","a198","a199","a200","a201","a202","a203","a204","a205","a206","a207","a208","a209","a210","a211","a212","a213","a214","a215","a216","a217","a218","a219","a220","a221","a222","a223","a224","a225","a226","a227","a228","a229","a230","a231","a232","a233","a234","a235","a236","a237","a238","a239","a240","a241","a242","a243","a244","a245","a246","a247","a248","a249","a250","a251","a252","a253","a254","a255"
template <int SH_, int NW_, int WR_, int NB16_, bool GU_, int KORD_, int NS_, int PDT_, int FD_, int BARPOS_, bool ASMACC_ = false>
struct GCfg {
  static constexpr bool ASMACC = ASMACC_;
  static constexpr int SH = SH_, NW = NW_, WR = WR_, NB16 = NB16_, KORD = KORD_, NS = NS_, PDT = PDT_, FD = FD_, BARPOS = BARPOS_;
  static constexpr bool GU = GU_;
  static constexpr int STEPS = SH == 32 ? 4 : 2;
  static constexpr int MR = WR / SH;
  static constexpr int NB = SH == 32 ? NB16 / 2 : NB16;
  static constexpr int NR16 = GU ? NB16 / 2 : NB16;
  static constexpr int PIECES = NB16 * 2;
  static constexpr int PB = (PIECES + NW - 1) / NW;
  static constexpr int STAGE = PB * NW * 1024;
  static constexpr int REGION = STAGE / 2;
  static constexpr int LDSB = NS * STAGE;
  static constexpr int NOPS = STEPS * MR + PB;
  static constexpr int GS = NB * MR;
  static constexpr int G = STEPS * GS;
  static constexpr int BSTEP = STEPS - FD;
  static constexpr int GB = BSTEP * GS + (BARPOS + 1) * MR - 1;
  static constexpr int dma_lo(int ks) { return (PB * ks) / STEPS; }
  static constexpr int step_ops(int ks) { return MR + dma_lo(ks + 1) - dma_lo(ks); }
  static constexpr int step_first(int ks) { return ks * MR + dma_lo(ks); }
  static constexpr int op_step(int j) { int ks = 0; while (ks < STEPS - 1 && j >= step_first(ks + 1)) ++ks; return ks; }
  static constexpr int op_gap(int j) { const int ks = op_step(j), i = j - step_first(ks), n = step_ops(ks); return ks * GS + (i * GS) / n; }
  static constexpr int ops_before(int g) { int c = 0; for (int j = 0; j < NOPS; ++j) if (op_gap(j) < g) ++c; return c; }
  static constexpr int NA(int ks) {
    const int ts = (ks + 1) % STEPS, jt = step_first(ts) + MR - 1;
    const int full = (ks == STEPS - 1) ? PDT - 1 : PDT;
    return (NOPS - 1 - jt) + (full - 1) * NOPS + ops_before(ks * GS);
  }
  static constexpr int NBW() { return (NS - 3) * NOPS + ops_before(GB); }
  static_assert(NS >= 3 && PDT >= 2 && FD <= STEPS - 0 && BSTEP >= 0, "");
  using acc_t = std::conditional_t<SH == 32, f32x16, f32x4>;
};

template <class C>
struct GState {
  typename C::acc_t acc[C::NB][C::MR];
  bf16x8 a[C::PDT * C::STEPS][C::MR];
  s16x4 blo[C::FD][C::NB], bhi[C::FD][C::NB];
  __amdgpu_buffer_rsrc_t srdA, srdB;
  unsigned voffA[C::MR];
  unsigned voffB[C::GU ? C::PB : 1];
  unsigned sB[C::GU ? 1 : C::PB];
  unsigned m0base, vrd, ktileB;
  unsigned long long tch[3]; unsigned tch_off;
  __amdgpu_buffer_rsrc_t srdK, srdV; unsigned kvm0, kvso, vk[2], vv;
};
struct GIter { unsigned va_cur, va_nxt, sA, sA0, m0_fill, sK_fill; };

template <class C, int SL, int OFF, int MRI> DI void a_load1(GState<C>& g, unsigned sA) {
  asm volatile("buffer_load_dwordx4 %0, %1, %2, %3 offen offset:%4" : "=v"(g.a[SL][MRI]) : "v"(g.voffA[MRI]), "s"(g.srdA), "s"(sA), "i"(OFF) : "memory");
}
template <class C, int Q> DI void b_dma1(GState<C>& g, unsigned m0v, unsigned sK) {
  if constexpr (C::GU)
    asm volatile("s_mov_b32 m0, %0\n\ts_nop 0\n\tbuffer_load_dwordx4 %1, %2, %3 offen lds" :: "s"(m0v + 1024u * Q), "v"(g.voffB[Q]), "s"(g.srdB), "s"(sK) : "memory");
  else
    asm volatile("s_mov_b32 m0, %0\n\ts_nop 0\n\tbuffer_load_dwordx4 %1, %2, %3 offen lds" :: "s"(m0v + 1024u * Q), "v"(g.voffB[0]), "s"(g.srdB), "s"(sK + g.sB[Q]) : "memory");
}
template <class C> DI void g_dummy(GState<C>& g) {
  asm volatile("s_mov_b32 m0, %0\n\ts_nop 0\n\tbuffer_load_dwordx4 %1, %2, %3 offen lds" :: "s"(g.m0base + (unsigned)(C::NS - 1) * C::STAGE), "v"(g.voffA[0]), "s"(g.srdA), "s"(0x80000000u) : "memory");
}
template <class C, int TI, int J, bool VA, bool VB> DI void g_op(GState<C>& g, const GIter& it) {
  constexpr int ks = C::op_step(J), i = J - C::step_first(ks);
  if constexpr (i < C::MR) {
    constexpr int PD = C::PDT * C::STEPS, SL = (TI * C::STEPS + ks - 1 + PD) % PD;
    if constexpr (VA) { if constexpr (ks == 0) a_load1<C, SL, 0, i>(g, it.sA0); else a_load1<C, SL, (ks - 1) * 1024, i>(g, it.sA); } else g_dummy<C>(g);
  } else {
    if constexpr (VB) b_dma1<C, C::dma_lo(ks) + i - C::MR>(g, it.m0_fill, it.sK_fill); else g_dummy<C>(g);
  }
}
template <class C, int TI, int GI, int... Js> DI void g_ops_at(GState<C>& g, const GIter& it, iseq<Js...>) {
  ((C::op_gap(Js) == GI ? g_op<C, TI, Js, true, true>(g, it) : (void)0), ...);
}
template <class C> constexpr int b_off(int KS, int NBI, int t) {
  if (C::SH == 32) {
    return ((2 * KS + t) * C::NR16 + (C::GU ? NBI : 2 * NBI)) * 256;
  } else {
    const int kb = 4 * KS + (C::KORD == 0 ? 2 * t : t);
    return (kb * C::NR16 + (C::GU ? NBI / 2 : NBI)) * 256 + ((C::GU && (NBI & 1)) ? C::REGION : 0);
  }
}
template <class C, int BUF, int KS, int NBI> DI void b_read(GState<C>& g, unsigned va) {
  g.blo[BUF][NBI] = __builtin_amdgcn_ds_read_tr16_b64_v4i16((LAS s16x4*)(size_t)(va + b_off<C>(KS, NBI, 0)));
  g.bhi[BUF][NBI] = __builtin_amdgcn_ds_read_tr16_b64_v4i16((LAS s16x4*)(size_t)(va + b_off<C>(KS, NBI, 1)));
}
template <class C, int BUF, int KS, int... Ns> DI void b_read_all(GState<C>& g, unsigned va, iseq<Ns...>) { (b_read<C, BUF, KS, Ns>(g, va), ...); }

template <class C, int N, int SL> DI void a_wait(GState<C>& g) {
  static_assert(N >= 0 && N < 64, "vmcnt range");
  if constexpr (C::MR == 1) asm volatile("s_waitcnt vmcnt(%1)" : "+v"(g.a[SL][0]) : "i"(N) : "memory");
  else if constexpr (C::MR == 2) asm volatile("s_waitcnt vmcnt(%2)" : "+v"(g.a[SL][0]), "+v"(g.a[SL][1]) : "i"(N) : "memory");
  else asm volatile("s_waitcnt vmcnt(%4)" : "+v"(g.a[SL][0]), "+v"(g.a[SL][1]), "+v"(g.a[SL][2]), "+v"(g.a[SL][3]) : "i"(N) : "memory");
}

template <class C, int TI, int GI> DI void g_gap(GState<C>& g, const GIter& it) {
  constexpr int KS = GI / C::GS, r = GI % C::GS, NBI = r / C::MR, MRI = r % C::MR;
  constexpr int SL = TI * C::STEPS + KS, BUF = (C::FD == 1) ? 0 : (KS & 1), KN = (KS + C::FD) % C::STEPS;
  if constexpr (r == 0) { a_wait<C, C::NA(KS), SL>(g); GAP(); }
  {
    const bf16x8 bf = __builtin_shufflevector(g.blo[BUF][NBI], g.bhi[BUF][NBI], 0, 1, 2, 3, 4, 5, 6, 7);
    if constexpr (C::ASMACC && C::SH == 32) { constexpr int ao = 16 * (C::MR * NBI + MRI);
      asm volatile("v_mfma_f32_32x32x16_bf16 a[%c2:%c3], %0, %1, a[%c2:%c3]" :: "v"(bf), "v"(g.a[SL][MRI]), "i"(ao), "i"(ao + 15)); }
    else if constexpr (C::ASMACC) { constexpr int ao = 4 * (C::MR * NBI + MRI);
      asm volatile("v_mfma_f32_16x16x32_bf16 a[%c2:%c3], %0, %1, a[%c2:%c3]" :: "v"(bf), "v"(g.a[SL][MRI]), "i"(ao), "i"(ao + 3)); }
    else if constexpr (C::SH == 32) g.acc[NBI][MRI] = __builtin_amdgcn_mfma_f32_32x32x16_bf16(bf, g.a[SL][MRI], g.acc[NBI][MRI], 0, 0, 0);
    else if constexpr (C::GU) asm volatile("v_mfma_f32_16x16x32_bf16 %0, %1, %2, %0" : "+v"(g.acc[NBI][MRI]) : "v"(bf), "v"(g.a[SL][MRI]));
    else g.acc[NBI][MRI] = __builtin_amdgcn_mfma_f32_16x16x32_bf16(bf, g.a[SL][MRI], g.acc[NBI][MRI], 0, 0, 0);
  }
  if constexpr (GI == C::GB) {
    GAP();
    static_assert(C::NBW() < 64, "");
    asm volatile("s_waitcnt vmcnt(%0) lgkmcnt(0)\n\ts_barrier" :: "i"(C::NBW()) : "memory");
    GAP();
    b_read_all<C, BUF, KN>(g, it.va_nxt, make_iseq<C::BARPOS + 1>{});
  } else if constexpr (MRI == C::MR - 1) {
    if constexpr (KS == C::BSTEP) { if constexpr (NBI > C::BARPOS) b_read<C, BUF, KN, NBI>(g, it.va_nxt); }
    else if constexpr (KS > C::BSTEP) b_read<C, BUF, KN, NBI>(g, it.va_nxt);
    else b_read<C, BUF, KN, NBI>(g, it.va_cur);
  }
  g_ops_at<C, TI, GI>(g, it, make_iseq<C::NOPS>{});
  GAP();
}
template <class C, int TI, int... Gs> DI void g_gaps(GState<C>& g, const GIter& it, iseq<Gs...>) { (g_gap<C, TI, Gs>(g, it), ...); }
template <class C, int TI> DI void g_tile(GState<C>& g, int t, unsigned& stg, unsigned& pstg) {
  unsigned nstg = stg + C::STAGE; if (nstg == C::NS * C::STAGE) nstg = 0;
  GIter it;
  it.va_cur = g.vrd + stg; it.va_nxt = g.vrd + nstg;
  it.sA = (unsigned)(C::STEPS * 1024) * (unsigned)(t + C::PDT); it.sA0 = it.sA - 1024u;
  it.m0_fill = g.m0base + pstg; it.sK_fill = g.ktileB * (unsigned)(t - 1 + C::NS);
  g_gaps<C, TI>(g, it, make_iseq<C::G>{});
  pstg = stg; stg = nstg;
}
template <class C, int... TIs> DI void g_body(GState<C>& g, int t, unsigned& stg, unsigned& pstg, iseq<TIs...>) { (g_tile<C, TIs>(g, t + TIs, stg, pstg), ...); }

template <class C, int I> DI void g_touch(GState<C>& g) {
  asm volatile("s_mov_b32 m0, %0\n\ts_nop 0\n\tglobal_load_lds_dword %1, %2" :: "s"(g.m0base + (unsigned)(C::NS - 1) * C::STAGE), "v"(g.tch_off), "s"(g.tch[I]) : "memory");
}
template <class C, int I> DI void g_kv(GState<C>& g) {
  constexpr int q = I & 3;
  if constexpr ((I >> 2) == 1) asm volatile("s_mov_b32 m0, %0\n\ts_nop 0\n\tbuffer_load_dwordx4 %1, %2, %3 offen lds" :: "s"(g.kvm0 + 0x8000u + 1024u * q), "v"(g.vv), "s"(g.srdV), "s"(g.kvso + 2048u * (q >> 1) + 128u * (q & 1)) : "memory");
  else asm volatile("s_mov_b32 m0, %0\n\ts_nop 0\n\tbuffer_load_dwordx4 %1, %2, %3 offen offset:%c4 lds" :: "s"(g.kvm0 + (I >> 3) * 0x4000u), "v"(g.vk[q & 1]), "s"(g.srdK), "s"(g.kvso + (I >> 3) * 16384u), "i"(1024 * q) : "memory");
}
template <class C> constexpr int dummy_index(int V, int J) {
  const int ks = C::op_step(J), i = J - C::step_first(ks);
  if (i >= C::MR || (C::STEPS * (V + C::PDT) + ks - 1) >= 0) return -1;
  return (V + (C::NS - 1)) * C::STEPS * C::MR + ks * C::MR + i;
}
template <class C, int V, int J> DI void g_virt_op(GState<C>& g, const GIter& it) {
  constexpr int ks = C::op_step(J);
  constexpr int TI = ((V % C::PDT) + C::PDT) % C::PDT;
  constexpr bool VA = (C::STEPS * (V + C::PDT) + ks - 1) >= 0, VB = (V - 1 + C::NS) >= 0;
  if constexpr (V == -1 && J == C::ops_before(C::GB)) asm volatile("s_waitcnt vmcnt(%0)\n\ts_barrier" :: "i"(C::NBW()) : "memory");
  constexpr int di = C::ASMACC ? dummy_index<C>(V, J) : -1;
  if constexpr (di >= 0 && di < 3) g_touch<C, di>(g);
  else if constexpr (di >= KV_FIRST && di < KV_FIRST + 12) g_kv<C, di - KV_FIRST>(g);
  else g_op<C, TI, J, VA, VB>(g, it);
}
template <class C, int V, int... Js> DI void g_virt(GState<C>& g, iseq<Js...>) {
  GIter it; constexpr int tb = V - 1 + C::NS;
  it.va_cur = it.va_nxt = 0; it.sA = (unsigned)(C::STEPS * 1024) * (unsigned)(V + C::PDT >= 0 ? V + C::PDT : 0); it.sA0 = it.sA - 1024u;
  it.m0_fill = g.m0base + (unsigned)(tb >= 0 ? tb : 0) * C::STAGE; it.sK_fill = g.ktileB * (unsigned)(tb >= 0 ? tb : 0);
  (g_virt_op<C, V, Js>(g, it), ...);
  if constexpr (V == -1 && C::ops_before(C::GB) == C::NOPS) asm volatile("s_waitcnt vmcnt(%0)\n\ts_barrier" :: "i"(C::NBW()) : "memory");
}
template <class C, int... Vs> DI void g_prologue(GState<C>& g, iseq<Vs...>) { constexpr int NV = sizeof...(Vs); (g_virt<C, Vs - NV>(g, make_iseq<C::NOPS>{}), ...); }

template <class C, bool PSWAP = false> DI void g_setup(GState<C>& g, unsigned ldsb, unsigned lane, unsigned wid, unsigned ldb) {
  g.m0base = rfl(ldsb + wid * (unsigned)C::PB * 1024u);
  g.ktileB = 64u * ldb;
  const unsigned q = (lane & 15u) >> 2, p0 = lane & 3u, p = PSWAP ? (((p0 & 1u) << 1) | (p0 >> 1)) : p0, g4 = lane >> 4;
  if constexpr (C::SH == 32) {
    const unsigned h = lane >> 5, blk = g4 & 1u;
    g.vrd = ldsb + blk * (C::GU ? (unsigned)C::REGION : 256u) + ((4u * h + q) ^ (4u * blk)) * 32u + 8u * p;
  } else {
    g.vrd = ldsb + (g4 >> 1) * (C::KORD == 0 ? 1u : 2u) * (unsigned)(C::NR16 * 256) + (4u * (g4 & 1u) + q) * 32u + 8u * p;
  }
  if constexpr (C::GU) {
    const unsigned wr = wid % (C::NW / 2), up = wid / (C::NW / 2);
#pragma unroll
    for (int qq = 0; qq < C::PB; ++qq) {
      const unsigned P = wr * C::PB + qq, s = 4u * P + g4 * 0u + (lane >> 4), kb = s / (unsigned)C::NR16, nb = s % (unsigned)C::NR16;
      const unsigned row = ((lane >> 1) & 7u) ^ ((C::SH == 32 && up) ? 4u : 0u);
      g.voffB[qq] = (P < (unsigned)(C::NR16 * 2)) ? (8u * kb + row) * ldb + 32u * nb + 16u * (lane & 1u) : 0x80000000u;
    }
  } else {
    static_assert(C::GU || (C::NR16 % 4 == 0), "");
    const unsigned sub = lane >> 4;
    const unsigned row = ((lane >> 1) & 7u) ^ ((C::SH == 32 && (sub & 1u)) ? 4u : 0u);
    g.voffB[0] = row * ldb + 32u * sub + 16u * (lane & 1u);
#pragma unroll
    for (int qq = 0; qq < C::PB; ++qq) { const unsigned P = wid * C::PB + qq, kb = P / (unsigned)(C::NR16 / 4), j = P % (unsigned)(C::NR16 / 4); g.sB[qq] = rfl(8u * kb * ldb + 128u * j); }
  }
}

template <int R> DI void acc_zero1() { asm volatile("v_accvgpr_write_b32 a[%c0], 0" :: "i"(R) : W4_ACLOB); }
template <int... Rs> DI void acc_zero(iseq<Rs...>) { (acc_zero1<Rs>(), ...); asm volatile("s_nop 1"); }
template <class C> DI void gemm_run(GState<C>& g, int NT) {
  constexpr int NV = C::PDT > C::NS - 1 ? C::PDT : C::NS - 1;
  if constexpr (!C::ASMACC) {
#pragma unroll
    for (int n = 0; n < C::NB; ++n)
#pragma unroll
      for (int m = 0; m < C::MR; ++m)
#pragma unroll
        for (int e = 0; e < (C::SH == 32 ? 16 : 4); ++e) g.acc[n][m][e] = 0.f;
  }
  g_prologue<C>(g, make_iseq<NV>{});
  if constexpr (C::ASMACC) acc_zero(make_iseq<C::NB * C::MR * (C::SH == 32 ? 16 : 4)>{});
  b_read_all<C, 0, 0>(g, g.vrd, make_iseq<C::NB>{});
  if constexpr (C::FD == 2) b_read_all<C, 1, 1>(g, g.vrd, make_iseq<C::NB>{});
  asm volatile("s_nop 1");
  GAP();
  unsigned stg = 0, pstg = (C::NS - 1) * C::STAGE;
  for (int t = 0; t < NT; t += C::PDT) g_body<C>(g, t, stg, pstg, make_iseq<C::PDT>{});
  asm volatile("s_waitcnt vmcnt(0) lgkmcnt(0)\n\ts_nop 7\n\ts_nop 7" ::: "memory");
#pragma unroll
  for (int i = 0; i < C::PDT * C::STEPS; ++i)
#pragma unroll
    for (int m = 0; m < C::MR; ++m) asm volatile("" :: "v"(g.a[i][m]));
  GAP();
}
typedef __bf16 abf16x8 __attribute__((ext_vector_type(8)));
typedef __bf16 abf16x4 __attribute__((ext_vector_type(4)));
#define ATTN_THR 64.0f
constexpr unsigned KRS = 256u, KTILEB = 64u * KRS;
#define KRING 0x0000u
#define VRING 0x8000u
#define QGEMM_LDS 0x10000u
DI float max3f(float a, float b, float c) { float r; asm("v_max3_f32 %0, %1, %2, %3" : "=v"(r) : "v"(a), "v"(b), "v"(c)); return r; }
DI float max2f(float a, float b) { float r; asm("v_max_f32_e32 %0, %1, %2" : "=v"(r) : "v"(a), "v"(b)); return r; }
DI float fadd_s(float a, float b) { float r; asm("v_add_f32_e32 %0, %1, %2" : "=v"(r) : "v"(a), "v"(b)); return r; }
DI float fmul_s(float a, float b) { float r; asm("v_mul_f32_e32 %0, %1, %2" : "=v"(r) : "v"(a), "v"(b)); return r; }
DI float fsub_s(float a, float b) { float r; asm("v_sub_f32_e32 %0, %1, %2" : "=v"(r) : "v"(a), "v"(b)); return r; }
DI float w4_aone() { unsigned l; asm volatile("v_mbcnt_lo_u32_b32 %0, -1, 0\n\tv_mbcnt_hi_u32_b32 %0, -1, %0" : "=v"(l)); return l < 32u ? 1.f : 0.f; }
DI void negm_bcast(f32x16& negm, float aone, float neg_mhat) {
  asm volatile("s_nop 1\n\tv_mfma_f32_32x32x2_f32 %0, %1, %2, 0\n\ts_nop 7\n\ts_nop 7\n\ts_nop 3" : "+v"(negm) : "v"(aone), "v"(neg_mhat));
}
struct W4C {
  unsigned vkb[4], vvb, vksrc[2], vvsrc[4], m0k, m0v, sofk, sofv;
  __amdgpu_buffer_rsrc_t srdK, srdV;
  f32x16 S[2][2][2];
  f32x16 NEGM[2];
  abf16x8 Vf[4][4];
  unsigned pk[2][4][4];
  abf16x4 vlo[4][4], vup[4][4];
  float mhat[2], fct[2]; bool pend;
  float rs[2][2];
  float mA, mB;
  unsigned sk_m0, sk_so, sv_m0, sv_so;
  unsigned pf_lo, pf_hi, pf_off, pf_m0;
};
constexpr int AO_(int db, int qb) { return 16 * (2 * db + qb); }
constexpr int AQ_(int qb, int d0) { return 128 + 32 * qb + 4 * d0; }
constexpr int AK_(int kb, int d0) { return 192 + 32 * kb + 4 * d0; }

template <int I> DI void tr_read(W4C& c, unsigned va) {
  constexpr int ks = I >> 3, db = (I >> 1) & 3;
  if constexpr ((I & 1) == 0) asm volatile("ds_read_b64_tr_b16 %0, %1 offset:%2" : "=v"(c.vlo[db][ks]) : "v"(va), "i"(4096 * ks + 512 * db) : "memory");
  else                        asm volatile("ds_read_b64_tr_b16 %0, %1 offset:%2" : "=v"(c.vup[db][ks]) : "v"(va), "i"(4096 * ks + 2048 + 512 * db) : "memory");
}
template <int I, int SLOT> DI void k_read(W4C& c) {
  constexpr int kb = I >> 3, d0 = I & 7;
  asm volatile("ds_read_b128 a[%c1:%c2], %0 offset:%c3" :: "v"(c.vkb[d0 & 3]), "i"(AK_(kb, d0)), "i"(AK_(kb, d0) + 3), "i"(SLOT * 0x4000 + 8192 * kb + 128 * (d0 >> 2)) : "memory");
}
template <int X, bool FIRST, int M> DI void qk_mfma(W4C& c) {
  constexpr int d0 = M >> 2, kb = (M >> 1) & 1, qb = M & 1, ak = AK_(kb, d0), aq = AQ_(qb, d0);
  if constexpr (d0 == 0) {
    if constexpr (FIRST) asm volatile("v_mfma_f32_32x32x16_bf16 %0, a[%c1:%c2], a[%c3:%c4], 0" : "=&v"(c.S[X][kb][qb]) : "i"(ak), "i"(ak + 3), "i"(aq), "i"(aq + 3));
    else                 asm volatile("v_mfma_f32_32x32x16_bf16 %0, a[%c2:%c3], a[%c4:%c5], %1" : "=&v"(c.S[X][kb][qb]) : "v"(c.NEGM[qb]), "i"(ak), "i"(ak + 3), "i"(aq), "i"(aq + 3));
  } else asm volatile("v_mfma_f32_32x32x16_bf16 %0, a[%c1:%c2], a[%c3:%c4], %0" : "+v"(c.S[X][kb][qb]) : "i"(ak), "i"(ak + 3), "i"(aq), "i"(aq + 3));
}
template <int M, bool ZERO> DI void pv_mfma(W4C& c) {
  constexpr int ks = M >> 3, db = (M >> 1) & 3, qb = M & 1, ao = AO_(db, qb);
  const u32x4 pw = {c.pk[qb][ks][0], c.pk[qb][ks][1], c.pk[qb][ks][2], c.pk[qb][ks][3]};
  if constexpr (ZERO && ks == 0) asm volatile("v_mfma_f32_32x32x16_bf16 a[%c2:%c3], %0, %1, 0" :: "v"(c.Vf[db][ks]), "v"(pw), "i"(ao), "i"(ao + 15));
  else                         asm volatile("v_mfma_f32_32x32x16_bf16 a[%c2:%c3], %0, %1, a[%c2:%c3]" :: "v"(c.Vf[db][ks]), "v"(pw), "i"(ao), "i"(ao + 15));
}
template <int X, int KB, int E> DI void op_exp(W4C& c) { c.S[X][KB][E >> 4][E & 15] = __builtin_amdgcn_exp2f(c.S[X][KB][E >> 4][E & 15]); }
template <int X, int KB, int E> DI void op_add(W4C& c) { asm volatile("v_add_f32_e32 %0, %0, %1" : "+v"(c.rs[E >> 4][E & 1]) : "v"(c.S[X][KB][E >> 4][E & 15])); }
template <int X, int KB, int J> DI void op_cvt(W4C& c) {
  constexpr int qb = J >> 3, ks = 2 * KB + ((J >> 2) & 1), p = J & 3, r = 2 * (J & 7);
  c.pk[qb][ks][p] = cvtpk(c.S[X][KB][qb][r], c.S[X][KB][qb][r + 1]);
  if constexpr (KB == 1) asm volatile("" : "+v"(c.pk[qb][ks][p]));
}
template <int X, int I> DI float sval(W4C& c) { return c.S[X][I >> 5][(I >> 4) & 1][I & 15]; }
template <int X, int G> DI void op_max4(W4C& c) {
  if constexpr (G == 0) asm volatile("v_max3_f32 %0, %2, %3, %4\n\tv_max3_f32 %1, %5, %6, %7\n\tv_max3_f32 %0, %0, %8, %9" : "=&v"(c.mA), "=&v"(c.mB)
                                     : "v"(sval<X, 0>(c)), "v"(sval<X, 1>(c)), "v"(sval<X, 2>(c)), "v"(sval<X, 3>(c)), "v"(sval<X, 4>(c)), "v"(sval<X, 5>(c)), "v"(sval<X, 6>(c)), "v"(sval<X, 7>(c)));
  else asm volatile("v_max3_f32 %1, %1, %2, %3\n\tv_max3_f32 %0, %0, %4, %5\n\tv_max3_f32 %1, %1, %6, %7\n\tv_max3_f32 %0, %0, %8, %9" : "+v"(c.mA), "+v"(c.mB)
                    : "v"(sval<X, 8 * G>(c)), "v"(sval<X, 8 * G + 1>(c)), "v"(sval<X, 8 * G + 2>(c)), "v"(sval<X, 8 * G + 3>(c)), "v"(sval<X, 8 * G + 4>(c)), "v"(sval<X, 8 * G + 5>(c)), "v"(sval<X, 8 * G + 6>(c)), "v"(sval<X, 8 * G + 7>(c)));
}
constexpr int EB0 = 10;
constexpr int eb_lo(int g) { return g <= EB0 ? 0 : (32 * (g - EB0)) / (32 - EB0); }
template <int X, bool FIRST, int M> DI void gapA(W4C& c, unsigned va, const int t) {
  constexpr int Xp = X ^ 1;
  qk_mfma<X, FIRST, M>(c);
  GAP();
  if constexpr (!FIRST) {
    op_exp<Xp, 1, M>(c);
    constexpr int base = M + (M > 16) + (M > 18) + (M > 20) + (M > 22);
    if constexpr (M <= 27) tr_read<base>(c, va);
    if constexpr (M < 16) op_cvt<Xp, 0, M>(c);
    else if constexpr (M >= 17 && M <= 23 && (M & 1)) op_cvt<Xp, 1, (M - 17) / 2>(c);
    else if constexpr (M >= 25 && M <= 28) op_cvt<Xp, 1, 8 + M - 25>(c);
    if constexpr (M == 16 || M == 18 || M == 20 || M == 22) tr_read<base + 1>(c, va);
    if constexpr (M == 0) { op_add<Xp, 0, eb_lo(31)>(c); if constexpr (eb_lo(31) + 1 < 32) op_add<Xp, 0, eb_lo(31) + 1>(c); }
    else op_add<Xp, 1, M - 1>(c);
  }
  if constexpr (M == 29) { c.sk_m0 = c.m0k + (unsigned)X * 0x4000u; c.sk_so = KTILEB * (unsigned)(t + 2) + c.sofk; asm volatile("" : "+s"(c.sk_m0), "+s"(c.sk_so)); }
  if constexpr (M == 30) { c.sv_m0 = c.m0v + (unsigned)Xp * 0x4000u; c.sv_so = KTILEB * (unsigned)(t + 1) + c.sofv; asm volatile("" : "+s"(c.sv_m0), "+s"(c.sv_so)); }
  if constexpr (M == 31) asm volatile("s_mov_b32 m0, %0" :: "s"(c.sk_m0) : "memory");
  if constexpr (!FIRST && M == 8) asm volatile("" :: "v"(c.NEGM[0]), "v"(c.NEGM[1]));
  GAP();
}
#define W4_VLO "+v"(c.vlo[0][0]), "+v"(c.vlo[0][1]), "+v"(c.vlo[0][2]), "+v"(c.vlo[0][3]), "+v"(c.vlo[1][0]), "+v"(c.vlo[1][1]), "+v"(c.vlo[1][2]), "+v"(c.vlo[1][3]), \
               "+v"(c.vlo[2][0]), "+v"(c.vlo[2][1]), "+v"(c.vlo[2][2]), "+v"(c.vlo[2][3]), "+v"(c.vlo[3][0]), "+v"(c.vlo[3][1]), "+v"(c.vlo[3][2]), "+v"(c.vlo[3][3])
#define W4_VUP "+v"(c.vup[0][0]), "+v"(c.vup[0][1]), "+v"(c.vup[0][2]), "+v"(c.vup[0][3]), "+v"(c.vup[1][0]), "+v"(c.vup[1][1]), "+v"(c.vup[1][2]), "+v"(c.vup[1][3]), \
               "+v"(c.vup[2][0]), "+v"(c.vup[2][1]), "+v"(c.vup[2][2]), "+v"(c.vup[2][3]), "+v"(c.vup[3][0]), "+v"(c.vup[3][1]), "+v"(c.vup[3][2]), "+v"(c.vup[3][3])
template <int X, bool FIRST, int... Ms> DI void phaseA_(W4C& c, const int t, iseq<Ms...>) {
  const unsigned va = c.vvb + (unsigned)(X ^ 1) * 0x4000u;
  (gapA<X, FIRST, Ms>(c, va, t), ...);
  if constexpr (FIRST) asm volatile("s_waitcnt vmcnt(0) lgkmcnt(0)\n\ts_barrier\n\ts_nop 7" ::: "memory");
  else { asm volatile("s_waitcnt vmcnt(%c16) lgkmcnt(0)" : W4_VLO : "i"(X == 1 ? 1 : 0) : "memory"); asm volatile("s_barrier\n\ts_nop 1" : W4_VUP :: "memory"); }
  GAP();
  if constexpr (!FIRST) {
#pragma unroll
    for (int ks = 0; ks < 4; ++ks)
#pragma unroll
      for (int db = 0; db < 4; ++db) c.Vf[db][ks] = __builtin_shufflevector(c.vlo[db][ks], c.vup[db][ks], 0, 1, 2, 3, 4, 5, 6, 7);
  }
}
template <int X, bool FIRST> DI void phaseA(W4C& c, const int t) { phaseA_<X, FIRST>(c, t, make_iseq<32>{}); }

template <int X, bool INIT> DI void new_reference(W4C& c) {
#pragma unroll
  for (int q = 0; q < 2; ++q) {
    float m0 = max3f(c.S[X][0][q][0], c.S[X][0][q][1], c.S[X][1][q][0]), m1 = max2f(c.S[X][1][q][1], c.S[X][0][q][2]);
#pragma unroll
    for (int e = 2; e < 16; e += 2) { m0 = max3f(m0, c.S[X][0][q][e], c.S[X][0][q][e + 1]); m1 = max3f(m1, c.S[X][1][q][e], c.S[X][1][q][e + 1]); }
    const float mm = max2f(m0, m1);
    auto pr = __builtin_amdgcn_permlane32_swap(__float_as_uint(mm), __float_as_uint(mm), false, false);
    const float rmax = max2f(__uint_as_float(pr[0]), __uint_as_float(pr[1]));
    const float dl = INIT ? rmax : max2f(rmax, 0.f);
    if constexpr (INIT) { c.mhat[q] = dl; c.rs[q][0] = 0.f; c.rs[q][1] = 0.f; c.fct[q] = 1.f; }
    else { c.mhat[q] = fadd_s(c.mhat[q], dl); c.fct[q] = __builtin_amdgcn_exp2f(-dl); c.rs[q][0] = c.rs[q][0] * c.fct[q]; c.rs[q][1] = c.rs[q][1] * c.fct[q]; }
#pragma unroll
    for (int kb = 0; kb < 2; ++kb) { _Pragma("unroll") for (int e = 0; e < 16; ++e) c.S[X][kb][q][e] = fsub_s(c.S[X][kb][q][e], dl); }
    negm_bcast(c.NEGM[q], w4_aone(), -c.mhat[q]);
  }
  asm volatile("s_nop 1");
}
template <int X, bool PEEL, bool ZERO1, int M> DI void gapB(W4C& c, const int t) {
  constexpr int Xp = X ^ 1;
  if constexpr (!PEEL) { pv_mfma<M, ZERO1>(c); GAP(); }
  if constexpr (!PEEL && M == 0) op_add<Xp, 1, 31>(c);
  constexpr int kq = M == 0 ? 0 : M == 11 ? 1 : M == 13 ? 2 : M == 15 ? 3 : -1, vq = M == 18 ? 0 : M == 20 ? 1 : M == 22 ? 2 : M == 24 ? 3 : -1;
  if constexpr (kq >= 0) asm volatile("buffer_load_dwordx4 %0, %1, %2 offen offset:%c3 lds" :: "v"(c.vksrc[kq & 1]), "s"(c.srdK), "s"(c.sk_so), "i"(1024 * kq) : "memory");
  if constexpr (M == 16) asm volatile("s_mov_b32 m0, %0" :: "s"(c.sv_m0) : "memory");
  if constexpr (vq >= 0) asm volatile("buffer_load_dwordx4 %0, %1, %2 offen offset:%c3 lds" :: "v"(c.vvsrc[vq]), "s"(c.srdV), "s"(c.sv_so), "i"(1024 * vq) : "memory");
  if constexpr (M >= 1 && M <= 8) {
    if constexpr (!PEEL) { op_max4<X, M - 1>(c); op_cvt<Xp, 1, (M <= 4 ? 4 + M - 1 : 12 + M - 5)>(c); }
    k_read<M - 1, Xp>(c);
  }
  if constexpr (M == 9) {
    if constexpr (PEEL) new_reference<X, true>(c);
    else {
      const float mm = max2f(c.mA, c.mB);
      if (__builtin_expect(__any((int)(mm > ATTN_THR)), 0)) { new_reference<X, false>(c); c.pend = true; }
    }
  }
  if constexpr (M >= EB0) {
    constexpr int e0 = eb_lo(M), e1 = eb_lo(M + 1), p0 = eb_lo(M - 1);
    op_exp<X, 0, e0>(c);
    if constexpr (M > EB0) op_add<X, 0, p0>(c);
    if constexpr (e0 + 1 < e1) op_exp<X, 0, e0 + 1>(c);
    if constexpr (M > EB0 && p0 + 1 < e0) op_add<X, 0, p0 + 1>(c);
    constexpr int kr = M == 10 ? 8 : M == 12 ? 9 : M == 14 ? 10 : M == 17 ? 11 : M == 19 ? 12 : M == 21 ? 13 : M == 23 ? 14 : M == 25 ? 15 : -1;
    if constexpr (kr >= 0) k_read<kr, Xp>(c);
  }
  if constexpr (X == 0 && M == 26) {
    unsigned plo, phi;
    asm volatile("v_readlane_b32 %0, %2, %4\n\tv_readlane_b32 %1, %3, %4\n\ts_mov_b32 m0, %5" : "=s"(plo), "=s"(phi) : "v"(c.pf_lo), "v"(c.pf_hi), "s"((unsigned)(t >> 1)), "s"(c.pf_m0) : "memory");
    c.sk_m0 = plo; c.sk_so = phi;
  }
  if constexpr (X == 0 && M == 28) {
    const unsigned long long pb = ((unsigned long long)c.sk_so << 32) | c.sk_m0;
    asm volatile("global_load_lds_dword %0, %1" :: "v"(c.pf_off), "s"(pb) : "memory");
  }
  if constexpr (M == 31) asm volatile("" :: "v"(c.S[X][0][0]), "v"(c.S[X][0][1]));
  GAP();
}
template <int R> DI void resc4(float f) {
  float t0, t1, t2, t3;
  asm volatile("v_accvgpr_read_b32 %0, a[%c5]\n\tv_accvgpr_read_b32 %1, a[%c6]\n\tv_accvgpr_read_b32 %2, a[%c7]\n\tv_accvgpr_read_b32 %3, a[%c8]\n\t"
               "v_mul_f32 %0, %4, %0\n\tv_mul_f32 %1, %4, %1\n\tv_mul_f32 %2, %4, %2\n\tv_mul_f32 %3, %4, %3\n\t"
               "v_accvgpr_write_b32 a[%c5], %0\n\tv_accvgpr_write_b32 a[%c6], %1\n\tv_accvgpr_write_b32 a[%c7], %2\n\tv_accvgpr_write_b32 a[%c8], %3"
               : "=&v"(t0), "=&v"(t1), "=&v"(t2), "=&v"(t3) : "v"(f), "i"(R), "i"(R + 1), "i"(R + 2), "i"(R + 3) : W4_ACLOB);
}
template <int... Is> DI void resc_all(W4C& c, iseq<Is...>) { (resc4<4 * Is>(c.fct[(Is >> 2) & 1]), ...); }
template <int X, bool PEEL, bool ZERO1, int... Ms> DI void phaseB_(W4C& c, const int t, iseq<Ms...>) {
  (gapB<X, PEEL, ZERO1, Ms>(c, t), ...);
  if constexpr (!PEEL) {
    if (__builtin_expect(c.pend, 0)) {
      asm volatile("s_nop 7\n\ts_nop 7" ::: W4_ACLOB);
      resc_all(c, make_iseq<32>{});
      asm volatile("s_nop 1" ::: W4_ACLOB);
      c.pend = false;
    }
  }
  asm volatile("s_waitcnt lgkmcnt(0)" ::: "memory");
  GAP();
}
template <int X, bool PEEL, bool ZERO1> DI void phaseB(W4C& c, const int t) { phaseB_<X, PEEL, ZERO1>(c, t, make_iseq<32>{}); }
template <int... Is> DI void k_reads0(W4C& c, iseq<Is...>) { (k_read<Is, 0>(c), ...); }
template <int... Is> DI void tr_reads(W4C& c, unsigned va, iseq<Is...>) { (tr_read<Is>(c, va), ...); }
template <int... Is> DI void drain_exp(W4C& c, iseq<Is...>) { (op_exp<1, 1, Is>(c), ...); }
template <int... Is> DI void drain_cvt0(W4C& c, iseq<Is...>) { (op_cvt<1, 0, Is>(c), ...); }
template <int... Is> DI void drain_cvt1(W4C& c, iseq<Is...>) { (op_cvt<1, 1, Is>(c), ...); }
template <int... Is> DI void drain_add(W4C& c, iseq<Is...>) { (op_add<1, 1, Is>(c), ...); }
template <int... Ms> DI void pv_all(W4C& c, iseq<Ms...>) { (pv_mfma<Ms, false>(c), ...); }
template <int R> DI u32x4 acc_pack8(float f) {
  float o[8];
  asm volatile("v_accvgpr_read_b32 %0, a[%c8]\n\tv_accvgpr_read_b32 %1, a[%c9]\n\tv_accvgpr_read_b32 %2, a[%c10]\n\tv_accvgpr_read_b32 %3, a[%c11]\n\t"
               "v_accvgpr_read_b32 %4, a[%c12]\n\tv_accvgpr_read_b32 %5, a[%c13]\n\tv_accvgpr_read_b32 %6, a[%c14]\n\tv_accvgpr_read_b32 %7, a[%c15]"
               : "=v"(o[0]), "=v"(o[1]), "=v"(o[2]), "=v"(o[3]), "=v"(o[4]), "=v"(o[5]), "=v"(o[6]), "=v"(o[7])
               : "i"(R), "i"(R + 1), "i"(R + 2), "i"(R + 3), "i"(R + 4), "i"(R + 5), "i"(R + 6), "i"(R + 7));
  u32x4 w = {cvtpk(o[0] * f, o[1] * f), cvtpk(o[2] * f, o[3] * f), cvtpk(o[4] * f, o[5] * f), cvtpk(o[6] * f, o[7] * f)};
  return w;
}
template <int I> DI void q_conv1(float C) {
  constexpr int qb = I >> 3, d0 = I & 7, aq = AQ_(qb, d0);
  const u32x4 w = acc_pack8<4 * (4 * d0 + 2 * qb)>(C);
  auto s0 = __builtin_amdgcn_permlane16_swap(w[0], w[2], false, false); auto s1 = __builtin_amdgcn_permlane16_swap(w[1], w[3], false, false);
  asm volatile("v_accvgpr_write_b32 a[%c4], %0\n\tv_accvgpr_write_b32 a[%c5], %1\n\tv_accvgpr_write_b32 a[%c6], %2\n\tv_accvgpr_write_b32 a[%c7], %3"
               :: "v"(s0[0]), "v"(s1[0]), "v"(s0[1]), "v"(s1[1]), "i"(aq), "i"(aq + 1), "i"(aq + 2), "i"(aq + 3) : W4_ACLOB_HI);
}
template <int... Is> DI void q_conv(float C, iseq<Is...>) { (q_conv1<Is>(C), ...); }
template <int I> DI void o_store1(__amdgpu_buffer_rsrc_t srd, unsigned vo, float il0, float il1) {
  constexpr int qb = I >> 3, db = (I >> 1) & 3, s = I & 1;
  const u32x4 w = acc_pack8<AO_(db, qb) + 8 * s>(qb ? il1 : il0);
  __builtin_amdgcn_raw_buffer_store_b128(w, srd, vo + qb * (256u * 1024u) + db * 1024 + s * 512, 0, WT);
}
template <int... Is> DI void o_store(__amdgpu_buffer_rsrc_t srd, unsigned vo, float il0, float il1, iseq<Is...>) { (o_store1<Is>(srd, vo, il0, il1), ...); }

using QCfg = GCfg<16, 4, 64, 8, false, 1, 6, 2, 1, 1, true>;
constexpr int SHM_ATTN = 0x10000 + 6 * 16384;

#define ST(i)
__global__ __launch_bounds__(256, 1) void k_attn(const u16* __restrict__ H1f, const u16* __restrict__ Wq, const u16* __restrict__ Kc, const u16* __restrict__ Vc, u16* __restrict__ Of,
                                                        const u16* __restrict__ Wo, const u16* __restrict__ Wg, const u16* __restrict__ Wu, const u16* __restrict__ Wd, const u16* __restrict__ Wm) {
  ST(0); KST_BEGIN(2);
  extern __shared__ __attribute__((aligned(16))) char lds[];
  const unsigned tid = threadIdx.x, wid = rfl(tid >> 6);
  const int bid = blockIdx.x, xcd = bid & 7, j = bid >> 3;
  const int head = 4 * xcd + (j & 3), y = j >> 2, kvh = head >> 3;
  const unsigned ldsb = (unsigned)(size_t)lds;
  {
    const unsigned lane = tid & 63u;
    GState<QCfg> g;
    const int mb0 = y * 16 + wid * 4;
    constexpr unsigned mbstride = 1024u * 32u;
    g.srdA = mk_srd((const char*)H1f + (size_t)mb0 * mbstride, 4 * mbstride);
#pragma unroll
    for (int m = 0; m < 4; ++m) g.voffA[m] = lane * 16u + m * mbstride;
    constexpr unsigned ldb = 4096u * 2u;
    g.srdB = mk_srd((const char*)Wq + (size_t)head * 256, 1024u * ldb - (unsigned)head * 256u);
    g_setup<QCfg, false>(g, ldsb + QGEMM_LDS, lane, wid, ldb);
    { const unsigned gw = blockIdx.x * 4u + wid;
      g.tch[0] = (unsigned long long)(size_t)((const char*)Wq + (size_t)gw * 4096); g.tch[1] = g.tch[0] + (4096u * 1024u);
      g.tch[2] = (unsigned long long)(size_t)((gw < 512u ? (const char*)Kc : (const char*)Vc) + (size_t)(gw & 511u) * 4096);
      g.tch_off = lane * 64u; }
    g.srdK = mk_srd((const char*)Kc + (size_t)kvh * (2048u * 256u), 2048u * 256u);
    g.srdV = mk_srd((const char*)Vc + (size_t)kvh * (2048u * 256u), 2048u * 256u);
    g.kvm0 = rfl(ldsb + KRING + 4096u * wid); g.kvso = rfl(KRS * 16u * wid);
#pragma unroll
    for (int par = 0; par < 2; ++par) g.vk[par] = KRS * (lane >> 4) + 16u * ((lane & 15u) ^ (4u * par + (lane >> 4)));
    g.vv = KRS * ((lane >> 2) & 7u) + 64u * (lane >> 5) + 16u * (lane & 3u);
    gemm_run<QCfg>(g, 16);
  }
  ST(1);
  unsigned lane; asm volatile("v_mbcnt_lo_u32_b32 %0, -1, 0\n\tv_mbcnt_hi_u32_b32 %0, -1, %0" : "=v"(lane));
  const unsigned l32 = lane & 31u, hi = lane >> 5;
  constexpr int T = 32;
  W4C c;
#pragma unroll
  for (int jj = 0; jj < 4; ++jj) c.vkb[jj] = ldsb + KRING + 256u * l32 + 16u * ((2u * jj + hi) ^ (l32 & 7u));
  c.vvb = ldsb + VRING + (((lane & 3u) << 3) | (((lane >> 2) & 3u) << 6) | (((lane >> 4) & 1u) << 5) | (hi << 8));
#pragma unroll
  for (int par = 0; par < 2; ++par) c.vksrc[par] = KRS * (lane >> 4) + 16u * ((lane & 15u) ^ (4u * par + (lane >> 4)));
#pragma unroll
  for (int q = 0; q < 4; ++q) c.vvsrc[q] = KRS * ((lane >> 2) & 7u) + 64u * hi + 16u * (lane & 3u) + KRS * 8u * (q >> 1) + 128u * (q & 1) + (3072u - 1024u * q);
  c.m0k = rfl(ldsb + KRING + 4096u * wid); c.m0v = rfl(ldsb + VRING + 4096u * wid);
  c.sofk = rfl(KRS * 16u * wid); c.sofv = rfl(KRS * 16u * wid) - 3072u;
  c.srdK = mk_srd((const char*)Kc + (size_t)kvh * (2048u * 256u), 2048u * 256u);
  c.srdV = mk_srd((const char*)Vc + (size_t)kvh * (2048u * 256u), 2048u * 256u);
  {
    const unsigned gw = blockIdx.x * 4u + wid;
    const char* p;
    const unsigned c2 = gw + 1024u, c2c = c2 < 1792u ? c2 : gw;
    if (lane == 0) p = (const char*)Wo + (size_t)gw * 4096; else if (lane == 1) p = (const char*)Wo + (size_t)c2 * 4096;
    else if (lane == 2) p = (const char*)Wg + (size_t)gw * 4096; else if (lane == 3) p = (const char*)Wg + (size_t)c2c * 4096;
    else if (lane == 4) p = (const char*)Wu + (size_t)gw * 4096; else if (lane == 5) p = (const char*)Wu + (size_t)c2c * 4096;
    else if (lane == 6) p = (const char*)Wd + (size_t)gw * 4096; else if (lane == 7) p = (const char*)Wd + (size_t)c2c * 4096;
    else if (lane < 11) p = (const char*)Wm + (size_t)(gw + 1024u * (lane - 8u)) * 4096;
    else p = (const char*)Wo + (size_t)gw * 4096;
    c.pf_lo = (unsigned)(size_t)p; c.pf_hi = (unsigned)((size_t)p >> 32);
    c.pf_off = lane * 64u; c.pf_m0 = rfl(ldsb + QGEMM_LDS + 256u * wid);
  }
  asm volatile("s_barrier" ::: "memory");
  k_reads0(c, make_iseq<16>{});
  q_conv(0.12751743082f, make_iseq<16>{});
  asm volatile("s_waitcnt lgkmcnt(0)\n\ts_nop 1" ::: "memory");
  c.mhat[0] = c.mhat[1] = 0.f; c.fct[0] = c.fct[1] = 1.f; c.pend = false; c.rs[0][0] = c.rs[0][1] = c.rs[1][0] = c.rs[1][1] = 0.f;
  ST(2);
  phaseA<0, true>(c, 0);  phaseB<0, true, false>(c, 0);
  phaseA<1, false>(c, 1); phaseB<1, false, true>(c, 1);
  for (int t = 2; t < T; t += 2) {
    phaseA<0, false>(c, t);     phaseB<0, false, false>(c, t);
    phaseA<1, false>(c, t + 1); phaseB<1, false, false>(c, t + 1);
  }
  ST(3);
  float lsum[2];
  {
    tr_reads(c, c.vvb + 0x4000u, make_iseq<32>{});
    op_add<1, 0, eb_lo(31)>(c); if constexpr (eb_lo(31) + 1 < 32) op_add<1, 0, eb_lo(31) + 1>(c);
    drain_exp(c, make_iseq<32>{});
    drain_cvt0(c, make_iseq<16>{});
    drain_cvt1(c, make_iseq<16>{});
    GAP(); asm volatile("s_nop 1"); GAP();
    drain_add(c, make_iseq<32>{});
    asm volatile("s_waitcnt lgkmcnt(0)" : W4_VLO :: "memory"); asm volatile("" : W4_VUP :: "memory");
    GAP();
#pragma unroll
    for (int ks = 0; ks < 4; ++ks)
#pragma unroll
      for (int db = 0; db < 4; ++db) c.Vf[db][ks] = __builtin_shufflevector(c.vlo[db][ks], c.vup[db][ks], 0, 1, 2, 3, 4, 5, 6, 7);
    asm volatile("s_nop 1" ::: "memory");
    pv_all(c, make_iseq<32>{});
#pragma unroll
    for (int qb = 0; qb < 2; ++qb) { const float rs = c.rs[qb][0] + c.rs[qb][1];
      auto pr = __builtin_amdgcn_permlane32_swap(__float_as_uint(rs), __float_as_uint(rs), false, false);
      lsum[qb] = __uint_as_float(pr[0]) + __uint_as_float(pr[1]); }
    asm volatile("s_nop 7\n\ts_nop 7" ::: "memory");
  }
  {
    unsigned lb; asm volatile("v_mbcnt_lo_u32_b32 %0, -1, 0\n\tv_mbcnt_hi_u32_b32 %0, -1, %0" : "=v"(lb));
    const float il0 = __builtin_amdgcn_rcpf(lsum[0]), il1 = __builtin_amdgcn_rcpf(lsum[1]);
    o_store(mk_srd(Of, 16u << 20), (((unsigned)(y * 16 + wid * 4) + ((lb >> 4) & 1u)) * 128u + (unsigned)head * 4u) * 1024u + (lb >> 5) * 256u + (lb & 15u) * 16u, il0, il1, make_iseq<16>{});
  }
  ST(4); KST_END(2);
}
template <int N> DI void touch(const char* p, int stride) {
  unsigned d[N];
#pragma unroll
  for (int i = 0; i < N; ++i) d[i] = *(const unsigned*)(p + (size_t)i * stride);
#pragma unroll
  for (int i = 0; i < N; ++i) asm volatile("" :: "v"(d[i]));
}

template <int NT, int KL> DI void mod_body(const u16* __restrict__ c, const u16* __restrict__ wmod, float* __restrict__ dst, int cb, int kh, float* red  ) {
  const int tid = threadIdx.x, cg = tid & 7, kg = tid >> 3, wv = tid >> 6;
  constexpr int NG = NT / 8, NI = KL / NG;
  u32x4 w[NI]; u16 cv[NI];
#pragma unroll
  for (int i = 0; i < NI; ++i) { const int k = kh * KL + kg + NG * i; w[i] = *(const u32x4*)(wmod + (size_t)k * 6144 + cb * 64 + cg * 8); cv[i] = c[k]; }
  float acc[8] = {0, 0, 0, 0, 0, 0, 0, 0};
#pragma unroll
  for (int i = 0; i < NI; ++i) {
    const float xx = bf2f(cv[i]), s = rbf(xx / (1.f + __expf(-xx)));
#pragma unroll
    for (int jj = 0; jj < 4; ++jj) { acc[2 * jj] += s * __uint_as_float(w[i][jj] << 16); acc[2 * jj + 1] += s * __uint_as_float(w[i][jj] & 0xffff0000u); }
  }
#pragma unroll
  for (int jj = 0; jj < 8; ++jj) { float a = acc[jj]; a += __shfl_xor(a, 8); a += __shfl_xor(a, 16); a += __shfl_xor(a, 32); acc[jj] = a; }
  if ((tid & 63) < 8) {
#pragma unroll
    for (int jj = 0; jj < 8; ++jj) red[wv * 64 + cg * 8 + jj] = acc[jj];
  }
  __syncthreads();
  if (tid < 64) { float s = 0;
#pragma unroll
    for (int g = 0; g < NT / 64; ++g) s += red[g * 64 + tid];
    dst[tid] = s; }
}
__global__ __launch_bounds__(512) void k_pre(const u16* __restrict__ c, const u16* __restrict__ wmod, float* __restrict__ modA, const u16* __restrict__ x, unsigned* __restrict__ flg) {
  __shared__ float red[8 * 64];
  const int b = blockIdx.x;
  KST_BEGIN(0);
  unsigned dx = 0;
  if (threadIdx.x == 0) flg[b * 64] = 0u;
  if (threadIdx.x < 256) dx = *(const unsigned*)((const char*)x + (size_t)b * 16384 + threadIdx.x * 64);
  mod_body<512, 128>(c, wmod, modA + (b & 7) * 2048 + (b >> 3) * 64, b >> 3, b & 7, red);
  asm volatile("" :: "v"(dx));
  KST_END(0);
}
DI f32x4 modv(const float* __restrict__ modp, int n) { const f32x4 a = *(const f32x4*)(modp + n), b = *(const f32x4*)(modp + 6144 + n); f32x4 o; for (int e = 0; e < 4; ++e) o[e] = rbf(a[e] + b[e]); return o; }
DI void unpack8(const u32x4& w, float* v) {
#pragma unroll
  for (int j = 0; j < 4; ++j) { v[2 * j] = __uint_as_float(w[j] << 16); v[2 * j + 1] = __uint_as_float(w[j] & 0xffff0000u); }
}
DI u32x4 pack8(const float* o) { return (u32x4){cvtpk(o[0], o[1]), cvtpk(o[2], o[3]), cvtpk(o[4], o[5]), cvtpk(o[6], o[7])}; }

DI void rbf2(float& a, float& b) { const unsigned w = cvtpk(a, b); a = __uint_as_float(w << 16); b = __uint_as_float(w & 0xffff0000u); }
DI void rbf8(float* v) { rbf2(v[0], v[1]); rbf2(v[2], v[3]); rbf2(v[4], v[5]); rbf2(v[6], v[7]); }
DI void modv8(const float* __restrict__ modp, int n, float* o) {
  const f32x4 a0 = *(const f32x4*)(modp + n), b0 = *(const f32x4*)(modp + 6144 + n), a1 = *(const f32x4*)(modp + n + 8), b1 = *(const f32x4*)(modp + 6144 + n + 8);
#pragma unroll
  for (int e = 0; e < 4; ++e) { o[e] = a0[e] + b0[e]; o[4 + e] = a1[e] + b1[e]; }
  rbf8(o);
}
template <int MODE>
__global__ __launch_bounds__(1024) void k_ln(const u16* __restrict__ x, const u16* __restrict__ Pf, const float* __restrict__ modp, u16* __restrict__ xf, u16* __restrict__ hf) {
  __shared__ float red[2][16][8];
  __shared__ __attribute__((aligned(16))) float msum[MODE == 0 ? 2048 : 4];
  const int tid = threadIdx.x;
  KST_BEGIN(MODE == 0 ? 1 : 4);
  const int r = tid & 7, c = tid >> 3, h = c & 1, wid = tid >> 6;
  const int row = blockIdx.x * 8 + r, mb = row >> 5, ml = row & 31;
  const size_t fo = ((size_t)mb * 64 + (c >> 1)) * 512 + (h * 32 + ml) * 8;
  const int n = 16 * (c >> 1) + 4 * h;
  float v[8], sh[8], sc[8];
  if (MODE == 0) {
    const u32x4 xw = *(const u32x4*)(x + (size_t)row * 1024 + 8 * c);
    { f32x2 a = {0.f, 0.f};
#pragma unroll
      for (int ks = 0; ks < 8; ++ks) a += *(const f32x2*)(modp + ks * 2048 + 2 * tid);
      float a0 = a[0], a1 = a[1]; rbf2(a0, a1); *(f32x2*)(msum + 2 * tid) = (f32x2){a0, a1}; }
    const unsigned s0 = h ? xw[0] : xw[2], s1 = h ? xw[1] : xw[3];
    const unsigned g0 = __shfl_xor(s0, 8), g1 = __shfl_xor(s1, 8);
    const u32x4 f = h ? (u32x4){g0, g1, xw[2], xw[3]} : (u32x4){xw[0], xw[1], g0, g1};
    __builtin_amdgcn_raw_buffer_store_b128(f, mk_srd(xf, 4u << 20), (unsigned)fo * 2u, 0, WT);
    unpack8(f, v);
  } else {
    const u32x4 xw = *(const u32x4*)(xf + fo), p0 = *(const u32x4*)(Pf + fo), p1 = *(const u32x4*)(Pf + fo + (size_t)2048 * 1024);
    float ga[8], a[8], b[8];
    modv8(modp, 3072 + n, sh); modv8(modp, 4096 + n, sc); modv8(modp, 2048 + n, ga);
    unpack8(xw, v); unpack8(p0, a); unpack8(p1, b);
#pragma unroll
    for (int j = 0; j < 8; ++j) a[j] += b[j];
    rbf8(a);
#pragma unroll
    for (int j = 0; j < 8; ++j) a[j] *= ga[j];
    rbf8(a);
#pragma unroll
    for (int j = 0; j < 8; ++j) v[j] += a[j];
    const u32x4 w = pack8(v);
    __builtin_amdgcn_raw_buffer_store_b128(w, mk_srd(xf, 4u << 20), (unsigned)fo * 2u, 0, WT);
    unpack8(w, v);
  }
  float s = ((v[0] + v[1]) + (v[2] + v[3])) + ((v[4] + v[5]) + (v[6] + v[7]));
  s += __shfl_xor(s, 8); s += __shfl_xor(s, 16); s += __shfl_xor(s, 32);
  if ((tid & 63) < 8) red[0][wid][r] = s;
  __syncthreads();
  if (MODE == 0) {
    const f32x4 a0 = *(const f32x4*)(msum + n), a1 = *(const f32x4*)(msum + n + 8), b0 = *(const f32x4*)(msum + 1024 + n), b1 = *(const f32x4*)(msum + 1024 + n + 8);
#pragma unroll
    for (int e = 0; e < 4; ++e) { sh[e] = a0[e]; sh[4 + e] = a1[e]; sc[e] = b0[e]; sc[4 + e] = b1[e]; }
  }
  float mean = 0;
#pragma unroll
  for (int w = 0; w < 16; ++w) mean += red[0][w][r];
  mean *= (1.f / 1024.f);
  float qv = 0;
#pragma unroll
  for (int j = 0; j < 8; ++j) { const float d = v[j] - mean; qv += d * d; }
  qv += __shfl_xor(qv, 8); qv += __shfl_xor(qv, 16); qv += __shfl_xor(qv, 32);
  if ((tid & 63) < 8) red[1][wid][r] = qv;
  __syncthreads();
  float var = 0;
#pragma unroll
  for (int w = 0; w < 16; ++w) var += red[1][w][r];
  var = rbf(var * (1.f / 1024.f));
  const float mb16 = rbf(mean), rsd = 1.f / rbf(sqrtf(rbf(var + 1e-5f)));
#pragma unroll
  for (int j = 0; j < 8; ++j) { v[j] -= mb16; sc[j] += 1.f; }
  rbf8(v); rbf8(sc);
#pragma unroll
  for (int j = 0; j < 8; ++j) v[j] *= rsd;
  rbf8(v);
#pragma unroll
  for (int j = 0; j < 8; ++j) v[j] *= sc[j];
  rbf8(v);
#pragma unroll
  for (int j = 0; j < 8; ++j) v[j] += sh[j];
  const unsigned ho = (((unsigned)(row >> 4) * 32u + (unsigned)(c >> 2)) * 1024u + (unsigned)(c & 3) * 256u + (unsigned)(row & 15) * 16u);
  __builtin_amdgcn_raw_buffer_store_b128(pack8(v), mk_srd(hf, 4u << 20), ho, 0, WT);
  KST_END(MODE == 0 ? 1 : 4);
}
using SCfg = GCfg<16, 4, 32, 8, false, 1, 4, 4, 1, 1>;
template <int K, bool FIN, int NT0>
__global__ __launch_bounds__(256, 2) void k_gemm_sk(const u16* __restrict__ Af, const u16* __restrict__ B, u16* __restrict__ Pf,
                                                    const u16* __restrict__ cvec, const u16* __restrict__ wmod, float* __restrict__ modp,
                                                    const u16* __restrict__ x1f, u16* __restrict__ out, unsigned* __restrict__ flg) {
  using C = SCfg;
  extern __shared__ __attribute__((aligned(16))) char lds[];
  KST_BEGIN(K == 4096 ? 3 : 6);
  if (K == 4096 && blockIdx.x >= 256) { const int b = blockIdx.x - 256; mod_body<256, 512>(cvec, wmod, modp + (b & 1) * 6144 + (32 + (b >> 1)) * 64, 32 + (b >> 1), b & 1, (float*)lds); KST_END(3); return; }
  constexpr int N = 1024;
  const unsigned tid = threadIdx.x, lane = tid & 63u, wid = rfl(tid >> 6);
  const int bid = blockIdx.x, xcd = bid & 7, j = bid >> 3;
  const int kh = xcd & 1, mg = xcd >> 1;
  const int mt = mg * 4 + (j & 3), nt = j >> 2;
  const int k0 = kh ? NT0 * 64 : 0, klen = kh ? K - NT0 * 64 : NT0 * 64;
  GState<C> g;
  const int mb0 = mt * 4 + wid;
  constexpr unsigned mbstride = (unsigned)K * 32u;
  g.srdA = mk_srd((const char*)Af + (size_t)(2 * mb0) * mbstride + (size_t)k0 * 32, mbstride + (unsigned)klen * 32u);
  g.voffA[0] = lane * 16u; g.voffA[1] = lane * 16u + mbstride;
  constexpr unsigned ldb = (unsigned)N * 2u;
  g.srdB = mk_srd((const char*)B + (size_t)k0 * ldb + (size_t)nt * 256, (unsigned)klen * ldb - (unsigned)nt * 256u);
  g_setup<C>(g, (unsigned)(size_t)lds, lane, wid, ldb);
  gemm_run<C>(g, klen / 64);
  const unsigned h = (lane >> 4) & 1u, hi = lane >> 5;
  const unsigned fo = (((unsigned)mb0 * 64u + (unsigned)nt * 8u + hi) * 512u + (h * 32u + (lane & 15u)) * 8u) * 2u;
  float acc[4][2][8];
#pragma unroll
  for (int i = 0; i < 4; ++i)
#pragma unroll
    for (int m = 0; m < 2; ++m)
#pragma unroll
      for (int e = 0; e < 4; ++e) {
        auto sw = __builtin_amdgcn_permlane32_swap(__float_as_uint(g.acc[2 * i][m][e]), __float_as_uint(g.acc[2 * i + 1][m][e]), false, false);
        acc[i][m][e] = __uint_as_float(sw[0]); acc[i][m][4 + e] = __uint_as_float(sw[1]);
      }
  if constexpr (!FIN) {
    const __amdgpu_buffer_rsrc_t srdP = mk_srd(Pf, 8u << 20);
#pragma unroll
    for (int i = 0; i < 4; ++i)
#pragma unroll
      for (int m = 0; m < 2; ++m) __builtin_amdgcn_raw_buffer_store_b128(pack8(acc[i][m]), srdP, (unsigned)kh * (4u << 20) + fo + i * 2048 + m * 256, 0, WT);
  } else {
    const __amdgpu_buffer_rsrc_t srdP = mk_srd(Pf, 4u << 20);
    unsigned* flag = flg + (128 + mt * 8 + nt) * 64;
    if (kh == 0) {
#pragma unroll
      for (int i = 0; i < 4; ++i)
#pragma unroll
        for (int m = 0; m < 2; ++m) __builtin_amdgcn_raw_buffer_store_b128(pack8(acc[i][m]), srdP, fo + i * 2048 + m * 256, 0, 16);
      asm volatile("s_waitcnt vmcnt(0)" ::: "memory");
      __syncthreads();
      if (tid == 0) __hip_atomic_store(flag, 1u, __ATOMIC_RELAXED, __HIP_MEMORY_SCOPE_AGENT);
    } else {
      u32x4 xw[4][2]; float gb[4][8];
#pragma unroll
      for (int i = 0; i < 4; ++i) { xw[i][0] = *(const u32x4*)((const char*)x1f + fo + i * 2048); xw[i][1] = *(const u32x4*)((const char*)x1f + fo + i * 2048 + 256);
        modv8(modp, 5120 + nt * 128 + 16 * (2 * i + hi) + 4 * h, gb[i]); }
      if (wid == 0) {
        for (unsigned spins = 0; spins < (1u << 22); ++spins) {
          if ((unsigned)__builtin_amdgcn_readfirstlane(__hip_atomic_load(flag, __ATOMIC_RELAXED, __HIP_MEMORY_SCOPE_AGENT)) != 0u) break;
          __builtin_amdgcn_s_sleep(1);
        }
      }
      asm volatile("" ::: "memory"); __builtin_amdgcn_s_barrier(); asm volatile("" ::: "memory");
      u32x4 pw[4][2];
#pragma unroll
      for (int i = 0; i < 4; ++i)
#pragma unroll
        for (int m = 0; m < 2; ++m) pw[i][m] = __builtin_amdgcn_raw_buffer_load_b128(srdP, fo + i * 2048 + m * 256, 0, 16);
      const unsigned oo = (((unsigned)mb0 * 32u + (lane & 15u)) * 1024u + (unsigned)nt * 128u + 16u * hi + 8u * h) * 2u;
      const __amdgpu_buffer_rsrc_t srdOut = mk_srd(out, 4u << 20);
#pragma unroll
      for (int i = 0; i < 4; ++i)
#pragma unroll
        for (int m = 0; m < 2; ++m) {
          float v[8], a[8];
          unpack8(xw[i][m], v); unpack8(pw[i][m], a);
#pragma unroll
          for (int jj = 0; jj < 8; ++jj) a[jj] += acc[i][m][jj];
          rbf8(a);
#pragma unroll
          for (int jj = 0; jj < 8; ++jj) a[jj] *= gb[i][jj];
          rbf8(a);
#pragma unroll
          for (int jj = 0; jj < 8; ++jj) v[jj] += a[jj];
          const u32x4 w = pack8(v);
          const unsigned s0 = h ? w[0] : w[2], s1 = h ? w[1] : w[3];
          const unsigned g0 = __shfl_xor(s0, 16), g1 = __shfl_xor(s1, 16);
          const u32x4 f = h ? (u32x4){g0, g1, w[2], w[3]} : (u32x4){w[0], w[1], g0, g1};
          __builtin_amdgcn_raw_buffer_store_b128(f, srdOut, oo + 64u * i + 32768u * m, 0, WT);
        }
    }
  }
  KST_END(K == 4096 ? 3 : 6);
}
#define SK2_NT0 28
DI float silu_mul(float g, float u) { return g * __builtin_amdgcn_rcpf(1.f + __builtin_amdgcn_exp2f(g * -1.4426950408889634f)) * u; }
#define U_NS 3
#define U_BP 3
using UCfg = GCfg<16, 8, 32, 14, true, 1, U_NS, 2, 1, U_BP>;
__global__ __launch_bounds__(512, 2) void k_gemm_gu(const u16* __restrict__ Af, const u16* __restrict__ Wg, const u16* __restrict__ Wu, u16* __restrict__ Act) {
  using C = UCfg;
  extern __shared__ __attribute__((aligned(16))) char lds[];
  constexpr int K = 1024, F = 3584;
  KST_BEGIN(5);
  const unsigned tid = threadIdx.x, wid = rfl(tid >> 6);
  const int bid = blockIdx.x, xcd = bid & 7, j = bid >> 3;
  const int mt = (xcd & 1) * 4 + (j & 3), nt = (xcd >> 1) * 8 + (j >> 2);
  GState<C> g;
  const int mb0 = mt * 8 + wid;
  {
    const unsigned lane = tid & 63u;
    constexpr unsigned mbstride = (unsigned)K * 32u;
    g.srdA = mk_srd((const char*)Af + (size_t)(2 * mb0) * mbstride, 2 * mbstride);
    g.voffA[0] = lane * 16u; g.voffA[1] = lane * 16u + mbstride;
    constexpr unsigned ldb = F * 2u;
    const bool up = wid >= 4;
    g.srdB = mk_srd((const char*)(up ? Wu : Wg) + (size_t)nt * 224, (unsigned)K * ldb - (unsigned)nt * 224u);
    g_setup<C>(g, (unsigned)(size_t)lds, lane, wid, ldb);
  }
  gemm_run<C>(g, K / 64);
  unsigned lane; asm volatile("v_mbcnt_lo_u32_b32 %0, -1, 0\n\tv_mbcnt_hi_u32_b32 %0, -1, %0" : "=v"(lane));
  const __amdgpu_buffer_rsrc_t srdO = mk_srd(Act, 2048u * F * 2u);
  const unsigned vo = ((unsigned)mb0 * 2u + (lane >> 5)) * (unsigned)((F >> 5) * 1024) + (lane & 31u) * 16u;
#pragma unroll
  for (int n = 0; n < 7; ++n) {
    unsigned q[2][2];
#pragma unroll
    for (int m = 0; m < 2; ++m) {
      float o[4];
#pragma unroll
      for (int e = 0; e < 4; ++e) o[e] = silu_mul(g.acc[2 * n][m][e], g.acc[2 * n + 1][m][e]);
      q[m][0] = cvtpk(o[0], o[1]); q[m][1] = cvtpk(o[2], o[3]);
    }
    auto s0 = __builtin_amdgcn_permlane32_swap(q[0][0], q[1][0], false, false); auto s1 = __builtin_amdgcn_permlane32_swap(q[0][1], q[1][1], false, false);
    const unsigned c16 = (unsigned)(nt * 7 + n);
    __builtin_amdgcn_raw_buffer_store_b128((u32x4){s0[0], s1[0], s0[1], s1[1]}, srdO, vo + (c16 >> 1) * 1024u + (c16 & 1u) * 512u, 0, WT);
  }
  KST_END(5);
}

extern "C" void kernel_launch(void* const* d_in, const int* in_sizes, int n_in, void* d_out, int out_size, void* d_ws, size_t ws_size, hipStream_t stream) {
  const u16* x = (const u16*)d_in[0]; const u16* kc = (const u16*)d_in[1]; const u16* vc = (const u16*)d_in[2];
  const u16* wq = (const u16*)d_in[3]; const u16* wo = (const u16*)d_in[4];
  const u16* wg = (const u16*)d_in[6]; const u16* wu = (const u16*)d_in[7]; const u16* wd = (const u16*)d_in[8];
  const u16* c = (const u16*)d_in[9]; const u16* wmod = (const u16*)d_in[10];
  u16* out = (u16*)d_out;
  char* ws = (char*)d_ws;
  float* mod = (float*)ws;
  u16* hf = (u16*)(ws + (1 << 20));
  u16* x1f = (u16*)(ws + (8 << 20));
  u16* of = (u16*)(ws + (16 << 20));
  u16* P = (u16*)(ws + (32 << 20));
  u16* act = (u16*)(ws + (64 << 20));
  hipFuncSetAttribute((const void*)k_attn, hipFuncAttributeMaxDynamicSharedMemorySize, SHM_ATTN);
  hipFuncSetAttribute((const void*)k_gemm_sk<4096, false, 32>, hipFuncAttributeMaxDynamicSharedMemorySize, SCfg::LDSB);
  hipFuncSetAttribute((const void*)k_gemm_sk<3584, true, SK2_NT0>, hipFuncAttributeMaxDynamicSharedMemorySize, SCfg::LDSB);
  hipFuncSetAttribute((const void*)k_gemm_gu, hipFuncAttributeMaxDynamicSharedMemorySize, UCfg::LDSB);
  float* modA = (float*)(ws + (256 << 10));
  unsigned* flg = (unsigned*)(ws + (512 << 10));
  u16* P2 = (u16*)(ws + (48 << 20));
  k_pre<<<256, 512, 0, stream>>>(c, wmod, modA, x, flg);
  k_ln<0><<<256, 1024, 0, stream>>>(x, nullptr, modA, x1f, hf);
  k_attn<<<256, 256, SHM_ATTN, stream>>>(hf, wq, kc, vc, of, wo, wg, wu, wd, wmod);
  k_gemm_sk<4096, false, 32><<<256 + 128, 256, SCfg::LDSB, stream>>>(of, wo, P, c, wmod, mod, nullptr, nullptr, nullptr);
  k_ln<1><<<256, 1024, 0, stream>>>(x, P, mod, x1f, hf);
  k_gemm_gu<<<256, 512, UCfg::LDSB, stream>>>(hf, wg, wu, act);
  k_gemm_sk<3584, true, SK2_NT0><<<256, 256, SCfg::LDSB, stream>>>(act, wd, P2, nullptr, nullptr, mod, x1f, out, flg);
}
```

```cpp
#include <hip/hip_runtime.h>
#include <utility>
#include <type_traits>
typedef unsigned short u16;
typedef short bf16x8 __attribute__((ext_vector_type(8)));
typedef short s16x4 __attribute__((ext_vector_type(4)));
typedef float f32x4 __attribute__((ext_vector_type(4)));
typedef float f32x16 __attribute__((ext_vector_type(16)));
typedef unsigned u32x4 __attribute__((ext_vector_type(4)));
typedef unsigned u32x2 __attribute__((ext_vector_type(2)));
typedef float f32x2 __attribute__((ext_vector_type(2)));
typedef __bf16 bf16x2_t __attribute__((ext_vector_type(2)));
#define DI __device__ __forceinline__
#define LAS __attribute__((address_space(3)))
#define GAP() __builtin_amdgcn_sched_barrier(0)
template <int... Is> using iseq = std::integer_sequence<int, Is...>;
template <int N> using make_iseq = std::make_integer_sequence<int, N>;

#define KST_BEGIN(k)
#define KST_END(k)
DI unsigned rfl(unsigned x) { return __builtin_amdgcn_readfirstlane(x); }
DI float bf2f(u16 v) { return __uint_as_float(((unsigned)v) << 16); }
DI unsigned cvtpk(float lo, float hi) { f32x2 v = {lo, hi}; bf16x2_t b = __builtin_convertvector(v, bf16x2_t); return __builtin_bit_cast(unsigned, b); }
DI float rbf(float f);
DI float rbf(float f) { return __uint_as_float(cvtpk(f, 0.f) << 16); }
#define KV_FIRST 12
#define WT 16
DI __amdgpu_buffer_rsrc_t mk_srd(const void* p, unsigned bytes) { return __builtin_amdgcn_make_buffer_rsrc((void*)p, (short)0, (int)bytes, 0x00020000); }

#define W4_ACLOB "a0","a1","a2","a3","a4","a5","a6","a7","a8","a9","a10","a11","a12","a13","a14","a15","a16","a17","a18","a19","a20","a21","a22","a23","a24","a25","a26","a27","a28","a29","a30","a31","a32","a33","a34","a35","a36","a37","a38","a39","a40","a41","a42","a43","a44","a45","a46","a47","a48","a49","a50","a51","a52","a53","a54","a55","a56","a57","a58","a59","a60","a61","a62","a63","a64","a65","a66","a67","a68","a69","a70","a71","a72","a73","a74","a75","a76","a77","a78","a79","a80","a81","a82","a83","a84","a85","a86","a87","a88","a89","a90","a91","a92","a93","a94","a95","a96","a97","a98","a99","a100","a101","a102","a103","a104","a105","a106","a107","a108","a109","a110","a111","a112","a113","a114","a115","a116","a117","a118","a119","a120","a121","a122","a123","a124","a125","a126","a127"
#define W4_ACLOB_HI "a128","a129","a130","a131","a132","a133","a134","a135","a136","a137","a138","a139","a140","a141","a142","a143","a144","a145","a146","a147","a148","a149","a150","a151","a152","a153","a154","a155","a156","a157","a158","a159","a160","a161","a162","a163","a164","a165","a166","a167","a168","a169","a170","a171","a172","a173","a174","a175","a176","a177","a178","a179","a180","a181","a182","a183","a184","a185","a186","a187","a188","a189","a190","a191","a192","a193","a194","a195","a196","a197","a198","a199","a200","a201","a202","a203","a204","a205","a206","a207","a208","a209","a210","a211","a212","a213","a214","a215","a216","a217","a218","a219","a220","a221","a222","a223","a224","a225","a226","a227","a228","a229","a230","a231","a232","a233","a234","a235","a236","a237","a238","a239","a240","a241","a242","a243","a244","a245","a246","a247","a248","a249","a250","a251","a252","a253","a254","a255"
template <int SH_, int NW_, int WR_, int NB16_, bool GU_, int KORD_, int NS_, int PDT_, int FD_, int BARPOS_, bool ASMACC_ = false>
struct GCfg {
  static constexpr bool ASMACC = ASMACC_;
  static constexpr int SH = SH_, NW = NW_, WR = WR_, NB16 = NB16_, KORD = KORD_, NS = NS_, PDT = PDT_, FD = FD_, BARPOS = BARPOS_;
  static constexpr bool GU = GU_;
  static constexpr int STEPS = SH == 32 ? 4 : 2;
  static constexpr int MR = WR / SH;
  static constexpr int NB = SH == 32 ? NB16 / 2 : NB16;
  static constexpr int NR16 = GU ? NB16 / 2 : NB16;
  static constexpr int PIECES = NB16 * 2;
  static constexpr int PB = (PIECES + NW - 1) / NW;
  static constexpr int STAGE = PB * NW * 1024;
  static constexpr int REGION = STAGE / 2;
  static constexpr int LDSB = NS * STAGE;
  static constexpr int NOPS = STEPS * MR + PB;
  static constexpr int GS = NB * MR;
  static constexpr int G = STEPS * GS;
  static constexpr int BSTEP = STEPS - FD;
  static constexpr int GB = BSTEP * GS + (BARPOS + 1) * MR - 1;
  static constexpr int dma_lo(int ks) { return (PB * ks) / STEPS; }
  static constexpr int step_ops(int ks) { return MR + dma_lo(ks + 1) - dma_lo(ks); }
  static constexpr int step_first(int ks) { return ks * MR + dma_lo(ks); }
  static constexpr int op_step(int j) { int ks = 0; while (ks < STEPS - 1 && j >= step_first(ks + 1)) ++ks; return ks; }
  static constexpr int op_gap(int j) { const int ks = op_step(j), i = j - step_first(ks), n = step_ops(ks); return ks * GS + (i * GS) / n; }
  static constexpr int ops_before(int g) { int c = 0; for (int j = 0; j < NOPS; ++j) if (op_gap(j) < g) ++c; return c; }
  static constexpr int NA(int ks) {
    const int ts = (ks + 1) % STEPS, jt = step_first(ts) + MR - 1;
    const int full = (ks == STEPS - 1) ? PDT - 1 : PDT;
    return (NOPS - 1 - jt) + (full - 1) * NOPS + ops_before(ks * GS);
  }
  static constexpr int NBW() { return (NS - 3) * NOPS + ops_before(GB); }
  static_assert(NS >= 3 && PDT >= 2 && FD <= STEPS - 0 && BSTEP >= 0, "");
  using acc_t = std::conditional_t<SH == 32, f32x16, f32x4>;
};

template <class C>
struct GState {
  typename C::acc_t acc[C::NB][C::MR];
  bf16x8 a[C::PDT * C::STEPS][C::MR];
  s16x4 blo[C::FD][C::NB], bhi[C::FD][C::NB];
  __amdgpu_buffer_rsrc_t srdA, srdB;
  unsigned voffA[C::MR];
  unsigned voffB[C::GU ? C::PB : 1];
  unsigned sB[C::GU ? 1 : C::PB];
  unsigned m0base, vrd, ktileB;
  unsigned long long tch[3]; unsigned tch_off;
  __amdgpu_buffer_rsrc_t srdK, srdV; unsigned kvm0, kvso, vk[4], vv;
};
struct GIter { unsigned va_cur, va_nxt, sA, sA0, m0_fill, sK_fill; };

template <class C, int SL, int OFF, int MRI> DI void a_load1(GState<C>& g, unsigned sA) {
  asm volatile("buffer_load_dwordx4 %0, %1, %2, %3 offen offset:%4" : "=v"(g.a[SL][MRI]) : "v"(g.voffA[MRI]), "s"(g.srdA), "s"(sA), "i"(OFF) : "memory");
}
template <class C, int Q> DI void b_dma1(GState<C>& g, unsigned m0v, unsigned sK) {
  if constexpr (C::GU)
    asm volatile("s_mov_b32 m0, %0\n\ts_nop 0\n\tbuffer_load_dwordx4 %1, %2, %3 offen lds" :: "s"(m0v + 1024u * Q), "v"(g.voffB[Q]), "s"(g.srdB), "s"(sK) : "memory");
  else
    asm volatile("s_mov_b32 m0, %0\n\ts_nop 0\n\tbuffer_load_dwordx4 %1, %2, %3 offen lds" :: "s"(m0v + 1024u * Q), "v"(g.voffB[0]), "s"(g.srdB), "s"(sK + g.sB[Q]) : "memory");
}
template <class C> DI void g_dummy(GState<C>& g) {
  asm volatile("s_mov_b32 m0, %0\n\ts_nop 0\n\tbuffer_load_dwordx4 %1, %2, %3 offen lds" :: "s"(g.m0base + (unsigned)(C::NS - 1) * C::STAGE), "v"(g.voffA[0]), "s"(g.srdA), "s"(0x80000000u) : "memory");
}
template <class C, int TI, int J, bool VA, bool VB> DI void g_op(GState<C>& g, const GIter& it) {
  constexpr int ks = C::op_step(J), i = J - C::step_first(ks);
  if constexpr (i < C::MR) {
    constexpr int PD = C::PDT * C::STEPS, SL = (TI * C::STEPS + ks - 1 + PD) % PD;
    if constexpr (VA) { if constexpr (ks == 0) a_load1<C, SL, 0, i>(g, it.sA0); else a_load1<C, SL, (ks - 1) * 1024, i>(g, it.sA); } else g_dummy<C>(g);
  } else {
    if constexpr (VB) b_dma1<C, C::dma_lo(ks) + i - C::MR>(g, it.m0_fill, it.sK_fill); else g_dummy<C>(g);
  }
}
template <class C, int TI, int GI, int... Js> DI void g_ops_at(GState<C>& g, const GIter& it, iseq<Js...>) {
  ((C::op_gap(Js) == GI ? g_op<C, TI, Js, true, true>(g, it) : (void)0), ...);
}
template <class C> constexpr int b_off(int KS, int NBI, int t) {
  if (C::SH == 32) {
    return ((2 * KS + t) * C::NR16 + (C::GU ? NBI : 2 * NBI)) * 256;
  } else {
    const int kb = 4 * KS + (C::KORD == 0 ? 2 * t : t);
    if (C::ASMACC) return (kb * C::NR16 + (NBI & ~1)) * 256 + 8 * (NBI & 1);
    return (kb * C::NR16 + (C::GU ? NBI / 2 : NBI)) * 256 + ((C::GU && (NBI & 1)) ? C::REGION : 0);
  }
}
template <class C, int BUF, int KS, int NBI> DI void b_read(GState<C>& g, unsigned va) {
  g.blo[BUF][NBI] = __builtin_amdgcn_ds_read_tr16_b64_v4i16((LAS s16x4*)(size_t)(va + b_off<C>(KS, NBI, 0)));
  g.bhi[BUF][NBI] = __builtin_amdgcn_ds_read_tr16_b64_v4i16((LAS s16x4*)(size_t)(va + b_off<C>(KS, NBI, 1)));
}
template <class C, int BUF, int KS, int... Ns> DI void b_read_all(GState<C>& g, unsigned va, iseq<Ns...>) { (b_read<C, BUF, KS, Ns>(g, va), ...); }

template <class C, int N, int SL> DI void a_wait(GState<C>& g) {
  static_assert(N >= 0 && N < 64, "vmcnt range");
  if constexpr (C::MR == 1) asm volatile("s_waitcnt vmcnt(%1)" : "+v"(g.a[SL][0]) : "i"(N) : "memory");
  else if constexpr (C::MR == 2) asm volatile("s_waitcnt vmcnt(%2)" : "+v"(g.a[SL][0]), "+v"(g.a[SL][1]) : "i"(N) : "memory");
  else asm volatile("s_waitcnt vmcnt(%4)" : "+v"(g.a[SL][0]), "+v"(g.a[SL][1]), "+v"(g.a[SL][2]), "+v"(g.a[SL][3]) : "i"(N) : "memory");
}

template <class C, int TI, int GI> DI void g_gap(GState<C>& g, const GIter& it) {
  constexpr int KS = GI / C::GS, r = GI % C::GS, NBI = r / C::MR, MRI = r % C::MR;
  constexpr int SL = TI * C::STEPS + KS, BUF = (C::FD == 1) ? 0 : (KS & 1), KN = (KS + C::FD) % C::STEPS;
  if constexpr (r == 0) { a_wait<C, C::NA(KS), SL>(g); GAP(); }
  {
    const bf16x8 bf = __builtin_shufflevector(g.blo[BUF][NBI], g.bhi[BUF][NBI], 0, 1, 2, 3, 4, 5, 6, 7);
    if constexpr (C::ASMACC && C::SH == 32) { constexpr int ao = 16 * (C::MR * NBI + MRI);
      asm volatile("v_mfma_f32_32x32x16_bf16 a[%c2:%c3], %0, %1, a[%c2:%c3]" :: "v"(bf), "v"(g.a[SL][MRI]), "i"(ao), "i"(ao + 15)); }
    else if constexpr (C::ASMACC) { constexpr int ao = 4 * (C::MR * NBI + MRI);
      asm volatile("v_mfma_f32_16x16x32_bf16 a[%c2:%c3], %0, %1, a[%c2:%c3]" :: "v"(bf), "v"(g.a[SL][MRI]), "i"(ao), "i"(ao + 3)); }
    else if constexpr (C::SH == 32) g.acc[NBI][MRI] = __builtin_amdgcn_mfma_f32_32x32x16_bf16(bf, g.a[SL][MRI], g.acc[NBI][MRI], 0, 0, 0);
    else if constexpr (C::GU) asm volatile("v_mfma_f32_16x16x32_bf16 %0, %1, %2, %0" : "+v"(g.acc[NBI][MRI]) : "v"(bf), "v"(g.a[SL][MRI]));
    else g.acc[NBI][MRI] = __builtin_amdgcn_mfma_f32_16x16x32_bf16(bf, g.a[SL][MRI], g.acc[NBI][MRI], 0, 0, 0);
  }
  if constexpr (GI == C::GB) {
    GAP();
    static_assert(C::NBW() < 64, "");
    asm volatile("s_waitcnt vmcnt(%0) lgkmcnt(0)\n\ts_barrier" :: "i"(C::NBW()) : "memory");
    GAP();
    b_read_all<C, BUF, KN>(g, it.va_nxt, make_iseq<C::BARPOS + 1>{});
  } else if constexpr (MRI == C::MR - 1) {
    if constexpr (KS == C::BSTEP) { if constexpr (NBI > C::BARPOS) b_read<C, BUF, KN, NBI>(g, it.va_nxt); }
    else if constexpr (KS > C::BSTEP) b_read<C, BUF, KN, NBI>(g, it.va_nxt);
    else b_read<C, BUF, KN, NBI>(g, it.va_cur);
  }
  g_ops_at<C, TI, GI>(g, it, make_iseq<C::NOPS>{});
  GAP();
}
template <class C, int TI, int... Gs> DI void g_gaps(GState<C>& g, const GIter& it, iseq<Gs...>) { (g_gap<C, TI, Gs>(g, it), ...); }
template <class C, int TI> DI void g_tile(GState<C>& g, int t, unsigned& stg, unsigned& pstg) {
  unsigned nstg = stg + C::STAGE; if (nstg == C::NS * C::STAGE) nstg = 0;
  GIter it;
  it.va_cur = g.vrd + stg; it.va_nxt = g.vrd + nstg;
  it.sA = (unsigned)(C::STEPS * 1024) * (unsigned)(t + C::PDT); it.sA0 = it.sA - 1024u;
  it.m0_fill = g.m0base + pstg; it.sK_fill = g.ktileB * (unsigned)(t - 1 + C::NS);
  g_gaps<C, TI>(g, it, make_iseq<C::G>{});
  pstg = stg; stg = nstg;
}
template <class C, int... TIs> DI void g_body(GState<C>& g, int t, unsigned& stg, unsigned& pstg, iseq<TIs...>) { (g_tile<C, TIs>(g, t + TIs, stg, pstg), ...); }

template <class C, int I> DI void g_touch(GState<C>& g) {
  asm volatile("s_mov_b32 m0, %0\n\ts_nop 0\n\tglobal_load_lds_dword %1, %2" :: "s"(g.m0base + (unsigned)(C::NS - 1) * C::STAGE), "v"(g.tch_off), "s"(g.tch[I]) : "memory");
}
template <class C, int I> DI void g_kv(GState<C>& g) {
  constexpr int q = I & 3;
  if constexpr ((I >> 2) == 1) asm volatile("s_mov_b32 m0, %0\n\ts_nop 0\n\tbuffer_load_dwordx4 %1, %2, %3 offen lds" :: "s"(g.kvm0 + 0x8000u + 1024u * q), "v"(g.vv), "s"(g.srdV), "s"(g.kvso + 2048u * (q >> 1) + 128u * (q & 1)) : "memory");
  else asm volatile("s_mov_b32 m0, %0\n\ts_nop 0\n\tbuffer_load_dwordx4 %1, %2, %3 offen offset:%c4 lds" :: "s"(g.kvm0 + (I >> 3) * 0x4000u), "v"(g.vk[q]), "s"(g.srdK), "s"(g.kvso + (I >> 3) * 16384u), "i"(1024 * q) : "memory");
}
template <class C> constexpr int dummy_index(int V, int J) {
  const int ks = C::op_step(J), i = J - C::step_first(ks);
  if (i >= C::MR || (C::STEPS * (V + C::PDT) + ks - 1) >= 0) return -1;
  return (V + (C::NS - 1)) * C::STEPS * C::MR + ks * C::MR + i;
}
template <class C, int V, int J> DI void g_virt_op(GState<C>& g, const GIter& it) {
  constexpr int ks = C::op_step(J);
  constexpr int TI = ((V % C::PDT) + C::PDT) % C::PDT;
  constexpr bool VA = (C::STEPS * (V + C::PDT) + ks - 1) >= 0, VB = (V - 1 + C::NS) >= 0;
  if constexpr (V == -1 && J == C::ops_before(C::GB)) asm volatile("s_waitcnt vmcnt(%0)\n\ts_barrier" :: "i"(C::NBW()) : "memory");
  constexpr int di = C::ASMACC ? dummy_index<C>(V, J) : -1;
  if constexpr (di >= 0 && di < 3) g_touch<C, di>(g);
  else if constexpr (di >= KV_FIRST && di < KV_FIRST + 12) g_kv<C, di - KV_FIRST>(g);
  else g_op<C, TI, J, VA, VB>(g, it);
}
template <class C, int V, int... Js> DI void g_virt(GState<C>& g, iseq<Js...>) {
  GIter it; constexpr int tb = V - 1 + C::NS;
  it.va_cur = it.va_nxt = 0; it.sA = (unsigned)(C::STEPS * 1024) * (unsigned)(V + C::PDT >= 0 ? V + C::PDT : 0); it.sA0 = it.sA - 1024u;
  it.m0_fill = g.m0base + (unsigned)(tb >= 0 ? tb : 0) * C::STAGE; it.sK_fill = g.ktileB * (unsigned)(tb >= 0 ? tb : 0);
  (g_virt_op<C, V, Js>(g, it), ...);
  if constexpr (V == -1 && C::ops_before(C::GB) == C::NOPS) asm volatile("s_waitcnt vmcnt(%0)\n\ts_barrier" :: "i"(C::NBW()) : "memory");
}
template <class C, int... Vs> DI void g_prologue(GState<C>& g, iseq<Vs...>) { constexpr int NV = sizeof...(Vs); (g_virt<C, Vs - NV>(g, make_iseq<C::NOPS>{}), ...); }

template <class C, bool PSWAP = false> DI void g_setup(GState<C>& g, unsigned ldsb, unsigned lane, unsigned wid, unsigned ldb) {
  g.m0base = rfl(ldsb + wid * (unsigned)C::PB * 1024u);
  g.ktileB = 64u * ldb;
  const unsigned q = (lane & 15u) >> 2, p0 = lane & 3u, p = PSWAP ? (((p0 & 1u) << 1) | (p0 >> 1)) : p0, g4 = lane >> 4;
  if constexpr (C::SH == 32) {
    const unsigned h = lane >> 5, blk = g4 & 1u;
    g.vrd = ldsb + blk * (C::GU ? (unsigned)C::REGION : 256u) + ((4u * h + q) ^ (4u * blk)) * 32u + 8u * p;
  } else {
    g.vrd = ldsb + (g4 >> 1) * (C::KORD == 0 ? 1u : 2u) * (unsigned)(C::NR16 * 256) + (4u * (g4 & 1u) + q) * 32u + (PSWAP ? (p0 >> 1) * 256u + (p0 & 1u) * 16u : 8u * p0);
  }
  if constexpr (C::GU) {
    const unsigned wr = wid % (C::NW / 2), up = wid / (C::NW / 2);
#pragma unroll
    for (int qq = 0; qq < C::PB; ++qq) {
      const unsigned P = wr * C::PB + qq, s = 4u * P + g4 * 0u + (lane >> 4), kb = s / (unsigned)C::NR16, nb = s % (unsigned)C::NR16;
      const unsigned row = ((lane >> 1) & 7u) ^ ((C::SH == 32 && up) ? 4u : 0u);
      g.voffB[qq] = (P < (unsigned)(C::NR16 * 2)) ? (8u * kb + row) * ldb + 32u * nb + 16u * (lane & 1u) : 0x80000000u;
    }
  } else {
    static_assert(C::GU || (C::NR16 % 4 == 0), "");
    const unsigned sub = lane >> 4;
    const unsigned row = ((lane >> 1) & 7u) ^ ((C::SH == 32 && (sub & 1u)) ? 4u : 0u);
    g.voffB[0] = row * ldb + 32u * sub + 16u * (lane & 1u);
#pragma unroll
    for (int qq = 0; qq < C::PB; ++qq) { const unsigned P = wid * C::PB + qq, kb = P / (unsigned)(C::NR16 / 4), j = P % (unsigned)(C::NR16 / 4); g.sB[qq] = rfl(8u * kb * ldb + 128u * j); }
  }
}

template <int R> DI void acc_zero1() { asm volatile("v_accvgpr_write_b32 a[%c0], 0" :: "i"(R) : W4_ACLOB); }
template <int... Rs> DI void acc_zero(iseq<Rs...>) { (acc_zero1<Rs>(), ...); asm volatile("s_nop 1"); }
template <class C> DI void gemm_run(GState<C>& g, int NT) {
  constexpr int NV = C::PDT > C::NS - 1 ? C::PDT : C::NS - 1;
  if constexpr (!C::ASMACC) {
#pragma unroll
    for (int n = 0; n < C::NB; ++n)
#pragma unroll
      for (int m = 0; m < C::MR; ++m)
#pragma unroll
        for (int e = 0; e < (C::SH == 32 ? 16 : 4); ++e) g.acc[n][m][e] = 0.f;
  }
  g_prologue<C>(g, make_iseq<NV>{});
  if constexpr (C::ASMACC) acc_zero(make_iseq<C::NB * C::MR * (C::SH == 32 ? 16 : 4)>{});
  b_read_all<C, 0, 0>(g, g.vrd, make_iseq<C::NB>{});
  if constexpr (C::FD == 2) b_read_all<C, 1, 1>(g, g.vrd, make_iseq<C::NB>{});
  asm volatile("s_nop 1");
  GAP();
  unsigned stg = 0, pstg = (C::NS - 1) * C::STAGE;
  for (int t = 0; t < NT; t += C::PDT) g_body<C>(g, t, stg, pstg, make_iseq<C::PDT>{});
  asm volatile("s_waitcnt vmcnt(0) lgkmcnt(0)\n\ts_nop 7\n\ts_nop 7" ::: "memory");
#pragma unroll
  for (int i = 0; i < C::PDT * C::STEPS; ++i)
#pragma unroll
    for (int m = 0; m < C::MR; ++m) asm volatile("" :: "v"(g.a[i][m]));
  GAP();
}
typedef __bf16 abf16x8 __attribute__((ext_vector_type(8)));
typedef __bf16 abf16x4 __attribute__((ext_vector_type(4)));
#define ATTN_THR 64.0f
constexpr unsigned KRS = 256u, KTILEB = 64u * KRS;
#define KRING 0x0000u
#define VRING 0x8000u
#define QGEMM_LDS 0x10000u
DI float max3f(float a, float b, float c) { float r; asm("v_max3_f32 %0, %1, %2, %3" : "=v"(r) : "v"(a), "v"(b), "v"(c)); return r; }
DI float max2f(float a, float b) { float r; asm("v_max_f32_e32 %0, %1, %2" : "=v"(r) : "v"(a), "v"(b)); return r; }
DI float fadd_s(float a, float b) { float r; asm("v_add_f32_e32 %0, %1, %2" : "=v"(r) : "v"(a), "v"(b)); return r; }
DI float fmul_s(float a, float b) { float r; asm("v_mul_f32_e32 %0, %1, %2" : "=v"(r) : "v"(a), "v"(b)); return r; }
DI float fsub_s(float a, float b) { float r; asm("v_sub_f32_e32 %0, %1, %2" : "=v"(r) : "v"(a), "v"(b)); return r; }
struct W4C {
  unsigned vkb[4], vvb, vksrc[4], vvsrc[4], m0k, m0v, sofk, sofv;
  __amdgpu_buffer_rsrc_t srdK, srdV;
  f32x4 S[2][4][4];
  f32x4 NEGM[4];
  abf16x8 Vf[8][2];
  unsigned pk[4][2][4];
  abf16x4 vlo[8][2], vup[8][2];
  float mhat[4], fct[4]; bool pend;
  f32x4 LS[4];
  u32x4 ones;
  float mx[4];
  unsigned sk_m0, sk_so, sv_m0, sv_so;
  unsigned pf_lo, pf_hi, pf_off, pf_m0;
};
constexpr int AO_(int n, int j) { return 4 * (4 * n + j); }
constexpr int AQ_(int j, int s) { return 128 + 16 * j + 4 * s; }
constexpr int AK_(int i, int s) { return 192 + 16 * i + 4 * s; }

template <int I, int SLOT> DI void tr_read(W4C& c) {
  constexpr int u = I >> 4, n = (I >> 1) & 7, t = I & 1, off = SLOT * 0x4000 + (4 * u + 2 * t) * 2048 + n * 256;
  if constexpr (t == 0) asm volatile("ds_read_b64_tr_b16 %0, %1 offset:%2" : "=v"(c.vlo[n][u]) : "v"(c.vvb), "i"(off) : "memory");
  else                  asm volatile("ds_read_b64_tr_b16 %0, %1 offset:%2" : "=v"(c.vup[n][u]) : "v"(c.vvb), "i"(off) : "memory");
}
template <int I, int SLOT> DI void k_read(W4C& c) {
  constexpr int s = I >> 2, i = I & 3;
  asm volatile("ds_read_b128 a[%c1:%c2], %0 offset:%c3" :: "v"(c.vkb[s]), "i"(AK_(i, s)), "i"(AK_(i, s) + 3), "i"(SLOT * 0x4000 + 4096 * i) : "memory");
}
template <int X, bool FIRST, int M> DI void qk_mfma(W4C& c) {
  constexpr int s = M >> 4, i = (M >> 2) & 3, j = M & 3, ak = AK_(i, s), aq = AQ_(j, s);
  if constexpr (s == 0) {
    if constexpr (FIRST) asm volatile("v_mfma_f32_16x16x32_bf16 %0, a[%c1:%c2], a[%c3:%c4], 0" : "=&v"(c.S[X][i][j]) : "i"(ak), "i"(ak + 3), "i"(aq), "i"(aq + 3));
    else                 asm volatile("v_mfma_f32_16x16x32_bf16 %0, a[%c2:%c3], a[%c4:%c5], %1" : "=&v"(c.S[X][i][j]) : "v"(c.NEGM[j]), "i"(ak), "i"(ak + 3), "i"(aq), "i"(aq + 3));
  } else asm volatile("v_mfma_f32_16x16x32_bf16 %0, a[%c1:%c2], a[%c3:%c4], %0" : "+v"(c.S[X][i][j]) : "i"(ak), "i"(ak + 3), "i"(aq), "i"(aq + 3));
}
template <int M, bool ZERO> DI void pv_mfma(W4C& c) {
  constexpr int u = M >> 5, n = (M >> 2) & 7, j = M & 3, ao = AO_(n, j);
  const u32x4 pw = {c.pk[j][u][0], c.pk[j][u][1], c.pk[j][u][2], c.pk[j][u][3]};
  if constexpr (ZERO && u == 0) asm volatile("v_mfma_f32_16x16x32_bf16 a[%c2:%c3], %0, %1, 0" :: "v"(c.Vf[n][u]), "v"(pw), "i"(ao), "i"(ao + 3));
  else                        asm volatile("v_mfma_f32_16x16x32_bf16 a[%c2:%c3], %0, %1, a[%c2:%c3]" :: "v"(c.Vf[n][u]), "v"(pw), "i"(ao), "i"(ao + 3));
}
template <int X, int H, int E> DI void op_exp(W4C& c) { constexpr int i = 2 * H + (E >> 4), j = (E >> 2) & 3, r = E & 3; c.S[X][i][j][r] = __builtin_amdgcn_exp2f(c.S[X][i][j][r]); }
template <int J, int U> DI void ls_mfma(W4C& c) {
  const u32x4 pw = {c.pk[J][U][0], c.pk[J][U][1], c.pk[J][U][2], c.pk[J][U][3]};
  asm volatile("v_mfma_f32_16x16x32_bf16 %0, %1, %2, %0" : "+v"(c.LS[J]) : "v"(c.ones), "v"(pw));
}
template <int X, int H, int J> DI void op_cvt(W4C& c) {
  constexpr int ih = J >> 3, j = (J >> 1) & 3, r = 2 * (J & 1), p = 2 * ih + (J & 1);
  c.pk[j][H][p] = cvtpk(c.S[X][2 * H + ih][j][r], c.S[X][2 * H + ih][j][r + 1]);
  if constexpr (H == 1) asm volatile("" : "+v"(c.pk[j][H][p]));
}
template <int X, int I> DI float sval(W4C& c) { return c.S[X][I >> 4][(I >> 2) & 3][I & 3]; }
template <int X, int G> DI void op_max2(W4C& c) {
  constexpr int a = 2 * (G & 1);
  if constexpr (G < 2) asm volatile("v_max3_f32 %0, %2, %3, %4\n\tv_max3_f32 %1, %5, %6, %7" : "=&v"(c.mx[a]), "=&v"(c.mx[a + 1])
                                    : "v"(sval<X, 6 * G>(c)), "v"(sval<X, 6 * G + 1>(c)), "v"(sval<X, 6 * G + 2>(c)), "v"(sval<X, 6 * G + 3>(c)), "v"(sval<X, 6 * G + 4>(c)), "v"(sval<X, 6 * G + 5>(c)));
  else if constexpr (G < 15) asm volatile("v_max3_f32 %0, %0, %2, %3\n\tv_max3_f32 %1, %1, %4, %5" : "+v"(c.mx[a]), "+v"(c.mx[a + 1])
                    : "v"(sval<X, 4 * G + 4>(c)), "v"(sval<X, 4 * G + 5>(c)), "v"(sval<X, 4 * G + 6>(c)), "v"(sval<X, 4 * G + 7>(c)));
  else asm volatile("v_max3_f32 %0, %0, %1, %2\n\tv_max_f32 %0, %0, %3" : "+v"(c.mx[0]) : "v"(c.mx[1]), "v"(c.mx[2]), "v"(c.mx[3]));
}
constexpr int NGB = 72;
constexpr int EB0 = 18;
constexpr int eb_lo(int g) { return g <= EB0 ? 0 : (32 * (g - EB0)) / (NGB - EB0); }
#define CVT1A 8
template <int X, bool FIRST, int M> DI void gapA(W4C& c, const int t) {
  constexpr int Xp = X ^ 1;
  qk_mfma<X, FIRST, M>(c);
  GAP();
  if constexpr (!FIRST) {
    if constexpr ((M & 1) == 0) op_exp<Xp, 1, M / 2>(c);
    else {
      constexpr int o = M >> 1;
      if constexpr (o < 8) { tr_read<2 * o, Xp>(c); tr_read<2 * o + 1, Xp>(c); }
      else if constexpr (o < 24) { tr_read<8 + o, Xp>(c); op_cvt<Xp, 0, o - 8>(c); }
      else { op_cvt<Xp, 1, o - 24>(c); }
    }
  }
  if constexpr (M == 51) { c.sk_m0 = c.m0k + (unsigned)X * 0x4000u; asm volatile("" : "+s"(c.sk_m0)); }
  if constexpr (M == 53) { c.sk_so = KTILEB * (unsigned)(t + 2) + c.sofk; asm volatile("" : "+s"(c.sk_so)); }
  if constexpr (M == 55) { c.sv_m0 = c.m0v + (unsigned)Xp * 0x4000u; asm volatile("" : "+s"(c.sv_m0)); }
  if constexpr (M == 57) { c.sv_so = KTILEB * (unsigned)(t + 1) + c.sofv; asm volatile("" : "+s"(c.sv_so)); }
  if constexpr (M == 61) asm volatile("s_mov_b32 m0, %0" :: "s"(c.sk_m0) : "memory");
  if constexpr (!FIRST && M == 24) asm volatile("" :: "v"(c.NEGM[0]), "v"(c.NEGM[1]), "v"(c.NEGM[2]), "v"(c.NEGM[3]));
  GAP();
}
#define W4_VLO "+v"(c.vlo[0][0]), "+v"(c.vlo[1][0]), "+v"(c.vlo[2][0]), "+v"(c.vlo[3][0]), "+v"(c.vlo[4][0]), "+v"(c.vlo[5][0]), "+v"(c.vlo[6][0]), "+v"(c.vlo[7][0]), \
               "+v"(c.vlo[0][1]), "+v"(c.vlo[1][1]), "+v"(c.vlo[2][1]), "+v"(c.vlo[3][1]), "+v"(c.vlo[4][1]), "+v"(c.vlo[5][1]), "+v"(c.vlo[6][1]), "+v"(c.vlo[7][1])
#define W4_VUP "+v"(c.vup[0][0]), "+v"(c.vup[1][0]), "+v"(c.vup[2][0]), "+v"(c.vup[3][0]), "+v"(c.vup[4][0]), "+v"(c.vup[5][0]), "+v"(c.vup[6][0]), "+v"(c.vup[7][0]), \
               "+v"(c.vup[0][1]), "+v"(c.vup[1][1]), "+v"(c.vup[2][1]), "+v"(c.vup[3][1]), "+v"(c.vup[4][1]), "+v"(c.vup[5][1]), "+v"(c.vup[6][1]), "+v"(c.vup[7][1])
DI void v_concat(W4C& c) {
#pragma unroll
  for (int u = 0; u < 2; ++u)
#pragma unroll
    for (int n = 0; n < 8; ++n) c.Vf[n][u] = __builtin_shufflevector(c.vlo[n][u], c.vup[n][u], 0, 1, 2, 3, 4, 5, 6, 7);
}
template <int X, bool FIRST, int... Ms> DI void phaseA_(W4C& c, const int t, iseq<Ms...>) {
  (gapA<X, FIRST, Ms>(c, t), ...);
  if constexpr (FIRST) asm volatile("s_waitcnt vmcnt(0) lgkmcnt(0)\n\ts_barrier\n\ts_nop 7" ::: "memory");
  else { asm volatile("s_waitcnt vmcnt(%c16) lgkmcnt(0)" : W4_VLO : "i"(X == 1 ? 1 : 0) : "memory"); asm volatile("s_barrier\n\ts_nop 1" : W4_VUP :: "memory"); }
  GAP();
  if constexpr (!FIRST) v_concat(c);
}
template <int X, bool FIRST> DI void phaseA(W4C& c, const int t) { phaseA_<X, FIRST>(c, t, make_iseq<64>{}); }

template <int X, bool INIT> DI void new_reference(W4C& c) {
#pragma unroll
  for (int j = 0; j < 4; ++j) {
    float m0 = max3f(c.S[X][0][j][0], c.S[X][0][j][1], c.S[X][0][j][2]), m1 = max2f(c.S[X][0][j][3], c.S[X][1][j][0]);
    m0 = max3f(m0, c.S[X][1][j][1], c.S[X][1][j][2]); m1 = max3f(m1, c.S[X][1][j][3], c.S[X][2][j][0]);
    m0 = max3f(m0, c.S[X][2][j][1], c.S[X][2][j][2]); m1 = max3f(m1, c.S[X][2][j][3], c.S[X][3][j][0]);
    m0 = max3f(m0, c.S[X][3][j][1], c.S[X][3][j][2]); m1 = max2f(m1, c.S[X][3][j][3]);
    float mm = max2f(m0, m1);
    mm = max2f(mm, __shfl_xor(mm, 16)); mm = max2f(mm, __shfl_xor(mm, 32));
    const float dl = INIT ? mm : max2f(mm, 0.f);
    if constexpr (INIT) { c.mhat[j] = dl; c.fct[j] = 1.f; }
    else { c.mhat[j] = fadd_s(c.mhat[j], dl); c.fct[j] = __builtin_amdgcn_exp2f(-dl); }
#pragma unroll
    for (int i = 0; i < 4; ++i) { _Pragma("unroll") for (int r = 0; r < 4; ++r) c.S[X][i][j][r] = fsub_s(c.S[X][i][j][r], dl); }
    const float nm = -c.mhat[j];
    c.NEGM[j] = (f32x4){nm, nm, nm, nm};
    asm volatile("" : "+v"(c.NEGM[j]));
  }
  asm volatile("s_nop 1");
}
constexpr int pvi(int G) { return G < 28 ? G : G - 4; }
template <int X, bool PEEL, bool ZERO1, int M> DI void gapB(W4C& c, const int t) {
  constexpr int Xp = X ^ 1;
  if constexpr (!PEEL) {
    if constexpr (M >= 28 && M < 32) ls_mfma<M - 28, 0>(c); else if constexpr (M >= 68) ls_mfma<M - 68, 1>(c); else pv_mfma<pvi(M), ZERO1>(c);
    GAP();
  }
  constexpr int kq = M == 0 ? 0 : M == 22 ? 1 : M == 27 ? 2 : M == 32 ? 3 : -1, vq = M == 40 ? 0 : M == 45 ? 1 : M == 50 ? 2 : M == 55 ? 3 : -1;
  if constexpr (kq >= 0) asm volatile("buffer_load_dwordx4 %0, %1, %2 offen offset:%c3 lds" :: "v"(c.vksrc[kq]), "s"(c.srdK), "s"(c.sk_so), "i"(1024 * kq) : "memory");
  if constexpr (M == 37) asm volatile("s_mov_b32 m0, %0" :: "s"(c.sv_m0) : "memory");
  if constexpr (vq >= 0) asm volatile("buffer_load_dwordx4 %0, %1, %2 offen offset:%c3 lds" :: "v"(c.vvsrc[vq]), "s"(c.srdV), "s"(c.sv_so), "i"(1024 * vq) : "memory");
  if constexpr (!PEEL && M >= 1 && M <= 16) { op_max2<X, M - 1>(c); if constexpr (M & 1) op_cvt<Xp, 1, 8 + (M - 1) / 2>(c); }
  if constexpr (M == 17) {
    if constexpr (PEEL) new_reference<X, true>(c);
    else if (__builtin_expect(__any((int)(c.mx[0] > ATTN_THR)), 0)) { new_reference<X, false>(c); c.pend = true; GAP(); }
  }
  if constexpr (M >= EB0) {
    constexpr int e0 = eb_lo(M), e1 = eb_lo(M + 1);
    if constexpr (e0 < e1) op_exp<X, 0, e0>(c);
    else {
      constexpr int fi = (M - EB0) - e0;
      if constexpr (fi < 16) k_read<fi, Xp>(c);
    }
  }
  if constexpr (X == 0 && M == 60) {
    unsigned plo, phi;
    asm volatile("v_readlane_b32 %0, %2, %4\n\tv_readlane_b32 %1, %3, %4\n\ts_mov_b32 m0, %5" : "=s"(plo), "=s"(phi) : "v"(c.pf_lo), "v"(c.pf_hi), "s"((unsigned)(t >> 1)), "s"(c.pf_m0) : "memory");
    c.sk_m0 = plo; c.sk_so = phi;
  }
  if constexpr (X == 0 && M == 65) {
    const unsigned long long pb = ((unsigned long long)c.sk_so << 32) | c.sk_m0;
    asm volatile("global_load_lds_dword %0, %1" :: "v"(c.pf_off), "s"(pb) : "memory");
  }
  if constexpr (M == NGB - 1) asm volatile("" :: "v"(c.S[X][0][0]), "v"(c.S[X][0][1]), "v"(c.S[X][0][2]), "v"(c.S[X][0][3]), "v"(c.S[X][1][0]), "v"(c.S[X][1][1]), "v"(c.S[X][1][2]), "v"(c.S[X][1][3]));
  GAP();
}
template <int R> DI void resc4(float f) {
  float t0, t1, t2, t3;
  asm volatile("v_accvgpr_read_b32 %0, a[%c5]\n\tv_accvgpr_read_b32 %1, a[%c6]\n\tv_accvgpr_read_b32 %2, a[%c7]\n\tv_accvgpr_read_b32 %3, a[%c8]\n\t"
               "v_mul_f32 %0, %4, %0\n\tv_mul_f32 %1, %4, %1\n\tv_mul_f32 %2, %4, %2\n\tv_mul_f32 %3, %4, %3\n\t"
               "v_accvgpr_write_b32 a[%c5], %0\n\tv_accvgpr_write_b32 a[%c6], %1\n\tv_accvgpr_write_b32 a[%c7], %2\n\tv_accvgpr_write_b32 a[%c8], %3"
               : "=&v"(t0), "=&v"(t1), "=&v"(t2), "=&v"(t3) : "v"(f), "i"(R), "i"(R + 1), "i"(R + 2), "i"(R + 3) : W4_ACLOB);
}
template <int... Is> DI void resc_all(W4C& c, iseq<Is...>) { (resc4<4 * Is>(c.fct[Is & 3]), ...); }
template <int X, bool PEEL, bool ZERO1, int... Ms> DI void phaseB_(W4C& c, const int t, iseq<Ms...>) {
  (gapB<X, PEEL, ZERO1, Ms>(c, t), ...);
  if constexpr (!PEEL) {
    if (__builtin_expect(c.pend, 0)) {
      asm volatile("s_nop 7\n\ts_nop 7" ::: W4_ACLOB);
      resc_all(c, make_iseq<32>{});
#pragma unroll
      for (int j = 0; j < 4; ++j) { c.LS[j] = c.LS[j] * c.fct[j]; asm volatile("" : "+v"(c.LS[j])); }
      asm volatile("s_nop 1" ::: W4_ACLOB);
      c.pend = false;
    }
  }
  asm volatile("s_waitcnt lgkmcnt(0)" ::: "memory");
  GAP();
}
template <int X, bool PEEL, bool ZERO1> DI void phaseB(W4C& c, const int t) { phaseB_<X, PEEL, ZERO1>(c, t, make_iseq<NGB>{}); }
template <int... Is> DI void k_reads0(W4C& c, iseq<Is...>) { (k_read<Is, 0>(c), ...); }
template <int... Is> DI void tr_reads1(W4C& c, iseq<Is...>) { (tr_read<Is, 1>(c), ...); }
template <int... Is> DI void drain_exp(W4C& c, iseq<Is...>) { (op_exp<1, 1, Is>(c), ...); }
template <int... Is> DI void drain_cvt0(W4C& c, iseq<Is...>) { (op_cvt<1, 0, Is>(c), ...); }
template <int... Is> DI void drain_cvt1(W4C& c, iseq<Is...>) { (op_cvt<1, 1, Is>(c), ...); }
template <int... Ms> DI void pv_all(W4C& c, iseq<Ms...>) { (pv_mfma<Ms, false>(c), ...); }
template <int R0, int R1> DI u32x4 acc_pack8(float f) {
  float o[8];
  asm volatile("v_accvgpr_read_b32 %0, a[%c8]\n\tv_accvgpr_read_b32 %1, a[%c9]\n\tv_accvgpr_read_b32 %2, a[%c10]\n\tv_accvgpr_read_b32 %3, a[%c11]\n\t"
               "v_accvgpr_read_b32 %4, a[%c12]\n\tv_accvgpr_read_b32 %5, a[%c13]\n\tv_accvgpr_read_b32 %6, a[%c14]\n\tv_accvgpr_read_b32 %7, a[%c15]"
               : "=v"(o[0]), "=v"(o[1]), "=v"(o[2]), "=v"(o[3]), "=v"(o[4]), "=v"(o[5]), "=v"(o[6]), "=v"(o[7])
               : "i"(R0), "i"(R0 + 1), "i"(R0 + 2), "i"(R0 + 3), "i"(R1), "i"(R1 + 1), "i"(R1 + 2), "i"(R1 + 3));
  u32x4 w = {cvtpk(o[0] * f, o[1] * f), cvtpk(o[2] * f, o[3] * f), cvtpk(o[4] * f, o[5] * f), cvtpk(o[6] * f, o[7] * f)};
  return w;
}
template <int I> DI void q_conv1(float C) {
  constexpr int j = I >> 2, s = I & 3, aq = AQ_(j, s);
  const u32x4 w = acc_pack8<AO_(2 * s, j), AO_(2 * s + 1, j)>(C);
  asm volatile("v_accvgpr_write_b32 a[%c4], %0\n\tv_accvgpr_write_b32 a[%c5], %1\n\tv_accvgpr_write_b32 a[%c6], %2\n\tv_accvgpr_write_b32 a[%c7], %3"
               :: "v"(w[0]), "v"(w[1]), "v"(w[2]), "v"(w[3]), "i"(aq), "i"(aq + 1), "i"(aq + 2), "i"(aq + 3) : W4_ACLOB_HI);
}
template <int... Is> DI void q_conv(float C, iseq<Is...>) { (q_conv1<Is>(C), ...); }
template <int I> DI void o_store1(__amdgpu_buffer_rsrc_t srd, unsigned vo, const float (&il)[4]) {
  constexpr int j = I >> 2, u = I & 3;
  const u32x4 w = acc_pack8<AO_(2 * u, j), AO_(2 * u + 1, j)>(il[j]);
  __builtin_amdgcn_raw_buffer_store_b128(w, srd, vo + j * (128u * 1024u) + u * 1024, 0, WT);
}
template <int... Is> DI void o_store(__amdgpu_buffer_rsrc_t srd, unsigned vo, const float (&il)[4], iseq<Is...>) { (o_store1<Is>(srd, vo, il), ...); }

using QCfg = GCfg<16, 4, 64, 8, false, 1, 6, 2, 1, 1, true>;
constexpr int SHM_ATTN = 0x10000 + 6 * 16384;

#define ST(i)
__global__ __launch_bounds__(256, 1) void k_attn(const u16* __restrict__ H1f, const u16* __restrict__ Wq, const u16* __restrict__ Kc, const u16* __restrict__ Vc, u16* __restrict__ Of,
                                                        const u16* __restrict__ Wo, const u16* __restrict__ Wg, const u16* __restrict__ Wu, const u16* __restrict__ Wd, const u16* __restrict__ Wm) {
  ST(0); KST_BEGIN(2);
  extern __shared__ __attribute__((aligned(16))) char lds[];
  const unsigned tid = threadIdx.x, wid = rfl(tid >> 6);
  const int bid = blockIdx.x, xcd = bid & 7, j = bid >> 3;
  const int head = 4 * xcd + (j & 3), y = j >> 2, kvh = head >> 3;
  const unsigned ldsb = (unsigned)(size_t)lds;
  {
    const unsigned lane = tid & 63u;
    GState<QCfg> g;
    const int mb0 = y * 16 + wid * 4;
    constexpr unsigned mbstride = 1024u * 32u;
    g.srdA = mk_srd((const char*)H1f + (size_t)mb0 * mbstride, 4 * mbstride);
#pragma unroll
    for (int m = 0; m < 4; ++m) g.voffA[m] = lane * 16u + m * mbstride;
    constexpr unsigned ldb = 4096u * 2u;
    g.srdB = mk_srd((const char*)Wq + (size_t)head * 256, 1024u * ldb - (unsigned)head * 256u);
    g_setup<QCfg, true>(g, ldsb + QGEMM_LDS, lane, wid, ldb);
    { const unsigned gw = blockIdx.x * 4u + wid;
      g.tch[0] = (unsigned long long)(size_t)((const char*)Wq + (size_t)gw * 4096); g.tch[1] = g.tch[0] + (4096u * 1024u);
      g.tch[2] = (unsigned long long)(size_t)((gw < 512u ? (const char*)Kc : (const char*)Vc) + (size_t)(gw & 511u) * 4096);
      g.tch_off = lane * 64u; }
    g.srdK = mk_srd((const char*)Kc + (size_t)kvh * (2048u * 256u), 2048u * 256u);
    g.srdV = mk_srd((const char*)Vc + (size_t)kvh * (2048u * 256u), 2048u * 256u);
    g.kvm0 = rfl(ldsb + KRING + 4096u * wid); g.kvso = rfl(KRS * 16u * wid);
#pragma unroll
    for (int q = 0; q < 4; ++q) g.vk[q] = KRS * (lane >> 4) + 16u * ((lane & 15u) ^ (4u * q + (lane >> 4)));
    g.vv = KRS * ((lane >> 1) & 7u) + 32u * (lane >> 4) + 16u * (lane & 1u);
    gemm_run<QCfg>(g, 16);
  }
  ST(1);
  unsigned lane; asm volatile("v_mbcnt_lo_u32_b32 %0, -1, 0\n\tv_mbcnt_hi_u32_b32 %0, -1, %0" : "=v"(lane));
  constexpr int T = 32;
  W4C c;
  { const unsigned r15 = lane & 15u, g4 = lane >> 4;
#pragma unroll
    for (int s = 0; s < 4; ++s) c.vkb[s] = ldsb + KRING + 256u * r15 + 16u * ((4u * s + g4) ^ r15);
    c.vvb = ldsb + VRING + (g4 >> 1) * 2048u + (4u * (g4 & 1u) + (r15 >> 2)) * 32u + 8u * (lane & 3u);
#pragma unroll
    for (int q = 0; q < 4; ++q) {
      c.vksrc[q] = KRS * g4 + 16u * (r15 ^ (4u * q + g4));
      c.vvsrc[q] = KRS * ((lane >> 1) & 7u) + 32u * g4 + 16u * (lane & 1u) + KRS * 8u * (q >> 1) + 128u * (q & 1) + (3072u - 1024u * q);
    } }
  c.m0k = rfl(ldsb + KRING + 4096u * wid); c.m0v = rfl(ldsb + VRING + 4096u * wid);
  c.sofk = rfl(KRS * 16u * wid); c.sofv = rfl(KRS * 16u * wid) - 3072u;
  c.srdK = mk_srd((const char*)Kc + (size_t)kvh * (2048u * 256u), 2048u * 256u);
  c.srdV = mk_srd((const char*)Vc + (size_t)kvh * (2048u * 256u), 2048u * 256u);
  {
    const unsigned gw = blockIdx.x * 4u + wid;
    const char* p;
    const unsigned c2 = gw + 1024u, c2c = c2 < 1792u ? c2 : gw;
    if (lane == 0) p = (const char*)Wo + (size_t)gw * 4096; else if (lane == 1) p = (const char*)Wo + (size_t)c2 * 4096;
    else if (lane == 2) p = (const char*)Wg + (size_t)gw * 4096; else if (lane == 3) p = (const char*)Wg + (size_t)c2c * 4096;
    else if (lane == 4) p = (const char*)Wu + (size_t)gw * 4096; else if (lane == 5) p = (const char*)Wu + (size_t)c2c * 4096;
    else if (lane == 6) p = (const char*)Wd + (size_t)gw * 4096; else if (lane == 7) p = (const char*)Wd + (size_t)c2c * 4096;
    else if (lane < 11) p = (const char*)Wm + (size_t)(gw + 1024u * (lane - 8u)) * 4096;
    else p = (const char*)Wo + (size_t)gw * 4096;
    c.pf_lo = (unsigned)(size_t)p; c.pf_hi = (unsigned)((size_t)p >> 32);
    c.pf_off = lane * 64u; c.pf_m0 = rfl(ldsb + QGEMM_LDS + 256u * wid);
  }
  asm volatile("s_barrier" ::: "memory");
  k_reads0(c, make_iseq<16>{});
  q_conv(0.12751743082f, make_iseq<16>{});
  asm volatile("s_waitcnt lgkmcnt(0)\n\ts_nop 1" ::: "memory");
  c.pend = false; c.ones = (u32x4){0x3f803f80u, 0x3f803f80u, 0x3f803f80u, 0x3f803f80u}; asm volatile("" : "+v"(c.ones));
#pragma unroll
  for (int j = 0; j < 4; ++j) { c.LS[j] = (f32x4){0.f, 0.f, 0.f, 0.f}; asm volatile("" : "+v"(c.LS[j])); }
  ST(2);
  phaseA<0, true>(c, 0);  phaseB<0, true, false>(c, 0);
  phaseA<1, false>(c, 1); phaseB<1, false, true>(c, 1);
  for (int t = 2; t < T; t += 2) {
    phaseA<0, false>(c, t);     phaseB<0, false, false>(c, t);
    phaseA<1, false>(c, t + 1); phaseB<1, false, false>(c, t + 1);
  }
  ST(3);
  float il[4];
  {
    tr_reads1(c, make_iseq<32>{});
    drain_exp(c, make_iseq<32>{});
    drain_cvt0(c, make_iseq<16>{});
    drain_cvt1(c, make_iseq<16>{});
    asm volatile("s_waitcnt lgkmcnt(0)" : W4_VLO :: "memory"); asm volatile("" : W4_VUP :: "memory");
    GAP();
    v_concat(c);
    asm volatile("s_nop 1" ::: "memory");
    ls_mfma<0, 0>(c); ls_mfma<1, 0>(c); ls_mfma<2, 0>(c); ls_mfma<3, 0>(c); ls_mfma<0, 1>(c); ls_mfma<1, 1>(c); ls_mfma<2, 1>(c); ls_mfma<3, 1>(c);
    pv_all(c, make_iseq<64>{});
    asm volatile("s_nop 7\n\ts_nop 7" : "+v"(c.LS[0]), "+v"(c.LS[1]), "+v"(c.LS[2]), "+v"(c.LS[3]) :: "memory");
#pragma unroll
    for (int j = 0; j < 4; ++j) il[j] = __builtin_amdgcn_rcpf(c.LS[j][0]);
  }
  {
    unsigned lb; asm volatile("v_mbcnt_lo_u32_b32 %0, -1, 0\n\tv_mbcnt_hi_u32_b32 %0, -1, %0" : "=v"(lb));
    o_store(mk_srd(Of, 16u << 20), ((unsigned)(y * 16 + wid * 4) * 128u + (unsigned)head * 4u) * 1024u + lb * 16u, il, make_iseq<16>{});
  }
  ST(4); KST_END(2);
}
template <int N> DI void touch(const char* p, int stride) {
  unsigned d[N];
#pragma unroll
  for (int i = 0; i < N; ++i) d[i] = *(const unsigned*)(p + (size_t)i * stride);
#pragma unroll
  for (int i = 0; i < N; ++i) asm volatile("" :: "v"(d[i]));
}

template <int NT, int KL> DI void mod_body(const u16* __restrict__ c, const u16* __restrict__ wmod, float* __restrict__ dst, int cb, int kh, float* red  ) {
  const int tid = threadIdx.x, cg = tid & 7, kg = tid >> 3, wv = tid >> 6;
  constexpr int NG = NT / 8, NI = KL / NG;
  u32x4 w[NI]; u16 cv[NI];
#pragma unroll
  for (int i = 0; i < NI; ++i) { const int k = kh * KL + kg + NG * i; w[i] = *(const u32x4*)(wmod + (size_t)k * 6144 + cb * 64 + cg * 8); cv[i] = c[k]; }
  float acc[8] = {0, 0, 0, 0, 0, 0, 0, 0};
#pragma unroll
  for (int i = 0; i < NI; ++i) {
    const float xx = bf2f(cv[i]), s = rbf(xx / (1.f + __expf(-xx)));
#pragma unroll
    for (int jj = 0; jj < 4; ++jj) { acc[2 * jj] += s * __uint_as_float(w[i][jj] << 16); acc[2 * jj + 1] += s * __uint_as_float(w[i][jj] & 0xffff0000u); }
  }
#pragma unroll
  for (int jj = 0; jj < 8; ++jj) { float a = acc[jj]; a += __shfl_xor(a, 8); a += __shfl_xor(a, 16); a += __shfl_xor(a, 32); acc[jj] = a; }
  if ((tid & 63) < 8) {
#pragma unroll
    for (int jj = 0; jj < 8; ++jj) red[wv * 64 + cg * 8 + jj] = acc[jj];
  }
  __syncthreads();
  if (tid < 64) { float s = 0;
#pragma unroll
    for (int g = 0; g < NT / 64; ++g) s += red[g * 64 + tid];
    dst[tid] = s; }
}
__global__ __launch_bounds__(512) void k_pre(const u16* __restrict__ c, const u16* __restrict__ wmod, float* __restrict__ modA, const u16* __restrict__ x, unsigned* __restrict__ flg) {
  __shared__ float red[8 * 64];
  const int b = blockIdx.x;
  KST_BEGIN(0);
  unsigned dx = 0;
  if (threadIdx.x == 0) flg[b * 64] = 0u;
  if (threadIdx.x < 256) dx = *(const unsigned*)((const char*)x + (size_t)b * 16384 + threadIdx.x * 64);
  mod_body<512, 128>(c, wmod, modA + (b & 7) * 2048 + (b >> 3) * 64, b >> 3, b & 7, red);
  asm volatile("" :: "v"(dx));
  KST_END(0);
}
DI f32x4 modv(const float* __restrict__ modp, int n) { const f32x4 a = *(const f32x4*)(modp + n), b = *(const f32x4*)(modp + 6144 + n); f32x4 o; for (int e = 0; e < 4; ++e) o[e] = rbf(a[e] + b[e]); return o; }
DI void unpack8(const u32x4& w, float* v) {
#pragma unroll
  for (int j = 0; j < 4; ++j) { v[2 * j] = __uint_as_float(w[j] << 16); v[2 * j + 1] = __uint_as_float(w[j] & 0xffff0000u); }
}
DI u32x4 pack8(const float* o) { return (u32x4){cvtpk(o[0], o[1]), cvtpk(o[2], o[3]), cvtpk(o[4], o[5]), cvtpk(o[6], o[7])}; }

DI void rbf2(float& a, float& b) { const unsigned w = cvtpk(a, b); a = __uint_as_float(w << 16); b = __uint_as_float(w & 0xffff0000u); }
DI void rbf8(float* v) { rbf2(v[0], v[1]); rbf2(v[2], v[3]); rbf2(v[4], v[5]); rbf2(v[6], v[7]); }
DI void modv8(const float* __restrict__ modp, int n, float* o) {
  const f32x4 a0 = *(const f32x4*)(modp + n), b0 = *(const f32x4*)(modp + 6144 + n), a1 = *(const f32x4*)(modp + n + 8), b1 = *(const f32x4*)(modp + 6144 + n + 8);
#pragma unroll
  for (int e = 0; e < 4; ++e) { o[e] = a0[e] + b0[e]; o[4 + e] = a1[e] + b1[e]; }
  rbf8(o);
}
template <int MODE>
__global__ __launch_bounds__(1024) void k_ln(const u16* __restrict__ x, const u16* __restrict__ Pf, const float* __restrict__ modp, u16* __restrict__ xf, u16* __restrict__ hf) {
  __shared__ float red[2][16][8];
  __shared__ __attribute__((aligned(16))) float msum[MODE == 0 ? 2048 : 4];
  const int tid = threadIdx.x;
  KST_BEGIN(MODE == 0 ? 1 : 4);
  const int r = tid & 7, c = tid >> 3, h = c & 1, wid = tid >> 6;
  const int row = blockIdx.x * 8 + r, mb = row >> 5, ml = row & 31;
  const size_t fo = ((size_t)mb * 64 + (c >> 1)) * 512 + (h * 32 + ml) * 8;
  const int n = 16 * (c >> 1) + 4 * h;
  float v[8], sh[8], sc[8];
  if (MODE == 0) {
    const u32x4 xw = *(const u32x4*)(x + (size_t)row * 1024 + 8 * c);
    { f32x2 a = {0.f, 0.f};
#pragma unroll
      for (int ks = 0; ks < 8; ++ks) a += *(const f32x2*)(modp + ks * 2048 + 2 * tid);
      float a0 = a[0], a1 = a[1]; rbf2(a0, a1); *(f32x2*)(msum + 2 * tid) = (f32x2){a0, a1}; }
    const unsigned s0 = h ? xw[0] : xw[2], s1 = h ? xw[1] : xw[3];
    const unsigned g0 = __shfl_xor(s0, 8), g1 = __shfl_xor(s1, 8);
    const u32x4 f = h ? (u32x4){g0, g1, xw[2], xw[3]} : (u32x4){xw[0], xw[1], g0, g1};
    __builtin_amdgcn_raw_buffer_store_b128(f, mk_srd(xf, 4u << 20), (unsigned)fo * 2u, 0, WT);
    unpack8(f, v);
  } else {
    const u32x4 xw = *(const u32x4*)(xf + fo), p0 = *(const u32x4*)(Pf + fo), p1 = *(const u32x4*)(Pf + fo + (size_t)2048 * 1024);
    float ga[8], a[8], b[8];
    modv8(modp, 3072 + n, sh); modv8(modp, 4096 + n, sc); modv8(modp, 2048 + n, ga);
    unpack8(xw, v); unpack8(p0, a); unpack8(p1, b);
#pragma unroll
    for (int j = 0; j < 8; ++j) a[j] += b[j];
    rbf8(a);
#pragma unroll
    for (int j = 0; j < 8; ++j) a[j] *= ga[j];
    rbf8(a);
#pragma unroll
    for (int j = 0; j < 8; ++j) v[j] += a[j];
    const u32x4 w = pack8(v);
    __builtin_amdgcn_raw_buffer_store_b128(w, mk_srd(xf, 4u << 20), (unsigned)fo * 2u, 0, WT);
    unpack8(w, v);
  }
  float s = ((v[0] + v[1]) + (v[2] + v[3])) + ((v[4] + v[5]) + (v[6] + v[7]));
  s += __shfl_xor(s, 8); s += __shfl_xor(s, 16); s += __shfl_xor(s, 32);
  if ((tid & 63) < 8) red[0][wid][r] = s;
  __syncthreads();
  if (MODE == 0) {
    const f32x4 a0 = *(const f32x4*)(msum + n), a1 = *(const f32x4*)(msum + n + 8), b0 = *(const f32x4*)(msum + 1024 + n), b1 = *(const f32x4*)(msum + 1024 + n + 8);
#pragma unroll
    for (int e = 0; e < 4; ++e) { sh[e] = a0[e]; sh[4 + e] = a1[e]; sc[e] = b0[e]; sc[4 + e] = b1[e]; }
  }
  float mean = 0;
#pragma unroll
  for (int w = 0; w < 16; ++w) mean += red[0][w][r];
  mean *= (1.f / 1024.f);
  float qv = 0;
#pragma unroll
  for (int j = 0; j < 8; ++j) { const float d = v[j] - mean; qv += d * d; }
  qv += __shfl_xor(qv, 8); qv += __shfl_xor(qv, 16); qv += __shfl_xor(qv, 32);
  if ((tid & 63) < 8) red[1][wid][r] = qv;
  __syncthreads();
  float var = 0;
#pragma unroll
  for (int w = 0; w < 16; ++w) var += red[1][w][r];
  var = rbf(var * (1.f / 1024.f));
  const float mb16 = rbf(mean), rsd = 1.f / rbf(sqrtf(rbf(var + 1e-5f)));
#pragma unroll
  for (int j = 0; j < 8; ++j) { v[j] -= mb16; sc[j] += 1.f; }
  rbf8(v); rbf8(sc);
#pragma unroll
  for (int j = 0; j < 8; ++j) v[j] *= rsd;
  rbf8(v);
#pragma unroll
  for (int j = 0; j < 8; ++j) v[j] *= sc[j];
  rbf8(v);
#pragma unroll
  for (int j = 0; j < 8; ++j) v[j] += sh[j];
  const unsigned ho = (((unsigned)(row >> 4) * 32u + (unsigned)(c >> 2)) * 1024u + (unsigned)(c & 3) * 256u + (unsigned)(row & 15) * 16u);
  __builtin_amdgcn_raw_buffer_store_b128(pack8(v), mk_srd(hf, 4u << 20), ho, 0, WT);
  KST_END(MODE == 0 ? 1 : 4);
}
template <int KORD> using SCfgT = GCfg<16, 4, 32, 8, false, KORD, 4, 4, 1, 1>;
using SCfg = SCfgT<1>;
template <int K, bool FIN, int NT0>
__global__ __launch_bounds__(256, 2) void k_gemm_sk(const u16* __restrict__ Af, const u16* __restrict__ B, u16* __restrict__ Pf,
                                                    const u16* __restrict__ cvec, const u16* __restrict__ wmod, float* __restrict__ modp,
                                                    const u16* __restrict__ x1f, u16* __restrict__ out, unsigned* __restrict__ flg) {
  using C = SCfgT<K == 4096 ? 0 : 1>;
  extern __shared__ __attribute__((aligned(16))) char lds[];
  KST_BEGIN(K == 4096 ? 3 : 6);
  if (K == 4096 && blockIdx.x >= 256) { const int b = blockIdx.x - 256; mod_body<256, 512>(cvec, wmod, modp + (b & 1) * 6144 + (32 + (b >> 1)) * 64, 32 + (b >> 1), b & 1, (float*)lds); KST_END(3); return; }
  constexpr int N = 1024;
  const unsigned tid = threadIdx.x, lane = tid & 63u, wid = rfl(tid >> 6);
  const int bid = blockIdx.x, xcd = bid & 7, j = bid >> 3;
  const int kh = xcd & 1, mg = xcd >> 1;
  const int mt = mg * 4 + (j & 3), nt = j >> 2;
  const int k0 = kh ? NT0 * 64 : 0, klen = kh ? K - NT0 * 64 : NT0 * 64;
  GState<C> g;
  const int mb0 = mt * 4 + wid;
  constexpr unsigned mbstride = (unsigned)K * 32u;
  g.srdA = mk_srd((const char*)Af + (size_t)(2 * mb0) * mbstride + (size_t)k0 * 32, mbstride + (unsigned)klen * 32u);
  g.voffA[0] = lane * 16u; g.voffA[1] = lane * 16u + mbstride;
  constexpr unsigned ldb = (unsigned)N * 2u;
  g.srdB = mk_srd((const char*)B + (size_t)k0 * ldb + (size_t)nt * 256, (unsigned)klen * ldb - (unsigned)nt * 256u);
  g_setup<C>(g, (unsigned)(size_t)lds, lane, wid, ldb);
  gemm_run<C>(g, klen / 64);
  const unsigned h = (lane >> 4) & 1u, hi = lane >> 5;
  const unsigned fo = (((unsigned)mb0 * 64u + (unsigned)nt * 8u + hi) * 512u + (h * 32u + (lane & 15u)) * 8u) * 2u;
  float acc[4][2][8];
#pragma unroll
  for (int i = 0; i < 4; ++i)
#pragma unroll
    for (int m = 0; m < 2; ++m)
#pragma unroll
      for (int e = 0; e < 4; ++e) {
        auto sw = __builtin_amdgcn_permlane32_swap(__float_as_uint(g.acc[2 * i][m][e]), __float_as_uint(g.acc[2 * i + 1][m][e]), false, false);
        acc[i][m][e] = __uint_as_float(sw[0]); acc[i][m][4 + e] = __uint_as_float(sw[1]);
      }
  if constexpr (!FIN) {
    const __amdgpu_buffer_rsrc_t srdP = mk_srd(Pf, 8u << 20);
#pragma unroll
    for (int i = 0; i < 4; ++i)
#pragma unroll
      for (int m = 0; m < 2; ++m) __builtin_amdgcn_raw_buffer_store_b128(pack8(acc[i][m]), srdP, (unsigned)kh * (4u << 20) + fo + i * 2048 + m * 256, 0, WT);
  } else {
    const __amdgpu_buffer_rsrc_t srdP = mk_srd(Pf, 4u << 20);
    unsigned* flag = flg + (128 + mt * 8 + nt) * 64;
    if (kh == 0) {
#pragma unroll
      for (int i = 0; i < 4; ++i)
#pragma unroll
        for (int m = 0; m < 2; ++m) __builtin_amdgcn_raw_buffer_store_b128(pack8(acc[i][m]), srdP, fo + i * 2048 + m * 256, 0, 16);
      asm volatile("s_waitcnt vmcnt(0)" ::: "memory");
      __syncthreads();
      if (tid == 0) __hip_atomic_store(flag, 1u, __ATOMIC_RELAXED, __HIP_MEMORY_SCOPE_AGENT);
    } else {
      u32x4 xw[4][2]; float gb[4][8];
#pragma unroll
      for (int i = 0; i < 4; ++i) { xw[i][0] = *(const u32x4*)((const char*)x1f + fo + i * 2048); xw[i][1] = *(const u32x4*)((const char*)x1f + fo + i * 2048 + 256);
        modv8(modp, 5120 + nt * 128 + 16 * (2 * i + hi) + 4 * h, gb[i]); }
      if (wid == 0) {
        for (unsigned spins = 0; spins < (1u << 22); ++spins) {
          if ((unsigned)__builtin_amdgcn_readfirstlane(__hip_atomic_load(flag, __ATOMIC_RELAXED, __HIP_MEMORY_SCOPE_AGENT)) != 0u) break;
          __builtin_amdgcn_s_sleep(1);
        }
      }
      asm volatile("" ::: "memory"); __builtin_amdgcn_s_barrier(); asm volatile("" ::: "memory");
      u32x4 pw[4][2];
#pragma unroll
      for (int i = 0; i < 4; ++i)
#pragma unroll
        for (int m = 0; m < 2; ++m) pw[i][m] = __builtin_amdgcn_raw_buffer_load_b128(srdP, fo + i * 2048 + m * 256, 0, 16);
      const unsigned oo = (((unsigned)mb0 * 32u + (lane & 15u)) * 1024u + (unsigned)nt * 128u + 16u * hi + 8u * h) * 2u;
      const __amdgpu_buffer_rsrc_t srdOut = mk_srd(out, 4u << 20);
#pragma unroll
      for (int i = 0; i < 4; ++i)
#pragma unroll
        for (int m = 0; m < 2; ++m) {
          float v[8], a[8];
          unpack8(xw[i][m], v); unpack8(pw[i][m], a);
#pragma unroll
          for (int jj = 0; jj < 8; ++jj) a[jj] += acc[i][m][jj];
          rbf8(a);
#pragma unroll
          for (int jj = 0; jj < 8; ++jj) a[jj] *= gb[i][jj];
          rbf8(a);
#pragma unroll
          for (int jj = 0; jj < 8; ++jj) v[jj] += a[jj];
          const u32x4 w = pack8(v);
          const unsigned s0 = h ? w[0] : w[2], s1 = h ? w[1] : w[3];
          const unsigned g0 = __shfl_xor(s0, 16), g1 = __shfl_xor(s1, 16);
          const u32x4 f = h ? (u32x4){g0, g1, w[2], w[3]} : (u32x4){w[0], w[1], g0, g1};
          __builtin_amdgcn_raw_buffer_store_b128(f, srdOut, oo + 64u * i + 32768u * m, 0, WT);
        }
    }
  }
  KST_END(K == 4096 ? 3 : 6);
}
#define SK2_NT0 28
DI float silu_mul(float g, float u) { return g * __builtin_amdgcn_rcpf(1.f + __builtin_amdgcn_exp2f(g * -1.4426950408889634f)) * u; }
#define U_NS 3
#define U_BP 3
using UCfg = GCfg<16, 8, 32, 14, true, 1, U_NS, 2, 1, U_BP>;
__global__ __launch_bounds__(512, 2) void k_gemm_gu(const u16* __restrict__ Af, const u16* __restrict__ Wg, const u16* __restrict__ Wu, u16* __restrict__ Act) {
  using C = UCfg;
  extern __shared__ __attribute__((aligned(16))) char lds[];
  constexpr int K = 1024, F = 3584;
  KST_BEGIN(5);
  const unsigned tid = threadIdx.x, wid = rfl(tid >> 6);
  const int bid = blockIdx.x, xcd = bid & 7, j = bid >> 3;
  const int mt = (xcd & 1) * 4 + (j & 3), nt = (xcd >> 1) * 8 + (j >> 2);
  GState<C> g;
  const int mb0 = mt * 8 + wid;
  {
    const unsigned lane = tid & 63u;
    constexpr unsigned mbstride = (unsigned)K * 32u;
    g.srdA = mk_srd((const char*)Af + (size_t)(2 * mb0) * mbstride, 2 * mbstride);
    g.voffA[0] = lane * 16u; g.voffA[1] = lane * 16u + mbstride;
    constexpr unsigned ldb = F * 2u;
    const bool up = wid >= 4;
    g.srdB = mk_srd((const char*)(up ? Wu : Wg) + (size_t)nt * 224, (unsigned)K * ldb - (unsigned)nt * 224u);
    g_setup<C>(g, (unsigned)(size_t)lds, lane, wid, ldb);
  }
  gemm_run<C>(g, K / 64);
  unsigned lane; asm volatile("v_mbcnt_lo_u32_b32 %0, -1, 0\n\tv_mbcnt_hi_u32_b32 %0, -1, %0" : "=v"(lane));
  const __amdgpu_buffer_rsrc_t srdO = mk_srd(Act, 2048u * F * 2u);
  const unsigned vo = ((unsigned)mb0 * 2u + (lane >> 5)) * (unsigned)((F >> 5) * 1024) + (lane & 31u) * 16u;
#pragma unroll
  for (int n = 0; n < 7; ++n) {
    unsigned q[2][2];
#pragma unroll
    for (int m = 0; m < 2; ++m) {
      float o[4];
#pragma unroll
      for (int e = 0; e < 4; ++e) o[e] = silu_mul(g.acc[2 * n][m][e], g.acc[2 * n + 1][m][e]);
      q[m][0] = cvtpk(o[0], o[1]); q[m][1] = cvtpk(o[2], o[3]);
    }
    auto s0 = __builtin_amdgcn_permlane32_swap(q[0][0], q[1][0], false, false); auto s1 = __builtin_amdgcn_permlane32_swap(q[0][1], q[1][1], false, false);
    const unsigned c16 = (unsigned)(nt * 7 + n);
    __builtin_amdgcn_raw_buffer_store_b128((u32x4){s0[0], s1[0], s0[1], s1[1]}, srdO, vo + (c16 >> 1) * 1024u + (c16 & 1u) * 512u, 0, WT);
  }
  KST_END(5);
}

extern "C" void kernel_launch(void* const* d_in, const int* in_sizes, int n_in, void* d_out, int out_size, void* d_ws, size_t ws_size, hipStream_t stream) {
  const u16* x = (const u16*)d_in[0]; const u16* kc = (const u16*)d_in[1]; const u16* vc = (const u16*)d_in[2];
  const u16* wq = (const u16*)d_in[3]; const u16* wo = (const u16*)d_in[4];
  const u16* wg = (const u16*)d_in[6]; const u16* wu = (const u16*)d_in[7]; const u16* wd = (const u16*)d_in[8];
  const u16* c = (const u16*)d_in[9]; const u16* wmod = (const u16*)d_in[10];
  u16* out = (u16*)d_out;
  char* ws = (char*)d_ws;
  float* mod = (float*)ws;
  u16* hf = (u16*)(ws + (1 << 20));
  u16* x1f = (u16*)(ws + (8 << 20));
  u16* of = (u16*)(ws + (16 << 20));
  u16* P = (u16*)(ws + (32 << 20));
  u16* act = (u16*)(ws + (64 << 20));
  hipFuncSetAttribute((const void*)k_attn, hipFuncAttributeMaxDynamicSharedMemorySize, SHM_ATTN);
  hipFuncSetAttribute((const void*)k_gemm_sk<4096, false, 32>, hipFuncAttributeMaxDynamicSharedMemorySize, SCfg::LDSB);
  hipFuncSetAttribute((const void*)k_gemm_sk<3584, true, SK2_NT0>, hipFuncAttributeMaxDynamicSharedMemorySize, SCfg::LDSB);
  hipFuncSetAttribute((const void*)k_gemm_gu, hipFuncAttributeMaxDynamicSharedMemorySize, UCfg::LDSB);
  float* modA = (float*)(ws + (256 << 10));
  unsigned* flg = (unsigned*)(ws + (512 << 10));
  u16* P2 = (u16*)(ws + (48 << 20));
  k_pre<<<256, 512, 0, stream>>>(c, wmod, modA, x, flg);
  k_ln<0><<<256, 1024, 0, stream>>>(x, nullptr, modA, x1f, hf);
  k_attn<<<256, 256, SHM_ATTN, stream>>>(hf, wq, kc, vc, of, wo, wg, wu, wd, wmod);
  k_gemm_sk<4096, false, 32><<<256 + 128, 256, SCfg::LDSB, stream>>>(of, wo, P, c, wmod, mod, nullptr, nullptr, nullptr);
  k_ln<1><<<256, 1024, 0, stream>>>(x, P, mod, x1f, hf);
  k_gemm_gu<<<256, 512, UCfg::LDSB, stream>>>(hf, wg, wu, act);
  k_gemm_sk<3584, true, SK2_NT0><<<256, 256, SCfg::LDSB, stream>>>(act, wd, P2, nullptr, nullptr, mod, x1f, out, flg);
}
```

```cpp
#include <hip/hip_runtime.h>
#include <utility>
#include <type_traits>
typedef unsigned short u16;
typedef short bf16x8 __attribute__((ext_vector_type(8)));
typedef short s16x4 __attribute__((ext_vector_type(4)));
typedef float f32x4 __attribute__((ext_vector_type(4)));
typedef float f32x16 __attribute__((ext_vector_type(16)));
typedef unsigned u32x4 __attribute__((ext_vector_type(4)));
typedef unsigned u32x2 __attribute__((ext_vector_type(2)));
typedef float f32x2 __attribute__((ext_vector_type(2)));
typedef __bf16 bf16x2_t __attribute__((ext_vector_type(2)));
#define DI __device__ __forceinline__
#define LAS __attribute__((address_space(3)))
#define GAP() __builtin_amdgcn_sched_barrier(0)
template <int... Is> using iseq = std::integer_sequence<int, Is...>;
template <int N> using make_iseq = std::make_integer_sequence<int, N>;

#define KST_BEGIN(k)
#define KST_END(k)
DI unsigned rfl(unsigned x) { return __builtin_amdgcn_readfirstlane(x); }
DI float bf2f(u16 v) { return __uint_as_float(((unsigned)v) << 16); }
DI unsigned cvtpk(float lo, float hi) { f32x2 v = {lo, hi}; bf16x2_t b = __builtin_convertvector(v, bf16x2_t); return __builtin_bit_cast(unsigned, b); }
DI float rbf(float f);
DI float rbf(float f) { return __uint_as_float(cvtpk(f, 0.f) << 16); }
#define KV_FIRST 12
#define WT 16
DI __amdgpu_buffer_rsrc_t mk_srd(const void* p, unsigned bytes) { return __builtin_amdgcn_make_buffer_rsrc((void*)p, (short)0, (int)bytes, 0x00020000); }

#define W4_ACLOB "a0","a1","a2","a3","a4","a5","a6","a7","a8","a9","a10","a11","a12","a13","a14","a15","a16","a17","a18","a19","a20","a21","a22","a23","a24","a25","a26","a27","a28","a29","a30","a31","a32","a33","a34","a35","a36","a37","a38","a39","a40","a41","a42","a43","a44","a45","a46","a47","a48","a49","a50","a51","a52","a53","a54","a55","a56","a57","a58","a59","a60","a61","a62","a63","a64","a65","a66","a67","a68","a69","a70","a71","a72","a73","a74","a75","a76","a77","a78","a79","a80","a81","a82","a83","a84","a85","a86","a87","a88","a89","a90","a91","a92","a93","a94","a95","a96","a97","a98","a99","a100","a101","a102","a103","a104","a105","a106","a107","a108","a109","a110","a111","a112","a113","a114","a115","a116","a117","a118","a119","a120","a121","a122","a123","a124","a125","a126","a127"
#define W4_ACLOB_HI "a128","a129","a130","a131","a132","a133","a134","a135","a136","a137","a138","a139","a140","a141","a142","a143","a144","a145","a146","a147","a148","a149","a150","a151","a152","a153","a154","a155","a156","a157","a158","a159","a160","a161","a162","a163","a164","a165","a166","a167","a168","a169","a170","a171","a172","a173","a174","a175","a176","a177","a178","a179","a180","a181","a182","a183","a184","a185","a186","a187","a188","a189","a190","a191","a192","a193","a194","a195","a196","a197","a198","a199","a200","a201","a202","a203","a204","a205","a206","a207","a208","a209","a210","a211","a212","a213","a214","a215","a216","a217","a218","a219","a220","a221","a222","a223","a224","a225","a226","a227","a228","a229","a230","a231","a232","a233","a234","a235","a236","a237","a238","a239","a240","a241","a242","a243","a244","a245","a246","a247","a248","a249","a250","a251","a252","a253","a254","a255"
template <int SH_, int NW_, int WR_, int NB16_, bool GU_, int KORD_, int NS_, int PDT_, int FD_, int BARPOS_, bool ASMACC_ = false>
struct GCfg {
  static constexpr bool ASMACC = ASMACC_;
  static constexpr int SH = SH_, NW = NW_, WR = WR_, NB16 = NB16_, KORD = KORD_, NS = NS_, PDT = PDT_, FD = FD_, BARPOS = BARPOS_;
  static constexpr bool GU = GU_;
  static constexpr int STEPS = SH == 32 ? 4 : 2;
  static constexpr int MR = WR / SH;
  static constexpr int NB = SH == 32 ? NB16 / 2 : NB16;
  static constexpr int NR16 = GU ? NB16 / 2 : NB16;
  static constexpr int PIECES = NB16 * 2;
  static constexpr int PB = (PIECES + NW - 1) / NW;
  static constexpr int STAGE = PB * NW * 1024;
  static constexpr int REGION = STAGE / 2;
  static constexpr int LDSB = NS * STAGE;
  static constexpr int NOPS = STEPS * MR + PB;
  static constexpr int GS = NB * MR;
  static constexpr int G = STEPS * GS;
  static constexpr int BSTEP = STEPS - FD;
  static constexpr int GB = BSTEP * GS + (BARPOS + 1) * MR - 1;
  static constexpr int dma_lo(int ks) { return (PB * ks) / STEPS; }
  static constexpr int step_ops(int ks) { return MR + dma_lo(ks + 1) - dma_lo(ks); }
  static constexpr int step_first(int ks) { return ks * MR + dma_lo(ks); }
  static constexpr int op_step(int j) { int ks = 0; while (ks < STEPS - 1 && j >= step_first(ks + 1)) ++ks; return ks; }
  static constexpr int op_gap(int j) { const int ks = op_step(j), i = j - step_first(ks), n = step_ops(ks); return ks * GS + (i * GS) / n; }
  static constexpr int ops_before(int g) { int c = 0; for (int j = 0; j < NOPS; ++j) if (op_gap(j) < g) ++c; return c; }
  static constexpr int NA(int ks) {
    const int ts = (ks + 1) % STEPS, jt = step_first(ts) + MR - 1;
    const int full = (ks == STEPS - 1) ? PDT - 1 : PDT;
    return (NOPS - 1 - jt) + (full - 1) * NOPS + ops_before(ks * GS);
  }
  static constexpr int NBW() { return (NS - 3) * NOPS + ops_before(GB); }
  static_assert(NS >= 3 && PDT >= 2 && FD <= STEPS - 0 && BSTEP >= 0, "");
  using acc_t = std::conditional_t<SH == 32, f32x16, f32x4>;
};

template <class C>
struct GState {
  typename C::acc_t acc[C::NB][C::MR];
  bf16x8 a[C::PDT * C::STEPS][C::MR];
  s16x4 blo[C::FD][C::NB], bhi[C::FD][C::NB];
  __amdgpu_buffer_rsrc_t srdA, srdB;
  unsigned voffA[C::MR];
  unsigned voffB[C::GU ? C::PB : 1];
  unsigned sB[C::GU ? 1 : C::PB];
  unsigned m0base, vrd, ktileB;
  unsigned long long tch[3]; unsigned tch_off;
  __amdgpu_buffer_rsrc_t srdK, srdV; unsigned kvm0, kvso, vk[4], vv;
};
struct GIter { unsigned va_cur, va_nxt, sA, sA0, m0_fill, sK_fill; };

template <class C, int SL, int OFF, int MRI> DI void a_load1(GState<C>& g, unsigned sA) {
  asm volatile("buffer_load_dwordx4 %0, %1, %2, %3 offen offset:%4" : "=v"(g.a[SL][MRI]) : "v"(g.voffA[MRI]), "s"(g.srdA), "s"(sA), "i"(OFF) : "memory");
}
template <class C, int Q> DI void b_dma1(GState<C>& g, unsigned m0v, unsigned sK) {
  if constexpr (C::GU)
    asm volatile("s_mov_b32 m0, %0\n\ts_nop 0\n\tbuffer_load_dwordx4 %1, %2, %3 offen lds" :: "s"(m0v + 1024u * Q), "v"(g.voffB[Q]), "s"(g.srdB), "s"(sK) : "memory");
  else
    asm volatile("s_mov_b32 m0, %0\n\ts_nop 0\n\tbuffer_load_dwordx4 %1, %2, %3 offen lds" :: "s"(m0v + 1024u * Q), "v"(g.voffB[0]), "s"(g.srdB), "s"(sK + g.sB[Q]) : "memory");
}
template <class C> DI void g_dummy(GState<C>& g) {
  asm volatile("s_mov_b32 m0, %0\n\ts_nop 0\n\tbuffer_load_dwordx4 %1, %2, %3 offen lds" :: "s"(g.m0base + (unsigned)(C::NS - 1) * C::STAGE), "v"(g.voffA[0]), "s"(g.srdA), "s"(0x80000000u) : "memory");
}
template <class C, int TI, int J, bool VA, bool VB> DI void g_op(GState<C>& g, const GIter& it) {
  constexpr int ks = C::op_step(J), i = J - C::step_first(ks);
  if constexpr (i < C::MR) {
    constexpr int PD = C::PDT * C::STEPS, SL = (TI * C::STEPS + ks - 1 + PD) % PD;
    if constexpr (VA) { if constexpr (ks == 0) a_load1<C, SL, 0, i>(g, it.sA0); else a_load1<C, SL, (ks - 1) * 1024, i>(g, it.sA); } else g_dummy<C>(g);
  } else {
    if constexpr (VB) b_dma1<C, C::dma_lo(ks) + i - C::MR>(g, it.m0_fill, it.sK_fill); else g_dummy<C>(g);
  }
}
template <class C, int TI, int GI, int... Js> DI void g_ops_at(GState<C>& g, const GIter& it, iseq<Js...>) {
  ((C::op_gap(Js) == GI ? g_op<C, TI, Js, true, true>(g, it) : (void)0), ...);
}
template <class C> constexpr int b_off(int KS, int NBI, int t) {
  if (C::SH == 32) {
    return ((2 * KS + t) * C::NR16 + (C::GU ? NBI : 2 * NBI)) * 256;
  } else {
    const int kb = 4 * KS + (C::KORD == 0 ? 2 * t : t);
    if (C::ASMACC) return (kb * C::NR16 + (NBI & ~1)) * 256 + 8 * (NBI & 1);
    return (kb * C::NR16 + (C::GU ? NBI / 2 : NBI)) * 256 + ((C::GU && (NBI & 1)) ? C::REGION : 0);
  }
}
template <class C, int BUF, int KS, int NBI> DI void b_read(GState<C>& g, unsigned va) {
  g.blo[BUF][NBI] = __builtin_amdgcn_ds_read_tr16_b64_v4i16((LAS s16x4*)(size_t)(va + b_off<C>(KS, NBI, 0)));
  g.bhi[BUF][NBI] = __builtin_amdgcn_ds_read_tr16_b64_v4i16((LAS s16x4*)(size_t)(va + b_off<C>(KS, NBI, 1)));
}
template <class C, int BUF, int KS, int... Ns> DI void b_read_all(GState<C>& g, unsigned va, iseq<Ns...>) { (b_read<C, BUF, KS, Ns>(g, va), ...); }

template <class C, int N, int SL> DI void a_wait(GState<C>& g) {
  static_assert(N >= 0 && N < 64, "vmcnt range");
  if constexpr (C::MR == 1) asm volatile("s_waitcnt vmcnt(%1)" : "+v"(g.a[SL][0]) : "i"(N) : "memory");
  else if constexpr (C::MR == 2) asm volatile("s_waitcnt vmcnt(%2)" : "+v"(g.a[SL][0]), "+v"(g.a[SL][1]) : "i"(N) : "memory");
  else asm volatile("s_waitcnt vmcnt(%4)" : "+v"(g.a[SL][0]), "+v"(g.a[SL][1]), "+v"(g.a[SL][2]), "+v"(g.a[SL][3]) : "i"(N) : "memory");
}

template <class C, int TI, int GI> DI void g_gap(GState<C>& g, const GIter& it) {
  constexpr int KS = GI / C::GS, r = GI % C::GS, NBI = r / C::MR, MRI = r % C::MR;
  constexpr int SL = TI * C::STEPS + KS, BUF = (C::FD == 1) ? 0 : (KS & 1), KN = (KS + C::FD) % C::STEPS;
  if constexpr (r == 0) { a_wait<C, C::NA(KS), SL>(g); GAP(); }
  {
    const bf16x8 bf = __builtin_shufflevector(g.blo[BUF][NBI], g.bhi[BUF][NBI], 0, 1, 2, 3, 4, 5, 6, 7);
    if constexpr (C::ASMACC && C::SH == 32) { constexpr int ao = 16 * (C::MR * NBI + MRI);
      asm volatile("v_mfma_f32_32x32x16_bf16 a[%c2:%c3], %0, %1, a[%c2:%c3]" :: "v"(bf), "v"(g.a[SL][MRI]), "i"(ao), "i"(ao + 15)); }
    else if constexpr (C::ASMACC) { constexpr int ao = 4 * (C::MR * NBI + MRI);
      asm volatile("v_mfma_f32_16x16x32_bf16 a[%c2:%c3], %0, %1, a[%c2:%c3]" :: "v"(bf), "v"(g.a[SL][MRI]), "i"(ao), "i"(ao + 3)); }
    else if constexpr (C::SH == 32) g.acc[NBI][MRI] = __builtin_amdgcn_mfma_f32_32x32x16_bf16(bf, g.a[SL][MRI], g.acc[NBI][MRI], 0, 0, 0);
    else if constexpr (C::GU) asm volatile("v_mfma_f32_16x16x32_bf16 %0, %1, %2, %0" : "+v"(g.acc[NBI][MRI]) : "v"(bf), "v"(g.a[SL][MRI]));
    else g.acc[NBI][MRI] = __builtin_amdgcn_mfma_f32_16x16x32_bf16(bf, g.a[SL][MRI], g.acc[NBI][MRI], 0, 0, 0);
  }
  if constexpr (GI == C::GB) {
    GAP();
    static_assert(C::NBW() < 64, "");
    asm volatile("s_waitcnt vmcnt(%0) lgkmcnt(0)\n\ts_barrier" :: "i"(C::NBW()) : "memory");
    GAP();
    b_read_all<C, BUF, KN>(g, it.va_nxt, make_iseq<C::BARPOS + 1>{});
  } else if constexpr (MRI == C::MR - 1) {
    if constexpr (KS == C::BSTEP) { if constexpr (NBI > C::BARPOS) b_read<C, BUF, KN, NBI>(g, it.va_nxt); }
    else if constexpr (KS > C::BSTEP) b_read<C, BUF, KN, NBI>(g, it.va_nxt);
    else b_read<C, BUF, KN, NBI>(g, it.va_cur);
  }
  g_ops_at<C, TI, GI>(g, it, make_iseq<C::NOPS>{});
  GAP();
}
template <class C, int TI, int... Gs> DI void g_gaps(GState<C>& g, const GIter& it, iseq<Gs...>) { (g_gap<C, TI, Gs>(g, it), ...); }
template <class C, int TI> DI void g_tile(GState<C>& g, int t, unsigned& stg, unsigned& pstg) {
  unsigned nstg = stg + C::STAGE; if (nstg == C::NS * C::STAGE) nstg = 0;
  GIter it;
  it.va_cur = g.vrd + stg; it.va_nxt = g.vrd + nstg;
  it.sA = (unsigned)(C::STEPS * 1024) * (unsigned)(t + C::PDT); it.sA0 = it.sA - 1024u;
  it.m0_fill = g.m0base + pstg; it.sK_fill = g.ktileB * (unsigned)(t - 1 + C::NS);
  g_gaps<C, TI>(g, it, make_iseq<C::G>{});
  pstg = stg; stg = nstg;
}
template <class C, int... TIs> DI void g_body(GState<C>& g, int t, unsigned& stg, unsigned& pstg, iseq<TIs...>) { (g_tile<C, TIs>(g, t + TIs, stg, pstg), ...); }

template <class C, int I> DI void g_touch(GState<C>& g) {
  asm volatile("s_mov_b32 m0, %0\n\ts_nop 0\n\tglobal_load_lds_dword %1, %2" :: "s"(g.m0base + (unsigned)(C::NS - 1) * C::STAGE), "v"(g.tch_off), "s"(g.tch[I]) : "memory");
}
template <class C, int I> DI void g_kv(GState<C>& g) {
  constexpr int q = I & 3;
  if constexpr ((I >> 2) == 1) asm volatile("s_mov_b32 m0, %0\n\ts_nop 0\n\tbuffer_load_dwordx4 %1, %2, %3 offen lds" :: "s"(g.kvm0 + 0x8000u + 1024u * q), "v"(g.vv), "s"(g.srdV), "s"(g.kvso + 2048u * (q >> 1) + 128u * (q & 1)) : "memory");
  else asm volatile("s_mov_b32 m0, %0\n\ts_nop 0\n\tbuffer_load_dwordx4 %1, %2, %3 offen offset:%c4 lds" :: "s"(g.kvm0 + (I >> 3) * 0x4000u), "v"(g.vk[q]), "s"(g.srdK), "s"(g.kvso + (I >> 3) * 16384u), "i"(1024 * q) : "memory");
}
template <class C> constexpr int dummy_index(int V, int J) {
  const int ks = C::op_step(J), i = J - C::step_first(ks);
  if (i >= C::MR || (C::STEPS * (V + C::PDT) + ks - 1) >= 0) return -1;
  return (V + (C::NS - 1)) * C::STEPS * C::MR + ks * C::MR + i;
}
template <class C, int V, int J> DI void g_virt_op(GState<C>& g, const GIter& it) {
  constexpr int ks = C::op_step(J);
  constexpr int TI = ((V % C::PDT) + C::PDT) % C::PDT;
  constexpr bool VA = (C::STEPS * (V + C::PDT) + ks - 1) >= 0, VB = (V - 1 + C::NS) >= 0;
  if constexpr (V == -1 && J == C::ops_before(C::GB)) asm volatile("s_waitcnt vmcnt(%0)\n\ts_barrier" :: "i"(C::NBW()) : "memory");
  constexpr int di = C::ASMACC ? dummy_index<C>(V, J) : -1;
  if constexpr (di >= 0 && di < 3) g_touch<C, di>(g);
  else if constexpr (di >= KV_FIRST && di < KV_FIRST + 12) g_kv<C, di - KV_FIRST>(g);
  else g_op<C, TI, J, VA, VB>(g, it);
}
template <class C, int V, int... Js> DI void g_virt(GState<C>& g, iseq<Js...>) {
  GIter it; constexpr int tb = V - 1 + C::NS;
  it.va_cur = it.va_nxt = 0; it.sA = (unsigned)(C::STEPS * 1024) * (unsigned)(V + C::PDT >= 0 ? V + C::PDT : 0); it.sA0 = it.sA - 1024u;
  it.m0_fill = g.m0base + (unsigned)(tb >= 0 ? tb : 0) * C::STAGE; it.sK_fill = g.ktileB * (unsigned)(tb >= 0 ? tb : 0);
  (g_virt_op<C, V, Js>(g, it), ...);
  if constexpr (V == -1 && C::ops_before(C::GB) == C::NOPS) asm volatile("s_waitcnt vmcnt(%0)\n\ts_barrier" :: "i"(C::NBW()) : "memory");
}
template <class C, int... Vs> DI void g_prologue(GState<C>& g, iseq<Vs...>) { constexpr int NV = sizeof...(Vs); (g_virt<C, Vs - NV>(g, make_iseq<C::NOPS>{}), ...); }

template <class C, bool PSWAP = false> DI void g_setup(GState<C>& g, unsigned ldsb, unsigned lane, unsigned wid, unsigned ldb) {
  g.m0base = rfl(ldsb + wid * (unsigned)C::PB * 1024u);
  g.ktileB = 64u * ldb;
  const unsigned q = (lane & 15u) >> 2, p0 = lane & 3u, p = PSWAP ? (((p0 & 1u) << 1) | (p0 >> 1)) : p0, g4 = lane >> 4;
  if constexpr (C::SH == 32) {
    const unsigned h = lane >> 5, blk = g4 & 1u;
    g.vrd = ldsb + blk * (C::GU ? (unsigned)C::REGION : 256u) + ((4u * h + q) ^ (4u * blk)) * 32u + 8u * p;
  } else {
    g.vrd = ldsb + (g4 >> 1) * (C::KORD == 0 ? 1u : 2u) * (unsigned)(C::NR16 * 256) + (4u * (g4 & 1u) + q) * 32u + (PSWAP ? (p0 >> 1) * 256u + (p0 & 1u) * 16u : 8u * p0);
  }
  if constexpr (C::GU) {
    const unsigned wr = wid % (C::NW / 2), up = wid / (C::NW / 2);
#pragma unroll
    for (int qq = 0; qq < C::PB; ++qq) {
      const unsigned P = wr * C::PB + qq, s = 4u * P + g4 * 0u + (lane >> 4), kb = s / (unsigned)C::NR16, nb = s % (unsigned)C::NR16;
      const unsigned row = ((lane >> 1) & 7u) ^ ((C::SH == 32 && up) ? 4u : 0u);
      g.voffB[qq] = (P < (unsigned)(C::NR16 * 2)) ? (8u * kb + row) * ldb + 32u * nb + 16u * (lane & 1u) : 0x80000000u;
    }
  } else {
    static_assert(C::GU || (C::NR16 % 4 == 0), "");
    const unsigned sub = lane >> 4;
    const unsigned row = ((lane >> 1) & 7u) ^ ((C::SH == 32 && (sub & 1u)) ? 4u : 0u);
    g.voffB[0] = row * ldb + 32u * sub + 16u * (lane & 1u);
#pragma unroll
    for (int qq = 0; qq < C::PB; ++qq) { const unsigned P = wid * C::PB + qq, kb = P / (unsigned)(C::NR16 / 4), j = P % (unsigned)(C::NR16 / 4); g.sB[qq] = rfl(8u * kb * ldb + 128u * j); }
  }
}

template <int R> DI void acc_zero1() { asm volatile("v_accvgpr_write_b32 a[%c0], 0" :: "i"(R) : W4_ACLOB); }
template <int... Rs> DI void acc_zero(iseq<Rs...>) { (acc_zero1<Rs>(), ...); asm volatile("s_nop 1"); }
template <class C> DI void gemm_run(GState<C>& g, int NT) {
  constexpr int NV = C::PDT > C::NS - 1 ? C::PDT : C::NS - 1;
  if constexpr (!C::ASMACC) {
#pragma unroll
    for (int n = 0; n < C::NB; ++n)
#pragma unroll
      for (int m = 0; m < C::MR; ++m)
#pragma unroll
        for (int e = 0; e < (C::SH == 32 ? 16 : 4); ++e) g.acc[n][m][e] = 0.f;
  }
  g_prologue<C>(g, make_iseq<NV>{});
  if constexpr (C::ASMACC) acc_zero(make_iseq<C::NB * C::MR * (C::SH == 32 ? 16 : 4)>{});
  b_read_all<C, 0, 0>(g, g.vrd, make_iseq<C::NB>{});
  if constexpr (C::FD == 2) b_read_all<C, 1, 1>(g, g.vrd, make_iseq<C::NB>{});
  asm volatile("s_nop 1");
  GAP();
  unsigned stg = 0, pstg = (C::NS - 1) * C::STAGE;
  for (int t = 0; t < NT; t += C::PDT) g_body<C>(g, t, stg, pstg, make_iseq<C::PDT>{});
  asm volatile("s_waitcnt vmcnt(0) lgkmcnt(0)\n\ts_nop 7\n\ts_nop 7" ::: "memory");
#pragma unroll
  for (int i = 0; i < C::PDT * C::STEPS; ++i)
#pragma unroll
    for (int m = 0; m < C::MR; ++m) asm volatile("" :: "v"(g.a[i][m]));
  GAP();
}
typedef __bf16 abf16x8 __attribute__((ext_vector_type(8)));
typedef __bf16 abf16x4 __attribute__((ext_vector_type(4)));
#define ATTN_THR 64.0f
constexpr unsigned KRS = 256u, KTILEB = 64u * KRS;
#define KRING 0x0000u
#define VRING 0x8000u
#define QGEMM_LDS 0x10000u
DI float max3f(float a, float b, float c) { float r; asm("v_max3_f32 %0, %1, %2, %3" : "=v"(r) : "v"(a), "v"(b), "v"(c)); return r; }
DI float max2f(float a, float b) { float r; asm("v_max_f32_e32 %0, %1, %2" : "=v"(r) : "v"(a), "v"(b)); return r; }
DI float fadd_s(float a, float b) { float r; asm("v_add_f32_e32 %0, %1, %2" : "=v"(r) : "v"(a), "v"(b)); return r; }
DI float fmul_s(float a, float b) { float r; asm("v_mul_f32_e32 %0, %1, %2" : "=v"(r) : "v"(a), "v"(b)); return r; }
DI float fsub_s(float a, float b) { float r; asm("v_sub_f32_e32 %0, %1, %2" : "=v"(r) : "v"(a), "v"(b)); return r; }
struct W4C {
  unsigned vkb[4], vvb, vksrc[4], vvsrc[4], m0k, m0v, sofk, sofv;
  __amdgpu_buffer_rsrc_t srdK, srdV;
  f32x4 S[2][4][4];
  f32x4 NEGM[4];
  abf16x8 Vf[8][2];
  unsigned pk[4][2][4];
  abf16x4 vlo[8][2], vup[8][2];
  float mhat[4], fct[4]; bool pend;
  f32x4 LS[4];
  u32x4 ones;
  float mx[4];
  unsigned sk_m0, sk_so, sv_m0, sv_so;
  unsigned pf_lo, pf_hi, pf_off, pf_m0;
};
constexpr int AO_(int n, int j) { return 4 * (4 * n + j); }
constexpr int AQ_(int j, int s) { return 128 + 16 * j + 4 * s; }
constexpr int AK_(int i, int s) { return 192 + 16 * i + 4 * s; }

template <int I, int SLOT> DI void tr_read(W4C& c) {
  constexpr int u = I >> 4, n = (I >> 1) & 7, t = I & 1, off = SLOT * 0x4000 + (4 * u + 2 * t) * 2048 + n * 256;
  if constexpr (t == 0) asm volatile("ds_read_b64_tr_b16 %0, %1 offset:%2" : "=v"(c.vlo[n][u]) : "v"(c.vvb), "i"(off) : "memory");
  else                  asm volatile("ds_read_b64_tr_b16 %0, %1 offset:%2" : "=v"(c.vup[n][u]) : "v"(c.vvb), "i"(off) : "memory");
}
template <int I, int SLOT> DI void k_read(W4C& c) {
  constexpr int s = I >> 2, i = I & 3;
  asm volatile("ds_read_b128 a[%c1:%c2], %0 offset:%c3" :: "v"(c.vkb[s]), "i"(AK_(i, s)), "i"(AK_(i, s) + 3), "i"(SLOT * 0x4000 + 4096 * i) : "memory");
}
template <int X, bool FIRST, int M> DI void qk_mfma(W4C& c) {
  constexpr int s = M >> 4, i = (M >> 2) & 3, j = M & 3, ak = AK_(i, s), aq = AQ_(j, s);
  if constexpr (s == 0) {
    if constexpr (FIRST) asm volatile("v_mfma_f32_16x16x32_bf16 %0, a[%c1:%c2], a[%c3:%c4], 0" : "=&v"(c.S[X][i][j]) : "i"(ak), "i"(ak + 3), "i"(aq), "i"(aq + 3));
    else                 asm volatile("v_mfma_f32_16x16x32_bf16 %0, a[%c2:%c3], a[%c4:%c5], %1" : "=&v"(c.S[X][i][j]) : "v"(c.NEGM[j]), "i"(ak), "i"(ak + 3), "i"(aq), "i"(aq + 3));
  } else asm volatile("v_mfma_f32_16x16x32_bf16 %0, a[%c1:%c2], a[%c3:%c4], %0" : "+v"(c.S[X][i][j]) : "i"(ak), "i"(ak + 3), "i"(aq), "i"(aq + 3));
}
template <int M, bool ZERO> DI void pv_mfma(W4C& c) {
  constexpr int u = M >> 5, n = (M >> 2) & 7, j = M & 3, ao = AO_(n, j);
  const u32x4 pw = {c.pk[j][u][0], c.pk[j][u][1], c.pk[j][u][2], c.pk[j][u][3]};
  if constexpr (ZERO && u == 0) asm volatile("v_mfma_f32_16x16x32_bf16 a[%c2:%c3], %0, %1, 0" :: "v"(c.Vf[n][u]), "v"(pw), "i"(ao), "i"(ao + 3));
  else                        asm volatile("v_mfma_f32_16x16x32_bf16 a[%c2:%c3], %0, %1, a[%c2:%c3]" :: "v"(c.Vf[n][u]), "v"(pw), "i"(ao), "i"(ao + 3));
}
template <int X, int H, int E> DI void op_exp(W4C& c) { constexpr int i = 2 * H + (E >> 4), j = (E >> 2) & 3, r = E & 3; c.S[X][i][j][r] = __builtin_amdgcn_exp2f(c.S[X][i][j][r]); }
template <int J, int U> DI void ls_mfma(W4C& c) {
  const u32x4 pw = {c.pk[J][U][0], c.pk[J][U][1], c.pk[J][U][2], c.pk[J][U][3]};
  asm volatile("v_mfma_f32_16x16x32_bf16 %0, %1, %2, %0" : "+v"(c.LS[J]) : "v"(c.ones), "v"(pw));
}
template <int X, int H, int J> DI void op_cvt(W4C& c) {
  constexpr int ih = J >> 3, j = (J >> 1) & 3, r = 2 * (J & 1), p = 2 * ih + (J & 1);
  c.pk[j][H][p] = cvtpk(c.S[X][2 * H + ih][j][r], c.S[X][2 * H + ih][j][r + 1]);
  if constexpr (H == 1) asm volatile("" : "+v"(c.pk[j][H][p]));
}
template <int X, int I> DI float sval(W4C& c) { return c.S[X][I >> 4][(I >> 2) & 3][I & 3]; }
template <int X, int G> DI void op_max2(W4C& c) {
  constexpr int a = 2 * (G & 1);
  if constexpr (G < 2) asm volatile("v_max3_f32 %0, %2, %3, %4\n\tv_max3_f32 %1, %5, %6, %7" : "=&v"(c.mx[a]), "=&v"(c.mx[a + 1])
                                    : "v"(sval<X, 6 * G>(c)), "v"(sval<X, 6 * G + 1>(c)), "v"(sval<X, 6 * G + 2>(c)), "v"(sval<X, 6 * G + 3>(c)), "v"(sval<X, 6 * G + 4>(c)), "v"(sval<X, 6 * G + 5>(c)));
  else if constexpr (G < 15) asm volatile("v_max3_f32 %0, %0, %2, %3\n\tv_max3_f32 %1, %1, %4, %5" : "+v"(c.mx[a]), "+v"(c.mx[a + 1])
                    : "v"(sval<X, 4 * G + 4>(c)), "v"(sval<X, 4 * G + 5>(c)), "v"(sval<X, 4 * G + 6>(c)), "v"(sval<X, 4 * G + 7>(c)));
  else asm volatile("v_max3_f32 %0, %0, %1, %2\n\tv_max_f32 %0, %0, %3" : "+v"(c.mx[0]) : "v"(c.mx[1]), "v"(c.mx[2]), "v"(c.mx[3]));
}
constexpr int NGB = 72;
constexpr int EB0 = 18;
constexpr int eb_lo(int g) { return g <= EB0 ? 0 : (32 * (g - EB0)) / (NGB - EB0); }
#define CVT1A 8
template <int X, bool FIRST, int M> DI void gapA(W4C& c, const int t) {
  constexpr int Xp = X ^ 1;
  qk_mfma<X, FIRST, M>(c);
  GAP();
  if constexpr (!FIRST) {
    if constexpr ((M & 1) == 0) op_exp<Xp, 1, M / 2>(c);
    else {
      constexpr int o = M >> 1;
      if constexpr (o < 8) { tr_read<2 * o, Xp>(c); tr_read<2 * o + 1, Xp>(c); }
      else if constexpr (o < 24) { tr_read<8 + o, Xp>(c); op_cvt<Xp, 0, o - 8>(c); }
      else { op_cvt<Xp, 1, o - 24>(c); }
    }
  }
  if constexpr (M == 51) { c.sk_m0 = c.m0k + (unsigned)X * 0x4000u; asm volatile("" : "+s"(c.sk_m0)); }
  if constexpr (M == 53) { c.sk_so = KTILEB * (unsigned)(t + 2) + c.sofk; asm volatile("" : "+s"(c.sk_so)); }
  if constexpr (M == 55) { c.sv_m0 = c.m0v + (unsigned)Xp * 0x4000u; asm volatile("" : "+s"(c.sv_m0)); }
  if constexpr (M == 57) { c.sv_so = KTILEB * (unsigned)(t + 1) + c.sofv; asm volatile("" : "+s"(c.sv_so)); }
  if constexpr (M == 61) asm volatile("s_mov_b32 m0, %0" :: "s"(c.sk_m0) : "memory");
  if constexpr (!FIRST && M == 24) asm volatile("" :: "v"(c.NEGM[0]), "v"(c.NEGM[1]), "v"(c.NEGM[2]), "v"(c.NEGM[3]));
  GAP();
}
#define W4_VLO "+v"(c.vlo[0][0]), "+v"(c.vlo[1][0]), "+v"(c.vlo[2][0]), "+v"(c.vlo[3][0]), "+v"(c.vlo[4][0]), "+v"(c.vlo[5][0]), "+v"(c.vlo[6][0]), "+v"(c.vlo[7][0]), \
               "+v"(c.vlo[0][1]), "+v"(c.vlo[1][1]), "+v"(c.vlo[2][1]), "+v"(c.vlo[3][1]), "+v"(c.vlo[4][1]), "+v"(c.vlo[5][1]), "+v"(c.vlo[6][1]), "+v"(c.vlo[7][1])
#define W4_VUP "+v"(c.vup[0][0]), "+v"(c.vup[1][0]), "+v"(c.vup[2][0]), "+v"(c.vup[3][0]), "+v"(c.vup[4][0]), "+v"(c.vup[5][0]), "+v"(c.vup[6][0]), "+v"(c.vup[7][0]), \
               "+v"(c.vup[0][1]), "+v"(c.vup[1][1]), "+v"(c.vup[2][1]), "+v"(c.vup[3][1]), "+v"(c.vup[4][1]), "+v"(c.vup[5][1]), "+v"(c.vup[6][1]), "+v"(c.vup[7][1])
DI void v_concat(W4C& c) {
#pragma unroll
  for (int u = 0; u < 2; ++u)
#pragma unroll
    for (int n = 0; n < 8; ++n) c.Vf[n][u] = __builtin_shufflevector(c.vlo[n][u], c.vup[n][u], 0, 1, 2, 3, 4, 5, 6, 7);
}
template <int X, bool FIRST, int... Ms> DI void phaseA_(W4C& c, const int t, iseq<Ms...>) {
  (gapA<X, FIRST, Ms>(c, t), ...);
  if constexpr (FIRST) asm volatile("s_waitcnt vmcnt(0) lgkmcnt(0)\n\ts_barrier\n\ts_nop 7" ::: "memory");
  else { asm volatile("s_waitcnt vmcnt(%c16) lgkmcnt(0)" : W4_VLO : "i"(X == 1 ? 1 : 0) : "memory"); asm volatile("s_barrier\n\ts_nop 1" : W4_VUP :: "memory"); }
  GAP();
  if constexpr (!FIRST) v_concat(c);
}
template <int X, bool FIRST> DI void phaseA(W4C& c, const int t) { phaseA_<X, FIRST>(c, t, make_iseq<64>{}); }

template <int X, bool INIT> DI void new_reference(W4C& c) {
#pragma unroll
  for (int j = 0; j < 4; ++j) {
    float m0 = max3f(c.S[X][0][j][0], c.S[X][0][j][1], c.S[X][0][j][2]), m1 = max2f(c.S[X][0][j][3], c.S[X][1][j][0]);
    m0 = max3f(m0, c.S[X][1][j][1], c.S[X][1][j][2]); m1 = max3f(m1, c.S[X][1][j][3], c.S[X][2][j][0]);
    m0 = max3f(m0, c.S[X][2][j][1], c.S[X][2][j][2]); m1 = max3f(m1, c.S[X][2][j][3], c.S[X][3][j][0]);
    m0 = max3f(m0, c.S[X][3][j][1], c.S[X][3][j][2]); m1 = max2f(m1, c.S[X][3][j][3]);
    float mm = max2f(m0, m1);
    mm = max2f(mm, __shfl_xor(mm, 16)); mm = max2f(mm, __shfl_xor(mm, 32));
    const float dl = INIT ? mm : max2f(mm, 0.f);
    if constexpr (INIT) { c.mhat[j] = dl; c.fct[j] = 1.f; }
    else { c.mhat[j] = fadd_s(c.mhat[j], dl); c.fct[j] = __builtin_amdgcn_exp2f(-dl); }
#pragma unroll
    for (int i = 0; i < 4; ++i) { _Pragma("unroll") for (int r = 0; r < 4; ++r) c.S[X][i][j][r] = fsub_s(c.S[X][i][j][r], dl); }
    const float nm = -c.mhat[j];
    c.NEGM[j] = (f32x4){nm, nm, nm, nm};
    asm volatile("" : "+v"(c.NEGM[j]));
  }
  asm volatile("s_nop 1");
}
constexpr int pvi(int G) { return G < 28 ? G : G - 4; }
template <int X, bool PEEL, bool ZERO1, int M> DI void gapB(W4C& c, const int t) {
  constexpr int Xp = X ^ 1;
  if constexpr (!PEEL) {
    if constexpr (M >= 28 && M < 32) ls_mfma<M - 28, 0>(c); else if constexpr (M >= 68) ls_mfma<M - 68, 1>(c); else pv_mfma<pvi(M), ZERO1>(c);
    GAP();
  }
  constexpr int kq = M == 0 ? 0 : M == 22 ? 1 : M == 27 ? 2 : M == 32 ? 3 : -1, vq = M == 40 ? 0 : M == 45 ? 1 : M == 50 ? 2 : M == 55 ? 3 : -1;
  if constexpr (kq >= 0) asm volatile("buffer_load_dwordx4 %0, %1, %2 offen offset:%c3 lds" :: "v"(c.vksrc[kq]), "s"(c.srdK), "s"(c.sk_so), "i"(1024 * kq) : "memory");
  if constexpr (M == 37) asm volatile("s_mov_b32 m0, %0" :: "s"(c.sv_m0) : "memory");
  if constexpr (vq >= 0) asm volatile("buffer_load_dwordx4 %0, %1, %2 offen offset:%c3 lds" :: "v"(c.vvsrc[vq]), "s"(c.srdV), "s"(c.sv_so), "i"(1024 * vq) : "memory");
  if constexpr (!PEEL && M >= 1 && M <= 16) { op_max2<X, M - 1>(c); if constexpr (M & 1) op_cvt<Xp, 1, 8 + (M - 1) / 2>(c); }
  if constexpr (M == 17) {
    if constexpr (PEEL) new_reference<X, true>(c);
    else if (__builtin_expect(__any((int)(c.mx[0] > ATTN_THR)), 0)) { new_reference<X, false>(c); c.pend = true; GAP(); }
  }
  if constexpr (M >= EB0) {
    constexpr int e0 = eb_lo(M), e1 = eb_lo(M + 1);
    if constexpr (e0 < e1) op_exp<X, 0, e0>(c);
    else {
      constexpr int fi = (M - EB0) - e0;
      if constexpr (fi < 16) k_read<fi, Xp>(c);
    }
  }
  if constexpr (X == 0 && M == 60) {
    unsigned plo, phi;
    asm volatile("v_readlane_b32 %0, %2, %4\n\tv_readlane_b32 %1, %3, %4\n\ts_mov_b32 m0, %5" : "=s"(plo), "=s"(phi) : "v"(c.pf_lo), "v"(c.pf_hi), "s"((unsigned)(t >> 1)), "s"(c.pf_m0) : "memory");
    c.sk_m0 = plo; c.sk_so = phi;
  }
  if constexpr (X == 0 && M == 65) {
    const unsigned long long pb = ((unsigned long long)c.sk_so << 32) | c.sk_m0;
    asm volatile("global_load_lds_dword %0, %1" :: "v"(c.pf_off), "s"(pb) : "memory");
  }
  if constexpr (M == NGB - 1) asm volatile("" :: "v"(c.S[X][0][0]), "v"(c.S[X][0][1]), "v"(c.S[X][0][2]), "v"(c.S[X][0][3]), "v"(c.S[X][1][0]), "v"(c.S[X][1][1]), "v"(c.S[X][1][2]), "v"(c.S[X][1][3]));
  GAP();
}
template <int R> DI void resc4(float f) {
  float t0, t1, t2, t3;
  asm volatile("v_accvgpr_read_b32 %0, a[%c5]\n\tv_accvgpr_read_b32 %1, a[%c6]\n\tv_accvgpr_read_b32 %2, a[%c7]\n\tv_accvgpr_read_b32 %3, a[%c8]\n\t"
               "v_mul_f32 %0, %4, %0\n\tv_mul_f32 %1, %4, %1\n\tv_mul_f32 %2, %4, %2\n\tv_mul_f32 %3, %4, %3\n\t"
               "v_accvgpr_write_b32 a[%c5], %0\n\tv_accvgpr_write_b32 a[%c6], %1\n\tv_accvgpr_write_b32 a[%c7], %2\n\tv_accvgpr_write_b32 a[%c8], %3"
               : "=&v"(t0), "=&v"(t1), "=&v"(t2), "=&v"(t3) : "v"(f), "i"(R), "i"(R + 1), "i"(R + 2), "i"(R + 3) : W4_ACLOB);
}
template <int... Is> DI void resc_all(W4C& c, iseq<Is...>) { (resc4<4 * Is>(c.fct[Is & 3]), ...); }
template <int X, bool PEEL, bool ZERO1, int... Ms> DI void phaseB_(W4C& c, const int t, iseq<Ms...>) {
  (gapB<X, PEEL, ZERO1, Ms>(c, t), ...);
  if constexpr (!PEEL) {
    if (__builtin_expect(c.pend, 0)) {
      asm volatile("s_nop 7\n\ts_nop 7" ::: W4_ACLOB);
      resc_all(c, make_iseq<32>{});
#pragma unroll
      for (int j = 0; j < 4; ++j) { c.LS[j] = c.LS[j] * c.fct[j]; asm volatile("" : "+v"(c.LS[j])); }
      asm volatile("s_nop 1" ::: W4_ACLOB);
      c.pend = false;
    }
  }
  asm volatile("s_waitcnt lgkmcnt(0)" ::: "memory");
  GAP();
}
template <int X, bool PEEL, bool ZERO1> DI void phaseB(W4C& c, const int t) { phaseB_<X, PEEL, ZERO1>(c, t, make_iseq<NGB>{}); }
template <int... Is> DI void k_reads0(W4C& c, iseq<Is...>) { (k_read<Is, 0>(c), ...); }
template <int... Is> DI void tr_reads1(W4C& c, iseq<Is...>) { (tr_read<Is, 1>(c), ...); }
template <int... Is> DI void drain_exp(W4C& c, iseq<Is...>) { (op_exp<1, 1, Is>(c), ...); }
template <int... Is> DI void drain_cvt0(W4C& c, iseq<Is...>) { (op_cvt<1, 0, Is>(c), ...); }
template <int... Is> DI void drain_cvt1(W4C& c, iseq<Is...>) { (op_cvt<1, 1, Is>(c), ...); }
template <int... Ms> DI void pv_all(W4C& c, iseq<Ms...>) { (pv_mfma<Ms, false>(c), ...); }
template <int R0, int R1> DI u32x4 acc_pack8(float f) {
  float o[8];
  asm volatile("v_accvgpr_read_b32 %0, a[%c8]\n\tv_accvgpr_read_b32 %1, a[%c9]\n\tv_accvgpr_read_b32 %2, a[%c10]\n\tv_accvgpr_read_b32 %3, a[%c11]\n\t"
               "v_accvgpr_read_b32 %4, a[%c12]\n\tv_accvgpr_read_b32 %5, a[%c13]\n\tv_accvgpr_read_b32 %6, a[%c14]\n\tv_accvgpr_read_b32 %7, a[%c15]"
               : "=v"(o[0]), "=v"(o[1]), "=v"(o[2]), "=v"(o[3]), "=v"(o[4]), "=v"(o[5]), "=v"(o[6]), "=v"(o[7])
               : "i"(R0), "i"(R0 + 1), "i"(R0 + 2), "i"(R0 + 3), "i"(R1), "i"(R1 + 1), "i"(R1 + 2), "i"(R1 + 3));
  u32x4 w = {cvtpk(o[0] * f, o[1] * f), cvtpk(o[2] * f, o[3] * f), cvtpk(o[4] * f, o[5] * f), cvtpk(o[6] * f, o[7] * f)};
  return w;
}
template <int I> DI void q_conv1(float C) {
  constexpr int j = I >> 2, s = I & 3, aq = AQ_(j, s);
  const u32x4 w = acc_pack8<AO_(2 * s, j), AO_(2 * s + 1, j)>(C);
  asm volatile("v_accvgpr_write_b32 a[%c4], %0\n\tv_accvgpr_write_b32 a[%c5], %1\n\tv_accvgpr_write_b32 a[%c6], %2\n\tv_accvgpr_write_b32 a[%c7], %3"
               :: "v"(w[0]), "v"(w[1]), "v"(w[2]), "v"(w[3]), "i"(aq), "i"(aq + 1), "i"(aq + 2), "i"(aq + 3) : W4_ACLOB_HI);
}
template <int... Is> DI void q_conv(float C, iseq<Is...>) { (q_conv1<Is>(C), ...); }
template <int I> DI void o_store1(__amdgpu_buffer_rsrc_t srd, unsigned vo, const float (&il)[4]) {
  constexpr int j = I >> 2, u = I & 3;
  const u32x4 w = acc_pack8<AO_(2 * u, j), AO_(2 * u + 1, j)>(il[j]);
  __builtin_amdgcn_raw_buffer_store_b128(w, srd, vo + j * (128u * 1024u) + u * 1024, 0, WT);
}
template <int G, int I> DI void fin_step(W4C& c, __amdgpu_buffer_rsrc_t srd, unsigned vo, const float (&il)[4]) {
  if constexpr (G < 4) { pv_mfma<32 * (I >> 3) + 4 * (2 * G + ((I >> 2) & 1)) + (I & 3), false>(c); GAP(); }
  if constexpr (G > 0 && (I & 3) == 3) { o_store1<4 * (I >> 2) + (G - 1)>(srd, vo, il); GAP(); }
}
template <int G, int... Is> DI void fin_group(W4C& c, __amdgpu_buffer_rsrc_t srd, unsigned vo, const float (&il)[4], iseq<Is...>) { (fin_step<G, Is>(c, srd, vo, il), ...); }

using QCfg = GCfg<16, 4, 64, 8, false, 1, 6, 2, 1, 1, true>;
constexpr int SHM_ATTN = 0x10000 + 6 * 16384;

#define ST(i)
__global__ __launch_bounds__(256, 1) void k_attn(const u16* __restrict__ H1f, const u16* __restrict__ Wq, const u16* __restrict__ Kc, const u16* __restrict__ Vc, u16* __restrict__ Of,
                                                        const u16* __restrict__ Wo, const u16* __restrict__ Wg, const u16* __restrict__ Wu, const u16* __restrict__ Wd, const u16* __restrict__ Wm) {
  ST(0); KST_BEGIN(2);
  extern __shared__ __attribute__((aligned(16))) char lds[];
  const unsigned tid = threadIdx.x, wid = rfl(tid >> 6);
  const int bid = blockIdx.x, xcd = bid & 7, j = bid >> 3;
  const int head = 4 * xcd + (j & 3), y = j >> 2, kvh = head >> 3;
  const unsigned ldsb = (unsigned)(size_t)lds;
  {
    const unsigned lane = tid & 63u;
    GState<QCfg> g;
    const int mb0 = y * 16 + wid * 4;
    constexpr unsigned mbstride = 1024u * 32u;
    g.srdA = mk_srd((const char*)H1f + (size_t)mb0 * mbstride, 4 * mbstride);
#pragma unroll
    for (int m = 0; m < 4; ++m) g.voffA[m] = lane * 16u + m * mbstride;
    constexpr unsigned ldb = 4096u * 2u;
    g.srdB = mk_srd((const char*)Wq + (size_t)head * 256, 1024u * ldb - (unsigned)head * 256u);
    g_setup<QCfg, true>(g, ldsb + QGEMM_LDS, lane, wid, ldb);
    { const unsigned gw = blockIdx.x * 4u + wid;
      g.tch[0] = (unsigned long long)(size_t)((const char*)Wq + (size_t)gw * 4096); g.tch[1] = g.tch[0] + (4096u * 1024u);
      g.tch[2] = (unsigned long long)(size_t)((gw < 512u ? (const char*)Kc : (const char*)Vc) + (size_t)(gw & 511u) * 4096);
      g.tch_off = lane * 64u; }
    g.srdK = mk_srd((const char*)Kc + (size_t)kvh * (2048u * 256u), 2048u * 256u);
    g.srdV = mk_srd((const char*)Vc + (size_t)kvh * (2048u * 256u), 2048u * 256u);
    g.kvm0 = rfl(ldsb + KRING + 4096u * wid); g.kvso = rfl(KRS * 16u * wid);
#pragma unroll
    for (int q = 0; q < 4; ++q) g.vk[q] = KRS * (lane >> 4) + 16u * ((lane & 15u) ^ (4u * q + (lane >> 4)));
    g.vv = KRS * ((lane >> 1) & 7u) + 32u * (lane >> 4) + 16u * (lane & 1u);
    gemm_run<QCfg>(g, 16);
  }
  ST(1);
  unsigned lane; asm volatile("v_mbcnt_lo_u32_b32 %0, -1, 0\n\tv_mbcnt_hi_u32_b32 %0, -1, %0" : "=v"(lane));
  constexpr int T = 32;
  W4C c;
  { const unsigned r15 = lane & 15u, g4 = lane >> 4;
#pragma unroll
    for (int s = 0; s < 4; ++s) c.vkb[s] = ldsb + KRING + 256u * r15 + 16u * ((4u * s + g4) ^ r15);
    c.vvb = ldsb + VRING + (g4 >> 1) * 2048u + (4u * (g4 & 1u) + (r15 >> 2)) * 32u + 8u * (lane & 3u);
#pragma unroll
    for (int q = 0; q < 4; ++q) {
      c.vksrc[q] = KRS * g4 + 16u * (r15 ^ (4u * q + g4));
      c.vvsrc[q] = KRS * ((lane >> 1) & 7u) + 32u * g4 + 16u * (lane & 1u) + KRS * 8u * (q >> 1) + 128u * (q & 1) + (3072u - 1024u * q);
    } }
  c.m0k = rfl(ldsb + KRING + 4096u * wid); c.m0v = rfl(ldsb + VRING + 4096u * wid);
  c.sofk = rfl(KRS * 16u * wid); c.sofv = rfl(KRS * 16u * wid) - 3072u;
  c.srdK = mk_srd((const char*)Kc + (size_t)kvh * (2048u * 256u), 2048u * 256u);
  c.srdV = mk_srd((const char*)Vc + (size_t)kvh * (2048u * 256u), 2048u * 256u);
  {
    const unsigned gw = blockIdx.x * 4u + wid;
    const char* p;
    const unsigned c2 = gw + 1024u, c2c = c2 < 1792u ? c2 : gw;
    if (lane == 0) p = (const char*)Wo + (size_t)gw * 4096; else if (lane == 1) p = (const char*)Wo + (size_t)c2 * 4096;
    else if (lane == 2) p = (const char*)Wg + (size_t)gw * 4096; else if (lane == 3) p = (const char*)Wg + (size_t)c2c * 4096;
    else if (lane == 4) p = (const char*)Wu + (size_t)gw * 4096; else if (lane == 5) p = (const char*)Wu + (size_t)c2c * 4096;
    else if (lane == 6) p = (const char*)Wd + (size_t)gw * 4096; else if (lane == 7) p = (const char*)Wd + (size_t)c2c * 4096;
    else if (lane < 11) p = (const char*)Wm + (size_t)(gw + 1024u * (lane - 8u)) * 4096;
    else p = (const char*)Wo + (size_t)gw * 4096;
    c.pf_lo = (unsigned)(size_t)p; c.pf_hi = (unsigned)((size_t)p >> 32);
    c.pf_off = lane * 64u; c.pf_m0 = rfl(ldsb + QGEMM_LDS + 256u * wid);
  }
  asm volatile("s_barrier" ::: "memory");
  k_reads0(c, make_iseq<16>{});
  q_conv(0.12751743082f, make_iseq<16>{});
  asm volatile("s_waitcnt lgkmcnt(0)\n\ts_nop 1" ::: "memory");
  c.pend = false; c.ones = (u32x4){0x3f803f80u, 0x3f803f80u, 0x3f803f80u, 0x3f803f80u}; asm volatile("" : "+v"(c.ones));
#pragma unroll
  for (int j = 0; j < 4; ++j) { c.LS[j] = (f32x4){0.f, 0.f, 0.f, 0.f}; asm volatile("" : "+v"(c.LS[j])); }
  ST(2);
  phaseA<0, true>(c, 0);  phaseB<0, true, false>(c, 0);
  phaseA<1, false>(c, 1); phaseB<1, false, true>(c, 1);
  for (int t = 2; t < T; t += 2) {
    phaseA<0, false>(c, t);     phaseB<0, false, false>(c, t);
    phaseA<1, false>(c, t + 1); phaseB<1, false, false>(c, t + 1);
  }
  ST(3);
  float il[4];
  {
    tr_reads1(c, make_iseq<32>{});
    drain_exp(c, make_iseq<32>{});
    drain_cvt0(c, make_iseq<16>{});
    drain_cvt1(c, make_iseq<16>{});
    asm volatile("s_waitcnt lgkmcnt(0)" : W4_VLO :: "memory"); asm volatile("" : W4_VUP :: "memory");
    GAP();
    v_concat(c);
    asm volatile("s_nop 1" ::: "memory");
    ls_mfma<0, 0>(c); ls_mfma<1, 0>(c); ls_mfma<2, 0>(c); ls_mfma<3, 0>(c); ls_mfma<0, 1>(c); ls_mfma<1, 1>(c); ls_mfma<2, 1>(c); ls_mfma<3, 1>(c);
    unsigned lb; asm volatile("v_mbcnt_lo_u32_b32 %0, -1, 0\n\tv_mbcnt_hi_u32_b32 %0, -1, %0" : "=v"(lb));
    const unsigned vo = ((unsigned)(y * 16 + wid * 4) * 128u + (unsigned)head * 4u) * 1024u + lb * 16u;
    const __amdgpu_buffer_rsrc_t srdO = mk_srd(Of, 16u << 20);
    GAP();
    fin_group<0>(c, srdO, vo, il, make_iseq<16>{});
    asm volatile("" : "+v"(c.LS[0]), "+v"(c.LS[1]), "+v"(c.LS[2]), "+v"(c.LS[3]));
#pragma unroll
    for (int j = 0; j < 4; ++j) il[j] = __builtin_amdgcn_rcpf(c.LS[j][0]);
    GAP();
    fin_group<1>(c, srdO, vo, il, make_iseq<16>{});
    fin_group<2>(c, srdO, vo, il, make_iseq<16>{});
    fin_group<3>(c, srdO, vo, il, make_iseq<16>{});
    asm volatile("s_nop 7\n\ts_nop 1" ::: "memory");
    fin_group<4>(c, srdO, vo, il, make_iseq<16>{});
  }
  ST(4); KST_END(2);
}
template <int N> DI void touch(const char* p, int stride) {
  unsigned d[N];
#pragma unroll
  for (int i = 0; i < N; ++i) d[i] = *(const unsigned*)(p + (size_t)i * stride);
#pragma unroll
  for (int i = 0; i < N; ++i) asm volatile("" :: "v"(d[i]));
}

template <int NT, int KL> DI void mod_body(const u16* __restrict__ c, const u16* __restrict__ wmod, float* __restrict__ dst, int cb, int kh, float* red  ) {
  const int tid = threadIdx.x, cg = tid & 7, kg = tid >> 3, wv = tid >> 6;
  constexpr int NG = NT / 8, NI = KL / NG;
  u32x4 w[NI]; u16 cv[NI];
#pragma unroll
  for (int i = 0; i < NI; ++i) { const int k = kh * KL + kg + NG * i; w[i] = *(const u32x4*)(wmod + (size_t)k * 6144 + cb * 64 + cg * 8); cv[i] = c[k]; }
  float acc[8] = {0, 0, 0, 0, 0, 0, 0, 0};
#pragma unroll
  for (int i = 0; i < NI; ++i) {
    const float xx = bf2f(cv[i]), s = rbf(xx / (1.f + __expf(-xx)));
#pragma unroll
    for (int jj = 0; jj < 4; ++jj) { acc[2 * jj] += s * __uint_as_float(w[i][jj] << 16); acc[2 * jj + 1] += s * __uint_as_float(w[i][jj] & 0xffff0000u); }
  }
#pragma unroll
  for (int jj = 0; jj < 8; ++jj) { float a = acc[jj]; a += __shfl_xor(a, 8); a += __shfl_xor(a, 16); a += __shfl_xor(a, 32); acc[jj] = a; }
  if ((tid & 63) < 8) {
#pragma unroll
    for (int jj = 0; jj < 8; ++jj) red[wv * 64 + cg * 8 + jj] = acc[jj];
  }
  __syncthreads();
  if (tid < 64) { float s = 0;
#pragma unroll
    for (int g = 0; g < NT / 64; ++g) s += red[g * 64 + tid];
    dst[tid] = s; }
}
__global__ __launch_bounds__(512) void k_pre(const u16* __restrict__ c, const u16* __restrict__ wmod, float* __restrict__ modA, const u16* __restrict__ x, unsigned* __restrict__ flg) {
  __shared__ float red[8 * 64];
  const int b = blockIdx.x;
  KST_BEGIN(0);
  unsigned dx = 0;
  if (threadIdx.x == 0) flg[b * 64] = 0u;
  if (threadIdx.x < 256) dx = *(const unsigned*)((const char*)x + (size_t)b * 16384 + threadIdx.x * 64);
  mod_body<512, 128>(c, wmod, modA + (b & 7) * 2048 + (b >> 3) * 64, b >> 3, b & 7, red);
  asm volatile("" :: "v"(dx));
  KST_END(0);
}
DI f32x4 modv(const float* __restrict__ modp, int n) { const f32x4 a = *(const f32x4*)(modp + n), b = *(const f32x4*)(modp + 6144 + n); f32x4 o; for (int e = 0; e < 4; ++e) o[e] = rbf(a[e] + b[e]); return o; }
DI void unpack8(const u32x4& w, float* v) {
#pragma unroll
  for (int j = 0; j < 4; ++j) { v[2 * j] = __uint_as_float(w[j] << 16); v[2 * j + 1] = __uint_as_float(w[j] & 0xffff0000u); }
}
DI u32x4 pack8(const float* o) { return (u32x4){cvtpk(o[0], o[1]), cvtpk(o[2], o[3]), cvtpk(o[4], o[5]), cvtpk(o[6], o[7])}; }

DI void rbf2(float& a, float& b) { const unsigned w = cvtpk(a, b); a = __uint_as_float(w << 16); b = __uint_as_float(w & 0xffff0000u); }
DI void rbf8(float* v) { rbf2(v[0], v[1]); rbf2(v[2], v[3]); rbf2(v[4], v[5]); rbf2(v[6], v[7]); }
DI void modv8(const float* __restrict__ modp, int n, float* o) {
  const f32x4 a0 = *(const f32x4*)(modp + n), b0 = *(const f32x4*)(modp + 6144 + n), a1 = *(const f32x4*)(modp + n + 8), b1 = *(const f32x4*)(modp + 6144 + n + 8);
#pragma unroll
  for (int e = 0; e < 4; ++e) { o[e] = a0[e] + b0[e]; o[4 + e] = a1[e] + b1[e]; }
  rbf8(o);
}
template <int MODE>
__global__ __launch_bounds__(1024) void k_ln(const u16* __restrict__ x, const u16* __restrict__ Pf, const float* __restrict__ modp, u16* __restrict__ xf, u16* __restrict__ hf) {
  __shared__ float red[2][16][8];
  __shared__ __attribute__((aligned(16))) float msum[MODE == 0 ? 2048 : 4];
  const int tid = threadIdx.x;
  KST_BEGIN(MODE == 0 ? 1 : 4);
  const int r = tid & 7, c = tid >> 3, h = c & 1, wid = tid >> 6;
  const int row = blockIdx.x * 8 + r, mb = row >> 5, ml = row & 31;
  const size_t fo = ((size_t)mb * 64 + (c >> 1)) * 512 + (h * 32 + ml) * 8;
  const int n = 16 * (c >> 1) + 4 * h;
  float v[8], sh[8], sc[8];
  if (MODE == 0) {
    const u32x4 xw = *(const u32x4*)(x + (size_t)row * 1024 + 8 * c);
    { f32x2 a = {0.f, 0.f};
#pragma unroll
      for (int ks = 0; ks < 8; ++ks) a += *(const f32x2*)(modp + ks * 2048 + 2 * tid);
      float a0 = a[0], a1 = a[1]; rbf2(a0, a1); *(f32x2*)(msum + 2 * tid) = (f32x2){a0, a1}; }
    const unsigned s0 = h ? xw[0] : xw[2], s1 = h ? xw[1] : xw[3];
    const unsigned g0 = __shfl_xor(s0, 8), g1 = __shfl_xor(s1, 8);
    const u32x4 f = h ? (u32x4){g0, g1, xw[2], xw[3]} : (u32x4){xw[0], xw[1], g0, g1};
    __builtin_amdgcn_raw_buffer_store_b128(f, mk_srd(xf, 4u << 20), (unsigned)fo * 2u, 0, WT);
    unpack8(f, v);
  } else {
    const u32x4 xw = *(const u32x4*)(xf + fo), p0 = *(const u32x4*)(Pf + fo), p1 = *(const u32x4*)(Pf + fo + (size_t)2048 * 1024);
    float ga[8], a[8], b[8];
    modv8(modp, 3072 + n, sh); modv8(modp, 4096 + n, sc); modv8(modp, 2048 + n, ga);
    unpack8(xw, v); unpack8(p0, a); unpack8(p1, b);
#pragma unroll
    for (int j = 0; j < 8; ++j) a[j] += b[j];
    rbf8(a);
#pragma unroll
    for (int j = 0; j < 8; ++j) a[j] *= ga[j];
    rbf8(a);
#pragma unroll
    for (int j = 0; j < 8; ++j) v[j] += a[j];
    const u32x4 w = pack8(v);
    __builtin_amdgcn_raw_buffer_store_b128(w, mk_srd(xf, 4u << 20), (unsigned)fo * 2u, 0, WT);
    unpack8(w, v);
  }
  float s = ((v[0] + v[1]) + (v[2] + v[3])) + ((v[4] + v[5]) + (v[6] + v[7]));
  s += __shfl_xor(s, 8); s += __shfl_xor(s, 16); s += __shfl_xor(s, 32);
  if ((tid & 63) < 8) red[0][wid][r] = s;
  __syncthreads();
  if (MODE == 0) {
    const f32x4 a0 = *(const f32x4*)(msum + n), a1 = *(const f32x4*)(msum + n + 8), b0 = *(const f32x4*)(msum + 1024 + n), b1 = *(const f32x4*)(msum + 1024 + n + 8);
#pragma unroll
    for (int e = 0; e < 4; ++e) { sh[e] = a0[e]; sh[4 + e] = a1[e]; sc[e] = b0[e]; sc[4 + e] = b1[e]; }
  }
  float mean = 0;
#pragma unroll
  for (int w = 0; w < 16; ++w) mean += red[0][w][r];
  mean *= (1.f / 1024.f);
  float qv = 0;
#pragma unroll
  for (int j = 0; j < 8; ++j) { const float d = v[j] - mean; qv += d * d; }
  qv += __shfl_xor(qv, 8); qv += __shfl_xor(qv, 16); qv += __shfl_xor(qv, 32);
  if ((tid & 63) < 8) red[1][wid][r] = qv;
  __syncthreads();
  float var = 0;
#pragma unroll
  for (int w = 0; w < 16; ++w) var += red[1][w][r];
  var = rbf(var * (1.f / 1024.f));
  const float mb16 = rbf(mean), rsd = 1.f / rbf(sqrtf(rbf(var + 1e-5f)));
#pragma unroll
  for (int j = 0; j < 8; ++j) { v[j] -= mb16; sc[j] += 1.f; }
  rbf8(v); rbf8(sc);
#pragma unroll
  for (int j = 0; j < 8; ++j) v[j] *= rsd;
  rbf8(v);
#pragma unroll
  for (int j = 0; j < 8; ++j) v[j] *= sc[j];
  rbf8(v);
#pragma unroll
  for (int j = 0; j < 8; ++j) v[j] += sh[j];
  const unsigned ho = (((unsigned)(row >> 4) * 32u + (unsigned)(c >> 2)) * 1024u + (unsigned)(c & 3) * 256u + (unsigned)(row & 15) * 16u);
  __builtin_amdgcn_raw_buffer_store_b128(pack8(v), mk_srd(hf, 4u << 20), ho, 0, WT);
  KST_END(MODE == 0 ? 1 : 4);
}
template <int KORD> using SCfgT = GCfg<16, 4, 32, 8, false, KORD, 4, 4, 1, 1>;
using SCfg = SCfgT<1>;
template <int K, bool FIN, int NT0>
__global__ __launch_bounds__(256, 2) void k_gemm_sk(const u16* __restrict__ Af, const u16* __restrict__ B, u16* __restrict__ Pf,
                                                    const u16* __restrict__ cvec, const u16* __restrict__ wmod, float* __restrict__ modp,
                                                    const u16* __restrict__ x1f, u16* __restrict__ out, unsigned* __restrict__ flg) {
  using C = SCfgT<K == 4096 ? 0 : 1>;
  extern __shared__ __attribute__((aligned(16))) char lds[];
  KST_BEGIN(K == 4096 ? 3 : 6);
  if (K == 4096 && blockIdx.x >= 256) { const int b = blockIdx.x - 256; mod_body<256, 512>(cvec, wmod, modp + (b & 1) * 6144 + (32 + (b >> 1)) * 64, 32 + (b >> 1), b & 1, (float*)lds); KST_END(3); return; }
  constexpr int N = 1024;
  const unsigned tid = threadIdx.x, lane = tid & 63u, wid = rfl(tid >> 6);
  const int bid = blockIdx.x, xcd = bid & 7, j = bid >> 3;
  const int kh = xcd & 1, mg = xcd >> 1;
  const int mt = mg * 4 + (j & 3), nt = j >> 2;
  const int k0 = kh ? NT0 * 64 : 0, klen = kh ? K - NT0 * 64 : NT0 * 64;
  GState<C> g;
  const int mb0 = mt * 4 + wid;
  constexpr unsigned mbstride = (unsigned)K * 32u;
  g.srdA = mk_srd((const char*)Af + (size_t)(2 * mb0) * mbstride + (size_t)k0 * 32, mbstride + (unsigned)klen * 32u);
  g.voffA[0] = lane * 16u; g.voffA[1] = lane * 16u + mbstride;
  constexpr unsigned ldb = (unsigned)N * 2u;
  g.srdB = mk_srd((const char*)B + (size_t)k0 * ldb + (size_t)nt * 256, (unsigned)klen * ldb - (unsigned)nt * 256u);
  g_setup<C>(g, (unsigned)(size_t)lds, lane, wid, ldb);
  gemm_run<C>(g, klen / 64);
  const unsigned h = (lane >> 4) & 1u, hi = lane >> 5;
  const unsigned fo = (((unsigned)mb0 * 64u + (unsigned)nt * 8u + hi) * 512u + (h * 32u + (lane & 15u)) * 8u) * 2u;
  float acc[4][2][8];
#pragma unroll
  for (int i = 0; i < 4; ++i)
#pragma unroll
    for (int m = 0; m < 2; ++m)
#pragma unroll
      for (int e = 0; e < 4; ++e) {
        auto sw = __builtin_amdgcn_permlane32_swap(__float_as_uint(g.acc[2 * i][m][e]), __float_as_uint(g.acc[2 * i + 1][m][e]), false, false);
        acc[i][m][e] = __uint_as_float(sw[0]); acc[i][m][4 + e] = __uint_as_float(sw[1]);
      }
  if constexpr (!FIN) {
    const __amdgpu_buffer_rsrc_t srdP = mk_srd(Pf, 8u << 20);
#pragma unroll
    for (int i = 0; i < 4; ++i)
#pragma unroll
      for (int m = 0; m < 2; ++m) __builtin_amdgcn_raw_buffer_store_b128(pack8(acc[i][m]), srdP, (unsigned)kh * (4u << 20) + fo + i * 2048 + m * 256, 0, WT);
  } else {
    const __amdgpu_buffer_rsrc_t srdP = mk_srd(Pf, 4u << 20);
    unsigned* flag = flg + (128 + mt * 8 + nt) * 64;
    if (kh == 0) {
#pragma unroll
      for (int i = 0; i < 4; ++i)
#pragma unroll
        for (int m = 0; m < 2; ++m) __builtin_amdgcn_raw_buffer_store_b128(pack8(acc[i][m]), srdP, fo + i * 2048 + m * 256, 0, 16);
      asm volatile("s_waitcnt vmcnt(0)" ::: "memory");
      __syncthreads();
      if (tid == 0) __hip_atomic_store(flag, 1u, __ATOMIC_RELAXED, __HIP_MEMORY_SCOPE_AGENT);
    } else {
      u32x4 xw[4][2]; float gb[4][8];
#pragma unroll
      for (int i = 0; i < 4; ++i) { xw[i][0] = *(const u32x4*)((const char*)x1f + fo + i * 2048); xw[i][1] = *(const u32x4*)((const char*)x1f + fo + i * 2048 + 256);
        modv8(modp, 5120 + nt * 128 + 16 * (2 * i + hi) + 4 * h, gb[i]); }
      if (wid == 0) {
        for (unsigned spins = 0; spins < (1u << 22); ++spins) {
          if ((unsigned)__builtin_amdgcn_readfirstlane(__hip_atomic_load(flag, __ATOMIC_RELAXED, __HIP_MEMORY_SCOPE_AGENT)) != 0u) break;
          __builtin_amdgcn_s_sleep(1);
        }
      }
      asm volatile("" ::: "memory"); __builtin_amdgcn_s_barrier(); asm volatile("" ::: "memory");
      u32x4 pw[4][2];
#pragma unroll
      for (int i = 0; i < 4; ++i)
#pragma unroll
        for (int m = 0; m < 2; ++m) pw[i][m] = __builtin_amdgcn_raw_buffer_load_b128(srdP, fo + i * 2048 + m * 256, 0, 16);
      const unsigned oo = (((unsigned)mb0 * 32u + (lane & 15u)) * 1024u + (unsigned)nt * 128u + 16u * hi + 8u * h) * 2u;
      const __amdgpu_buffer_rsrc_t srdOut = mk_srd(out, 4u << 20);
#pragma unroll
      for (int i = 0; i < 4; ++i)
#pragma unroll
        for (int m = 0; m < 2; ++m) {
          float v[8], a[8];
          unpack8(xw[i][m], v); unpack8(pw[i][m], a);
#pragma unroll
          for (int jj = 0; jj < 8; ++jj) a[jj] += acc[i][m][jj];
          rbf8(a);
#pragma unroll
          for (int jj = 0; jj < 8; ++jj) a[jj] *= gb[i][jj];
          rbf8(a);
#pragma unroll
          for (int jj = 0; jj < 8; ++jj) v[jj] += a[jj];
          const u32x4 w = pack8(v);
          const unsigned s0 = h ? w[0] : w[2], s1 = h ? w[1] : w[3];
          const unsigned g0 = __shfl_xor(s0, 16), g1 = __shfl_xor(s1, 16);
          const u32x4 f = h ? (u32x4){g0, g1, w[2], w[3]} : (u32x4){w[0], w[1], g0, g1};
          __builtin_amdgcn_raw_buffer_store_b128(f, srdOut, oo + 64u * i + 32768u * m, 0, WT);
        }
    }
  }
  KST_END(K == 4096 ? 3 : 6);
}
#define SK2_NT0 28
DI float silu_mul(float g, float u) { return g * __builtin_amdgcn_rcpf(1.f + __builtin_amdgcn_exp2f(g * -1.4426950408889634f)) * u; }
#define U_NS 3
#define U_BP 3
using UCfg = GCfg<16, 8, 32, 14, true, 1, U_NS, 2, 1, U_BP>;
__global__ __launch_bounds__(512, 2) void k_gemm_gu(const u16* __restrict__ Af, const u16* __restrict__ Wg, const u16* __restrict__ Wu, u16* __restrict__ Act) {
  using C = UCfg;
  extern __shared__ __attribute__((aligned(16))) char lds[];
  constexpr int K = 1024, F = 3584;
  KST_BEGIN(5);
  const unsigned tid = threadIdx.x, wid = rfl(tid >> 6);
  const int bid = blockIdx.x, xcd = bid & 7, j = bid >> 3;
  const int mt = (xcd & 1) * 4 + (j & 3), nt = (xcd >> 1) * 8 + (j >> 2);
  GState<C> g;
  const int mb0 = mt * 8 + wid;
  {
    const unsigned lane = tid & 63u;
    constexpr unsigned mbstride = (unsigned)K * 32u;
    g.srdA = mk_srd((const char*)Af + (size_t)(2 * mb0) * mbstride, 2 * mbstride);
    g.voffA[0] = lane * 16u; g.voffA[1] = lane * 16u + mbstride;
    constexpr unsigned ldb = F * 2u;
    const bool up = wid >= 4;
    g.srdB = mk_srd((const char*)(up ? Wu : Wg) + (size_t)nt * 224, (unsigned)K * ldb - (unsigned)nt * 224u);
    g_setup<C>(g, (unsigned)(size_t)lds, lane, wid, ldb);
  }
  gemm_run<C>(g, K / 64);
  unsigned lane; asm volatile("v_mbcnt_lo_u32_b32 %0, -1, 0\n\tv_mbcnt_hi_u32_b32 %0, -1, %0" : "=v"(lane));
  const __amdgpu_buffer_rsrc_t srdO = mk_srd(Act, 2048u * F * 2u);
  const unsigned vo = ((unsigned)mb0 * 2u + (lane >> 5)) * (unsigned)((F >> 5) * 1024) + (lane & 31u) * 16u;
#pragma unroll
  for (int n = 0; n < 7; ++n) {
    unsigned q[2][2];
#pragma unroll
    for (int m = 0; m < 2; ++m) {
      float o[4];
#pragma unroll
      for (int e = 0; e < 4; ++e) o[e] = silu_mul(g.acc[2 * n][m][e], g.acc[2 * n + 1][m][e]);
      q[m][0] = cvtpk(o[0], o[1]); q[m][1] = cvtpk(o[2], o[3]);
    }
    auto s0 = __builtin_amdgcn_permlane32_swap(q[0][0], q[1][0], false, false); auto s1 = __builtin_amdgcn_permlane32_swap(q[0][1], q[1][1], false, false);
    const unsigned c16 = (unsigned)(nt * 7 + n);
    __builtin_amdgcn_raw_buffer_store_b128((u32x4){s0[0], s1[0], s0[1], s1[1]}, srdO, vo + (c16 >> 1) * 1024u + (c16 & 1u) * 512u, 0, WT);
  }
  KST_END(5);
}

extern "C" void kernel_launch(void* const* d_in, const int* in_sizes, int n_in, void* d_out, int out_size, void* d_ws, size_t ws_size, hipStream_t stream) {
  const u16* x = (const u16*)d_in[0]; const u16* kc = (const u16*)d_in[1]; const u16* vc = (const u16*)d_in[2];
  const u16* wq = (const u16*)d_in[3]; const u16* wo = (const u16*)d_in[4];
  const u16* wg = (const u16*)d_in[6]; const u16* wu = (const u16*)d_in[7]; const u16* wd = (const u16*)d_in[8];
  const u16* c = (const u16*)d_in[9]; const u16* wmod = (const u16*)d_in[10];
  u16* out = (u16*)d_out;
  char* ws = (char*)d_ws;
  float* mod = (float*)ws;
  u16* hf = (u16*)(ws + (1 << 20));
  u16* x1f = (u16*)(ws + (8 << 20));
  u16* of = (u16*)(ws + (16 << 20));
  u16* P = (u16*)(ws + (32 << 20));
  u16* act = (u16*)(ws + (64 << 20));
  hipFuncSetAttribute((const void*)k_attn, hipFuncAttributeMaxDynamicSharedMemorySize, SHM_ATTN);
  hipFuncSetAttribute((const void*)k_gemm_sk<4096, false, 32>, hipFuncAttributeMaxDynamicSharedMemorySize, SCfg::LDSB);
  hipFuncSetAttribute((const void*)k_gemm_sk<3584, true, SK2_NT0>, hipFuncAttributeMaxDynamicSharedMemorySize, SCfg::LDSB);
  hipFuncSetAttribute((const void*)k_gemm_gu, hipFuncAttributeMaxDynamicSharedMemorySize, UCfg::LDSB);
  float* modA = (float*)(ws + (256 << 10));
  unsigned* flg = (unsigned*)(ws + (512 << 10));
  u16* P2 = (u16*)(ws + (48 << 20));
  k_pre<<<256, 512, 0, stream>>>(c, wmod, modA, x, flg);
  k_ln<0><<<256, 1024, 0, stream>>>(x, nullptr, modA, x1f, hf);
  k_attn<<<256, 256, SHM_ATTN, stream>>>(hf, wq, kc, vc, of, wo, wg, wu, wd, wmod);
  k_gemm_sk<4096, false, 32><<<256 + 128, 256, SCfg::LDSB, stream>>>(of, wo, P, c, wmod, mod, nullptr, nullptr, nullptr);
  k_ln<1><<<256, 1024, 0, stream>>>(x, P, mod, x1f, hf);
  k_gemm_gu<<<256, 512, UCfg::LDSB, stream>>>(hf, wg, wu, act);
  k_gemm_sk<3584, true, SK2_NT0><<<256, 256, SCfg::LDSB, stream>>>(act, wd, P2, nullptr, nullptr, mod, x1f, out, flg);
}
```

```cpp
#include <hip/hip_runtime.h>
#include <utility>
#include <type_traits>
typedef unsigned short u16;
typedef short bf16x8 __attribute__((ext_vector_type(8)));
typedef short s16x4 __attribute__((ext_vector_type(4)));
typedef float f32x4 __attribute__((ext_vector_type(4)));
typedef float f32x16 __attribute__((ext_vector_type(16)));
typedef unsigned u32x4 __attribute__((ext_vector_type(4)));
typedef unsigned u32x2 __attribute__((ext_vector_type(2)));
typedef float f32x2 __attribute__((ext_vector_type(2)));
typedef __bf16 bf16x2_t __attribute__((ext_vector_type(2)));
#define DI __device__ __forceinline__
#define LAS __attribute__((address_space(3)))
#define GAP() __builtin_amdgcn_sched_barrier(0)
template <int... Is> using iseq = std::integer_sequence<int, Is...>;
template <int N> using make_iseq = std::make_integer_sequence<int, N>;

#define KST_BEGIN(k)
#define KST_END(k)
DI unsigned rfl(unsigned x) { return __builtin_amdgcn_readfirstlane(x); }
DI float bf2f(u16 v) { return __uint_as_float(((unsigned)v) << 16); }
DI unsigned cvtpk(float lo, float hi) { f32x2 v = {lo, hi}; bf16x2_t b = __builtin_convertvector(v, bf16x2_t); return __builtin_bit_cast(unsigned, b); }
DI float rbf(float f);
DI float rbf(float f) { return __uint_as_float(cvtpk(f, 0.f) << 16); }
#define KV_FIRST 12
#define HPAD 0u
#define OPAD 0u
#define WT 16
DI __amdgpu_buffer_rsrc_t mk_srd(const void* p, unsigned bytes) { return __builtin_amdgcn_make_buffer_rsrc((void*)p, (short)0, (int)bytes, 0x00020000); }

#define W4_ACLOB "a0","a1","a2","a3","a4","a5","a6","a7","a8","a9","a10","a11","a12","a13","a14","a15","a16","a17","a18","a19","a20","a21","a22","a23","a24","a25","a26","a27","a28","a29","a30","a31","a32","a33","a34","a35","a36","a37","a38","a39","a40","a41","a42","a43","a44","a45","a46","a47","a48","a49","a50","a51","a52","a53","a54","a55","a56","a57","a58","a59","a60","a61","a62","a63","a64","a65","a66","a67","a68","a69","a70","a71","a72","a73","a74","a75","a76","a77","a78","a79","a80","a81","a82","a83","a84","a85","a86","a87","a88","a89","a90","a91","a92","a93","a94","a95","a96","a97","a98","a99","a100","a101","a102","a103","a104","a105","a106","a107","a108","a109","a110","a111","a112","a113","a114","a115","a116","a117","a118","a119","a120","a121","a122","a123","a124","a125","a126","a127"
#define W4_ACLOB_HI "a128","a129","a130","a131","a132","a133","a134","a135","a136","a137","a138","a139","a140","a141","a142","a143","a144","a145","a146","a147","a148","a149","a150","a151","a152","a153","a154","a155","a156","a157","a158","a159","a160","a161","a162","a163","a164","a165","a166","a167","a168","a169","a170","a171","a172","a173","a174","a175","a176","a177","a178","a179","a180","a181","a182","a183","a184","a185","a186","a187","a188","a189","a190","a191","a192","a193","a194","a195","a196","a197","a198","a199","a200","a201","a202","a203","a204","a205","a206","a207","a208","a209","a210","a211","a212","a213","a214","a215","a216","a217","a218","a219","a220","a221","a222","a223","a224","a225","a226","a227","a228","a229","a230","a231","a232","a233","a234","a235","a236","a237","a238","a239","a240","a241","a242","a243","a244","a245","a246","a247","a248","a249","a250","a251","a252","a253","a254","a255"
template <int SH_, int NW_, int WR_, int NB16_, bool GU_, int KORD_, int NS_, int PDT_, int FD_, int BARPOS_, bool ASMACC_ = false>
struct GCfg {
  static constexpr bool ASMACC = ASMACC_;
  static constexpr int SH = SH_, NW = NW_, WR = WR_, NB16 = NB16_, KORD = KORD_, NS = NS_, PDT = PDT_, FD = FD_, BARPOS = BARPOS_;
  static constexpr bool GU = GU_;
  static constexpr int STEPS = SH == 32 ? 4 : 2;
  static constexpr int MR = WR / SH;
  static constexpr int NB = SH == 32 ? NB16 / 2 : NB16;
  static constexpr int NR16 = GU ? NB16 / 2 : NB16;
  static constexpr int PIECES = NB16 * 2;
  static constexpr int PB = (PIECES + NW - 1) / NW;
  static constexpr int STAGE = PB * NW * 1024;
  static constexpr int REGION = STAGE / 2;
  static constexpr int LDSB = NS * STAGE;
  static constexpr int NOPS = STEPS * MR + PB;
  static constexpr int GS = NB * MR;
  static constexpr int G = STEPS * GS;
  static constexpr int BSTEP = STEPS - FD;
  static constexpr int GB = BSTEP * GS + (BARPOS + 1) * MR - 1;
  static constexpr int dma_lo(int ks) { return (PB * ks) / STEPS; }
  static constexpr int step_ops(int ks) { return MR + dma_lo(ks + 1) - dma_lo(ks); }
  static constexpr int step_first(int ks) { return ks * MR + dma_lo(ks); }
  static constexpr int op_step(int j) { int ks = 0; while (ks < STEPS - 1 && j >= step_first(ks + 1)) ++ks; return ks; }
  static constexpr int op_gap(int j) { const int ks = op_step(j), i = j - step_first(ks), n = step_ops(ks); return ks * GS + (i * GS) / n; }
  static constexpr int ops_before(int g) { int c = 0; for (int j = 0; j < NOPS; ++j) if (op_gap(j) < g) ++c; return c; }
  static constexpr int NA(int ks) {
    const int ts = (ks + 1) % STEPS, jt = step_first(ts) + MR - 1;
    const int full = (ks == STEPS - 1) ? PDT - 1 : PDT;
    return (NOPS - 1 - jt) + (full - 1) * NOPS + ops_before(ks * GS);
  }
  static constexpr int NBW() { return (NS - 3) * NOPS + ops_before(GB); }
  static_assert(NS >= 3 && PDT >= 2 && FD <= STEPS - 0 && BSTEP >= 0, "");
  using acc_t = std::conditional_t<SH == 32, f32x16, f32x4>;
};

template <class C>
struct GState {
  typename C::acc_t acc[C::NB][C::MR];
  bf16x8 a[C::PDT * C::STEPS][C::MR];
  s16x4 blo[C::FD][C::NB], bhi[C::FD][C::NB];
  __amdgpu_buffer_rsrc_t srdA, srdB;
  unsigned voffA[C::MR];
  unsigned voffB[C::GU ? C::PB : 1];
  unsigned sB[C::GU ? 1 : C::PB];
  unsigned m0base, vrd, ktileB;
  unsigned long long tch[3]; unsigned tch_off;
  __amdgpu_buffer_rsrc_t srdK, srdV; unsigned kvm0, kvso, vk[4], vv;
};
struct GIter { unsigned va_cur, va_nxt, sA, sA0, m0_fill, sK_fill; };

template <class C, int SL, int OFF, int MRI> DI void a_load1(GState<C>& g, unsigned sA) {
  asm volatile("buffer_load_dwordx4 %0, %1, %2, %3 offen offset:%4" : "=v"(g.a[SL][MRI]) : "v"(g.voffA[MRI]), "s"(g.srdA), "s"(sA), "i"(OFF) : "memory");
}
template <class C, int Q> DI void b_dma1(GState<C>& g, unsigned m0v, unsigned sK) {
  if constexpr (C::GU)
    asm volatile("s_mov_b32 m0, %0\n\ts_nop 0\n\tbuffer_load_dwordx4 %1, %2, %3 offen lds" :: "s"(m0v + 1024u * Q), "v"(g.voffB[Q]), "s"(g.srdB), "s"(sK) : "memory");
  else
    asm volatile("s_mov_b32 m0, %0\n\ts_nop 0\n\tbuffer_load_dwordx4 %1, %2, %3 offen lds" :: "s"(m0v + 1024u * Q), "v"(g.voffB[0]), "s"(g.srdB), "s"(sK + g.sB[Q]) : "memory");
}
template <class C> DI void g_dummy(GState<C>& g) {
  asm volatile("s_mov_b32 m0, %0\n\ts_nop 0\n\tbuffer_load_dwordx4 %1, %2, %3 offen lds" :: "s"(g.m0base + (unsigned)(C::NS - 1) * C::STAGE), "v"(g.voffA[0]), "s"(g.srdA), "s"(0x80000000u) : "memory");
}
template <class C, int TI, int J, bool VA, bool VB> DI void g_op(GState<C>& g, const GIter& it) {
  constexpr int ks = C::op_step(J), i = J - C::step_first(ks);
  if constexpr (i < C::MR) {
    constexpr int PD = C::PDT * C::STEPS, SL = (TI * C::STEPS + ks - 1 + PD) % PD;
    if constexpr (VA) { if constexpr (ks == 0) a_load1<C, SL, 0, i>(g, it.sA0); else a_load1<C, SL, (ks - 1) * 1024, i>(g, it.sA); } else g_dummy<C>(g);
  } else {
    if constexpr (VB) b_dma1<C, C::dma_lo(ks) + i - C::MR>(g, it.m0_fill, it.sK_fill); else g_dummy<C>(g);
  }
}
template <class C, int TI, int GI, int... Js> DI void g_ops_at(GState<C>& g, const GIter& it, iseq<Js...>) {
  ((C::op_gap(Js) == GI ? g_op<C, TI, Js, true, true>(g, it) : (void)0), ...);
}
template <class C> constexpr int b_off(int KS, int NBI, int t) {
  if (C::SH == 32) {
    return ((2 * KS + t) * C::NR16 + (C::GU ? NBI : 2 * NBI)) * 256;
  } else {
    const int kb = 4 * KS + (C::KORD == 0 ? 2 * t : t);
    if (C::ASMACC) return (kb * C::NR16 + (NBI & ~1)) * 256 + 8 * (NBI & 1);
    return (kb * C::NR16 + (C::GU ? NBI / 2 : NBI)) * 256 + ((C::GU && (NBI & 1)) ? C::REGION : 0);
  }
}
template <class C, int BUF, int KS, int NBI> DI void b_read(GState<C>& g, unsigned va) {
  g.blo[BUF][NBI] = __builtin_amdgcn_ds_read_tr16_b64_v4i16((LAS s16x4*)(size_t)(va + b_off<C>(KS, NBI, 0)));
  g.bhi[BUF][NBI] = __builtin_amdgcn_ds_read_tr16_b64_v4i16((LAS s16x4*)(size_t)(va + b_off<C>(KS, NBI, 1)));
}
template <class C, int BUF, int KS, int... Ns> DI void b_read_all(GState<C>& g, unsigned va, iseq<Ns...>) { (b_read<C, BUF, KS, Ns>(g, va), ...); }

template <class C, int N, int SL> DI void a_wait(GState<C>& g) {
  static_assert(N >= 0 && N < 64, "vmcnt range");
  if constexpr (C::MR == 1) asm volatile("s_waitcnt vmcnt(%1)" : "+v"(g.a[SL][0]) : "i"(N) : "memory");
  else if constexpr (C::MR == 2) asm volatile("s_waitcnt vmcnt(%2)" : "+v"(g.a[SL][0]), "+v"(g.a[SL][1]) : "i"(N) : "memory");
  else asm volatile("s_waitcnt vmcnt(%4)" : "+v"(g.a[SL][0]), "+v"(g.a[SL][1]), "+v"(g.a[SL][2]), "+v"(g.a[SL][3]) : "i"(N) : "memory");
}

template <class C, int TI, int GI> DI void g_gap(GState<C>& g, const GIter& it) {
  constexpr int KS = GI / C::GS, r = GI % C::GS, NBI = r / C::MR, MRI = r % C::MR;
  constexpr int SL = TI * C::STEPS + KS, BUF = (C::FD == 1) ? 0 : (KS & 1), KN = (KS + C::FD) % C::STEPS;
  if constexpr (r == 0) { a_wait<C, C::NA(KS), SL>(g); GAP(); }
  {
    const bf16x8 bf = __builtin_shufflevector(g.blo[BUF][NBI], g.bhi[BUF][NBI], 0, 1, 2, 3, 4, 5, 6, 7);
    if constexpr (C::ASMACC && C::SH == 32) { constexpr int ao = 16 * (C::MR * NBI + MRI);
      asm volatile("v_mfma_f32_32x32x16_bf16 a[%c2:%c3], %0, %1, a[%c2:%c3]" :: "v"(bf), "v"(g.a[SL][MRI]), "i"(ao), "i"(ao + 15)); }
    else if constexpr (C::ASMACC) { constexpr int ao = 4 * (C::MR * NBI + MRI);
      asm volatile("v_mfma_f32_16x16x32_bf16 a[%c2:%c3], %0, %1, a[%c2:%c3]" :: "v"(bf), "v"(g.a[SL][MRI]), "i"(ao), "i"(ao + 3)); }
    else if constexpr (C::SH == 32) g.acc[NBI][MRI] = __builtin_amdgcn_mfma_f32_32x32x16_bf16(bf, g.a[SL][MRI], g.acc[NBI][MRI], 0, 0, 0);
    else if constexpr (C::GU) asm volatile("v_mfma_f32_16x16x32_bf16 %0, %1, %2, %0" : "+v"(g.acc[NBI][MRI]) : "v"(bf), "v"(g.a[SL][MRI]));
    else g.acc[NBI][MRI] = __builtin_amdgcn_mfma_f32_16x16x32_bf16(bf, g.a[SL][MRI], g.acc[NBI][MRI], 0, 0, 0);
  }
  if constexpr (GI == C::GB) {
    GAP();
    static_assert(C::NBW() < 64, "");
    asm volatile("s_waitcnt vmcnt(%0) lgkmcnt(0)\n\ts_barrier" :: "i"(C::NBW()) : "memory");
    GAP();
    b_read_all<C, BUF, KN>(g, it.va_nxt, make_iseq<C::BARPOS + 1>{});
  } else if constexpr (MRI == C::MR - 1) {
    if constexpr (KS == C::BSTEP) { if constexpr (NBI > C::BARPOS) b_read<C, BUF, KN, NBI>(g, it.va_nxt); }
    else if constexpr (KS > C::BSTEP) b_read<C, BUF, KN, NBI>(g, it.va_nxt);
    else b_read<C, BUF, KN, NBI>(g, it.va_cur);
  }
  g_ops_at<C, TI, GI>(g, it, make_iseq<C::NOPS>{});
  GAP();
}
template <class C, int TI, int... Gs> DI void g_gaps(GState<C>& g, const GIter& it, iseq<Gs...>) { (g_gap<C, TI, Gs>(g, it), ...); }
template <class C, int TI> DI void g_tile(GState<C>& g, int t, unsigned& stg, unsigned& pstg) {
  unsigned nstg = stg + C::STAGE; if (nstg == C::NS * C::STAGE) nstg = 0;
  GIter it;
  it.va_cur = g.vrd + stg; it.va_nxt = g.vrd + nstg;
  it.sA = (unsigned)(C::STEPS * 1024) * (unsigned)(t + C::PDT); it.sA0 = it.sA - 1024u;
  it.m0_fill = g.m0base + pstg; it.sK_fill = g.ktileB * (unsigned)(t - 1 + C::NS);
  g_gaps<C, TI>(g, it, make_iseq<C::G>{});
  pstg = stg; stg = nstg;
}
template <class C, int... TIs> DI void g_body(GState<C>& g, int t, unsigned& stg, unsigned& pstg, iseq<TIs...>) { (g_tile<C, TIs>(g, t + TIs, stg, pstg), ...); }

template <class C, int I> DI void g_touch(GState<C>& g) {
  asm volatile("s_mov_b32 m0, %0\n\ts_nop 0\n\tglobal_load_lds_dword %1, %2" :: "s"(g.m0base + (unsigned)(C::NS - 1) * C::STAGE), "v"(g.tch_off), "s"(g.tch[I]) : "memory");
}
template <class C, int I> DI void g_kv(GState<C>& g) {
  constexpr int q = I & 3;
  if constexpr ((I >> 2) == 1) asm volatile("s_mov_b32 m0, %0\n\ts_nop 0\n\tbuffer_load_dwordx4 %1, %2, %3 offen lds" :: "s"(g.kvm0 + 0x8000u + 1024u * q), "v"(g.vv), "s"(g.srdV), "s"(g.kvso + 2048u * (q >> 1) + 128u * (q & 1)) : "memory");
  else asm volatile("s_mov_b32 m0, %0\n\ts_nop 0\n\tbuffer_load_dwordx4 %1, %2, %3 offen offset:%c4 lds" :: "s"(g.kvm0 + (I >> 3) * 0x4000u), "v"(g.vk[q]), "s"(g.srdK), "s"(g.kvso + (I >> 3) * 16384u), "i"(1024 * q) : "memory");
}
template <class C> constexpr int dummy_index(int V, int J) {
  const int ks = C::op_step(J), i = J - C::step_first(ks);
  if (i >= C::MR || (C::STEPS * (V + C::PDT) + ks - 1) >= 0) return -1;
  return (V + (C::NS - 1)) * C::STEPS * C::MR + ks * C::MR + i;
}
template <class C, int V, int J> DI void g_virt_op(GState<C>& g, const GIter& it) {
  constexpr int ks = C::op_step(J);
  constexpr int TI = ((V % C::PDT) + C::PDT) % C::PDT;
  constexpr bool VA = (C::STEPS * (V + C::PDT) + ks - 1) >= 0, VB = (V - 1 + C::NS) >= 0;
  if constexpr (V == -1 && J == C::ops_before(C::GB)) asm volatile("s_waitcnt vmcnt(%0)\n\ts_barrier" :: "i"(C::NBW()) : "memory");
  constexpr int di = C::ASMACC ? dummy_index<C>(V, J) : -1;
  if constexpr (di >= 0 && di < 3) g_touch<C, di>(g);
  else if constexpr (di >= KV_FIRST && di < KV_FIRST + 12) g_kv<C, di - KV_FIRST>(g);
  else g_op<C, TI, J, VA, VB>(g, it);
}
template <class C, int V, int... Js> DI void g_virt(GState<C>& g, iseq<Js...>) {
  GIter it; constexpr int tb = V - 1 + C::NS;
  it.va_cur = it.va_nxt = 0; it.sA = (unsigned)(C::STEPS * 1024) * (unsigned)(V + C::PDT >= 0 ? V + C::PDT : 0); it.sA0 = it.sA - 1024u;
  it.m0_fill = g.m0base + (unsigned)(tb >= 0 ? tb : 0) * C::STAGE; it.sK_fill = g.ktileB * (unsigned)(tb >= 0 ? tb : 0);
  (g_virt_op<C, V, Js>(g, it), ...);
  if constexpr (V == -1 && C::ops_before(C::GB) == C::NOPS) asm volatile("s_waitcnt vmcnt(%0)\n\ts_barrier" :: "i"(C::NBW()) : "memory");
}
template <class C, int... Vs> DI void g_prologue(GState<C>& g, iseq<Vs...>) { constexpr int NV = sizeof...(Vs); (g_virt<C, Vs - NV>(g, make_iseq<C::NOPS>{}), ...); }

template <class C, bool PSWAP = false> DI void g_setup(GState<C>& g, unsigned ldsb, unsigned lane, unsigned wid, unsigned ldb) {
  g.m0base = rfl(ldsb + wid * (unsigned)C::PB * 1024u);
  g.ktileB = 64u * ldb;
  const unsigned q = (lane & 15u) >> 2, p0 = lane & 3u, p = PSWAP ? (((p0 & 1u) << 1) | (p0 >> 1)) : p0, g4 = lane >> 4;
  if constexpr (C::SH == 32) {
    const unsigned h = lane >> 5, blk = g4 & 1u;
    g.vrd = ldsb + blk * (C::GU ? (unsigned)C::REGION : 256u) + ((4u * h + q) ^ (4u * blk)) * 32u + 8u * p;
  } else {
    g.vrd = ldsb + (g4 >> 1) * (C::KORD == 0 ? 1u : 2u) * (unsigned)(C::NR16 * 256) + (4u * (g4 & 1u) + q) * 32u + (PSWAP ? (p0 >> 1) * 256u + (p0 & 1u) * 16u : 8u * p0);
  }
  if constexpr (C::GU) {
    const unsigned wr = wid % (C::NW / 2), up = wid / (C::NW / 2);
#pragma unroll
    for (int qq = 0; qq < C::PB; ++qq) {
      const unsigned P = wr * C::PB + qq, s = 4u * P + g4 * 0u + (lane >> 4), kb = s / (unsigned)C::NR16, nb = s % (unsigned)C::NR16;
      const unsigned row = ((lane >> 1) & 7u) ^ ((C::SH == 32 && up) ? 4u : 0u);
      g.voffB[qq] = (P < (unsigned)(C::NR16 * 2)) ? (8u * kb + row) * ldb + 32u * nb + 16u * (lane & 1u) : 0x80000000u;
    }
  } else {
    static_assert(C::GU || (C::NR16 % 4 == 0), "");
    const unsigned sub = lane >> 4;
    const unsigned row = ((lane >> 1) & 7u) ^ ((C::SH == 32 && (sub & 1u)) ? 4u : 0u);
    g.voffB[0] = row * ldb + 32u * sub + 16u * (lane & 1u);
#pragma unroll
    for (int qq = 0; qq < C::PB; ++qq) { const unsigned P = wid * C::PB + qq, kb = P / (unsigned)(C::NR16 / 4), j = P % (unsigned)(C::NR16 / 4); g.sB[qq] = rfl(8u * kb * ldb + 128u * j); }
  }
}

template <int R> DI void acc_zero1() { asm volatile("v_accvgpr_write_b32 a[%c0], 0" :: "i"(R) : W4_ACLOB); }
template <int... Rs> DI void acc_zero(iseq<Rs...>) { (acc_zero1<Rs>(), ...); asm volatile("s_nop 1"); }
template <class C> DI void gemm_run(GState<C>& g, int NT) {
  constexpr int NV = C::PDT > C::NS - 1 ? C::PDT : C::NS - 1;
  if constexpr (!C::ASMACC) {
#pragma unroll
    for (int n = 0; n < C::NB; ++n)
#pragma unroll
      for (int m = 0; m < C::MR; ++m)
#pragma unroll
        for (int e = 0; e < (C::SH == 32 ? 16 : 4); ++e) g.acc[n][m][e] = 0.f;
  }
  g_prologue<C>(g, make_iseq<NV>{});
  if constexpr (C::ASMACC) acc_zero(make_iseq<C::NB * C::MR * (C::SH == 32 ? 16 : 4)>{});
  b_read_all<C, 0, 0>(g, g.vrd, make_iseq<C::NB>{});
  if constexpr (C::FD == 2) b_read_all<C, 1, 1>(g, g.vrd, make_iseq<C::NB>{});
  asm volatile("s_nop 1");
  GAP();
  unsigned stg = 0, pstg = (C::NS - 1) * C::STAGE;
  for (int t = 0; t < NT; t += C::PDT) g_body<C>(g, t, stg, pstg, make_iseq<C::PDT>{});
  asm volatile("s_waitcnt vmcnt(0) lgkmcnt(0)\n\ts_nop 7\n\ts_nop 7" ::: "memory");
#pragma unroll
  for (int i = 0; i < C::PDT * C::STEPS; ++i)
#pragma unroll
    for (int m = 0; m < C::MR; ++m) asm volatile("" :: "v"(g.a[i][m]));
  GAP();
}
typedef __bf16 abf16x8 __attribute__((ext_vector_type(8)));
typedef __bf16 abf16x4 __attribute__((ext_vector_type(4)));
#define ATTN_THR 64.0f
constexpr unsigned KRS = 256u, KTILEB = 64u * KRS;
#define KRING 0x0000u
#define VRING 0x8000u
#define QGEMM_LDS 0x10000u
DI float max3f(float a, float b, float c) { float r; asm("v_max3_f32 %0, %1, %2, %3" : "=v"(r) : "v"(a), "v"(b), "v"(c)); return r; }
DI float max2f(float a, float b) { float r; asm("v_max_f32_e32 %0, %1, %2" : "=v"(r) : "v"(a), "v"(b)); return r; }
DI float fadd_s(float a, float b) { float r; asm("v_add_f32_e32 %0, %1, %2" : "=v"(r) : "v"(a), "v"(b)); return r; }
DI float fmul_s(float a, float b) { float r; asm("v_mul_f32_e32 %0, %1, %2" : "=v"(r) : "v"(a), "v"(b)); return r; }
DI float fsub_s(float a, float b) { float r; asm("v_sub_f32_e32 %0, %1, %2" : "=v"(r) : "v"(a), "v"(b)); return r; }
struct W4C {
  unsigned vkb[4], vvb, vksrc[4], vvsrc[4], m0k, m0v, sofk, sofv;
  __amdgpu_buffer_rsrc_t srdK, srdV;
  f32x4 S[2][4][4];
  f32x4 NEGM[4];
  abf16x8 Vf[8][2];
  unsigned pk[4][2][4];
  abf16x4 vlo[8][2], vup[8][2];
  float mhat[4], fct[4]; bool pend;
  f32x4 LS[4];
  u32x4 ones;
  float mx[4];
  unsigned sk_m0, sk_so, sv_m0, sv_so;
  unsigned pf_lo, pf_hi, pf_off, pf_m0;
};
constexpr int AO_(int n, int j) { return 4 * (4 * n + j); }
constexpr int AQ_(int j, int s) { return 128 + 16 * j + 4 * s; }
constexpr int AK_(int i, int s) { return 192 + 16 * i + 4 * s; }

template <int I, int SLOT> DI void tr_read(W4C& c) {
  constexpr int u = I >> 4, n = (I >> 1) & 7, t = I & 1, off = SLOT * 0x4000 + (4 * u + 2 * t) * 2048 + n * 256;
  if constexpr (t == 0) asm volatile("ds_read_b64_tr_b16 %0, %1 offset:%2" : "=v"(c.vlo[n][u]) : "v"(c.vvb), "i"(off) : "memory");
  else                  asm volatile("ds_read_b64_tr_b16 %0, %1 offset:%2" : "=v"(c.vup[n][u]) : "v"(c.vvb), "i"(off) : "memory");
}
template <int I, int SLOT> DI void k_read(W4C& c) {
  constexpr int s = I >> 2, i = I & 3;
  asm volatile("ds_read_b128 a[%c1:%c2], %0 offset:%c3" :: "v"(c.vkb[s]), "i"(AK_(i, s)), "i"(AK_(i, s) + 3), "i"(SLOT * 0x4000 + 4096 * i) : "memory");
}
template <int X, bool FIRST, int M> DI void qk_mfma(W4C& c) {
  constexpr int s = M >> 4, i = (M >> 2) & 3, j = M & 3, ak = AK_(i, s), aq = AQ_(j, s);
  if constexpr (s == 0) {
    if constexpr (FIRST) asm volatile("v_mfma_f32_16x16x32_bf16 %0, a[%c1:%c2], a[%c3:%c4], 0" : "=&v"(c.S[X][i][j]) : "i"(ak), "i"(ak + 3), "i"(aq), "i"(aq + 3));
    else                 asm volatile("v_mfma_f32_16x16x32_bf16 %0, a[%c2:%c3], a[%c4:%c5], %1" : "=&v"(c.S[X][i][j]) : "v"(c.NEGM[j]), "i"(ak), "i"(ak + 3), "i"(aq), "i"(aq + 3));
  } else asm volatile("v_mfma_f32_16x16x32_bf16 %0, a[%c1:%c2], a[%c3:%c4], %0" : "+v"(c.S[X][i][j]) : "i"(ak), "i"(ak + 3), "i"(aq), "i"(aq + 3));
}
template <int M, bool ZERO> DI void pv_mfma(W4C& c) {
  constexpr int u = M >> 5, n = (M >> 2) & 7, j = M & 3, ao = AO_(n, j);
  const u32x4 pw = {c.pk[j][u][0], c.pk[j][u][1], c.pk[j][u][2], c.pk[j][u][3]};
  if constexpr (ZERO && u == 0) asm volatile("v_mfma_f32_16x16x32_bf16 a[%c2:%c3], %0, %1, 0" :: "v"(c.Vf[n][u]), "v"(pw), "i"(ao), "i"(ao + 3));
  else                        asm volatile("v_mfma_f32_16x16x32_bf16 a[%c2:%c3], %0, %1, a[%c2:%c3]" :: "v"(c.Vf[n][u]), "v"(pw), "i"(ao), "i"(ao + 3));
}
template <int X, int H, int E> DI void op_exp(W4C& c) { constexpr int i = 2 * H + (E >> 4), j = (E >> 2) & 3, r = E & 3; c.S[X][i][j][r] = __builtin_amdgcn_exp2f(c.S[X][i][j][r]); }
template <int J, int U> DI void ls_mfma(W4C& c) {
  const u32x4 pw = {c.pk[J][U][0], c.pk[J][U][1], c.pk[J][U][2], c.pk[J][U][3]};
  asm volatile("v_mfma_f32_16x16x32_bf16 %0, %1, %2, %0" : "+v"(c.LS[J]) : "v"(c.ones), "v"(pw));
}
template <int X, int H, int J> DI void op_cvt(W4C& c) {
  constexpr int ih = J >> 3, j = (J >> 1) & 3, r = 2 * (J & 1), p = 2 * ih + (J & 1);
  c.pk[j][H][p] = cvtpk(c.S[X][2 * H + ih][j][r], c.S[X][2 * H + ih][j][r + 1]);
  if constexpr (H == 1) asm volatile("" : "+v"(c.pk[j][H][p]));
}
template <int X, int I> DI float sval(W4C& c) { return c.S[X][I >> 4][(I >> 2) & 3][I & 3]; }
template <int X, int G> DI void op_max2(W4C& c) {
  constexpr int a = 2 * (G & 1);
  if constexpr (G < 2) asm volatile("v_max3_f32 %0, %2, %3, %4\n\tv_max3_f32 %1, %5, %6, %7" : "=&v"(c.mx[a]), "=&v"(c.mx[a + 1])
                                    : "v"(sval<X, 6 * G>(c)), "v"(sval<X, 6 * G + 1>(c)), "v"(sval<X, 6 * G + 2>(c)), "v"(sval<X, 6 * G + 3>(c)), "v"(sval<X, 6 * G + 4>(c)), "v"(sval<X, 6 * G + 5>(c)));
  else if constexpr (G < 15) asm volatile("v_max3_f32 %0, %0, %2, %3\n\tv_max3_f32 %1, %1, %4, %5" : "+v"(c.mx[a]), "+v"(c.mx[a + 1])
                    : "v"(sval<X, 4 * G + 4>(c)), "v"(sval<X, 4 * G + 5>(c)), "v"(sval<X, 4 * G + 6>(c)), "v"(sval<X, 4 * G + 7>(c)));
  else asm volatile("v_max3_f32 %0, %0, %1, %2\n\tv_max_f32 %0, %0, %3" : "+v"(c.mx[0]) : "v"(c.mx[1]), "v"(c.mx[2]), "v"(c.mx[3]));
}
constexpr int NGB = 72;
constexpr int EB0 = 18;
constexpr int eb_lo(int g) { return g <= EB0 ? 0 : (32 * (g - EB0)) / (NGB - EB0); }
#define CVT1A 8
template <int X, bool FIRST, int M> DI void gapA(W4C& c, const int t) {
  constexpr int Xp = X ^ 1;
  qk_mfma<X, FIRST, M>(c);
  GAP();
  if constexpr (!FIRST) {
    if constexpr ((M & 1) == 0) op_exp<Xp, 1, M / 2>(c);
    else {
      constexpr int o = M >> 1;
      if constexpr (o < 8) { tr_read<2 * o, Xp>(c); tr_read<2 * o + 1, Xp>(c); }
      else if constexpr (o < 24) { tr_read<8 + o, Xp>(c); op_cvt<Xp, 0, o - 8>(c); }
      else { op_cvt<Xp, 1, o - 24>(c); }
    }
  }
  if constexpr (M == 51) { c.sk_m0 = c.m0k + (unsigned)X * 0x4000u; asm volatile("" : "+s"(c.sk_m0)); }
  if constexpr (M == 53) { c.sk_so = KTILEB * (unsigned)(t + 2) + c.sofk; asm volatile("" : "+s"(c.sk_so)); }
  if constexpr (M == 55) { c.sv_m0 = c.m0v + (unsigned)Xp * 0x4000u; asm volatile("" : "+s"(c.sv_m0)); }
  if constexpr (M == 57) { c.sv_so = KTILEB * (unsigned)(t + 1) + c.sofv; asm volatile("" : "+s"(c.sv_so)); }
  if constexpr (M == 61) asm volatile("s_mov_b32 m0, %0" :: "s"(c.sk_m0) : "memory");
  if constexpr (!FIRST && M == 24) asm volatile("" :: "v"(c.NEGM[0]), "v"(c.NEGM[1]), "v"(c.NEGM[2]), "v"(c.NEGM[3]));
  GAP();
}
#define W4_VLO "+v"(c.vlo[0][0]), "+v"(c.vlo[1][0]), "+v"(c.vlo[2][0]), "+v"(c.vlo[3][0]), "+v"(c.vlo[4][0]), "+v"(c.vlo[5][0]), "+v"(c.vlo[6][0]), "+v"(c.vlo[7][0]), \
               "+v"(c.vlo[0][1]), "+v"(c.vlo[1][1]), "+v"(c.vlo[2][1]), "+v"(c.vlo[3][1]), "+v"(c.vlo[4][1]), "+v"(c.vlo[5][1]), "+v"(c.vlo[6][1]), "+v"(c.vlo[7][1])
#define W4_VUP "+v"(c.vup[0][0]), "+v"(c.vup[1][0]), "+v"(c.vup[2][0]), "+v"(c.vup[3][0]), "+v"(c.vup[4][0]), "+v"(c.vup[5][0]), "+v"(c.vup[6][0]), "+v"(c.vup[7][0]), \
               "+v"(c.vup[0][1]), "+v"(c.vup[1][1]), "+v"(c.vup[2][1]), "+v"(c.vup[3][1]), "+v"(c.vup[4][1]), "+v"(c.vup[5][1]), "+v"(c.vup[6][1]), "+v"(c.vup[7][1])
DI void v_concat(W4C& c) {
#pragma unroll
  for (int u = 0; u < 2; ++u)
#pragma unroll
    for (int n = 0; n < 8; ++n) c.Vf[n][u] = __builtin_shufflevector(c.vlo[n][u], c.vup[n][u], 0, 1, 2, 3, 4, 5, 6, 7);
}
template <int X, bool FIRST, int... Ms> DI void phaseA_(W4C& c, const int t, iseq<Ms...>) {
  (gapA<X, FIRST, Ms>(c, t), ...);
  if constexpr (FIRST) asm volatile("s_waitcnt vmcnt(0) lgkmcnt(0)\n\ts_barrier\n\ts_nop 7" ::: "memory");
  else { asm volatile("s_waitcnt vmcnt(%c16) lgkmcnt(0)" : W4_VLO : "i"(X == 1 ? 1 : 0) : "memory"); asm volatile("s_barrier\n\ts_nop 1" : W4_VUP :: "memory"); }
  GAP();
  if constexpr (!FIRST) v_concat(c);
}
template <int X, bool FIRST> DI void phaseA(W4C& c, const int t) { phaseA_<X, FIRST>(c, t, make_iseq<64>{}); }

template <int X, bool INIT> DI void new_reference(W4C& c) {
#pragma unroll
  for (int j = 0; j < 4; ++j) {
    float m0 = max3f(c.S[X][0][j][0], c.S[X][0][j][1], c.S[X][0][j][2]), m1 = max2f(c.S[X][0][j][3], c.S[X][1][j][0]);
    m0 = max3f(m0, c.S[X][1][j][1], c.S[X][1][j][2]); m1 = max3f(m1, c.S[X][1][j][3], c.S[X][2][j][0]);
    m0 = max3f(m0, c.S[X][2][j][1], c.S[X][2][j][2]); m1 = max3f(m1, c.S[X][2][j][3], c.S[X][3][j][0]);
    m0 = max3f(m0, c.S[X][3][j][1], c.S[X][3][j][2]); m1 = max2f(m1, c.S[X][3][j][3]);
    float mm = max2f(m0, m1);
    mm = max2f(mm, __shfl_xor(mm, 16)); mm = max2f(mm, __shfl_xor(mm, 32));
    const float dl = INIT ? mm : max2f(mm, 0.f);
    if constexpr (INIT) { c.mhat[j] = dl; c.fct[j] = 1.f; }
    else { c.mhat[j] = fadd_s(c.mhat[j], dl); c.fct[j] = __builtin_amdgcn_exp2f(-dl); }
#pragma unroll
    for (int i = 0; i < 4; ++i) { _Pragma("unroll") for (int r = 0; r < 4; ++r) c.S[X][i][j][r] = fsub_s(c.S[X][i][j][r], dl); }
    const float nm = -c.mhat[j];
    c.NEGM[j] = (f32x4){nm, nm, nm, nm};
    asm volatile("" : "+v"(c.NEGM[j]));
  }
  asm volatile("s_nop 1");
}
constexpr int pvi(int G) { return G < 28 ? G : G - 4; }
template <int X, bool PEEL, bool ZERO1, int M> DI void gapB(W4C& c, const int t) {
  constexpr int Xp = X ^ 1;
  if constexpr (!PEEL) {
    if constexpr (M >= 28 && M < 32) ls_mfma<M - 28, 0>(c); else if constexpr (M >= 68) ls_mfma<M - 68, 1>(c); else pv_mfma<pvi(M), ZERO1>(c);
    GAP();
  }
  constexpr int kq = M == 0 ? 0 : M == 22 ? 1 : M == 27 ? 2 : M == 32 ? 3 : -1, vq = M == 40 ? 0 : M == 45 ? 1 : M == 50 ? 2 : M == 55 ? 3 : -1;
  if constexpr (kq >= 0) asm volatile("buffer_load_dwordx4 %0, %1, %2 offen offset:%c3 lds" :: "v"(c.vksrc[kq]), "s"(c.srdK), "s"(c.sk_so), "i"(1024 * kq) : "memory");
  if constexpr (M == 37) asm volatile("s_mov_b32 m0, %0" :: "s"(c.sv_m0) : "memory");
  if constexpr (vq >= 0) asm volatile("buffer_load_dwordx4 %0, %1, %2 offen offset:%c3 lds" :: "v"(c.vvsrc[vq]), "s"(c.srdV), "s"(c.sv_so), "i"(1024 * vq) : "memory");
  if constexpr (!PEEL && M >= 1 && M <= 16) { op_max2<X, M - 1>(c); if constexpr (M & 1) op_cvt<Xp, 1, 8 + (M - 1) / 2>(c); }
  if constexpr (M == 17) {
    if constexpr (PEEL) new_reference<X, true>(c);
    else if (__builtin_expect(__any((int)(c.mx[0] > ATTN_THR)), 0)) { new_reference<X, false>(c); c.pend = true; GAP(); }
  }
  if constexpr (M >= EB0) {
    constexpr int e0 = eb_lo(M), e1 = eb_lo(M + 1);
    if constexpr (e0 < e1) op_exp<X, 0, e0>(c);
    else {
      constexpr int fi = (M - EB0) - e0;
      if constexpr (fi < 16) k_read<fi, Xp>(c);
    }
  }
  if constexpr (X == 0 && M == 60) {
    unsigned plo, phi;
    asm volatile("v_readlane_b32 %0, %2, %4\n\tv_readlane_b32 %1, %3, %4\n\ts_mov_b32 m0, %5" : "=s"(plo), "=s"(phi) : "v"(c.pf_lo), "v"(c.pf_hi), "s"((unsigned)(t >> 1)), "s"(c.pf_m0) : "memory");
    c.sk_m0 = plo; c.sk_so = phi;
  }
  if constexpr (X == 0 && M == 65) {
    const unsigned long long pb = ((unsigned long long)c.sk_so << 32) | c.sk_m0;
    asm volatile("global_load_lds_dword %0, %1" :: "v"(c.pf_off), "s"(pb) : "memory");
  }
  if constexpr (M == NGB - 1) asm volatile("" :: "v"(c.S[X][0][0]), "v"(c.S[X][0][1]), "v"(c.S[X][0][2]), "v"(c.S[X][0][3]), "v"(c.S[X][1][0]), "v"(c.S[X][1][1]), "v"(c.S[X][1][2]), "v"(c.S[X][1][3]));
  GAP();
}
template <int R> DI void resc4(float f) {
  float t0, t1, t2, t3;
  asm volatile("v_accvgpr_read_b32 %0, a[%c5]\n\tv_accvgpr_read_b32 %1, a[%c6]\n\tv_accvgpr_read_b32 %2, a[%c7]\n\tv_accvgpr_read_b32 %3, a[%c8]\n\t"
               "v_mul_f32 %0, %4, %0\n\tv_mul_f32 %1, %4, %1\n\tv_mul_f32 %2, %4, %2\n\tv_mul_f32 %3, %4, %3\n\t"
               "v_accvgpr_write_b32 a[%c5], %0\n\tv_accvgpr_write_b32 a[%c6], %1\n\tv_accvgpr_write_b32 a[%c7], %2\n\tv_accvgpr_write_b32 a[%c8], %3"
               : "=&v"(t0), "=&v"(t1), "=&v"(t2), "=&v"(t3) : "v"(f), "i"(R), "i"(R + 1), "i"(R + 2), "i"(R + 3) : W4_ACLOB);
}
template <int... Is> DI void resc_all(W4C& c, iseq<Is...>) { (resc4<4 * Is>(c.fct[Is & 3]), ...); }
template <int X, bool PEEL, bool ZERO1, int... Ms> DI void phaseB_(W4C& c, const int t, iseq<Ms...>) {
  (gapB<X, PEEL, ZERO1, Ms>(c, t), ...);
  if constexpr (!PEEL) {
    if (__builtin_expect(c.pend, 0)) {
      asm volatile("s_nop 7\n\ts_nop 7" ::: W4_ACLOB);
      resc_all(c, make_iseq<32>{});
#pragma unroll
      for (int j = 0; j < 4; ++j) { c.LS[j] = c.LS[j] * c.fct[j]; asm volatile("" : "+v"(c.LS[j])); }
      asm volatile("s_nop 1" ::: W4_ACLOB);
      c.pend = false;
    }
  }
  asm volatile("s_waitcnt lgkmcnt(0)" ::: "memory");
  GAP();
}
template <int X, bool PEEL, bool ZERO1> DI void phaseB(W4C& c, const int t) { phaseB_<X, PEEL, ZERO1>(c, t, make_iseq<NGB>{}); }
template <int... Is> DI void k_reads0(W4C& c, iseq<Is...>) { (k_read<Is, 0>(c), ...); }
template <int... Is> DI void tr_reads1(W4C& c, iseq<Is...>) { (tr_read<Is, 1>(c), ...); }
template <int... Is> DI void drain_exp(W4C& c, iseq<Is...>) { (op_exp<1, 1, Is>(c), ...); }
template <int... Is> DI void drain_cvt0(W4C& c, iseq<Is...>) { (op_cvt<1, 0, Is>(c), ...); }
template <int... Is> DI void drain_cvt1(W4C& c, iseq<Is...>) { (op_cvt<1, 1, Is>(c), ...); }
template <int... Ms> DI void pv_all(W4C& c, iseq<Ms...>) { (pv_mfma<Ms, false>(c), ...); }
template <int R0, int R1> DI u32x4 acc_pack8(float f) {
  float o[8];
  asm volatile("v_accvgpr_read_b32 %0, a[%c8]\n\tv_accvgpr_read_b32 %1, a[%c9]\n\tv_accvgpr_read_b32 %2, a[%c10]\n\tv_accvgpr_read_b32 %3, a[%c11]\n\t"
               "v_accvgpr_read_b32 %4, a[%c12]\n\tv_accvgpr_read_b32 %5, a[%c13]\n\tv_accvgpr_read_b32 %6, a[%c14]\n\tv_accvgpr_read_b32 %7, a[%c15]"
               : "=v"(o[0]), "=v"(o[1]), "=v"(o[2]), "=v"(o[3]), "=v"(o[4]), "=v"(o[5]), "=v"(o[6]), "=v"(o[7])
               : "i"(R0), "i"(R0 + 1), "i"(R0 + 2), "i"(R0 + 3), "i"(R1), "i"(R1 + 1), "i"(R1 + 2), "i"(R1 + 3));
  u32x4 w = {cvtpk(o[0] * f, o[1] * f), cvtpk(o[2] * f, o[3] * f), cvtpk(o[4] * f, o[5] * f), cvtpk(o[6] * f, o[7] * f)};
  return w;
}
template <int I> DI void q_conv1(float C) {
  constexpr int j = I >> 2, s = I & 3, aq = AQ_(j, s);
  const u32x4 w = acc_pack8<AO_(2 * s, j), AO_(2 * s + 1, j)>(C);
  asm volatile("v_accvgpr_write_b32 a[%c4], %0\n\tv_accvgpr_write_b32 a[%c5], %1\n\tv_accvgpr_write_b32 a[%c6], %2\n\tv_accvgpr_write_b32 a[%c7], %3"
               :: "v"(w[0]), "v"(w[1]), "v"(w[2]), "v"(w[3]), "i"(aq), "i"(aq + 1), "i"(aq + 2), "i"(aq + 3) : W4_ACLOB_HI);
}
template <int... Is> DI void q_conv(float C, iseq<Is...>) { (q_conv1<Is>(C), ...); }
template <int I> DI void o_store1(__amdgpu_buffer_rsrc_t srd, unsigned vo, const float (&il)[4]) {
  constexpr int j = I >> 2, u = I & 3;
  const u32x4 w = acc_pack8<AO_(2 * u, j), AO_(2 * u + 1, j)>(il[j]);
  __builtin_amdgcn_raw_buffer_store_b128(w, srd, vo + j * (128u * 1024u + OPAD) + u * 1024, 0, WT);
}
template <int G, int I> DI void fin_step(W4C& c, __amdgpu_buffer_rsrc_t srd, unsigned vo, const float (&il)[4]) {
  if constexpr (G < 4) { pv_mfma<32 * (I >> 3) + 4 * (2 * G + ((I >> 2) & 1)) + (I & 3), false>(c); GAP(); }
  if constexpr (G > 0 && (I & 3) == 3) { o_store1<4 * (I >> 2) + (G - 1)>(srd, vo, il); GAP(); }
}
template <int G, int... Is> DI void fin_group(W4C& c, __amdgpu_buffer_rsrc_t srd, unsigned vo, const float (&il)[4], iseq<Is...>) { (fin_step<G, Is>(c, srd, vo, il), ...); }

using QCfg = GCfg<16, 4, 64, 8, false, 1, 6, 2, 1, 1, true>;
constexpr int SHM_ATTN = 0x10000 + 6 * 16384;

#define ST(i)
__global__ __launch_bounds__(256, 1) void k_attn(const u16* __restrict__ H1f, const u16* __restrict__ Wq, const u16* __restrict__ Kc, const u16* __restrict__ Vc, u16* __restrict__ Of,
                                                        const u16* __restrict__ Wo, const u16* __restrict__ Wg, const u16* __restrict__ Wu, const u16* __restrict__ Wd, const u16* __restrict__ Wm) {
  ST(0); KST_BEGIN(2);
  extern __shared__ __attribute__((aligned(16))) char lds[];
  const unsigned tid = threadIdx.x, wid = rfl(tid >> 6);
  const int bid = blockIdx.x, xcd = bid & 7, j = bid >> 3;
  const int head = 4 * xcd + (j & 3), y = j >> 2, kvh = head >> 3;
  const unsigned ldsb = (unsigned)(size_t)lds;
  {
    const unsigned lane = tid & 63u;
    GState<QCfg> g;
    const int mb0 = y * 16 + wid * 4;
    constexpr unsigned mbstride = 1024u * 32u + HPAD;
    g.srdA = mk_srd((const char*)H1f + (size_t)mb0 * mbstride, 4 * mbstride);
#pragma unroll
    for (int m = 0; m < 4; ++m) g.voffA[m] = lane * 16u + m * mbstride;
    constexpr unsigned ldb = 4096u * 2u;
    g.srdB = mk_srd((const char*)Wq + (size_t)head * 256, 1024u * ldb - (unsigned)head * 256u);
    g_setup<QCfg, true>(g, ldsb + QGEMM_LDS, lane, wid, ldb);
    { const unsigned gw = blockIdx.x * 4u + wid;
      g.tch[0] = (unsigned long long)(size_t)((const char*)Wq + (size_t)gw * 4096); g.tch[1] = g.tch[0] + (4096u * 1024u);
      g.tch[2] = (unsigned long long)(size_t)((gw < 512u ? (const char*)Kc : (const char*)Vc) + (size_t)(gw & 511u) * 4096);
      g.tch_off = lane * 64u; }
    g.srdK = mk_srd((const char*)Kc + (size_t)kvh * (2048u * 256u), 2048u * 256u);
    g.srdV = mk_srd((const char*)Vc + (size_t)kvh * (2048u * 256u), 2048u * 256u);
    g.kvm0 = rfl(ldsb + KRING + 4096u * wid); g.kvso = rfl(KRS * 16u * wid);
#pragma unroll
    for (int q = 0; q < 4; ++q) g.vk[q] = KRS * (lane >> 4) + 16u * ((lane & 15u) ^ (4u * q + (lane >> 4)));
    g.vv = KRS * ((lane >> 1) & 7u) + 32u * (lane >> 4) + 16u * (lane & 1u);
    gemm_run<QCfg>(g, 16);
  }
  ST(1);
  unsigned lane; asm volatile("v_mbcnt_lo_u32_b32 %0, -1, 0\n\tv_mbcnt_hi_u32_b32 %0, -1, %0" : "=v"(lane));
  constexpr int T = 32;
  W4C c;
  { const unsigned r15 = lane & 15u, g4 = lane >> 4;
#pragma unroll
    for (int s = 0; s < 4; ++s) c.vkb[s] = ldsb + KRING + 256u * r15 + 16u * ((4u * s + g4) ^ r15);
    c.vvb = ldsb + VRING + (g4 >> 1) * 2048u + (4u * (g4 & 1u) + (r15 >> 2)) * 32u + 8u * (lane & 3u);
#pragma unroll
    for (int q = 0; q < 4; ++q) {
      c.vksrc[q] = KRS * g4 + 16u * (r15 ^ (4u * q + g4));
      c.vvsrc[q] = KRS * ((lane >> 1) & 7u) + 32u * g4 + 16u * (lane & 1u) + KRS * 8u * (q >> 1) + 128u * (q & 1) + (3072u - 1024u * q);
    } }
  c.m0k = rfl(ldsb + KRING + 4096u * wid); c.m0v = rfl(ldsb + VRING + 4096u * wid);
  c.sofk = rfl(KRS * 16u * wid); c.sofv = rfl(KRS * 16u * wid) - 3072u;
  c.srdK = mk_srd((const char*)Kc + (size_t)kvh * (2048u * 256u), 2048u * 256u);
  c.srdV = mk_srd((const char*)Vc + (size_t)kvh * (2048u * 256u), 2048u * 256u);
  {
    const unsigned gw = blockIdx.x * 4u + wid;
    const char* p;
    const unsigned c2 = gw + 1024u, c2c = c2 < 1792u ? c2 : gw;
    if (lane == 0) p = (const char*)Wo + (size_t)gw * 4096; else if (lane == 1) p = (const char*)Wo + (size_t)c2 * 4096;
    else if (lane == 2) p = (const char*)Wg + (size_t)gw * 4096; else if (lane == 3) p = (const char*)Wg + (size_t)c2c * 4096;
    else if (lane == 4) p = (const char*)Wu + (size_t)gw * 4096; else if (lane == 5) p = (const char*)Wu + (size_t)c2c * 4096;
    else if (lane == 6) p = (const char*)Wd + (size_t)gw * 4096; else if (lane == 7) p = (const char*)Wd + (size_t)c2c * 4096;
    else if (lane < 11) p = (const char*)Wm + (size_t)(gw + 1024u * (lane - 8u)) * 4096;
    else p = (const char*)Wo + (size_t)gw * 4096;
    c.pf_lo = (unsigned)(size_t)p; c.pf_hi = (unsigned)((size_t)p >> 32);
    c.pf_off = lane * 64u; c.pf_m0 = rfl(ldsb + QGEMM_LDS + 256u * wid);
  }
  asm volatile("s_barrier" ::: "memory");
  k_reads0(c, make_iseq<16>{});
  q_conv(0.12751743082f, make_iseq<16>{});
  asm volatile("s_waitcnt lgkmcnt(0)\n\ts_nop 1" ::: "memory");
  c.pend = false; c.ones = (u32x4){0x3f803f80u, 0x3f803f80u, 0x3f803f80u, 0x3f803f80u}; asm volatile("" : "+v"(c.ones));
#pragma unroll
  for (int j = 0; j < 4; ++j) { c.LS[j] = (f32x4){0.f, 0.f, 0.f, 0.f}; asm volatile("" : "+v"(c.LS[j])); }
  ST(2);
  phaseA<0, true>(c, 0);  phaseB<0, true, false>(c, 0);
  phaseA<1, false>(c, 1); phaseB<1, false, true>(c, 1);
  for (int t = 2; t < T; t += 2) {
    phaseA<0, false>(c, t);     phaseB<0, false, false>(c, t);
    phaseA<1, false>(c, t + 1); phaseB<1, false, false>(c, t + 1);
  }
  ST(3);
  float il[4];
  {
    tr_reads1(c, make_iseq<32>{});
    drain_exp(c, make_iseq<32>{});
    drain_cvt0(c, make_iseq<16>{});
    drain_cvt1(c, make_iseq<16>{});
    asm volatile("s_waitcnt lgkmcnt(0)" : W4_VLO :: "memory"); asm volatile("" : W4_VUP :: "memory");
    GAP();
    v_concat(c);
    asm volatile("s_nop 1" ::: "memory");
    ls_mfma<0, 0>(c); ls_mfma<1, 0>(c); ls_mfma<2, 0>(c); ls_mfma<3, 0>(c); ls_mfma<0, 1>(c); ls_mfma<1, 1>(c); ls_mfma<2, 1>(c); ls_mfma<3, 1>(c);
    unsigned lb; asm volatile("v_mbcnt_lo_u32_b32 %0, -1, 0\n\tv_mbcnt_hi_u32_b32 %0, -1, %0" : "=v"(lb));
    const unsigned vo = (unsigned)(y * 16 + wid * 4) * (128u * 1024u + OPAD) + (unsigned)head * 4096u + lb * 16u;
    const __amdgpu_buffer_rsrc_t srdO = mk_srd(Of, 128u * (128u * 1024u + OPAD));
    GAP();
    fin_group<0>(c, srdO, vo, il, make_iseq<16>{});
    asm volatile("" : "+v"(c.LS[0]), "+v"(c.LS[1]), "+v"(c.LS[2]), "+v"(c.LS[3]));
#pragma unroll
    for (int j = 0; j < 4; ++j) il[j] = __builtin_amdgcn_rcpf(c.LS[j][0]);
    GAP();
    fin_group<1>(c, srdO, vo, il, make_iseq<16>{});
    fin_group<2>(c, srdO, vo, il, make_iseq<16>{});
    fin_group<3>(c, srdO, vo, il, make_iseq<16>{});
    asm volatile("s_nop 7\n\ts_nop 1" ::: "memory");
    fin_group<4>(c, srdO, vo, il, make_iseq<16>{});
  }
  ST(4); KST_END(2);
}
template <int N> DI void touch(const char* p, int stride) {
  unsigned d[N];
#pragma unroll
  for (int i = 0; i < N; ++i) d[i] = *(const unsigned*)(p + (size_t)i * stride);
#pragma unroll
  for (int i = 0; i < N; ++i) asm volatile("" :: "v"(d[i]));
}

template <int NT, int KL> DI void mod_body(const u16* __restrict__ c, const u16* __restrict__ wmod, float* __restrict__ dst, int cb, int kh, float* red  ) {
  const int tid = threadIdx.x, cg = tid & 7, kg = tid >> 3, wv = tid >> 6;
  constexpr int NG = NT / 8, NI = KL / NG;
  u32x4 w[NI]; u16 cv[NI];
#pragma unroll
  for (int i = 0; i < NI; ++i) { const int k = kh * KL + kg + NG * i; w[i] = *(const u32x4*)(wmod + (size_t)k * 6144 + cb * 64 + cg * 8); cv[i] = c[k]; }
  float acc[8] = {0, 0, 0, 0, 0, 0, 0, 0};
#pragma unroll
  for (int i = 0; i < NI; ++i) {
    const float xx = bf2f(cv[i]), s = rbf(xx / (1.f + __expf(-xx)));
#pragma unroll
    for (int jj = 0; jj < 4; ++jj) { acc[2 * jj] += s * __uint_as_float(w[i][jj] << 16); acc[2 * jj + 1] += s * __uint_as_float(w[i][jj] & 0xffff0000u); }
  }
#pragma unroll
  for (int jj = 0; jj < 8; ++jj) { float a = acc[jj]; a += __shfl_xor(a, 8); a += __shfl_xor(a, 16); a += __shfl_xor(a, 32); acc[jj] = a; }
  if ((tid & 63) < 8) {
#pragma unroll
    for (int jj = 0; jj < 8; ++jj) red[wv * 64 + cg * 8 + jj] = acc[jj];
  }
  __syncthreads();
  if (tid < 64) { float s = 0;
#pragma unroll
    for (int g = 0; g < NT / 64; ++g) s += red[g * 64 + tid];
    dst[tid] = s; }
}
__global__ __launch_bounds__(512) void k_pre(const u16* __restrict__ c, const u16* __restrict__ wmod, float* __restrict__ modA, const u16* __restrict__ x, unsigned* __restrict__ flg) {
  __shared__ float red[8 * 64];
  const int b = blockIdx.x;
  KST_BEGIN(0);
  unsigned dx = 0;
  if (threadIdx.x == 0) flg[b * 64] = 0u;
  if (threadIdx.x < 256) dx = *(const unsigned*)((const char*)x + (size_t)b * 16384 + threadIdx.x * 64);
  mod_body<512, 128>(c, wmod, modA + (b & 7) * 2048 + (b >> 3) * 64, b >> 3, b & 7, red);
  asm volatile("" :: "v"(dx));
  KST_END(0);
}
DI f32x4 modv(const float* __restrict__ modp, int n) { const f32x4 a = *(const f32x4*)(modp + n), b = *(const f32x4*)(modp + 6144 + n); f32x4 o; for (int e = 0; e < 4; ++e) o[e] = rbf(a[e] + b[e]); return o; }
DI void unpack8(const u32x4& w, float* v) {
#pragma unroll
  for (int j = 0; j < 4; ++j) { v[2 * j] = __uint_as_float(w[j] << 16); v[2 * j + 1] = __uint_as_float(w[j] & 0xffff0000u); }
}
DI u32x4 pack8(const float* o) { return (u32x4){cvtpk(o[0], o[1]), cvtpk(o[2], o[3]), cvtpk(o[4], o[5]), cvtpk(o[6], o[7])}; }

DI void rbf2(float& a, float& b) { const unsigned w = cvtpk(a, b); a = __uint_as_float(w << 16); b = __uint_as_float(w & 0xffff0000u); }
DI void rbf8(float* v) { rbf2(v[0], v[1]); rbf2(v[2], v[3]); rbf2(v[4], v[5]); rbf2(v[6], v[7]); }
DI void modv8(const float* __restrict__ modp, int n, float* o) {
  const f32x4 a0 = *(const f32x4*)(modp + n), b0 = *(const f32x4*)(modp + 6144 + n), a1 = *(const f32x4*)(modp + n + 8), b1 = *(const f32x4*)(modp + 6144 + n + 8);
#pragma unroll
  for (int e = 0; e < 4; ++e) { o[e] = a0[e] + b0[e]; o[4 + e] = a1[e] + b1[e]; }
  rbf8(o);
}
template <int MODE>
__global__ __launch_bounds__(1024) void k_ln(const u16* __restrict__ x, const u16* __restrict__ Pf, const float* __restrict__ modp, u16* __restrict__ xf, u16* __restrict__ hf) {
  __shared__ float red[2][16][8];
  __shared__ __attribute__((aligned(16))) float msum[MODE == 0 ? 2048 : 4];
  const int tid = threadIdx.x;
  KST_BEGIN(MODE == 0 ? 1 : 4);
  const int r = tid & 7, c = tid >> 3, h = c & 1, wid = tid >> 6;
  const int row = blockIdx.x * 8 + r, mb = row >> 5, ml = row & 31;
  const size_t fo = ((size_t)mb * 64 + (c >> 1)) * 512 + (h * 32 + ml) * 8;
  const int n = 16 * (c >> 1) + 4 * h;
  float v[8], sh[8], sc[8];
  if (MODE == 0) {
    const u32x4 xw = *(const u32x4*)(x + (size_t)row * 1024 + 8 * c);
    { f32x2 a = {0.f, 0.f};
#pragma unroll
      for (int ks = 0; ks < 8; ++ks) a += *(const f32x2*)(modp + ks * 2048 + 2 * tid);
      float a0 = a[0], a1 = a[1]; rbf2(a0, a1); *(f32x2*)(msum + 2 * tid) = (f32x2){a0, a1}; }
    const unsigned s0 = h ? xw[0] : xw[2], s1 = h ? xw[1] : xw[3];
    const unsigned g0 = __shfl_xor(s0, 8), g1 = __shfl_xor(s1, 8);
    const u32x4 f = h ? (u32x4){g0, g1, xw[2], xw[3]} : (u32x4){xw[0], xw[1], g0, g1};
    __builtin_amdgcn_raw_buffer_store_b128(f, mk_srd(xf, 4u << 20), (unsigned)fo * 2u, 0, WT);
    unpack8(f, v);
  } else {
    const u32x4 xw = *(const u32x4*)(xf + fo), p0 = *(const u32x4*)(Pf + fo), p1 = *(const u32x4*)(Pf + fo + (size_t)2048 * 1024);
    float ga[8], a[8], b[8];
    modv8(modp, 3072 + n, sh); modv8(modp, 4096 + n, sc); modv8(modp, 2048 + n, ga);
    unpack8(xw, v); unpack8(p0, a); unpack8(p1, b);
#pragma unroll
    for (int j = 0; j < 8; ++j) a[j] += b[j];
    rbf8(a);
#pragma unroll
    for (int j = 0; j < 8; ++j) a[j] *= ga[j];
    rbf8(a);
#pragma unroll
    for (int j = 0; j < 8; ++j) v[j] += a[j];
    const u32x4 w = pack8(v);
    __builtin_amdgcn_raw_buffer_store_b128(w, mk_srd(xf, 4u << 20), (unsigned)fo * 2u, 0, WT);
    unpack8(w, v);
  }
  float s = ((v[0] + v[1]) + (v[2] + v[3])) + ((v[4] + v[5]) + (v[6] + v[7]));
  s += __shfl_xor(s, 8); s += __shfl_xor(s, 16); s += __shfl_xor(s, 32);
  if ((tid & 63) < 8) red[0][wid][r] = s;
  __syncthreads();
  if (MODE == 0) {
    const f32x4 a0 = *(const f32x4*)(msum + n), a1 = *(const f32x4*)(msum + n + 8), b0 = *(const f32x4*)(msum + 1024 + n), b1 = *(const f32x4*)(msum + 1024 + n + 8);
#pragma unroll
    for (int e = 0; e < 4; ++e) { sh[e] = a0[e]; sh[4 + e] = a1[e]; sc[e] = b0[e]; sc[4 + e] = b1[e]; }
  }
  float mean = 0;
#pragma unroll
  for (int w = 0; w < 16; ++w) mean += red[0][w][r];
  mean *= (1.f / 1024.f);
  float qv = 0;
#pragma unroll
  for (int j = 0; j < 8; ++j) { const float d = v[j] - mean; qv += d * d; }
  qv += __shfl_xor(qv, 8); qv += __shfl_xor(qv, 16); qv += __shfl_xor(qv, 32);
  if ((tid & 63) < 8) red[1][wid][r] = qv;
  __syncthreads();
  float var = 0;
#pragma unroll
  for (int w = 0; w < 16; ++w) var += red[1][w][r];
  var = rbf(var * (1.f / 1024.f));
  const float mb16 = rbf(mean), rsd = 1.f / rbf(sqrtf(rbf(var + 1e-5f)));
#pragma unroll
  for (int j = 0; j < 8; ++j) { v[j] -= mb16; sc[j] += 1.f; }
  rbf8(v); rbf8(sc);
#pragma unroll
  for (int j = 0; j < 8; ++j) v[j] *= rsd;
  rbf8(v);
#pragma unroll
  for (int j = 0; j < 8; ++j) v[j] *= sc[j];
  rbf8(v);
#pragma unroll
  for (int j = 0; j < 8; ++j) v[j] += sh[j];
  const unsigned ho = (unsigned)(row >> 4) * (32768u + HPAD) + (unsigned)(c >> 2) * 1024u + (unsigned)(c & 3) * 256u + (unsigned)(row & 15) * 16u;
  __builtin_amdgcn_raw_buffer_store_b128(pack8(v), mk_srd(hf, 128u * (32768u + HPAD)), ho, 0, WT);
  KST_END(MODE == 0 ? 1 : 4);
}
template <int KORD> using SCfgT = GCfg<16, 4, 32, 8, false, KORD, 4, 4, 1, 1>;
using SCfg = SCfgT<1>;
template <int K, bool FIN, int NT0>
__global__ __launch_bounds__(256, 2) void k_gemm_sk(const u16* __restrict__ Af, const u16* __restrict__ B, u16* __restrict__ Pf,
                                                    const u16* __restrict__ cvec, const u16* __restrict__ wmod, float* __restrict__ modp,
                                                    const u16* __restrict__ x1f, u16* __restrict__ out, unsigned* __restrict__ flg) {
  using C = SCfgT<K == 4096 ? 0 : 1>;
  extern __shared__ __attribute__((aligned(16))) char lds[];
  KST_BEGIN(K == 4096 ? 3 : 6);
  if (K == 4096 && blockIdx.x >= 256) { const int b = blockIdx.x - 256; mod_body<256, 512>(cvec, wmod, modp + (b & 1) * 6144 + (32 + (b >> 1)) * 64, 32 + (b >> 1), b & 1, (float*)lds); KST_END(3); return; }
  constexpr int N = 1024;
  const unsigned tid = threadIdx.x, lane = tid & 63u, wid = rfl(tid >> 6);
  const int bid = blockIdx.x, xcd = bid & 7, j = bid >> 3;
  const int kh = xcd & 1, mg = xcd >> 1;
  const int mt = mg * 4 + (j & 3), nt = j >> 2;
  const int k0 = kh ? NT0 * 64 : 0, klen = kh ? K - NT0 * 64 : NT0 * 64;
  GState<C> g;
  const int mb0 = mt * 4 + wid;
  constexpr unsigned mbstride = (unsigned)K * 32u + (K == 4096 ? OPAD : 0u);
  g.srdA = mk_srd((const char*)Af + (size_t)(2 * mb0) * mbstride + (size_t)k0 * 32, mbstride + (unsigned)klen * 32u);
  g.voffA[0] = lane * 16u; g.voffA[1] = lane * 16u + mbstride;
  constexpr unsigned ldb = (unsigned)N * 2u;
  g.srdB = mk_srd((const char*)B + (size_t)k0 * ldb + (size_t)nt * 256, (unsigned)klen * ldb - (unsigned)nt * 256u);
  g_setup<C>(g, (unsigned)(size_t)lds, lane, wid, ldb);
  gemm_run<C>(g, klen / 64);
  const unsigned h = (lane >> 4) & 1u, hi = lane >> 5;
  const unsigned fo = (((unsigned)mb0 * 64u + (unsigned)nt * 8u + hi) * 512u + (h * 32u + (lane & 15u)) * 8u) * 2u;
  float acc[4][2][8];
#pragma unroll
  for (int i = 0; i < 4; ++i)
#pragma unroll
    for (int m = 0; m < 2; ++m)
#pragma unroll
      for (int e = 0; e < 4; ++e) {
        auto sw = __builtin_amdgcn_permlane32_swap(__float_as_uint(g.acc[2 * i][m][e]), __float_as_uint(g.acc[2 * i + 1][m][e]), false, false);
        acc[i][m][e] = __uint_as_float(sw[0]); acc[i][m][4 + e] = __uint_as_float(sw[1]);
      }
  if constexpr (!FIN) {
    const __amdgpu_buffer_rsrc_t srdP = mk_srd(Pf, 8u << 20);
#pragma unroll
    for (int i = 0; i < 4; ++i)
#pragma unroll
      for (int m = 0; m < 2; ++m) __builtin_amdgcn_raw_buffer_store_b128(pack8(acc[i][m]), srdP, (unsigned)kh * (4u << 20) + fo + i * 2048 + m * 256, 0, WT);
  } else {
    const __amdgpu_buffer_rsrc_t srdP = mk_srd(Pf, 4u << 20);
    unsigned* flag = flg + (128 + mt * 8 + nt) * 64;
    if (kh == 0) {
#pragma unroll
      for (int i = 0; i < 4; ++i)
#pragma unroll
        for (int m = 0; m < 2; ++m) __builtin_amdgcn_raw_buffer_store_b128(pack8(acc[i][m]), srdP, fo + i * 2048 + m * 256, 0, 16);
      asm volatile("s_waitcnt vmcnt(0)" ::: "memory");
      __syncthreads();
      if (tid == 0) __hip_atomic_store(flag, 1u, __ATOMIC_RELAXED, __HIP_MEMORY_SCOPE_AGENT);
    } else {
      u32x4 xw[4][2]; float gb[4][8];
#pragma unroll
      for (int i = 0; i < 4; ++i) { xw[i][0] = *(const u32x4*)((const char*)x1f + fo + i * 2048); xw[i][1] = *(const u32x4*)((const char*)x1f + fo + i * 2048 + 256);
        modv8(modp, 5120 + nt * 128 + 16 * (2 * i + hi) + 4 * h, gb[i]); }
      if (wid == 0) {
        for (unsigned spins = 0; spins < (1u << 22); ++spins) {
          if ((unsigned)__builtin_amdgcn_readfirstlane(__hip_atomic_load(flag, __ATOMIC_RELAXED, __HIP_MEMORY_SCOPE_AGENT)) != 0u) break;
          __builtin_amdgcn_s_sleep(1);
        }
      }
      asm volatile("" ::: "memory"); __builtin_amdgcn_s_barrier(); asm volatile("" ::: "memory");
      u32x4 pw[4][2];
#pragma unroll
      for (int i = 0; i < 4; ++i)
#pragma unroll
        for (int m = 0; m < 2; ++m) pw[i][m] = __builtin_amdgcn_raw_buffer_load_b128(srdP, fo + i * 2048 + m * 256, 0, 16);
      const unsigned oo = (((unsigned)mb0 * 32u + (lane & 15u)) * 1024u + (unsigned)nt * 128u + 16u * hi + 8u * h) * 2u;
      const __amdgpu_buffer_rsrc_t srdOut = mk_srd(out, 4u << 20);
#pragma unroll
      for (int i = 0; i < 4; ++i)
#pragma unroll
        for (int m = 0; m < 2; ++m) {
          float v[8], a[8];
          unpack8(xw[i][m], v); unpack8(pw[i][m], a);
#pragma unroll
          for (int jj = 0; jj < 8; ++jj) a[jj] += acc[i][m][jj];
          rbf8(a);
#pragma unroll
          for (int jj = 0; jj < 8; ++jj) a[jj] *= gb[i][jj];
          rbf8(a);
#pragma unroll
          for (int jj = 0; jj < 8; ++jj) v[jj] += a[jj];
          const u32x4 w = pack8(v);
          const unsigned s0 = h ? w[0] : w[2], s1 = h ? w[1] : w[3];
          const unsigned g0 = __shfl_xor(s0, 16), g1 = __shfl_xor(s1, 16);
          const u32x4 f = h ? (u32x4){g0, g1, w[2], w[3]} : (u32x4){w[0], w[1], g0, g1};
          __builtin_amdgcn_raw_buffer_store_b128(f, srdOut, oo + 64u * i + 32768u * m, 0, WT);
        }
    }
  }
  KST_END(K == 4096 ? 3 : 6);
}
#define SK2_NT0 28
DI float silu_mul(float g, float u) { return g * __builtin_amdgcn_rcpf(1.f + __builtin_amdgcn_exp2f(g * -1.4426950408889634f)) * u; }
#define U_NS 3
#define U_BP 3
using UCfg = GCfg<16, 8, 32, 14, true, 1, U_NS, 2, 1, U_BP>;
__global__ __launch_bounds__(512, 2) void k_gemm_gu(const u16* __restrict__ Af, const u16* __restrict__ Wg, const u16* __restrict__ Wu, u16* __restrict__ Act) {
  using C = UCfg;
  extern __shared__ __attribute__((aligned(16))) char lds[];
  constexpr int K = 1024, F = 3584;
  KST_BEGIN(5);
  const unsigned tid = threadIdx.x, wid = rfl(tid >> 6);
  const int bid = blockIdx.x, xcd = bid & 7, j = bid >> 3;
  const int mt = (xcd & 1) * 4 + (j & 3), nt = (xcd >> 1) * 8 + (j >> 2);
  GState<C> g;
  const int mb0 = mt * 8 + wid;
  {
    const unsigned lane = tid & 63u;
    constexpr unsigned mbstride = (unsigned)K * 32u + HPAD;
    g.srdA = mk_srd((const char*)Af + (size_t)(2 * mb0) * mbstride, 2 * mbstride);
    g.voffA[0] = lane * 16u; g.voffA[1] = lane * 16u + mbstride;
    constexpr unsigned ldb = F * 2u;
    const bool up = wid >= 4;
    g.srdB = mk_srd((const char*)(up ? Wu : Wg) + (size_t)nt * 224, (unsigned)K * ldb - (unsigned)nt * 224u);
    g_setup<C>(g, (unsigned)(size_t)lds, lane, wid, ldb);
  }
  gemm_run<C>(g, K / 64);
  unsigned lane; asm volatile("v_mbcnt_lo_u32_b32 %0, -1, 0\n\tv_mbcnt_hi_u32_b32 %0, -1, %0" : "=v"(lane));
  const __amdgpu_buffer_rsrc_t srdO = mk_srd(Act, 2048u * F * 2u);
  const unsigned vo = ((unsigned)mb0 * 2u + (lane >> 5)) * (unsigned)((F >> 5) * 1024) + (lane & 31u) * 16u;
#pragma unroll
  for (int n = 0; n < 7; ++n) {
    unsigned q[2][2];
#pragma unroll
    for (int m = 0; m < 2; ++m) {
      float o[4];
#pragma unroll
      for (int e = 0; e < 4; ++e) o[e] = silu_mul(g.acc[2 * n][m][e], g.acc[2 * n + 1][m][e]);
      q[m][0] = cvtpk(o[0], o[1]); q[m][1] = cvtpk(o[2], o[3]);
    }
    auto s0 = __builtin_amdgcn_permlane32_swap(q[0][0], q[1][0], false, false); auto s1 = __builtin_amdgcn_permlane32_swap(q[0][1], q[1][1], false, false);
    const unsigned c16 = (unsigned)(nt * 7 + n);
    __builtin_amdgcn_raw_buffer_store_b128((u32x4){s0[0], s1[0], s0[1], s1[1]}, srdO, vo + (c16 >> 1) * 1024u + (c16 & 1u) * 512u, 0, WT);
  }
  KST_END(5);
}

extern "C" void kernel_launch(void* const* d_in, const int* in_sizes, int n_in, void* d_out, int out_size, void* d_ws, size_t ws_size, hipStream_t stream) {
  const u16* x = (const u16*)d_in[0]; const u16* kc = (const u16*)d_in[1]; const u16* vc = (const u16*)d_in[2];
  const u16* wq = (const u16*)d_in[3]; const u16* wo = (const u16*)d_in[4];
  const u16* wg = (const u16*)d_in[6]; const u16* wu = (const u16*)d_in[7]; const u16* wd = (const u16*)d_in[8];
  const u16* c = (const u16*)d_in[9]; const u16* wmod = (const u16*)d_in[10];
  u16* out = (u16*)d_out;
  char* ws = (char*)d_ws;
  float* mod = (float*)ws;
  u16* hf = (u16*)(ws + (1 << 20));
  u16* x1f = (u16*)(ws + (8 << 20));
  u16* of = (u16*)(ws + (96 << 20));
  u16* P = (u16*)(ws + (32 << 20));
  u16* act = (u16*)(ws + (64 << 20));
  hipFuncSetAttribute((const void*)k_attn, hipFuncAttributeMaxDynamicSharedMemorySize, SHM_ATTN);
  hipFuncSetAttribute((const void*)k_gemm_sk<4096, false, 32>, hipFuncAttributeMaxDynamicSharedMemorySize, SCfg::LDSB);
  hipFuncSetAttribute((const void*)k_gemm_sk<3584, true, SK2_NT0>, hipFuncAttributeMaxDynamicSharedMemorySize, SCfg::LDSB);
  hipFuncSetAttribute((const void*)k_gemm_gu, hipFuncAttributeMaxDynamicSharedMemorySize, UCfg::LDSB);
  float* modA = (float*)(ws + (256 << 10));
  unsigned* flg = (unsigned*)(ws + (512 << 10));
  u16* P2 = (u16*)(ws + (48 << 20));
  k_pre<<<256, 512, 0, stream>>>(c, wmod, modA, x, flg);
  k_ln<0><<<256, 1024, 0, stream>>>(x, nullptr, modA, x1f, hf);
  k_attn<<<256, 256, SHM_ATTN, stream>>>(hf, wq, kc, vc, of, wo, wg, wu, wd, wmod);
  k_gemm_sk<4096, false, 32><<<256 + 128, 256, SCfg::LDSB, stream>>>(of, wo, P, c, wmod, mod, nullptr, nullptr, nullptr);
  k_ln<1><<<256, 1024, 0, stream>>>(x, P, mod, x1f, hf);
  k_gemm_gu<<<256, 512, UCfg::LDSB, stream>>>(hf, wg, wu, act);
  k_gemm_sk<3584, true, SK2_NT0><<<256, 256, SCfg::LDSB, stream>>>(act, wd, P2, nullptr, nullptr, mod, x1f, out, flg);
}
```

```cpp
#include <hip/hip_runtime.h>
#include <utility>
#include <type_traits>
typedef unsigned short u16;
typedef short bf16x8 __attribute__((ext_vector_type(8)));
typedef short s16x4 __attribute__((ext_vector_type(4)));
typedef float f32x4 __attribute__((ext_vector_type(4)));
typedef float f32x16 __attribute__((ext_vector_type(16)));
typedef unsigned u32x4 __attribute__((ext_vector_type(4)));
typedef unsigned u32x2 __attribute__((ext_vector_type(2)));
typedef float f32x2 __attribute__((ext_vector_type(2)));
typedef __bf16 bf16x2_t __attribute__((ext_vector_type(2)));
#define DI __device__ __forceinline__
#define LAS __attribute__((address_space(3)))
#define GAP() __builtin_amdgcn_sched_barrier(0)
template <int... Is> using iseq = std::integer_sequence<int, Is...>;
template <int N> using make_iseq = std::make_integer_sequence<int, N>;

#define KST_BEGIN(k)
#define KST_END(k)
DI unsigned rfl(unsigned x) { return __builtin_amdgcn_readfirstlane(x); }
DI float bf2f(u16 v) { return __uint_as_float(((unsigned)v) << 16); }
DI unsigned cvtpk(float lo, float hi) { f32x2 v = {lo, hi}; bf16x2_t b = __builtin_convertvector(v, bf16x2_t); return __builtin_bit_cast(unsigned, b); }
DI float rbf(float f);
DI float rbf(float f) { return __uint_as_float(cvtpk(f, 0.f) << 16); }
#define KV_FIRST 12
#define WT 16
DI __amdgpu_buffer_rsrc_t mk_srd(const void* p, unsigned bytes) { return __builtin_amdgcn_make_buffer_rsrc((void*)p, (short)0, (int)bytes, 0x00020000); }

#define W4_ACLOB "a0","a1","a2","a3","a4","a5","a6","a7","a8","a9","a10","a11","a12","a13","a14","a15","a16","a17","a18","a19","a20","a21","a22","a23","a24","a25","a26","a27","a28","a29","a30","a31","a32","a33","a34","a35","a36","a37","a38","a39","a40","a41","a42","a43","a44","a45","a46","a47","a48","a49","a50","a51","a52","a53","a54","a55","a56","a57","a58","a59","a60","a61","a62","a63","a64","a65","a66","a67","a68","a69","a70","a71","a72","a73","a74","a75","a76","a77","a78","a79","a80","a81","a82","a83","a84","a85","a86","a87","a88","a89","a90","a91","a92","a93","a94","a95","a96","a97","a98","a99","a100","a101","a102","a103","a104","a105","a106","a107","a108","a109","a110","a111","a112","a113","a114","a115","a116","a117","a118","a119","a120","a121","a122","a123","a124","a125","a126","a127"
#define W4_ACLOB_HI "a128","a129","a130","a131","a132","a133","a134","a135","a136","a137","a138","a139","a140","a141","a142","a143","a144","a145","a146","a147","a148","a149","a150","a151","a152","a153","a154","a155","a156","a157","a158","a159","a160","a161","a162","a163","a164","a165","a166","a167","a168","a169","a170","a171","a172","a173","a174","a175","a176","a177","a178","a179","a180","a181","a182","a183","a184","a185","a186","a187","a188","a189","a190","a191","a192","a193","a194","a195","a196","a197","a198","a199","a200","a201","a202","a203","a204","a205","a206","a207","a208","a209","a210","a211","a212","a213","a214","a215","a216","a217","a218","a219","a220","a221","a222","a223","a224","a225","a226","a227","a228","a229","a230","a231","a232","a233","a234","a235","a236","a237","a238","a239","a240","a241","a242","a243","a244","a245","a246","a247","a248","a249","a250","a251","a252","a253","a254","a255"
template <int SH_, int NW_, int WR_, int NB16_, bool GU_, int KORD_, int NS_, int PDT_, int FD_, int BARPOS_, bool ASMACC_ = false>
struct GCfg {
  static constexpr bool ASMACC = ASMACC_;
  static constexpr int SH = SH_, NW = NW_, WR = WR_, NB16 = NB16_, KORD = KORD_, NS = NS_, PDT = PDT_, FD = FD_, BARPOS = BARPOS_;
  static constexpr bool GU = GU_;
  static constexpr int STEPS = SH == 32 ? 4 : 2;
  static constexpr int MR = WR / SH;
  static constexpr int NB = SH == 32 ? NB16 / 2 : NB16;
  static constexpr int NR16 = GU ? NB16 / 2 : NB16;
  static constexpr int PIECES = NB16 * 2;
  static constexpr int PB = (PIECES + NW - 1) / NW;
  static constexpr int STAGE = PB * NW * 1024;
  static constexpr int REGION = STAGE / 2;
  static constexpr int LDSB = NS * STAGE;
  static constexpr int NOPS = STEPS * MR + PB;
  static constexpr int GS = NB * MR;
  static constexpr int G = STEPS * GS;
  static constexpr int BSTEP = STEPS - FD;
  static constexpr int GB = BSTEP * GS + (BARPOS + 1) * MR - 1;
  static constexpr int dma_lo(int ks) { return (PB * ks) / STEPS; }
  static constexpr int step_ops(int ks) { return MR + dma_lo(ks + 1) - dma_lo(ks); }
  static constexpr int step_first(int ks) { return ks * MR + dma_lo(ks); }
  static constexpr int op_step(int j) { int ks = 0; while (ks < STEPS - 1 && j >= step_first(ks + 1)) ++ks; return ks; }
  static constexpr int op_gap(int j) { const int ks = op_step(j), i = j - step_first(ks), n = step_ops(ks); return ks * GS + (i * GS) / n; }
  static constexpr int ops_before(int g) { int c = 0; for (int j = 0; j < NOPS; ++j) if (op_gap(j) < g) ++c; return c; }
  static constexpr int NA(int ks) {
    const int ts = (ks + 1) % STEPS, jt = step_first(ts) + MR - 1;
    const int full = (ks == STEPS - 1) ? PDT - 1 : PDT;
    return (NOPS - 1 - jt) + (full - 1) * NOPS + ops_before(ks * GS);
  }
  static constexpr int NBW() { return (NS - 3) * NOPS + ops_before(GB); }
  static_assert(NS >= 3 && PDT >= 2 && FD <= STEPS - 0 && BSTEP >= 0, "");
  using acc_t = std::conditional_t<SH == 32, f32x16, f32x4>;
};

template <class C>
struct GState {
  typename C::acc_t acc[C::NB][C::MR];
  bf16x8 a[C::PDT * C::STEPS][C::MR];
  s16x4 blo[C::FD][C::NB], bhi[C::FD][C::NB];
  __amdgpu_buffer_rsrc_t srdA, srdB;
  unsigned voffA[C::MR];
  unsigned voffB[C::GU ? C::PB : 1];
  unsigned sB[C::GU ? 1 : C::PB];
  unsigned m0base, vrd, ktileB;
  unsigned long long tch[3]; unsigned tch_off;
  __amdgpu_buffer_rsrc_t srdK, srdV; unsigned kvm0, kvso, vk[4], vv;
};
struct GIter { unsigned va_cur, va_nxt, sA, sA0, m0_fill, sK_fill; };

template <class C, int SL, int OFF, int MRI> DI void a_load1(GState<C>& g, unsigned sA) {
  asm volatile("buffer_load_dwordx4 %0, %1, %2, %3 offen offset:%4" : "=v"(g.a[SL][MRI]) : "v"(g.voffA[MRI]), "s"(g.srdA), "s"(sA), "i"(OFF) : "memory");
}
template <class C, int Q> DI void b_dma1(GState<C>& g, unsigned m0v, unsigned sK) {
  if constexpr (C::GU)
    asm volatile("s_mov_b32 m0, %0\n\ts_nop 0\n\tbuffer_load_dwordx4 %1, %2, %3 offen lds" :: "s"(m0v + 1024u * Q), "v"(g.voffB[Q]), "s"(g.srdB), "s"(sK) : "memory");
  else
    asm volatile("s_mov_b32 m0, %0\n\ts_nop 0\n\tbuffer_load_dwordx4 %1, %2, %3 offen lds" :: "s"(m0v + 1024u * Q), "v"(g.voffB[0]), "s"(g.srdB), "s"(sK + g.sB[Q]) : "memory");
}
template <class C> DI void g_dummy(GState<C>& g) {
  asm volatile("s_mov_b32 m0, %0\n\ts_nop 0\n\tbuffer_load_dwordx4 %1, %2, %3 offen lds" :: "s"(g.m0base + (unsigned)(C::NS - 1) * C::STAGE), "v"(g.voffA[0]), "s"(g.srdA), "s"(0x80000000u) : "memory");
}
template <class C, int TI, int J, bool VA, bool VB> DI void g_op(GState<C>& g, const GIter& it) {
  constexpr int ks = C::op_step(J), i = J - C::step_first(ks);
  if constexpr (i < C::MR) {
    constexpr int PD = C::PDT * C::STEPS, SL = (TI * C::STEPS + ks - 1 + PD) % PD;
    if constexpr (VA) { if constexpr (ks == 0) a_load1<C, SL, 0, i>(g, it.sA0); else a_load1<C, SL, (ks - 1) * 1024, i>(g, it.sA); } else g_dummy<C>(g);
  } else {
    if constexpr (VB) b_dma1<C, C::dma_lo(ks) + i - C::MR>(g, it.m0_fill, it.sK_fill); else g_dummy<C>(g);
  }
}
template <class C, int TI, int GI, int... Js> DI void g_ops_at(GState<C>& g, const GIter& it, iseq<Js...>) {
  ((C::op_gap(Js) == GI ? g_op<C, TI, Js, true, true>(g, it) : (void)0), ...);
}
template <class C> constexpr int b_off(int KS, int NBI, int t) {
  if (C::SH == 32) {
    return ((2 * KS + t) * C::NR16 + (C::GU ? NBI : 2 * NBI)) * 256;
  } else {
    const int kb = 4 * KS + (C::KORD == 0 ? 2 * t : t);
    if (C::ASMACC) return (kb * C::NR16 + (NBI & ~1)) * 256 + 8 * (NBI & 1);
    return (kb * C::NR16 + (C::GU ? NBI / 2 : NBI)) * 256 + ((C::GU && (NBI & 1)) ? C::REGION : 0);
  }
}
template <class C, int BUF, int KS, int NBI> DI void b_read(GState<C>& g, unsigned va) {
  g.blo[BUF][NBI] = __builtin_amdgcn_ds_read_tr16_b64_v4i16((LAS s16x4*)(size_t)(va + b_off<C>(KS, NBI, 0)));
  g.bhi[BUF][NBI] = __builtin_amdgcn_ds_read_tr16_b64_v4i16((LAS s16x4*)(size_t)(va + b_off<C>(KS, NBI, 1)));
}
template <class C, int BUF, int KS, int... Ns> DI void b_read_all(GState<C>& g, unsigned va, iseq<Ns...>) { (b_read<C, BUF, KS, Ns>(g, va), ...); }

template <class C, int N, int SL> DI void a_wait(GState<C>& g) {
  static_assert(N >= 0 && N < 64, "vmcnt range");
  if constexpr (C::MR == 1) asm volatile("s_waitcnt vmcnt(%1)" : "+v"(g.a[SL][0]) : "i"(N) : "memory");
  else if constexpr (C::MR == 2) asm volatile("s_waitcnt vmcnt(%2)" : "+v"(g.a[SL][0]), "+v"(g.a[SL][1]) : "i"(N) : "memory");
  else asm volatile("s_waitcnt vmcnt(%4)" : "+v"(g.a[SL][0]), "+v"(g.a[SL][1]), "+v"(g.a[SL][2]), "+v"(g.a[SL][3]) : "i"(N) : "memory");
}

template <class C, int TI, int GI> DI void g_gap(GState<C>& g, const GIter& it) {
  constexpr int KS = GI / C::GS, r = GI % C::GS, NBI = r / C::MR, MRI = r % C::MR;
  constexpr int SL = TI * C::STEPS + KS, BUF = (C::FD == 1) ? 0 : (KS & 1), KN = (KS + C::FD) % C::STEPS;
  if constexpr (r == 0) { a_wait<C, C::NA(KS), SL>(g); GAP(); }
  {
    const bf16x8 bf = __builtin_shufflevector(g.blo[BUF][NBI], g.bhi[BUF][NBI], 0, 1, 2, 3, 4, 5, 6, 7);
    if constexpr (C::ASMACC && C::SH == 32) { constexpr int ao = 16 * (C::MR * NBI + MRI);
      asm volatile("v_mfma_f32_32x32x16_bf16 a[%c2:%c3], %0, %1, a[%c2:%c3]" :: "v"(bf), "v"(g.a[SL][MRI]), "i"(ao), "i"(ao + 15)); }
    else if constexpr (C::ASMACC) { constexpr int ao = 4 * (C::MR * NBI + MRI);
      asm volatile("v_mfma_f32_16x16x32_bf16 a[%c2:%c3], %0, %1, a[%c2:%c3]" :: "v"(bf), "v"(g.a[SL][MRI]), "i"(ao), "i"(ao + 3)); }
    else if constexpr (C::SH == 32) g.acc[NBI][MRI] = __builtin_amdgcn_mfma_f32_32x32x16_bf16(bf, g.a[SL][MRI], g.acc[NBI][MRI], 0, 0, 0);
    else if constexpr (C::GU) asm volatile("v_mfma_f32_16x16x32_bf16 %0, %1, %2, %0" : "+v"(g.acc[NBI][MRI]) : "v"(bf), "v"(g.a[SL][MRI]));
    else g.acc[NBI][MRI] = __builtin_amdgcn_mfma_f32_16x16x32_bf16(bf, g.a[SL][MRI], g.acc[NBI][MRI], 0, 0, 0);
  }
  if constexpr (GI == C::GB) {
    GAP();
    static_assert(C::NBW() < 64, "");
    asm volatile("s_waitcnt vmcnt(%0) lgkmcnt(0)\n\ts_barrier" :: "i"(C::NBW()) : "memory");
    GAP();
    b_read_all<C, BUF, KN>(g, it.va_nxt, make_iseq<C::BARPOS + 1>{});
  } else if constexpr (MRI == C::MR - 1) {
    if constexpr (KS == C::BSTEP) { if constexpr (NBI > C::BARPOS) b_read<C, BUF, KN, NBI>(g, it.va_nxt); }
    else if constexpr (KS > C::BSTEP) b_read<C, BUF, KN, NBI>(g, it.va_nxt);
    else b_read<C, BUF, KN, NBI>(g, it.va_cur);
  }
  g_ops_at<C, TI, GI>(g, it, make_iseq<C::NOPS>{});
  GAP();
}
template <class C, int TI, int... Gs> DI void g_gaps(GState<C>& g, const GIter& it, iseq<Gs...>) { (g_gap<C, TI, Gs>(g, it), ...); }
template <class C, int TI> DI void g_tile(GState<C>& g, int t, unsigned& stg, unsigned& pstg) {
  unsigned nstg = stg + C::STAGE; if (nstg == C::NS * C::STAGE) nstg = 0;
  GIter it;
  it.va_cur = g.vrd + stg; it.va_nxt = g.vrd + nstg;
  it.sA = (unsigned)(C::STEPS * 1024) * (unsigned)(t + C::PDT); it.sA0 = it.sA - 1024u;
  it.m0_fill = g.m0base + pstg; it.sK_fill = g.ktileB * (unsigned)(t - 1 + C::NS);
  g_gaps<C, TI>(g, it, make_iseq<C::G>{});
  pstg = stg; stg = nstg;
}
template <class C, int... TIs> DI void g_body(GState<C>& g, int t, unsigned& stg, unsigned& pstg, iseq<TIs...>) { (g_tile<C, TIs>(g, t + TIs, stg, pstg), ...); }

template <class C, int I> DI void g_touch(GState<C>& g) {
  asm volatile("s_mov_b32 m0, %0\n\ts_nop 0\n\tglobal_load_lds_dword %1, %2" :: "s"(g.m0base + (unsigned)(C::NS - 1) * C::STAGE), "v"(g.tch_off), "s"(g.tch[I]) : "memory");
}
template <class C, int I> DI void g_kv(GState<C>& g) {
  constexpr int q = I & 3;
  if constexpr ((I >> 2) == 1) asm volatile("s_mov_b32 m0, %0\n\ts_nop 0\n\tbuffer_load_dwordx4 %1, %2, %3 offen lds" :: "s"(g.kvm0 + 0x8000u + 1024u * q), "v"(g.vv), "s"(g.srdV), "s"(g.kvso + 2048u * (q >> 1) + 128u * (q & 1)) : "memory");
  else asm volatile("s_mov_b32 m0, %0\n\ts_nop 0\n\tbuffer_load_dwordx4 %1, %2, %3 offen offset:%c4 lds" :: "s"(g.kvm0 + (I >> 3) * 0x4000u), "v"(g.vk[q]), "s"(g.srdK), "s"(g.kvso + (I >> 3) * 16384u), "i"(1024 * q) : "memory");
}
template <class C> constexpr int dummy_index(int V, int J) {
  const int ks = C::op_step(J), i = J - C::step_first(ks);
  if (i >= C::MR || (C::STEPS * (V + C::PDT) + ks - 1) >= 0) return -1;
  return (V + (C::NS - 1)) * C::STEPS * C::MR + ks * C::MR + i;
}
template <class C, int V, int J> DI void g_virt_op(GState<C>& g, const GIter& it) {
  constexpr int ks = C::op_step(J);
  constexpr int TI = ((V % C::PDT) + C::PDT) % C::PDT;
  constexpr bool VA = (C::STEPS * (V + C::PDT) + ks - 1) >= 0, VB = (V - 1 + C::NS) >= 0;
  if constexpr (V == -1 && J == C::ops_before(C::GB)) asm volatile("s_waitcnt vmcnt(%0)\n\ts_barrier" :: "i"(C::NBW()) : "memory");
  constexpr int di = C::ASMACC ? dummy_index<C>(V, J) : -1;
  if constexpr (di >= 0 && di < 3) g_touch<C, di>(g);
  else if constexpr (di >= KV_FIRST && di < KV_FIRST + 12) g_kv<C, di - KV_FIRST>(g);
  else g_op<C, TI, J, VA, VB>(g, it);
}
template <class C, int V, int... Js> DI void g_virt(GState<C>& g, iseq<Js...>) {
  GIter it; constexpr int tb = V - 1 + C::NS;
  it.va_cur = it.va_nxt = 0; it.sA = (unsigned)(C::STEPS * 1024) * (unsigned)(V + C::PDT >= 0 ? V + C::PDT : 0); it.sA0 = it.sA - 1024u;
  it.m0_fill = g.m0base + (unsigned)(tb >= 0 ? tb : 0) * C::STAGE; it.sK_fill = g.ktileB * (unsigned)(tb >= 0 ? tb : 0);
  (g_virt_op<C, V, Js>(g, it), ...);
  if constexpr (V == -1 && C::ops_before(C::GB) == C::NOPS) asm volatile("s_waitcnt vmcnt(%0)\n\ts_barrier" :: "i"(C::NBW()) : "memory");
}
template <class C, int... Vs> DI void g_prologue(GState<C>& g, iseq<Vs...>) { constexpr int NV = sizeof...(Vs); (g_virt<C, Vs - NV>(g, make_iseq<C::NOPS>{}), ...); }

template <class C, bool PSWAP = false> DI void g_setup(GState<C>& g, unsigned ldsb, unsigned lane, unsigned wid, unsigned ldb) {
  g.m0base = rfl(ldsb + wid * (unsigned)C::PB * 1024u);
  g.ktileB = 64u * ldb;
  const unsigned q = (lane & 15u) >> 2, p0 = lane & 3u, p = PSWAP ? (((p0 & 1u) << 1) | (p0 >> 1)) : p0, g4 = lane >> 4;
  if constexpr (C::SH == 32) {
    const unsigned h = lane >> 5, blk = g4 & 1u;
    g.vrd = ldsb + blk * (C::GU ? (unsigned)C::REGION : 256u) + ((4u * h + q) ^ (4u * blk)) * 32u + 8u * p;
  } else {
    g.vrd = ldsb + (g4 >> 1) * (C::KORD == 0 ? 1u : 2u) * (unsigned)(C::NR16 * 256) + (4u * (g4 & 1u) + q) * 32u + (PSWAP ? (p0 >> 1) * 256u + (p0 & 1u) * 16u : 8u * p0);
  }
  if constexpr (C::GU) {
    const unsigned wr = wid % (C::NW / 2), up = wid / (C::NW / 2);
#pragma unroll
    for (int qq = 0; qq < C::PB; ++qq) {
      const unsigned P = wr * C::PB + qq, s = 4u * P + g4 * 0u + (lane >> 4), kb = s / (unsigned)C::NR16, nb = s % (unsigned)C::NR16;
      const unsigned row = ((lane >> 1) & 7u) ^ ((C::SH == 32 && up) ? 4u : 0u);
      g.voffB[qq] = (P < (unsigned)(C::NR16 * 2)) ? (8u * kb + row) * ldb + 32u * nb + 16u * (lane & 1u) : 0x80000000u;
    }
  } else {
    static_assert(C::GU || (C::NR16 % 4 == 0), "");
    const unsigned sub = lane >> 4;
    const unsigned row = ((lane >> 1) & 7u) ^ ((C::SH == 32 && (sub & 1u)) ? 4u : 0u);
    g.voffB[0] = row * ldb + 32u * sub + 16u * (lane & 1u);
#pragma unroll
    for (int qq = 0; qq < C::PB; ++qq) { const unsigned P = wid * C::PB + qq, kb = P / (unsigned)(C::NR16 / 4), j = P % (unsigned)(C::NR16 / 4); g.sB[qq] = rfl(8u * kb * ldb + 128u * j); }
  }
}

template <int R> DI void acc_zero1() { asm volatile("v_accvgpr_write_b32 a[%c0], 0" :: "i"(R) : W4_ACLOB); }
template <int... Rs> DI void acc_zero(iseq<Rs...>) { (acc_zero1<Rs>(), ...); asm volatile("s_nop 1"); }
template <class C> DI void gemm_run(GState<C>& g, int NT) {
  constexpr int NV = C::PDT > C::NS - 1 ? C::PDT : C::NS - 1;
  if constexpr (!C::ASMACC) {
#pragma unroll
    for (int n = 0; n < C::NB; ++n)
#pragma unroll
      for (int m = 0; m < C::MR; ++m)
#pragma unroll
        for (int e = 0; e < (C::SH == 32 ? 16 : 4); ++e) g.acc[n][m][e] = 0.f;
  }
  g_prologue<C>(g, make_iseq<NV>{});
  if constexpr (C::ASMACC) acc_zero(make_iseq<C::NB * C::MR * (C::SH == 32 ? 16 : 4)>{});
  b_read_all<C, 0, 0>(g, g.vrd, make_iseq<C::NB>{});
  if constexpr (C::FD == 2) b_read_all<C, 1, 1>(g, g.vrd, make_iseq<C::NB>{});
  asm volatile("s_nop 1");
  GAP();
  unsigned stg = 0, pstg = (C::NS - 1) * C::STAGE;
  for (int t = 0; t < NT; t += C::PDT) g_body<C>(g, t, stg, pstg, make_iseq<C::PDT>{});
  asm volatile("s_waitcnt vmcnt(0) lgkmcnt(0)\n\ts_nop 7\n\ts_nop 7" ::: "memory");
#pragma unroll
  for (int i = 0; i < C::PDT * C::STEPS; ++i)
#pragma unroll
    for (int m = 0; m < C::MR; ++m) asm volatile("" :: "v"(g.a[i][m]));
  GAP();
}
typedef __bf16 abf16x8 __attribute__((ext_vector_type(8)));
typedef __bf16 abf16x4 __attribute__((ext_vector_type(4)));
#define ATTN_THR 64.0f
constexpr unsigned KRS = 256u, KTILEB = 64u * KRS;
#define KRING 0x0000u
#define VRING 0x8000u
#define QGEMM_LDS 0x10000u
DI float max3f(float a, float b, float c) { float r; asm("v_max3_f32 %0, %1, %2, %3" : "=v"(r) : "v"(a), "v"(b), "v"(c)); return r; }
DI float max2f(float a, float b) { float r; asm("v_max_f32_e32 %0, %1, %2" : "=v"(r) : "v"(a), "v"(b)); return r; }
DI float fadd_s(float a, float b) { float r; asm("v_add_f32_e32 %0, %1, %2" : "=v"(r) : "v"(a), "v"(b)); return r; }
DI float fmul_s(float a, float b) { float r; asm("v_mul_f32_e32 %0, %1, %2" : "=v"(r) : "v"(a), "v"(b)); return r; }
DI float fsub_s(float a, float b) { float r; asm("v_sub_f32_e32 %0, %1, %2" : "=v"(r) : "v"(a), "v"(b)); return r; }
struct W4C {
  unsigned vkb[4], vvb, vksrc[4], vvsrc[4], m0k, m0v, sofk, sofv;
  __amdgpu_buffer_rsrc_t srdK, srdV;
  f32x4 S[2][4][4];
  f32x4 NEGM[4];
  abf16x8 Vf[8][2];
  unsigned pk[4][2][4];
  abf16x4 vlo[8][2], vup[8][2];
  float mhat[4], fct[4]; bool pend;
  f32x4 LS[4];
  u32x4 ones;
  float mx[4];
  unsigned sk_m0, sk_so, sv_m0, sv_so;
  unsigned pf_lo, pf_hi, pf_off, pf_m0;
};
constexpr int AO_(int n, int j) { return 4 * (4 * n + j); }
constexpr int AQ_(int j, int s) { return 128 + 16 * j + 4 * s; }
constexpr int AK_(int i, int s) { return 192 + 16 * i + 4 * s; }

template <int I, int SLOT> DI void tr_read(W4C& c) {
  constexpr int u = I >> 4, n = (I >> 1) & 7, t = I & 1, off = SLOT * 0x4000 + (4 * u + 2 * t) * 2048 + n * 256;
  if constexpr (t == 0) asm volatile("ds_read_b64_tr_b16 %0, %1 offset:%2" : "=v"(c.vlo[n][u]) : "v"(c.vvb), "i"(off) : "memory");
  else                  asm volatile("ds_read_b64_tr_b16 %0, %1 offset:%2" : "=v"(c.vup[n][u]) : "v"(c.vvb), "i"(off) : "memory");
}
template <int I, int SLOT> DI void k_read(W4C& c) {
  constexpr int s = I >> 2, i = I & 3;
  asm volatile("ds_read_b128 a[%c1:%c2], %0 offset:%c3" :: "v"(c.vkb[s]), "i"(AK_(i, s)), "i"(AK_(i, s) + 3), "i"(SLOT * 0x4000 + 4096 * i) : "memory");
}
template <int X, bool FIRST, int M> DI void qk_mfma(W4C& c) {
  constexpr int s = M >> 4, i = (M >> 2) & 3, j = M & 3, ak = AK_(i, s), aq = AQ_(j, s);
  if constexpr (s == 0) {
    if constexpr (FIRST) asm volatile("v_mfma_f32_16x16x32_bf16 %0, a[%c1:%c2], a[%c3:%c4], 0" : "=&v"(c.S[X][i][j]) : "i"(ak), "i"(ak + 3), "i"(aq), "i"(aq + 3));
    else                 asm volatile("v_mfma_f32_16x16x32_bf16 %0, a[%c2:%c3], a[%c4:%c5], %1" : "=&v"(c.S[X][i][j]) : "v"(c.NEGM[j]), "i"(ak), "i"(ak + 3), "i"(aq), "i"(aq + 3));
  } else asm volatile("v_mfma_f32_16x16x32_bf16 %0, a[%c1:%c2], a[%c3:%c4], %0" : "+v"(c.S[X][i][j]) : "i"(ak), "i"(ak + 3), "i"(aq), "i"(aq + 3));
}
template <int M, bool ZERO> DI void pv_mfma(W4C& c) {
  constexpr int u = M >> 5, n = (M >> 2) & 7, j = M & 3, ao = AO_(n, j);
  const u32x4 pw = {c.pk[j][u][0], c.pk[j][u][1], c.pk[j][u][2], c.pk[j][u][3]};
  if constexpr (ZERO && u == 0) asm volatile("v_mfma_f32_16x16x32_bf16 a[%c2:%c3], %0, %1, 0" :: "v"(c.Vf[n][u]), "v"(pw), "i"(ao), "i"(ao + 3));
  else                        asm volatile("v_mfma_f32_16x16x32_bf16 a[%c2:%c3], %0, %1, a[%c2:%c3]" :: "v"(c.Vf[n][u]), "v"(pw), "i"(ao), "i"(ao + 3));
}
template <int X, int H, int E> DI void op_exp(W4C& c) { constexpr int i = 2 * H + (E >> 4), j = (E >> 2) & 3, r = E & 3; c.S[X][i][j][r] = __builtin_amdgcn_exp2f(c.S[X][i][j][r]); }
template <int J, int U> DI void ls_mfma(W4C& c) {
  const u32x4 pw = {c.pk[J][U][0], c.pk[J][U][1], c.pk[J][U][2], c.pk[J][U][3]};
  asm volatile("v_mfma_f32_16x16x32_bf16 %0, %1, %2, %0" : "+v"(c.LS[J]) : "v"(c.ones), "v"(pw));
}
template <int X, int H, int J> DI void op_cvt(W4C& c) {
  constexpr int ih = J >> 3, j = (J >> 1) & 3, r = 2 * (J & 1), p = 2 * ih + (J & 1);
  c.pk[j][H][p] = cvtpk(c.S[X][2 * H + ih][j][r], c.S[X][2 * H + ih][j][r + 1]);
  if constexpr (H == 1) asm volatile("" : "+v"(c.pk[j][H][p]));
}
template <int X, int I> DI float sval(W4C& c) { return c.S[X][I >> 4][(I >> 2) & 3][I & 3]; }
template <int X, int G> DI void op_max2(W4C& c) {
  constexpr int a = 2 * (G & 1);
  if constexpr (G < 2) asm volatile("v_max3_f32 %0, %2, %3, %4\n\tv_max3_f32 %1, %5, %6, %7" : "=&v"(c.mx[a]), "=&v"(c.mx[a + 1])
                                    : "v"(sval<X, 6 * G>(c)), "v"(sval<X, 6 * G + 1>(c)), "v"(sval<X, 6 * G + 2>(c)), "v"(sval<X, 6 * G + 3>(c)), "v"(sval<X, 6 * G + 4>(c)), "v"(sval<X, 6 * G + 5>(c)));
  else if constexpr (G < 15) asm volatile("v_max3_f32 %0, %0, %2, %3\n\tv_max3_f32 %1, %1, %4, %5" : "+v"(c.mx[a]), "+v"(c.mx[a + 1])
                    : "v"(sval<X, 4 * G + 4>(c)), "v"(sval<X, 4 * G + 5>(c)), "v"(sval<X, 4 * G + 6>(c)), "v"(sval<X, 4 * G + 7>(c)));
  else asm volatile("v_max3_f32 %0, %0, %1, %2\n\tv_max_f32 %0, %0, %3" : "+v"(c.mx[0]) : "v"(c.mx[1]), "v"(c.mx[2]), "v"(c.mx[3]));
}
constexpr int NGB = 72;
constexpr int EB0 = 18;
constexpr int eb_lo(int g) { return g <= EB0 ? 0 : (32 * (g - EB0)) / (NGB - EB0); }
#define CVT1A 8
template <int X, bool FIRST, int M> DI void gapA(W4C& c, const int t) {
  constexpr int Xp = X ^ 1;
  qk_mfma<X, FIRST, M>(c);
  GAP();
  if constexpr (!FIRST) {
    if constexpr ((M & 1) == 0) op_exp<Xp, 1, M / 2>(c);
    else {
      constexpr int o = M >> 1;
      if constexpr (o < 8) { tr_read<2 * o, Xp>(c); tr_read<2 * o + 1, Xp>(c); }
      else if constexpr (o < 24) { tr_read<8 + o, Xp>(c); op_cvt<Xp, 0, o - 8>(c); }
      else { op_cvt<Xp, 1, o - 24>(c); }
    }
  }
  if constexpr (M == 51) { c.sk_m0 = c.m0k + (unsigned)X * 0x4000u; asm volatile("" : "+s"(c.sk_m0)); }
  if constexpr (M == 53) { c.sk_so = KTILEB * (unsigned)(t + 2) + c.sofk; asm volatile("" : "+s"(c.sk_so)); }
  if constexpr (M == 55) { c.sv_m0 = c.m0v + (unsigned)Xp * 0x4000u; asm volatile("" : "+s"(c.sv_m0)); }
  if constexpr (M == 57) { c.sv_so = KTILEB * (unsigned)(t + 1) + c.sofv; asm volatile("" : "+s"(c.sv_so)); }
  if constexpr (M == 61) asm volatile("s_mov_b32 m0, %0" :: "s"(c.sk_m0) : "memory");
  if constexpr (!FIRST && M == 24) asm volatile("" :: "v"(c.NEGM[0]), "v"(c.NEGM[1]), "v"(c.NEGM[2]), "v"(c.NEGM[3]));
  GAP();
}
#define W4_VLO "+v"(c.vlo[0][0]), "+v"(c.vlo[1][0]), "+v"(c.vlo[2][0]), "+v"(c.vlo[3][0]), "+v"(c.vlo[4][0]), "+v"(c.vlo[5][0]), "+v"(c.vlo[6][0]), "+v"(c.vlo[7][0]), \
               "+v"(c.vlo[0][1]), "+v"(c.vlo[1][1]), "+v"(c.vlo[2][1]), "+v"(c.vlo[3][1]), "+v"(c.vlo[4][1]), "+v"(c.vlo[5][1]), "+v"(c.vlo[6][1]), "+v"(c.vlo[7][1])
#define W4_VUP "+v"(c.vup[0][0]), "+v"(c.vup[1][0]), "+v"(c.vup[2][0]), "+v"(c.vup[3][0]), "+v"(c.vup[4][0]), "+v"(c.vup[5][0]), "+v"(c.vup[6][0]), "+v"(c.vup[7][0]), \
               "+v"(c.vup[0][1]), "+v"(c.vup[1][1]), "+v"(c.vup[2][1]), "+v"(c.vup[3][1]), "+v"(c.vup[4][1]), "+v"(c.vup[5][1]), "+v"(c.vup[6][1]), "+v"(c.vup[7][1])
DI void v_concat(W4C& c) {
#pragma unroll
  for (int u = 0; u < 2; ++u)
#pragma unroll
    for (int n = 0; n < 8; ++n) c.Vf[n][u] = __builtin_shufflevector(c.vlo[n][u], c.vup[n][u], 0, 1, 2, 3, 4, 5, 6, 7);
}
template <int X, bool FIRST, int... Ms> DI void phaseA_(W4C& c, const int t, iseq<Ms...>) {
  (gapA<X, FIRST, Ms>(c, t), ...);
  if constexpr (FIRST) asm volatile("s_waitcnt vmcnt(0) lgkmcnt(0)\n\ts_barrier\n\ts_nop 7" ::: "memory");
  else { asm volatile("s_waitcnt vmcnt(%c16) lgkmcnt(0)" : W4_VLO : "i"(X == 1 ? 1 : 0) : "memory"); asm volatile("s_barrier\n\ts_nop 1" : W4_VUP :: "memory"); }
  GAP();
  if constexpr (!FIRST) v_concat(c);
}
template <int X, bool FIRST> DI void phaseA(W4C& c, const int t) { phaseA_<X, FIRST>(c, t, make_iseq<64>{}); }

template <int X, bool INIT> DI void new_reference(W4C& c) {
#pragma unroll
  for (int j = 0; j < 4; ++j) {
    float m0 = max3f(c.S[X][0][j][0], c.S[X][0][j][1], c.S[X][0][j][2]), m1 = max2f(c.S[X][0][j][3], c.S[X][1][j][0]);
    m0 = max3f(m0, c.S[X][1][j][1], c.S[X][1][j][2]); m1 = max3f(m1, c.S[X][1][j][3], c.S[X][2][j][0]);
    m0 = max3f(m0, c.S[X][2][j][1], c.S[X][2][j][2]); m1 = max3f(m1, c.S[X][2][j][3], c.S[X][3][j][0]);
    m0 = max3f(m0, c.S[X][3][j][1], c.S[X][3][j][2]); m1 = max2f(m1, c.S[X][3][j][3]);
    float mm = max2f(m0, m1);
    mm = max2f(mm, __shfl_xor(mm, 16)); mm = max2f(mm, __shfl_xor(mm, 32));
    const float dl = INIT ? mm : max2f(mm, 0.f);
    if constexpr (INIT) { c.mhat[j] = dl; c.fct[j] = 1.f; }
    else { c.mhat[j] = fadd_s(c.mhat[j], dl); c.fct[j] = __builtin_amdgcn_exp2f(-dl); }
#pragma unroll
    for (int i = 0; i < 4; ++i) { _Pragma("unroll") for (int r = 0; r < 4; ++r) c.S[X][i][j][r] = fsub_s(c.S[X][i][j][r], dl); }
    const float nm = -c.mhat[j];
    c.NEGM[j] = (f32x4){nm, nm, nm, nm};
    asm volatile("" : "+v"(c.NEGM[j]));
  }
  asm volatile("s_nop 1");
}
constexpr int pvi(int G) { return G < 28 ? G : G - 4; }
template <int X, bool PEEL, bool ZERO1, int M> DI void gapB(W4C& c, const int t) {
  constexpr int Xp = X ^ 1;
  if constexpr (!PEEL) {
    if constexpr (M >= 28 && M < 32) ls_mfma<M - 28, 0>(c); else if constexpr (M >= 68) ls_mfma<M - 68, 1>(c); else pv_mfma<pvi(M), ZERO1>(c);
    GAP();
  }
  constexpr int kq = M == 0 ? 0 : M == 22 ? 1 : M == 27 ? 2 : M == 32 ? 3 : -1, vq = M == 40 ? 0 : M == 45 ? 1 : M == 50 ? 2 : M == 55 ? 3 : -1;
  if constexpr (kq >= 0) asm volatile("buffer_load_dwordx4 %0, %1, %2 offen offset:%c3 lds" :: "v"(c.vksrc[kq]), "s"(c.srdK), "s"(c.sk_so), "i"(1024 * kq) : "memory");
  if constexpr (M == 37) asm volatile("s_mov_b32 m0, %0" :: "s"(c.sv_m0) : "memory");
  if constexpr (vq >= 0) asm volatile("buffer_load_dwordx4 %0, %1, %2 offen offset:%c3 lds" :: "v"(c.vvsrc[vq]), "s"(c.srdV), "s"(c.sv_so), "i"(1024 * vq) : "memory");
  if constexpr (!PEEL && M >= 1 && M <= 16) { op_max2<X, M - 1>(c); if constexpr (M & 1) op_cvt<Xp, 1, 8 + (M - 1) / 2>(c); }
  if constexpr (M == 17) {
    if constexpr (PEEL) new_reference<X, true>(c);
    else if (__builtin_expect(__any((int)(c.mx[0] > ATTN_THR)), 0)) { new_reference<X, false>(c); c.pend = true; GAP(); }
  }
  if constexpr (M >= EB0) {
    constexpr int e0 = eb_lo(M), e1 = eb_lo(M + 1);
    if constexpr (e0 < e1) op_exp<X, 0, e0>(c);
    else {
      constexpr int fi = (M - EB0) - e0;
      if constexpr (fi < 16) k_read<fi, Xp>(c);
    }
  }
  if constexpr (X == 0 && M == 60) {
    unsigned plo, phi;
    asm volatile("v_readlane_b32 %0, %2, %4\n\tv_readlane_b32 %1, %3, %4\n\ts_mov_b32 m0, %5" : "=s"(plo), "=s"(phi) : "v"(c.pf_lo), "v"(c.pf_hi), "s"((unsigned)(t >> 1)), "s"(c.pf_m0) : "memory");
    c.sk_m0 = plo; c.sk_so = phi;
  }
  if constexpr (X == 0 && M == 65) {
    const unsigned long long pb = ((unsigned long long)c.sk_so << 32) | c.sk_m0;
    asm volatile("global_load_lds_dword %0, %1" :: "v"(c.pf_off), "s"(pb) : "memory");
  }
  if constexpr (M == NGB - 1) asm volatile("" :: "v"(c.S[X][0][0]), "v"(c.S[X][0][1]), "v"(c.S[X][0][2]), "v"(c.S[X][0][3]), "v"(c.S[X][1][0]), "v"(c.S[X][1][1]), "v"(c.S[X][1][2]), "v"(c.S[X][1][3]));
  GAP();
}
template <int R> DI void resc4(float f) {
  float t0, t1, t2, t3;
  asm volatile("v_accvgpr_read_b32 %0, a[%c5]\n\tv_accvgpr_read_b32 %1, a[%c6]\n\tv_accvgpr_read_b32 %2, a[%c7]\n\tv_accvgpr_read_b32 %3, a[%c8]\n\t"
               "v_mul_f32 %0, %4, %0\n\tv_mul_f32 %1, %4, %1\n\tv_mul_f32 %2, %4, %2\n\tv_mul_f32 %3, %4, %3\n\t"
               "v_accvgpr_write_b32 a[%c5], %0\n\tv_accvgpr_write_b32 a[%c6], %1\n\tv_accvgpr_write_b32 a[%c7], %2\n\tv_accvgpr_write_b32 a[%c8], %3"
               : "=&v"(t0), "=&v"(t1), "=&v"(t2), "=&v"(t3) : "v"(f), "i"(R), "i"(R + 1), "i"(R + 2), "i"(R + 3) : W4_ACLOB);
}
template <int... Is> DI void resc_all(W4C& c, iseq<Is...>) { (resc4<4 * Is>(c.fct[Is & 3]), ...); }
template <int X, bool PEEL, bool ZERO1, int... Ms> DI void phaseB_(W4C& c, const int t, iseq<Ms...>) {
  (gapB<X, PEEL, ZERO1, Ms>(c, t), ...);
  if constexpr (!PEEL) {
    if (__builtin_expect(c.pend, 0)) {
      asm volatile("s_nop 7\n\ts_nop 7" ::: W4_ACLOB);
      resc_all(c, make_iseq<32>{});
#pragma unroll
      for (int j = 0; j < 4; ++j) { c.LS[j] = c.LS[j] * c.fct[j]; asm volatile("" : "+v"(c.LS[j])); }
      asm volatile("s_nop 1" ::: W4_ACLOB);
      c.pend = false;
    }
  }
  asm volatile("s_waitcnt lgkmcnt(0)" ::: "memory");
  GAP();
}
template <int X, bool PEEL, bool ZERO1> DI void phaseB(W4C& c, const int t) { phaseB_<X, PEEL, ZERO1>(c, t, make_iseq<NGB>{}); }
template <int... Is> DI void k_reads0(W4C& c, iseq<Is...>) { (k_read<Is, 0>(c), ...); }
template <int... Is> DI void tr_reads1(W4C& c, iseq<Is...>) { (tr_read<Is, 1>(c), ...); }
template <int... Is> DI void drain_exp(W4C& c, iseq<Is...>) { (op_exp<1, 1, Is>(c), ...); }
template <int... Is> DI void drain_cvt0(W4C& c, iseq<Is...>) { (op_cvt<1, 0, Is>(c), ...); }
template <int... Is> DI void drain_cvt1(W4C& c, iseq<Is...>) { (op_cvt<1, 1, Is>(c), ...); }
template <int... Ms> DI void pv_all(W4C& c, iseq<Ms...>) { (pv_mfma<Ms, false>(c), ...); }
template <int R0, int R1> DI u32x4 acc_pack8(float f) {
  float o[8];
  asm volatile("v_accvgpr_read_b32 %0, a[%c8]\n\tv_accvgpr_read_b32 %1, a[%c9]\n\tv_accvgpr_read_b32 %2, a[%c10]\n\tv_accvgpr_read_b32 %3, a[%c11]\n\t"
               "v_accvgpr_read_b32 %4, a[%c12]\n\tv_accvgpr_read_b32 %5, a[%c13]\n\tv_accvgpr_read_b32 %6, a[%c14]\n\tv_accvgpr_read_b32 %7, a[%c15]"
               : "=v"(o[0]), "=v"(o[1]), "=v"(o[2]), "=v"(o[3]), "=v"(o[4]), "=v"(o[5]), "=v"(o[6]), "=v"(o[7])
               : "i"(R0), "i"(R0 + 1), "i"(R0 + 2), "i"(R0 + 3), "i"(R1), "i"(R1 + 1), "i"(R1 + 2), "i"(R1 + 3));
  u32x4 w = {cvtpk(o[0] * f, o[1] * f), cvtpk(o[2] * f, o[3] * f), cvtpk(o[4] * f, o[5] * f), cvtpk(o[6] * f, o[7] * f)};
  return w;
}
template <int I> DI void q_conv1(float C) {
  constexpr int j = I >> 2, s = I & 3, aq = AQ_(j, s);
  const u32x4 w = acc_pack8<AO_(2 * s, j), AO_(2 * s + 1, j)>(C);
  asm volatile("v_accvgpr_write_b32 a[%c4], %0\n\tv_accvgpr_write_b32 a[%c5], %1\n\tv_accvgpr_write_b32 a[%c6], %2\n\tv_accvgpr_write_b32 a[%c7], %3"
               :: "v"(w[0]), "v"(w[1]), "v"(w[2]), "v"(w[3]), "i"(aq), "i"(aq + 1), "i"(aq + 2), "i"(aq + 3) : W4_ACLOB_HI);
}
template <int... Is> DI void q_conv(float C, iseq<Is...>) { (q_conv1<Is>(C), ...); }
template <int I> DI void o_store1(__amdgpu_buffer_rsrc_t srd, unsigned vo, const float (&il)[4]) {
  constexpr int j = I >> 2, u = I & 3;
  const u32x4 w = acc_pack8<AO_(2 * u, j), AO_(2 * u + 1, j)>(il[j]);
  __builtin_amdgcn_raw_buffer_store_b128(w, srd, vo + j * (128u * 1024u) + u * 1024, 0, WT);
}
template <int G, int I> DI void fin_step(W4C& c, __amdgpu_buffer_rsrc_t srd, unsigned vo, const float (&il)[4]) {
  if constexpr (G < 4) { pv_mfma<32 * (I >> 3) + 4 * (2 * G + ((I >> 2) & 1)) + (I & 3), false>(c); GAP(); }
  if constexpr (G > 0 && (I & 3) == 3) { o_store1<4 * (I >> 2) + (G - 1)>(srd, vo, il); GAP(); }
}
template <int G, int... Is> DI void fin_group(W4C& c, __amdgpu_buffer_rsrc_t srd, unsigned vo, const float (&il)[4], iseq<Is...>) { (fin_step<G, Is>(c, srd, vo, il), ...); }

using QCfg = GCfg<16, 4, 64, 8, false, 1, 6, 2, 1, 1, true>;
constexpr int SHM_ATTN = 0x10000 + 6 * 16384;

#define ST(i)
__global__ __launch_bounds__(256, 1) void k_attn(const u16* __restrict__ H1f, const u16* __restrict__ Wq, const u16* __restrict__ Kc, const u16* __restrict__ Vc, u16* __restrict__ Of,
                                                        const u16* __restrict__ Wo, const u16* __restrict__ Wg, const u16* __restrict__ Wu, const u16* __restrict__ Wd, const u16* __restrict__ Wm) {
  ST(0); KST_BEGIN(2);
  extern __shared__ __attribute__((aligned(16))) char lds[];
  const unsigned tid = threadIdx.x, wid = rfl(tid >> 6);
  const int bid = blockIdx.x, xcd = bid & 7, j = bid >> 3;
  const int head = 4 * xcd + (j & 3), y = j >> 2, kvh = head >> 3;
  const unsigned ldsb = (unsigned)(size_t)lds;
  {
    const unsigned lane = tid & 63u;
    GState<QCfg> g;
    const int mb0 = y * 16 + wid * 4;
    constexpr unsigned mbstride = 1024u * 32u;
    g.srdA = mk_srd((const char*)H1f + (size_t)mb0 * mbstride, 4 * mbstride);
#pragma unroll
    for (int m = 0; m < 4; ++m) g.voffA[m] = lane * 16u + m * mbstride;
    constexpr unsigned ldb = 4096u * 2u;
    g.srdB = mk_srd((const char*)Wq + (size_t)head * 256, 1024u * ldb - (unsigned)head * 256u);
    g_setup<QCfg, true>(g, ldsb + QGEMM_LDS, lane, wid, ldb);
    { const unsigned gw = blockIdx.x * 4u + wid;
      g.tch[0] = (unsigned long long)(size_t)((const char*)Wq + (size_t)gw * 4096); g.tch[1] = g.tch[0] + (4096u * 1024u);
      g.tch[2] = (unsigned long long)(size_t)((gw < 512u ? (const char*)Kc : (const char*)Vc) + (size_t)(gw & 511u) * 4096);
      g.tch_off = lane * 64u; }
    g.srdK = mk_srd((const char*)Kc + (size_t)kvh * (2048u * 256u), 2048u * 256u);
    g.srdV = mk_srd((const char*)Vc + (size_t)kvh * (2048u * 256u), 2048u * 256u);
    g.kvm0 = rfl(ldsb + KRING + 4096u * wid); g.kvso = rfl(KRS * 16u * wid);
#pragma unroll
    for (int q = 0; q < 4; ++q) g.vk[q] = KRS * (lane >> 4) + 16u * ((lane & 15u) ^ (4u * q + (lane >> 4)));
    g.vv = KRS * ((lane >> 1) & 7u) + 32u * (lane >> 4) + 16u * (lane & 1u);
    gemm_run<QCfg>(g, 16);
  }
  ST(1);
  unsigned lane; asm volatile("v_mbcnt_lo_u32_b32 %0, -1, 0\n\tv_mbcnt_hi_u32_b32 %0, -1, %0" : "=v"(lane));
  constexpr int T = 32;
  W4C c;
  { const unsigned r15 = lane & 15u, g4 = lane >> 4;
#pragma unroll
    for (int s = 0; s < 4; ++s) c.vkb[s] = ldsb + KRING + 256u * r15 + 16u * ((4u * s + g4) ^ r15);
    c.vvb = ldsb + VRING + (g4 >> 1) * 2048u + (4u * (g4 & 1u) + (r15 >> 2)) * 32u + 8u * (lane & 3u);
#pragma unroll
    for (int q = 0; q < 4; ++q) {
      c.vksrc[q] = KRS * g4 + 16u * (r15 ^ (4u * q + g4));
      c.vvsrc[q] = KRS * ((lane >> 1) & 7u) + 32u * g4 + 16u * (lane & 1u) + KRS * 8u * (q >> 1) + 128u * (q & 1) + (3072u - 1024u * q);
    } }
  c.m0k = rfl(ldsb + KRING + 4096u * wid); c.m0v = rfl(ldsb + VRING + 4096u * wid);
  c.sofk = rfl(KRS * 16u * wid); c.sofv = rfl(KRS * 16u * wid) - 3072u;
  c.srdK = mk_srd((const char*)Kc + (size_t)kvh * (2048u * 256u), 2048u * 256u);
  c.srdV = mk_srd((const char*)Vc + (size_t)kvh * (2048u * 256u), 2048u * 256u);
  {
    const unsigned gw = blockIdx.x * 4u + wid;
    const char* p;
    const unsigned c2 = gw + 1024u, c2c = c2 < 1792u ? c2 : gw;
    if (lane == 0) p = (const char*)Wo + (size_t)gw * 4096; else if (lane == 1) p = (const char*)Wo + (size_t)c2 * 4096;
    else if (lane == 2) p = (const char*)Wg + (size_t)gw * 4096; else if (lane == 3) p = (const char*)Wg + (size_t)c2c * 4096;
    else if (lane == 4) p = (const char*)Wu + (size_t)gw * 4096; else if (lane == 5) p = (const char*)Wu + (size_t)c2c * 4096;
    else if (lane == 6) p = (const char*)Wd + (size_t)gw * 4096; else if (lane == 7) p = (const char*)Wd + (size_t)c2c * 4096;
    else if (lane < 11) p = (const char*)Wm + (size_t)(gw + 1024u * (lane - 8u)) * 4096;
    else p = (const char*)Wo + (size_t)gw * 4096;
    c.pf_lo = (unsigned)(size_t)p; c.pf_hi = (unsigned)((size_t)p >> 32);
    c.pf_off = lane * 64u; c.pf_m0 = rfl(ldsb + QGEMM_LDS + 256u * wid);
  }
  asm volatile("s_barrier" ::: "memory");
  k_reads0(c, make_iseq<16>{});
  q_conv(0.12751743082f, make_iseq<16>{});
  asm volatile("s_waitcnt lgkmcnt(0)\n\ts_nop 1" ::: "memory");
  c.pend = false; c.ones = (u32x4){0x3f803f80u, 0x3f803f80u, 0x3f803f80u, 0x3f803f80u}; asm volatile("" : "+v"(c.ones));
#pragma unroll
  for (int j = 0; j < 4; ++j) { c.LS[j] = (f32x4){0.f, 0.f, 0.f, 0.f}; asm volatile("" : "+v"(c.LS[j])); }
  ST(2);
  phaseA<0, true>(c, 0);  phaseB<0, true, false>(c, 0);
  phaseA<1, false>(c, 1); phaseB<1, false, true>(c, 1);
  for (int t = 2; t < T; t += 2) {
    phaseA<0, false>(c, t);     phaseB<0, false, false>(c, t);
    phaseA<1, false>(c, t + 1); phaseB<1, false, false>(c, t + 1);
  }
  ST(3);
  float il[4];
  {
    tr_reads1(c, make_iseq<32>{});
    drain_exp(c, make_iseq<32>{});
    drain_cvt0(c, make_iseq<16>{});
    drain_cvt1(c, make_iseq<16>{});
    asm volatile("s_waitcnt lgkmcnt(0)" : W4_VLO :: "memory"); asm volatile("" : W4_VUP :: "memory");
    GAP();
    v_concat(c);
    asm volatile("s_nop 1" ::: "memory");
    ls_mfma<0, 0>(c); ls_mfma<1, 0>(c); ls_mfma<2, 0>(c); ls_mfma<3, 0>(c); ls_mfma<0, 1>(c); ls_mfma<1, 1>(c); ls_mfma<2, 1>(c); ls_mfma<3, 1>(c);
    unsigned lb; asm volatile("v_mbcnt_lo_u32_b32 %0, -1, 0\n\tv_mbcnt_hi_u32_b32 %0, -1, %0" : "=v"(lb));
    const unsigned vo = ((unsigned)(y * 16 + wid * 4) * 128u + (unsigned)head * 4u) * 1024u + lb * 16u;
    const __amdgpu_buffer_rsrc_t srdO = mk_srd(Of, 16u << 20);
    GAP();
    fin_group<0>(c, srdO, vo, il, make_iseq<16>{});
    asm volatile("" : "+v"(c.LS[0]), "+v"(c.LS[1]), "+v"(c.LS[2]), "+v"(c.LS[3]));
#pragma unroll
    for (int j = 0; j < 4; ++j) il[j] = __builtin_amdgcn_rcpf(c.LS[j][0]);
    GAP();
    fin_group<1>(c, srdO, vo, il, make_iseq<16>{});
    fin_group<2>(c, srdO, vo, il, make_iseq<16>{});
    fin_group<3>(c, srdO, vo, il, make_iseq<16>{});
    asm volatile("s_nop 7\n\ts_nop 1" ::: "memory");
    fin_group<4>(c, srdO, vo, il, make_iseq<16>{});
  }
  ST(4); KST_END(2);
}
template <int N> DI void touch(const char* p, int stride) {
  unsigned d[N];
#pragma unroll
  for (int i = 0; i < N; ++i) d[i] = *(const unsigned*)(p + (size_t)i * stride);
#pragma unroll
  for (int i = 0; i < N; ++i) asm volatile("" :: "v"(d[i]));
}

template <int NT, int KL> DI void mod_body(const u16* __restrict__ c, const u16* __restrict__ wmod, float* __restrict__ dst, int cb, int kh, float* red  ) {
  const int tid = threadIdx.x, cg = tid & 7, kg = tid >> 3, wv = tid >> 6;
  constexpr int NG = NT / 8, NI = KL / NG;
  u32x4 w[NI]; u16 cv[NI];
#pragma unroll
  for (int i = 0; i < NI; ++i) { const int k = kh * KL + kg + NG * i; w[i] = *(const u32x4*)(wmod + (size_t)k * 6144 + cb * 64 + cg * 8); cv[i] = c[k]; }
  float acc[8] = {0, 0, 0, 0, 0, 0, 0, 0};
#pragma unroll
  for (int i = 0; i < NI; ++i) {
    const float xx = bf2f(cv[i]), s = rbf(xx / (1.f + __expf(-xx)));
#pragma unroll
    for (int jj = 0; jj < 4; ++jj) { acc[2 * jj] += s * __uint_as_float(w[i][jj] << 16); acc[2 * jj + 1] += s * __uint_as_float(w[i][jj] & 0xffff0000u); }
  }
#pragma unroll
  for (int jj = 0; jj < 8; ++jj) { float a = acc[jj]; a += __shfl_xor(a, 8); a += __shfl_xor(a, 16); a += __shfl_xor(a, 32); acc[jj] = a; }
  if ((tid & 63) < 8) {
#pragma unroll
    for (int jj = 0; jj < 8; ++jj) red[wv * 64 + cg * 8 + jj] = acc[jj];
  }
  __syncthreads();
  if (tid < 64) { float s = 0;
#pragma unroll
    for (int g = 0; g < NT / 64; ++g) s += red[g * 64 + tid];
    dst[tid] = s; }
}
__global__ __launch_bounds__(512) void k_pre(const u16* __restrict__ c, const u16* __restrict__ wmod, float* __restrict__ modA, const u16* __restrict__ x, unsigned* __restrict__ flg) {
  __shared__ float red[8 * 64];
  const int b = blockIdx.x;
  KST_BEGIN(0);
  unsigned dx = 0;
  if (threadIdx.x == 0) flg[b * 64] = 0u;
  if (threadIdx.x < 256) dx = *(const unsigned*)((const char*)x + (size_t)b * 16384 + threadIdx.x * 64);
  mod_body<512, 128>(c, wmod, modA + (b & 7) * 2048 + (b >> 3) * 64, b >> 3, b & 7, red);
  asm volatile("" :: "v"(dx));
  KST_END(0);
}
DI f32x4 modv(const float* __restrict__ modp, int n) { const f32x4 a = *(const f32x4*)(modp + n), b = *(const f32x4*)(modp + 6144 + n); f32x4 o; for (int e = 0; e < 4; ++e) o[e] = rbf(a[e] + b[e]); return o; }
DI void unpack8(const u32x4& w, float* v) {
#pragma unroll
  for (int j = 0; j < 4; ++j) { v[2 * j] = __uint_as_float(w[j] << 16); v[2 * j + 1] = __uint_as_float(w[j] & 0xffff0000u); }
}
DI u32x4 pack8(const float* o) { return (u32x4){cvtpk(o[0], o[1]), cvtpk(o[2], o[3]), cvtpk(o[4], o[5]), cvtpk(o[6], o[7])}; }

DI void rbf2(float& a, float& b) { const unsigned w = cvtpk(a, b); a = __uint_as_float(w << 16); b = __uint_as_float(w & 0xffff0000u); }
DI void rbf8(float* v) { rbf2(v[0], v[1]); rbf2(v[2], v[3]); rbf2(v[4], v[5]); rbf2(v[6], v[7]); }
DI void modv8(const float* __restrict__ modp, int n, float* o) {
  const f32x4 a0 = *(const f32x4*)(modp + n), b0 = *(const f32x4*)(modp + 6144 + n), a1 = *(const f32x4*)(modp + n + 8), b1 = *(const f32x4*)(modp + 6144 + n + 8);
#pragma unroll
  for (int e = 0; e < 4; ++e) { o[e] = a0[e] + b0[e]; o[4 + e] = a1[e] + b1[e]; }
  rbf8(o);
}
template <int MODE>
__global__ __launch_bounds__(1024) void k_ln(const u16* __restrict__ x, const u16* __restrict__ Pf, const float* __restrict__ modp, u16* __restrict__ xf, u16* __restrict__ hf) {
  __shared__ float red[2][16][8];
  __shared__ __attribute__((aligned(16))) float msum[MODE == 0 ? 2048 : 4];
  const int tid = threadIdx.x;
  KST_BEGIN(MODE == 0 ? 1 : 4);
  const int r = tid & 7, c = tid >> 3, h = c & 1, wid = tid >> 6;
  const int row = blockIdx.x * 8 + r, mb = row >> 5, ml = row & 31;
  const size_t fo = ((size_t)mb * 64 + (c >> 1)) * 512 + (h * 32 + ml) * 8;
  const int n = 16 * (c >> 1) + 4 * h;
  float v[8], sh[8], sc[8];
  if (MODE == 0) {
    const u32x4 xw = *(const u32x4*)(x + (size_t)row * 1024 + 8 * c);
    { f32x2 a = {0.f, 0.f};
#pragma unroll
      for (int ks = 0; ks < 8; ++ks) a += *(const f32x2*)(modp + ks * 2048 + 2 * tid);
      float a0 = a[0], a1 = a[1]; rbf2(a0, a1); *(f32x2*)(msum + 2 * tid) = (f32x2){a0, a1}; }
    const unsigned s0 = h ? xw[0] : xw[2], s1 = h ? xw[1] : xw[3];
    const unsigned g0 = __shfl_xor(s0, 8), g1 = __shfl_xor(s1, 8);
    const u32x4 f = h ? (u32x4){g0, g1, xw[2], xw[3]} : (u32x4){xw[0], xw[1], g0, g1};
    __builtin_amdgcn_raw_buffer_store_b128(f, mk_srd(xf, 4u << 20), (unsigned)fo * 2u, 0, WT);
    unpack8(f, v);
  } else {
    const u32x4 xw = *(const u32x4*)(xf + fo), p0 = *(const u32x4*)(Pf + fo), p1 = *(const u32x4*)(Pf + fo + (size_t)2048 * 1024);
    float ga[8], a[8], b[8];
    modv8(modp, 3072 + n, sh); modv8(modp, 4096 + n, sc); modv8(modp, 2048 + n, ga);
    unpack8(xw, v); unpack8(p0, a); unpack8(p1, b);
#pragma unroll
    for (int j = 0; j < 8; ++j) a[j] += b[j];
    rbf8(a);
#pragma unroll
    for (int j = 0; j < 8; ++j) a[j] *= ga[j];
    rbf8(a);
#pragma unroll
    for (int j = 0; j < 8; ++j) v[j] += a[j];
    const u32x4 w = pack8(v);
    __builtin_amdgcn_raw_buffer_store_b128(w, mk_srd(xf, 4u << 20), (unsigned)fo * 2u, 0, WT);
    unpack8(w, v);
  }
  float s = ((v[0] + v[1]) + (v[2] + v[3])) + ((v[4] + v[5]) + (v[6] + v[7]));
  s += __shfl_xor(s, 8); s += __shfl_xor(s, 16); s += __shfl_xor(s, 32);
  if ((tid & 63) < 8) red[0][wid][r] = s;
  __syncthreads();
  if (MODE == 0) {
    const f32x4 a0 = *(const f32x4*)(msum + n), a1 = *(const f32x4*)(msum + n + 8), b0 = *(const f32x4*)(msum + 1024 + n), b1 = *(const f32x4*)(msum + 1024 + n + 8);
#pragma unroll
    for (int e = 0; e < 4; ++e) { sh[e] = a0[e]; sh[4 + e] = a1[e]; sc[e] = b0[e]; sc[4 + e] = b1[e]; }
  }
  float mean = 0;
#pragma unroll
  for (int w = 0; w < 16; ++w) mean += red[0][w][r];
  mean *= (1.f / 1024.f);
  float qv = 0;
#pragma unroll
  for (int j = 0; j < 8; ++j) { const float d = v[j] - mean; qv += d * d; }
  qv += __shfl_xor(qv, 8); qv += __shfl_xor(qv, 16); qv += __shfl_xor(qv, 32);
  if ((tid & 63) < 8) red[1][wid][r] = qv;
  __syncthreads();
  float var = 0;
#pragma unroll
  for (int w = 0; w < 16; ++w) var += red[1][w][r];
  var = rbf(var * (1.f / 1024.f));
  const float mb16 = rbf(mean), rsd = 1.f / rbf(sqrtf(rbf(var + 1e-5f)));
#pragma unroll
  for (int j = 0; j < 8; ++j) { v[j] -= mb16; sc[j] += 1.f; }
  rbf8(v); rbf8(sc);
#pragma unroll
  for (int j = 0; j < 8; ++j) v[j] *= rsd;
  rbf8(v);
#pragma unroll
  for (int j = 0; j < 8; ++j) v[j] *= sc[j];
  rbf8(v);
#pragma unroll
  for (int j = 0; j < 8; ++j) v[j] += sh[j];
  const unsigned ho = (((unsigned)(row >> 4) * 32u + (unsigned)(c >> 2)) * 1024u + (unsigned)(c & 3) * 256u + (unsigned)(row & 15) * 16u);
  __builtin_amdgcn_raw_buffer_store_b128(pack8(v), mk_srd(hf, 4u << 20), ho, 0, WT);
  KST_END(MODE == 0 ? 1 : 4);
}
template <int KORD> using SCfgT = GCfg<16, 4, 32, 8, false, KORD, 4, 4, 1, 1>;
using SCfg = SCfgT<1>;
template <int K, bool FIN, int NT0>
__global__ __launch_bounds__(256, 2) void k_gemm_sk(const u16* __restrict__ Af, const u16* __restrict__ B, u16* __restrict__ Pf,
                                                    const u16* __restrict__ cvec, const u16* __restrict__ wmod, float* __restrict__ modp,
                                                    const u16* __restrict__ x1f, u16* __restrict__ out, unsigned* __restrict__ flg) {
  using C = SCfgT<K == 4096 ? 0 : 1>;
  extern __shared__ __attribute__((aligned(16))) char lds[];
  KST_BEGIN(K == 4096 ? 3 : 6);
  if (K == 4096 && blockIdx.x >= 256) { const int b = blockIdx.x - 256; mod_body<256, 512>(cvec, wmod, modp + (b & 1) * 6144 + (32 + (b >> 1)) * 64, 32 + (b >> 1), b & 1, (float*)lds); KST_END(3); return; }
  constexpr int N = 1024;
  const unsigned tid = threadIdx.x, lane = tid & 63u, wid = rfl(tid >> 6);
  const int bid = blockIdx.x, xcd = bid & 7, j = bid >> 3;
  const int kh = xcd & 1, mg = xcd >> 1;
  const int mt = mg * 4 + (j & 3), nt = j >> 2;
  const int k0 = kh ? NT0 * 64 : 0, klen = kh ? K - NT0 * 64 : NT0 * 64;
  GState<C> g;
  const int mb0 = mt * 4 + wid;
  constexpr unsigned mbstride = (unsigned)K * 32u;
  g.srdA = mk_srd((const char*)Af + (size_t)(2 * mb0) * mbstride + (size_t)k0 * 32, mbstride + (unsigned)klen * 32u);
  g.voffA[0] = lane * 16u; g.voffA[1] = lane * 16u + mbstride;
  constexpr unsigned ldb = (unsigned)N * 2u;
  g.srdB = mk_srd((const char*)B + (size_t)k0 * ldb + (size_t)nt * 256, (unsigned)klen * ldb - (unsigned)nt * 256u);
  g_setup<C>(g, (unsigned)(size_t)lds, lane, wid, ldb);
  gemm_run<C>(g, klen / 64);
  const unsigned h = (lane >> 4) & 1u, hi = lane >> 5;
  const unsigned fo = (((unsigned)mb0 * 64u + (unsigned)nt * 8u + hi) * 512u + (h * 32u + (lane & 15u)) * 8u) * 2u;
  float acc[4][2][8];
#pragma unroll
  for (int i = 0; i < 4; ++i)
#pragma unroll
    for (int m = 0; m < 2; ++m)
#pragma unroll
      for (int e = 0; e < 4; ++e) {
        auto sw = __builtin_amdgcn_permlane32_swap(__float_as_uint(g.acc[2 * i][m][e]), __float_as_uint(g.acc[2 * i + 1][m][e]), false, false);
        acc[i][m][e] = __uint_as_float(sw[0]); acc[i][m][4 + e] = __uint_as_float(sw[1]);
      }
  if constexpr (!FIN) {
    const __amdgpu_buffer_rsrc_t srdP = mk_srd(Pf, 8u << 20);
#pragma unroll
    for (int i = 0; i < 4; ++i)
#pragma unroll
      for (int m = 0; m < 2; ++m) __builtin_amdgcn_raw_buffer_store_b128(pack8(acc[i][m]), srdP, (unsigned)kh * (4u << 20) + fo + i * 2048 + m * 256, 0, WT);
  } else {
    const __amdgpu_buffer_rsrc_t srdP = mk_srd(Pf, 4u << 20);
    unsigned* flag = flg + (128 + mt * 8 + nt) * 64;
    if (kh == 0) {
#pragma unroll
      for (int i = 0; i < 4; ++i)
#pragma unroll
        for (int m = 0; m < 2; ++m) __builtin_amdgcn_raw_buffer_store_b128(pack8(acc[i][m]), srdP, fo + i * 2048 + m * 256, 0, 16);
      asm volatile("s_waitcnt vmcnt(0)" ::: "memory");
      __syncthreads();
      if (tid == 0) __hip_atomic_store(flag, 1u, __ATOMIC_RELAXED, __HIP_MEMORY_SCOPE_AGENT);
    } else {
      u32x4 xw[4][2]; float gb[4][8];
#pragma unroll
      for (int i = 0; i < 4; ++i) { xw[i][0] = *(const u32x4*)((const char*)x1f + fo + i * 2048); xw[i][1] = *(const u32x4*)((const char*)x1f + fo + i * 2048 + 256);
        modv8(modp, 5120 + nt * 128 + 16 * (2 * i + hi) + 4 * h, gb[i]); }
      if (wid == 0) {
        for (unsigned spins = 0; spins < (1u << 22); ++spins) {
          if ((unsigned)__builtin_amdgcn_readfirstlane(__hip_atomic_load(flag, __ATOMIC_RELAXED, __HIP_MEMORY_SCOPE_AGENT)) != 0u) break;
          __builtin_amdgcn_s_sleep(1);
        }
      }
      asm volatile("" ::: "memory"); __builtin_amdgcn_s_barrier(); asm volatile("" ::: "memory");
      u32x4 pw[4][2];
#pragma unroll
      for (int i = 0; i < 4; ++i)
#pragma unroll
        for (int m = 0; m < 2; ++m) pw[i][m] = __builtin_amdgcn_raw_buffer_load_b128(srdP, fo + i * 2048 + m * 256, 0, 16);
      const unsigned oo = (((unsigned)mb0 * 32u + (lane & 15u)) * 1024u + (unsigned)nt * 128u + 16u * hi + 8u * h) * 2u;
      const __amdgpu_buffer_rsrc_t srdOut = mk_srd(out, 4u << 20);
#pragma unroll
      for (int i = 0; i < 4; ++i)
#pragma unroll
        for (int m = 0; m < 2; ++m) {
          float v[8], a[8];
          unpack8(xw[i][m], v); unpack8(pw[i][m], a);
#pragma unroll
          for (int jj = 0; jj < 8; ++jj) a[jj] += acc[i][m][jj];
          rbf8(a);
#pragma unroll
          for (int jj = 0; jj < 8; ++jj) a[jj] *= gb[i][jj];
          rbf8(a);
#pragma unroll
          for (int jj = 0; jj < 8; ++jj) v[jj] += a[jj];
          const u32x4 w = pack8(v);
          const unsigned s0 = h ? w[0] : w[2], s1 = h ? w[1] : w[3];
          const unsigned g0 = __shfl_xor(s0, 16), g1 = __shfl_xor(s1, 16);
          const u32x4 f = h ? (u32x4){g0, g1, w[2], w[3]} : (u32x4){w[0], w[1], g0, g1};
          __builtin_amdgcn_raw_buffer_store_b128(f, srdOut, oo + 64u * i + 32768u * m, 0, WT);
        }
    }
  }
  KST_END(K == 4096 ? 3 : 6);
}
#define SK2_NT0 28
DI float silu_mul(float g, float u) { return g * __builtin_amdgcn_rcpf(1.f + __builtin_amdgcn_exp2f(g * -1.4426950408889634f)) * u; }
#define U_NS 3
#define U_BP 3
using UCfg = GCfg<16, 8, 32, 14, true, 1, U_NS, 2, 1, U_BP>;
__global__ __launch_bounds__(512, 2) void k_gemm_gu(const u16* __restrict__ Af, const u16* __restrict__ Wg, const u16* __restrict__ Wu, u16* __restrict__ Act) {
  using C = UCfg;
  extern __shared__ __attribute__((aligned(16))) char lds[];
  constexpr int K = 1024, F = 3584;
  KST_BEGIN(5);
  const unsigned tid = threadIdx.x, wid = rfl(tid >> 6);
  const int bid = blockIdx.x, xcd = bid & 7, j = bid >> 3;
  const int mt = (xcd & 1) * 4 + (j & 3), nt = (xcd >> 1) * 8 + (j >> 2);
  GState<C> g;
  const int mb0 = mt * 8 + wid;
  {
    const unsigned lane = tid & 63u;
    constexpr unsigned mbstride = (unsigned)K * 32u;
    g.srdA = mk_srd((const char*)Af + (size_t)(2 * mb0) * mbstride, 2 * mbstride);
    g.voffA[0] = lane * 16u; g.voffA[1] = lane * 16u + mbstride;
    constexpr unsigned ldb = F * 2u;
    const bool up = wid >= 4;
    g.srdB = mk_srd((const char*)(up ? Wu : Wg) + (size_t)nt * 224, (unsigned)K * ldb - (unsigned)nt * 224u);
    g_setup<C>(g, (unsigned)(size_t)lds, lane, wid, ldb);
  }
  gemm_run<C>(g, K / 64);
  unsigned lane; asm volatile("v_mbcnt_lo_u32_b32 %0, -1, 0\n\tv_mbcnt_hi_u32_b32 %0, -1, %0" : "=v"(lane));
  const __amdgpu_buffer_rsrc_t srdO = mk_srd(Act, 2048u * F * 2u);
  const unsigned vo = ((unsigned)mb0 * 2u + (lane >> 5)) * (unsigned)((F >> 5) * 1024) + (lane & 31u) * 16u;
#pragma unroll
  for (int n = 0; n < 7; ++n) {
    unsigned q[2][2];
#pragma unroll
    for (int m = 0; m < 2; ++m) {
      float o[4];
#pragma unroll
      for (int e = 0; e < 4; ++e) o[e] = silu_mul(g.acc[2 * n][m][e], g.acc[2 * n + 1][m][e]);
      q[m][0] = cvtpk(o[0], o[1]); q[m][1] = cvtpk(o[2], o[3]);
    }
    auto s0 = __builtin_amdgcn_permlane32_swap(q[0][0], q[1][0], false, false); auto s1 = __builtin_amdgcn_permlane32_swap(q[0][1], q[1][1], false, false);
    const unsigned c16 = (unsigned)(nt * 7 + n);
    __builtin_amdgcn_raw_buffer_store_b128((u32x4){s0[0], s1[0], s0[1], s1[1]}, srdO, vo + (c16 >> 1) * 1024u + (c16 & 1u) * 512u, 0, WT);
  }
  KST_END(5);
}

#define ALIASP 1
extern "C" void kernel_launch(void* const* d_in, const int* in_sizes, int n_in, void* d_out, int out_size, void* d_ws, size_t ws_size, hipStream_t stream) {
  const u16* x = (const u16*)d_in[0]; const u16* kc = (const u16*)d_in[1]; const u16* vc = (const u16*)d_in[2];
  const u16* wq = (const u16*)d_in[3]; const u16* wo = (const u16*)d_in[4];
  const u16* wg = (const u16*)d_in[6]; const u16* wu = (const u16*)d_in[7]; const u16* wd = (const u16*)d_in[8];
  const u16* c = (const u16*)d_in[9]; const u16* wmod = (const u16*)d_in[10];
  u16* out = (u16*)d_out;
#define WSTOP 0
  char* ws = (char*)d_ws + (WSTOP == 1 ? ((ws_size - (36u << 20)) & ~(size_t)0xfffff) : WSTOP == 2 ? ((ws_size / 2) & ~(size_t)0xfffff) : 0);
  float* mod = (float*)ws;
  u16* hf = (u16*)(ws + (1 << 20));
  u16* x1f = (u16*)(ws + (5 << 20));
  u16* of = (u16*)(ws + (9 << 20));
  u16* act = of;
  u16* P = (u16*)(ws + (25 << 20));
  hipFuncSetAttribute((const void*)k_attn, hipFuncAttributeMaxDynamicSharedMemorySize, SHM_ATTN);
  hipFuncSetAttribute((const void*)k_gemm_sk<4096, false, 32>, hipFuncAttributeMaxDynamicSharedMemorySize, SCfg::LDSB);
  hipFuncSetAttribute((const void*)k_gemm_sk<3584, true, SK2_NT0>, hipFuncAttributeMaxDynamicSharedMemorySize, SCfg::LDSB);
  hipFuncSetAttribute((const void*)k_gemm_gu, hipFuncAttributeMaxDynamicSharedMemorySize, UCfg::LDSB);
  float* modA = (float*)(ws + (256 << 10));
  unsigned* flg = (unsigned*)(ws + (512 << 10));
  u16* P2 = (u16*)(ws + (ALIASP ? (25 << 20) : (33 << 20) - (1 << 20) * 0));
  k_pre<<<256, 512, 0, stream>>>(c, wmod, modA, x, flg);
  k_ln<0><<<256, 1024, 0, stream>>>(x, nullptr, modA, x1f, hf);
  k_attn<<<256, 256, SHM_ATTN, stream>>>(hf, wq, kc, vc, of, wo, wg, wu, wd, wmod);
  k_gemm_sk<4096, false, 32><<<256 + 128, 256, SCfg::LDSB, stream>>>(of, wo, P, c, wmod, mod, nullptr, nullptr, nullptr);
  k_ln<1><<<256, 1024, 0, stream>>>(x, P, mod, x1f, hf);
  k_gemm_gu<<<256, 512, UCfg::LDSB, stream>>>(hf, wg, wu, act);
  k_gemm_sk<3584, true, SK2_NT0><<<256, 256, SCfg::LDSB, stream>>>(act, wd, P2, nullptr, nullptr, mod, x1f, out, flg);
}
```

```cpp
#include <hip/hip_runtime.h>
#include <utility>
#include <type_traits>
typedef unsigned short u16;
typedef short bf16x8 __attribute__((ext_vector_type(8)));
typedef short s16x4 __attribute__((ext_vector_type(4)));
typedef float f32x4 __attribute__((ext_vector_type(4)));
typedef float f32x16 __attribute__((ext_vector_type(16)));
typedef unsigned u32x4 __attribute__((ext_vector_type(4)));
typedef unsigned u32x2 __attribute__((ext_vector_type(2)));
typedef float f32x2 __attribute__((ext_vector_type(2)));
typedef __bf16 bf16x2_t __attribute__((ext_vector_type(2)));
#define DI __device__ __forceinline__
#define LAS __attribute__((address_space(3)))
#define GAP() __builtin_amdgcn_sched_barrier(0)
template <int... Is> using iseq = std::integer_sequence<int, Is...>;
template <int N> using make_iseq = std::make_integer_sequence<int, N>;

DI unsigned rfl(unsigned x) { return __builtin_amdgcn_readfirstlane(x); }
DI float bf2f(u16 v) { return __uint_as_float(((unsigned)v) << 16); }
DI unsigned cvtpk(float lo, float hi) { f32x2 v = {lo, hi}; bf16x2_t b = __builtin_convertvector(v, bf16x2_t); return __builtin_bit_cast(unsigned, b); }
DI float rbf(float f);
DI float rbf(float f) { return __uint_as_float(cvtpk(f, 0.f) << 16); }
#define KV_FIRST 12
#define WT 16
DI __amdgpu_buffer_rsrc_t mk_srd(const void* p, unsigned bytes) { return __builtin_amdgcn_make_buffer_rsrc((void*)p, (short)0, (int)bytes, 0x00020000); }

#define W4_ACLOB "a0","a1","a2","a3","a4","a5","a6","a7","a8","a9","a10","a11","a12","a13","a14","a15","a16","a17","a18","a19","a20","a21","a22","a23","a24","a25","a26","a27","a28","a29","a30","a31","a32","a33","a34","a35","a36","a37","a38","a39","a40","a41","a42","a43","a44","a45","a46","a47","a48","a49","a50","a51","a52","a53","a54","a55","a56","a57","a58","a59","a60","a61","a62","a63","a64","a65","a66","a67","a68","a69","a70","a71","a72","a73","a74","a75","a76","a77","a78","a79","a80","a81","a82","a83","a84","a85","a86","a87","a88","a89","a90","a91","a92","a93","a94","a95","a96","a97","a98","a99","a100","a101","a102","a103","a104","a105","a106","a107","a108","a109","a110","a111","a112","a113","a114","a115","a116","a117","a118","a119","a120","a121","a122","a123","a124","a125","a126","a127"
#define W4_ACLOB_HI "a128","a129","a130","a131","a132","a133","a134","a135","a136","a137","a138","a139","a140","a141","a142","a143","a144","a145","a146","a147","a148","a149","a150","a151","a152","a153","a154","a155","a156","a157","a158","a159","a160","a161","a162","a163","a164","a165","a166","a167","a168","a169","a170","a171","a172","a173","a174","a175","a176","a177","a178","a179","a180","a181","a182","a183","a184","a185","a186","a187","a188","a189","a190","a191","a192","a193","a194","a195","a196","a197","a198","a199","a200","a201","a202","a203","a204","a205","a206","a207","a208","a209","a210","a211","a212","a213","a214","a215","a216","a217","a218","a219","a220","a221","a222","a223","a224","a225","a226","a227","a228","a229","a230","a231","a232","a233","a234","a235","a236","a237","a238","a239","a240","a241","a242","a243","a244","a245","a246","a247","a248","a249","a250","a251","a252","a253","a254","a255"
template <int SH_, int NW_, int WR_, int NB16_, bool GU_, int KORD_, int NS_, int PDT_, int FD_, int BARPOS_, bool ASMACC_ = false>
struct GCfg {
  static constexpr bool ASMACC = ASMACC_;
  static constexpr int SH = SH_, NW = NW_, WR = WR_, NB16 = NB16_, KORD = KORD_, NS = NS_, PDT = PDT_, FD = FD_, BARPOS = BARPOS_;
  static constexpr bool GU = GU_;
  static constexpr int STEPS = SH == 32 ? 4 : 2;
  static constexpr int MR = WR / SH;
  static constexpr int NB = SH == 32 ? NB16 / 2 : NB16;
  static constexpr int NR16 = GU ? NB16 / 2 : NB16;
  static constexpr int PIECES = NB16 * 2;
  static constexpr int PB = (PIECES + NW - 1) / NW;
  static constexpr int STAGE = PB * NW * 1024;
  static constexpr int REGION = STAGE / 2;
  static constexpr int LDSB = NS * STAGE;
  static constexpr int NOPS = STEPS * MR + PB;
  static constexpr int GS = NB * MR;
  static constexpr int G = STEPS * GS;
  static constexpr int BSTEP = STEPS - FD;
  static constexpr int GB = BSTEP * GS + (BARPOS + 1) * MR - 1;
  static constexpr int dma_lo(int ks) { return (PB * ks) / STEPS; }
  static constexpr int step_ops(int ks) { return MR + dma_lo(ks + 1) - dma_lo(ks); }
  static constexpr int step_first(int ks) { return ks * MR + dma_lo(ks); }
  static constexpr int op_step(int j) { int ks = 0; while (ks < STEPS - 1 && j >= step_first(ks + 1)) ++ks; return ks; }
  static constexpr int op_gap(int j) { const int ks = op_step(j), i = j - step_first(ks), n = step_ops(ks); return ks * GS + (i * GS) / n; }
  static constexpr int ops_before(int g) { int c = 0; for (int j = 0; j < NOPS; ++j) if (op_gap(j) < g) ++c; return c; }
  static constexpr int NA(int ks) {
    const int ts = (ks + 1) % STEPS, jt = step_first(ts) + MR - 1;
    const int full = (ks == STEPS - 1) ? PDT - 1 : PDT;
    return (NOPS - 1 - jt) + (full - 1) * NOPS + ops_before(ks * GS);
  }
  static constexpr int NBW() { return (NS - 3) * NOPS + ops_before(GB); }
  static_assert(NS >= 3 && PDT >= 2 && FD <= STEPS - 0 && BSTEP >= 0, "");
  using acc_t = std::conditional_t<SH == 32, f32x16, f32x4>;
};

template <class C>
struct GState {
  typename C::acc_t acc[C::NB][C::MR];
  bf16x8 a[C::PDT * C::STEPS][C::MR];
  s16x4 blo[C::FD][C::NB], bhi[C::FD][C::NB];
  __amdgpu_buffer_rsrc_t srdA, srdB;
  unsigned voffA[C::MR];
  unsigned voffB[C::GU ? C::PB : 1];
  unsigned sB[C::GU ? 1 : C::PB];
  unsigned m0base, vrd, ktileB;
  unsigned long long tch[3]; unsigned tch_off;
  __amdgpu_buffer_rsrc_t srdK, srdV; unsigned kvm0, kvso, vk[4], vv;
};
struct GIter { unsigned va_cur, va_nxt, sA, sA0, m0_fill, sK_fill; };

template <class C, int SL, int OFF, int MRI> DI void a_load1(GState<C>& g, unsigned sA) {
  asm volatile("buffer_load_dwordx4 %0, %1, %2, %3 offen offset:%4" : "=v"(g.a[SL][MRI]) : "v"(g.voffA[MRI]), "s"(g.srdA), "s"(sA), "i"(OFF) : "memory");
}
template <class C, int Q> DI void b_dma1(GState<C>& g, unsigned m0v, unsigned sK) {
  if constexpr (C::GU)
    asm volatile("s_mov_b32 m0, %0\n\ts_nop 0\n\tbuffer_load_dwordx4 %1, %2, %3 offen lds" :: "s"(m0v + 1024u * Q), "v"(g.voffB[Q]), "s"(g.srdB), "s"(sK) : "memory");
  else
    asm volatile("s_mov_b32 m0, %0\n\ts_nop 0\n\tbuffer_load_dwordx4 %1, %2, %3 offen lds" :: "s"(m0v + 1024u * Q), "v"(g.voffB[0]), "s"(g.srdB), "s"(sK + g.sB[Q]) : "memory");
}
template <class C> DI void g_dummy(GState<C>& g) {
  asm volatile("s_mov_b32 m0, %0\n\ts_nop 0\n\tbuffer_load_dwordx4 %1, %2, %3 offen lds" :: "s"(g.m0base + (unsigned)(C::NS - 1) * C::STAGE), "v"(g.voffA[0]), "s"(g.srdA), "s"(0x80000000u) : "memory");
}
template <class C, int TI, int J, bool VA, bool VB> DI void g_op(GState<C>& g, const GIter& it) {
  constexpr int ks = C::op_step(J), i = J - C::step_first(ks);
  if constexpr (i < C::MR) {
    constexpr int PD = C::PDT * C::STEPS, SL = (TI * C::STEPS + ks - 1 + PD) % PD;
    if constexpr (VA) { if constexpr (ks == 0) a_load1<C, SL, 0, i>(g, it.sA0); else a_load1<C, SL, (ks - 1) * 1024, i>(g, it.sA); } else g_dummy<C>(g);
  } else {
    if constexpr (VB) b_dma1<C, C::dma_lo(ks) + i - C::MR>(g, it.m0_fill, it.sK_fill); else g_dummy<C>(g);
  }
}
template <class C, int TI, int GI, int... Js> DI void g_ops_at(GState<C>& g, const GIter& it, iseq<Js...>) {
  ((C::op_gap(Js) == GI ? g_op<C, TI, Js, true, true>(g, it) : (void)0), ...);
}
template <class C> constexpr int b_off(int KS, int NBI, int t) {
  if (C::SH == 32) {
    return ((2 * KS + t) * C::NR16 + (C::GU ? NBI : 2 * NBI)) * 256;
  } else {
    const int kb = 4 * KS + (C::KORD == 0 ? 2 * t : t);
    if (C::ASMACC) return (kb * C::NR16 + (NBI & ~1)) * 256 + 8 * (NBI & 1);
    return (kb * C::NR16 + (C::GU ? NBI / 2 : NBI)) * 256 + ((C::GU && (NBI & 1)) ? C::REGION : 0);
  }
}
template <class C, int BUF, int KS, int NBI> DI void b_read(GState<C>& g, unsigned va) {
  g.blo[BUF][NBI] = __builtin_amdgcn_ds_read_tr16_b64_v4i16((LAS s16x4*)(size_t)(va + b_off<C>(KS, NBI, 0)));
  g.bhi[BUF][NBI] = __builtin_amdgcn_ds_read_tr16_b64_v4i16((LAS s16x4*)(size_t)(va + b_off<C>(KS, NBI, 1)));
}
template <class C, int BUF, int KS, int... Ns> DI void b_read_all(GState<C>& g, unsigned va, iseq<Ns...>) { (b_read<C, BUF, KS, Ns>(g, va), ...); }

template <class C, int N, int SL> DI void a_wait(GState<C>& g) {
  static_assert(N >= 0 && N < 64, "vmcnt range");
  if constexpr (C::MR == 1) asm volatile("s_waitcnt vmcnt(%1)" : "+v"(g.a[SL][0]) : "i"(N) : "memory");
  else if constexpr (C::MR == 2) asm volatile("s_waitcnt vmcnt(%2)" : "+v"(g.a[SL][0]), "+v"(g.a[SL][1]) : "i"(N) : "memory");
  else asm volatile("s_waitcnt vmcnt(%4)" : "+v"(g.a[SL][0]), "+v"(g.a[SL][1]), "+v"(g.a[SL][2]), "+v"(g.a[SL][3]) : "i"(N) : "memory");
}

template <class C, int TI, int GI> DI void g_gap(GState<C>& g, const GIter& it) {
  constexpr int KS = GI / C::GS, r = GI % C::GS, NBI = r / C::MR, MRI = r % C::MR;
  constexpr int SL = TI * C::STEPS + KS, BUF = (C::FD == 1) ? 0 : (KS & 1), KN = (KS + C::FD) % C::STEPS;
  if constexpr (r == 0) { a_wait<C, C::NA(KS), SL>(g); GAP(); }
  {
    const bf16x8 bf = __builtin_shufflevector(g.blo[BUF][NBI], g.bhi[BUF][NBI], 0, 1, 2, 3, 4, 5, 6, 7);
    if constexpr (C::ASMACC && C::SH == 32) { constexpr int ao = 16 * (C::MR * NBI + MRI);
      asm volatile("v_mfma_f32_32x32x16_bf16 a[%c2:%c3], %0, %1, a[%c2:%c3]" :: "v"(bf), "v"(g.a[SL][MRI]), "i"(ao), "i"(ao + 15)); }
    else if constexpr (C::ASMACC) { constexpr int ao = 4 * (C::MR * NBI + MRI);
      asm volatile("v_mfma_f32_16x16x32_bf16 a[%c2:%c3], %0, %1, a[%c2:%c3]" :: "v"(bf), "v"(g.a[SL][MRI]), "i"(ao), "i"(ao + 3)); }
    else if constexpr (C::SH == 32) g.acc[NBI][MRI] = __builtin_amdgcn_mfma_f32_32x32x16_bf16(bf, g.a[SL][MRI], g.acc[NBI][MRI], 0, 0, 0);
    else if constexpr (C::GU) asm volatile("v_mfma_f32_16x16x32_bf16 %0, %1, %2, %0" : "+v"(g.acc[NBI][MRI]) : "v"(bf), "v"(g.a[SL][MRI]));
    else g.acc[NBI][MRI] = __builtin_amdgcn_mfma_f32_16x16x32_bf16(bf, g.a[SL][MRI], g.acc[NBI][MRI], 0, 0, 0);
  }
  if constexpr (GI == C::GB) {
    GAP();
    static_assert(C::NBW() < 64, "");
    asm volatile("s_waitcnt vmcnt(%0) lgkmcnt(0)\n\ts_barrier" :: "i"(C::NBW()) : "memory");
    GAP();
    b_read_all<C, BUF, KN>(g, it.va_nxt, make_iseq<C::BARPOS + 1>{});
  } else if constexpr (MRI == C::MR - 1) {
    if constexpr (KS == C::BSTEP) { if constexpr (NBI > C::BARPOS) b_read<C, BUF, KN, NBI>(g, it.va_nxt); }
    else if constexpr (KS > C::BSTEP) b_read<C, BUF, KN, NBI>(g, it.va_nxt);
    else b_read<C, BUF, KN, NBI>(g, it.va_cur);
  }
  g_ops_at<C, TI, GI>(g, it, make_iseq<C::NOPS>{});
  GAP();
}
template <class C, int TI, int... Gs> DI void g_gaps(GState<C>& g, const GIter& it, iseq<Gs...>) { (g_gap<C, TI, Gs>(g, it), ...); }
template <class C, int TI> DI void g_tile(GState<C>& g, int t, unsigned& stg, unsigned& pstg) {
  unsigned nstg = stg + C::STAGE; if (nstg == C::NS * C::STAGE) nstg = 0;
  GIter it;
  it.va_cur = g.vrd + stg; it.va_nxt = g.vrd + nstg;
  it.sA = (unsigned)(C::STEPS * 1024) * (unsigned)(t + C::PDT); it.sA0 = it.sA - 1024u;
  it.m0_fill = g.m0base + pstg; it.sK_fill = g.ktileB * (unsigned)(t - 1 + C::NS);
  g_gaps<C, TI>(g, it, make_iseq<C::G>{});
  pstg = stg; stg = nstg;
}
template <class C, int... TIs> DI void g_body(GState<C>& g, int t, unsigned& stg, unsigned& pstg, iseq<TIs...>) { (g_tile<C, TIs>(g, t + TIs, stg, pstg), ...); }

template <class C, int I> DI void g_touch(GState<C>& g) {
  asm volatile("s_mov_b32 m0, %0\n\ts_nop 0\n\tglobal_load_lds_dword %1, %2" :: "s"(g.m0base + (unsigned)(C::NS - 1) * C::STAGE), "v"(g.tch_off), "s"(g.tch[I]) : "memory");
}
template <class C, int I> DI void g_kv(GState<C>& g) {
  constexpr int q = I & 3;
  if constexpr ((I >> 2) == 1) asm volatile("s_mov_b32 m0, %0\n\ts_nop 0\n\tbuffer_load_dwordx4 %1, %2, %3 offen lds" :: "s"(g.kvm0 + 0x8000u + 1024u * q), "v"(g.vv), "s"(g.srdV), "s"(g.kvso + 2048u * (q >> 1) + 128u * (q & 1)) : "memory");
  else asm volatile("s_mov_b32 m0, %0\n\ts_nop 0\n\tbuffer_load_dwordx4 %1, %2, %3 offen offset:%c4 lds" :: "s"(g.kvm0 + (I >> 3) * 0x4000u), "v"(g.vk[q]), "s"(g.srdK), "s"(g.kvso + (I >> 3) * 16384u), "i"(1024 * q) : "memory");
}
template <class C> constexpr int dummy_index(int V, int J) {
  const int ks = C::op_step(J), i = J - C::step_first(ks);
  if (i >= C::MR || (C::STEPS * (V + C::PDT) + ks - 1) >= 0) return -1;
  return (V + (C::NS - 1)) * C::STEPS * C::MR + ks * C::MR + i;
}
template <class C, int V, int J> DI void g_virt_op(GState<C>& g, const GIter& it) {
  constexpr int ks = C::op_step(J);
  constexpr int TI = ((V % C::PDT) + C::PDT) % C::PDT;
  constexpr bool VA = (C::STEPS * (V + C::PDT) + ks - 1) >= 0, VB = (V - 1 + C::NS) >= 0;
  if constexpr (V == -1 && J == C::ops_before(C::GB)) asm volatile("s_waitcnt vmcnt(%0)\n\ts_barrier" :: "i"(C::NBW()) : "memory");
  constexpr int di = C::ASMACC ? dummy_index<C>(V, J) : -1;
  if constexpr (di >= 0 && di < 3) g_touch<C, di>(g);
  else if constexpr (di >= KV_FIRST && di < KV_FIRST + 12) g_kv<C, di - KV_FIRST>(g);
  else g_op<C, TI, J, VA, VB>(g, it);
}
template <class C, int V, int... Js> DI void g_virt(GState<C>& g, iseq<Js...>) {
  GIter it; constexpr int tb = V - 1 + C::NS;
  it.va_cur = it.va_nxt = 0; it.sA = (unsigned)(C::STEPS * 1024) * (unsigned)(V + C::PDT >= 0 ? V + C::PDT : 0); it.sA0 = it.sA - 1024u;
  it.m0_fill = g.m0base + (unsigned)(tb >= 0 ? tb : 0) * C::STAGE; it.sK_fill = g.ktileB * (unsigned)(tb >= 0 ? tb : 0);
  (g_virt_op<C, V, Js>(g, it), ...);
  if constexpr (V == -1 && C::ops_before(C::GB) == C::NOPS) asm volatile("s_waitcnt vmcnt(%0)\n\ts_barrier" :: "i"(C::NBW()) : "memory");
}
template <class C, int... Vs> DI void g_prologue(GState<C>& g, iseq<Vs...>) { constexpr int NV = sizeof...(Vs); (g_virt<C, Vs - NV>(g, make_iseq<C::NOPS>{}), ...); }

template <class C, bool PSWAP = false> DI void g_setup(GState<C>& g, unsigned ldsb, unsigned lane, unsigned wid, unsigned ldb) {
  g.m0base = rfl(ldsb + wid * (unsigned)C::PB * 1024u);
  g.ktileB = 64u * ldb;
  const unsigned q = (lane & 15u) >> 2, p0 = lane & 3u, p = PSWAP ? (((p0 & 1u) << 1) | (p0 >> 1)) : p0, g4 = lane >> 4;
  if constexpr (C::SH == 32) {
    const unsigned h = lane >> 5, blk = g4 & 1u;
    g.vrd = ldsb + blk * (C::GU ? (unsigned)C::REGION : 256u) + ((4u * h + q) ^ (4u * blk)) * 32u + 8u * p;
  } else {
    g.vrd = ldsb + (g4 >> 1) * (C::KORD == 0 ? 1u : 2u) * (unsigned)(C::NR16 * 256) + (4u * (g4 & 1u) + q) * 32u + (PSWAP ? (p0 >> 1) * 256u + (p0 & 1u) * 16u : 8u * p0);
  }
  if constexpr (C::GU) {
    const unsigned wr = wid % (C::NW / 2), up = wid / (C::NW / 2);
#pragma unroll
    for (int qq = 0; qq < C::PB; ++qq) {
      const unsigned P = wr * C::PB + qq, s = 4u * P + g4 * 0u + (lane >> 4), kb = s / (unsigned)C::NR16, nb = s % (unsigned)C::NR16;
      const unsigned row = ((lane >> 1) & 7u) ^ ((C::SH == 32 && up) ? 4u : 0u);
      g.voffB[qq] = (P < (unsigned)(C::NR16 * 2)) ? (8u * kb + row) * ldb + 32u * nb + 16u * (lane & 1u) : 0x80000000u;
    }
  } else {
    static_assert(C::GU || (C::NR16 % 4 == 0), "");
    const unsigned sub = lane >> 4;
    const unsigned row = ((lane >> 1) & 7u) ^ ((C::SH == 32 && (sub & 1u)) ? 4u : 0u);
    g.voffB[0] = row * ldb + 32u * sub + 16u * (lane & 1u);
#pragma unroll
    for (int qq = 0; qq < C::PB; ++qq) { const unsigned P = wid * C::PB + qq, kb = P / (unsigned)(C::NR16 / 4), j = P % (unsigned)(C::NR16 / 4); g.sB[qq] = rfl(8u * kb * ldb + 128u * j); }
  }
}

template <int R> DI void acc_zero1() { asm volatile("v_accvgpr_write_b32 a[%c0], 0" :: "i"(R) : W4_ACLOB); }
template <int... Rs> DI void acc_zero(iseq<Rs...>) { (acc_zero1<Rs>(), ...); asm volatile("s_nop 1"); }
template <class C> DI void gemm_run(GState<C>& g, int NT) {
  constexpr int NV = C::PDT > C::NS - 1 ? C::PDT : C::NS - 1;
  if constexpr (!C::ASMACC) {
#pragma unroll
    for (int n = 0; n < C::NB; ++n)
#pragma unroll
      for (int m = 0; m < C::MR; ++m)
#pragma unroll
        for (int e = 0; e < (C::SH == 32 ? 16 : 4); ++e) g.acc[n][m][e] = 0.f;
  }
  g_prologue<C>(g, make_iseq<NV>{});
  if constexpr (C::ASMACC) acc_zero(make_iseq<C::NB * C::MR * (C::SH == 32 ? 16 : 4)>{});
  b_read_all<C, 0, 0>(g, g.vrd, make_iseq<C::NB>{});
  if constexpr (C::FD == 2) b_read_all<C, 1, 1>(g, g.vrd, make_iseq<C::NB>{});
  asm volatile("s_nop 1");
  GAP();
  unsigned stg = 0, pstg = (C::NS - 1) * C::STAGE;
  for (int t = 0; t < NT; t += C::PDT) g_body<C>(g, t, stg, pstg, make_iseq<C::PDT>{});
  asm volatile("s_waitcnt vmcnt(0) lgkmcnt(0)\n\ts_nop 7\n\ts_nop 7" ::: "memory");
#pragma unroll
  for (int i = 0; i < C::PDT * C::STEPS; ++i)
#pragma unroll
    for (int m = 0; m < C::MR; ++m) asm volatile("" :: "v"(g.a[i][m]));
  GAP();
}
typedef __bf16 abf16x8 __attribute__((ext_vector_type(8)));
typedef __bf16 abf16x4 __attribute__((ext_vector_type(4)));
#define ATTN_THR 64.0f
constexpr unsigned KRS = 256u, KTILEB = 64u * KRS;
#define KRING 0x0000u
#define VRING 0x8000u
#define QGEMM_LDS 0x10000u
DI float max3f(float a, float b, float c) { float r; asm("v_max3_f32 %0, %1, %2, %3" : "=v"(r) : "v"(a), "v"(b), "v"(c)); return r; }
DI float max2f(float a, float b) { float r; asm("v_max_f32_e32 %0, %1, %2" : "=v"(r) : "v"(a), "v"(b)); return r; }
DI float fadd_s(float a, float b) { float r; asm("v_add_f32_e32 %0, %1, %2" : "=v"(r) : "v"(a), "v"(b)); return r; }
DI float fmul_s(float a, float b) { float r; asm("v_mul_f32_e32 %0, %1, %2" : "=v"(r) : "v"(a), "v"(b)); return r; }
DI float fsub_s(float a, float b) { float r; asm("v_sub_f32_e32 %0, %1, %2" : "=v"(r) : "v"(a), "v"(b)); return r; }
struct W4C {
  unsigned vkb[4], vvb, vksrc[4], vvsrc[4], m0k, m0v, sofk, sofv;
  __amdgpu_buffer_rsrc_t srdK, srdV;
  f32x4 S[2][4][4];
  f32x4 NEGM[4];
  abf16x8 Vf[8][2];
  unsigned pk[4][2][4];
  abf16x4 vlo[8][2], vup[8][2];
  float mhat[4], fct[4]; bool pend;
  f32x4 LS[4];
  u32x4 ones;
  float mx[4];
  unsigned sk_m0, sk_so, sv_m0, sv_so;
  unsigned pf_lo, pf_hi, pf_off, pf_m0;
};
constexpr int AO_(int n, int j) { return 4 * (4 * n + j); }
constexpr int AQ_(int j, int s) { return 128 + 16 * j + 4 * s; }
constexpr int AK_(int i, int s) { return 192 + 16 * i + 4 * s; }

template <int I, int SLOT> DI void tr_read(W4C& c) {
  constexpr int u = I >> 4, n = (I >> 1) & 7, t = I & 1, off = SLOT * 0x4000 + (4 * u + 2 * t) * 2048 + n * 256;
  if constexpr (t == 0) asm volatile("ds_read_b64_tr_b16 %0, %1 offset:%2" : "=v"(c.vlo[n][u]) : "v"(c.vvb), "i"(off) : "memory");
  else                  asm volatile("ds_read_b64_tr_b16 %0, %1 offset:%2" : "=v"(c.vup[n][u]) : "v"(c.vvb), "i"(off) : "memory");
}
template <int I, int SLOT> DI void k_read(W4C& c) {
  constexpr int s = I >> 2, i = I & 3;
  asm volatile("ds_read_b128 a[%c1:%c2], %0 offset:%c3" :: "v"(c.vkb[s]), "i"(AK_(i, s)), "i"(AK_(i, s) + 3), "i"(SLOT * 0x4000 + 4096 * i) : "memory");
}
template <int X, bool FIRST, int M> DI void qk_mfma(W4C& c) {
  constexpr int s = M >> 4, i = (M >> 2) & 3, j = M & 3, ak = AK_(i, s), aq = AQ_(j, s);
  if constexpr (s == 0) {
    if constexpr (FIRST) asm volatile("v_mfma_f32_16x16x32_bf16 %0, a[%c1:%c2], a[%c3:%c4], 0" : "=&v"(c.S[X][i][j]) : "i"(ak), "i"(ak + 3), "i"(aq), "i"(aq + 3));
    else                 asm volatile("v_mfma_f32_16x16x32_bf16 %0, a[%c2:%c3], a[%c4:%c5], %1" : "=&v"(c.S[X][i][j]) : "v"(c.NEGM[j]), "i"(ak), "i"(ak + 3), "i"(aq), "i"(aq + 3));
  } else asm volatile("v_mfma_f32_16x16x32_bf16 %0, a[%c1:%c2], a[%c3:%c4], %0" : "+v"(c.S[X][i][j]) : "i"(ak), "i"(ak + 3), "i"(aq), "i"(aq + 3));
}
template <int M, bool ZERO> DI void pv_mfma(W4C& c) {
  constexpr int u = M >> 5, n = (M >> 2) & 7, j = M & 3, ao = AO_(n, j);
  const u32x4 pw = {c.pk[j][u][0], c.pk[j][u][1], c.pk[j][u][2], c.pk[j][u][3]};
  if constexpr (ZERO && u == 0) asm volatile("v_mfma_f32_16x16x32_bf16 a[%c2:%c3], %0, %1, 0" :: "v"(c.Vf[n][u]), "v"(pw), "i"(ao), "i"(ao + 3));
  else                        asm volatile("v_mfma_f32_16x16x32_bf16 a[%c2:%c3], %0, %1, a[%c2:%c3]" :: "v"(c.Vf[n][u]), "v"(pw), "i"(ao), "i"(ao + 3));
}
template <int X, int H, int E> DI void op_exp(W4C& c) { constexpr int i = 2 * H + (E >> 4), j = (E >> 2) & 3, r = E & 3; c.S[X][i][j][r] = __builtin_amdgcn_exp2f(c.S[X][i][j][r]); }
template <int J, int U> DI void ls_mfma(W4C& c) {
  const u32x4 pw = {c.pk[J][U][0], c.pk[J][U][1], c.pk[J][U][2], c.pk[J][U][3]};
  asm volatile("v_mfma_f32_16x16x32_bf16 %0, %1, %2, %0" : "+v"(c.LS[J]) : "v"(c.ones), "v"(pw));
}
template <int X, int H, int J> DI void op_cvt(W4C& c) {
  constexpr int ih = J >> 3, j = (J >> 1) & 3, r = 2 * (J & 1), p = 2 * ih + (J & 1);
  c.pk[j][H][p] = cvtpk(c.S[X][2 * H + ih][j][r], c.S[X][2 * H + ih][j][r + 1]);
  if constexpr (H == 1) asm volatile("" : "+v"(c.pk[j][H][p]));
}
template <int X, int I> DI float sval(W4C& c) { return c.S[X][I >> 4][(I >> 2) & 3][I & 3]; }
template <int X, int G> DI void op_max2(W4C& c) {
  constexpr int a = 2 * (G & 1);
  if constexpr (G < 2) asm volatile("v_max3_f32 %0, %2, %3, %4\n\tv_max3_f32 %1, %5, %6, %7" : "=&v"(c.mx[a]), "=&v"(c.mx[a + 1])
                                    : "v"(sval<X, 6 * G>(c)), "v"(sval<X, 6 * G + 1>(c)), "v"(sval<X, 6 * G + 2>(c)), "v"(sval<X, 6 * G + 3>(c)), "v"(sval<X, 6 * G + 4>(c)), "v"(sval<X, 6 * G + 5>(c)));
  else if constexpr (G < 15) asm volatile("v_max3_f32 %0, %0, %2, %3\n\tv_max3_f32 %1, %1, %4, %5" : "+v"(c.mx[a]), "+v"(c.mx[a + 1])
                    : "v"(sval<X, 4 * G + 4>(c)), "v"(sval<X, 4 * G + 5>(c)), "v"(sval<X, 4 * G + 6>(c)), "v"(sval<X, 4 * G + 7>(c)));
  else asm volatile("v_max3_f32 %0, %0, %1, %2\n\tv_max_f32 %0, %0, %3" : "+v"(c.mx[0]) : "v"(c.mx[1]), "v"(c.mx[2]), "v"(c.mx[3]));
}
constexpr int NGB = 72;
constexpr int EB0 = 18;
constexpr int eb_lo(int g) { return g <= EB0 ? 0 : (32 * (g - EB0)) / (NGB - EB0); }
#define CVT1A 8
template <int X, bool FIRST, int M> DI void gapA(W4C& c, const int t) {
  constexpr int Xp = X ^ 1;
  qk_mfma<X, FIRST, M>(c);
  GAP();
  if constexpr (!FIRST) {
    if constexpr ((M & 1) == 0) op_exp<Xp, 1, M / 2>(c);
    else {
      constexpr int o = M >> 1;
      if constexpr (o < 8) { tr_read<2 * o, Xp>(c); tr_read<2 * o + 1, Xp>(c); }
      else if constexpr (o < 24) { tr_read<8 + o, Xp>(c); op_cvt<Xp, 0, o - 8>(c); }
      else { op_cvt<Xp, 1, o - 24>(c); }
    }
  }
  if constexpr (M == 51) { c.sk_m0 = c.m0k + (unsigned)X * 0x4000u; asm volatile("" : "+s"(c.sk_m0)); }
  if constexpr (M == 53) { c.sk_so = KTILEB * (unsigned)(t + 2) + c.sofk; asm volatile("" : "+s"(c.sk_so)); }
  if constexpr (M == 55) { c.sv_m0 = c.m0v + (unsigned)Xp * 0x4000u; asm volatile("" : "+s"(c.sv_m0)); }
  if constexpr (M == 57) { c.sv_so = KTILEB * (unsigned)(t + 1) + c.sofv; asm volatile("" : "+s"(c.sv_so)); }
  if constexpr (M == 61) asm volatile("s_mov_b32 m0, %0" :: "s"(c.sk_m0) : "memory");
  if constexpr (!FIRST && M == 24) asm volatile("" :: "v"(c.NEGM[0]), "v"(c.NEGM[1]), "v"(c.NEGM[2]), "v"(c.NEGM[3]));
  GAP();
}
#define W4_VLO "+v"(c.vlo[0][0]), "+v"(c.vlo[1][0]), "+v"(c.vlo[2][0]), "+v"(c.vlo[3][0]), "+v"(c.vlo[4][0]), "+v"(c.vlo[5][0]), "+v"(c.vlo[6][0]), "+v"(c.vlo[7][0]), \
               "+v"(c.vlo[0][1]), "+v"(c.vlo[1][1]), "+v"(c.vlo[2][1]), "+v"(c.vlo[3][1]), "+v"(c.vlo[4][1]), "+v"(c.vlo[5][1]), "+v"(c.vlo[6][1]), "+v"(c.vlo[7][1])
#define W4_VUP "+v"(c.vup[0][0]), "+v"(c.vup[1][0]), "+v"(c.vup[2][0]), "+v"(c.vup[3][0]), "+v"(c.vup[4][0]), "+v"(c.vup[5][0]), "+v"(c.vup[6][0]), "+v"(c.vup[7][0]), \
               "+v"(c.vup[0][1]), "+v"(c.vup[1][1]), "+v"(c.vup[2][1]), "+v"(c.vup[3][1]), "+v"(c.vup[4][1]), "+v"(c.vup[5][1]), "+v"(c.vup[6][1]), "+v"(c.vup[7][1])
DI void v_concat(W4C& c) {
#pragma unroll
  for (int u = 0; u < 2; ++u)
#pragma unroll
    for (int n = 0; n < 8; ++n) c.Vf[n][u] = __builtin_shufflevector(c.vlo[n][u], c.vup[n][u], 0, 1, 2, 3, 4, 5, 6, 7);
}
template <int X, bool FIRST, int... Ms> DI void phaseA_(W4C& c, const int t, iseq<Ms...>) {
  (gapA<X, FIRST, Ms>(c, t), ...);
  if constexpr (FIRST) asm volatile("s_waitcnt vmcnt(0) lgkmcnt(0)\n\ts_barrier\n\ts_nop 7" ::: "memory");
  else { asm volatile("s_waitcnt vmcnt(%c16) lgkmcnt(0)" : W4_VLO : "i"(X == 1 ? 1 : 0) : "memory"); asm volatile("s_barrier\n\ts_nop 1" : W4_VUP :: "memory"); }
  GAP();
  if constexpr (!FIRST) v_concat(c);
}
template <int X, bool FIRST> DI void phaseA(W4C& c, const int t) { phaseA_<X, FIRST>(c, t, make_iseq<64>{}); }

template <int X, bool INIT> DI void new_reference(W4C& c) {
#pragma unroll
  for (int j = 0; j < 4; ++j) {
    float m0 = max3f(c.S[X][0][j][0], c.S[X][0][j][1], c.S[X][0][j][2]), m1 = max2f(c.S[X][0][j][3], c.S[X][1][j][0]);
    m0 = max3f(m0, c.S[X][1][j][1], c.S[X][1][j][2]); m1 = max3f(m1, c.S[X][1][j][3], c.S[X][2][j][0]);
    m0 = max3f(m0, c.S[X][2][j][1], c.S[X][2][j][2]); m1 = max3f(m1, c.S[X][2][j][3], c.S[X][3][j][0]);
    m0 = max3f(m0, c.S[X][3][j][1], c.S[X][3][j][2]); m1 = max2f(m1, c.S[X][3][j][3]);
    float mm = max2f(m0, m1);
    mm = max2f(mm, __shfl_xor(mm, 16)); mm = max2f(mm, __shfl_xor(mm, 32));
    const float dl = INIT ? mm : max2f(mm, 0.f);
    if constexpr (INIT) { c.mhat[j] = dl; c.fct[j] = 1.f; }
    else { c.mhat[j] = fadd_s(c.mhat[j], dl); c.fct[j] = __builtin_amdgcn_exp2f(-dl); }
#pragma unroll
    for (int i = 0; i < 4; ++i) { _Pragma("unroll") for (int r = 0; r < 4; ++r) c.S[X][i][j][r] = fsub_s(c.S[X][i][j][r], dl); }
    const float nm = -c.mhat[j];
    c.NEGM[j] = (f32x4){nm, nm, nm, nm};
    asm volatile("" : "+v"(c.NEGM[j]));
  }
  asm volatile("s_nop 1");
}
constexpr int pvi(int G) { return G < 28 ? G : G - 4; }
template <int X, bool PEEL, bool ZERO1, int M> DI void gapB(W4C& c, const int t) {
  constexpr int Xp = X ^ 1;
  if constexpr (!PEEL) {
    if constexpr (M >= 28 && M < 32) ls_mfma<M - 28, 0>(c); else if constexpr (M >= 68) ls_mfma<M - 68, 1>(c); else pv_mfma<pvi(M), ZERO1>(c);
    GAP();
  }
  constexpr int kq = M == 0 ? 0 : M == 22 ? 1 : M == 27 ? 2 : M == 32 ? 3 : -1, vq = M == 40 ? 0 : M == 45 ? 1 : M == 50 ? 2 : M == 55 ? 3 : -1;
  if constexpr (kq >= 0) asm volatile("buffer_load_dwordx4 %0, %1, %2 offen offset:%c3 lds" :: "v"(c.vksrc[kq]), "s"(c.srdK), "s"(c.sk_so), "i"(1024 * kq) : "memory");
  if constexpr (M == 37) asm volatile("s_mov_b32 m0, %0" :: "s"(c.sv_m0) : "memory");
  if constexpr (vq >= 0) asm volatile("buffer_load_dwordx4 %0, %1, %2 offen offset:%c3 lds" :: "v"(c.vvsrc[vq]), "s"(c.srdV), "s"(c.sv_so), "i"(1024 * vq) : "memory");
  if constexpr (!PEEL && M >= 1 && M <= 16) { op_max2<X, M - 1>(c); if constexpr (M & 1) op_cvt<Xp, 1, 8 + (M - 1) / 2>(c); }
  if constexpr (M == 17) {
    if constexpr (PEEL) new_reference<X, true>(c);
    else if (__builtin_expect(__any((int)(c.mx[0] > ATTN_THR)), 0)) { new_reference<X, false>(c); c.pend = true; GAP(); }
  }
  if constexpr (M >= EB0) {
    constexpr int e0 = eb_lo(M), e1 = eb_lo(M + 1);
    if constexpr (e0 < e1) op_exp<X, 0, e0>(c);
    else {
      constexpr int fi = (M - EB0) - e0;
      if constexpr (fi < 16) k_read<fi, Xp>(c);
    }
  }
  if constexpr (X == 0 && M == 60) {
    unsigned plo, phi;
    asm volatile("v_readlane_b32 %0, %2, %4\n\tv_readlane_b32 %1, %3, %4\n\ts_mov_b32 m0, %5" : "=s"(plo), "=s"(phi) : "v"(c.pf_lo), "v"(c.pf_hi), "s"((unsigned)(t >> 1)), "s"(c.pf_m0) : "memory");
    c.sk_m0 = plo; c.sk_so = phi;
  }
  if constexpr (X == 0 && M == 65) {
    const unsigned long long pb = ((unsigned long long)c.sk_so << 32) | c.sk_m0;
    asm volatile("global_load_lds_dword %0, %1" :: "v"(c.pf_off), "s"(pb) : "memory");
  }
  if constexpr (M == NGB - 1) asm volatile("" :: "v"(c.S[X][0][0]), "v"(c.S[X][0][1]), "v"(c.S[X][0][2]), "v"(c.S[X][0][3]), "v"(c.S[X][1][0]), "v"(c.S[X][1][1]), "v"(c.S[X][1][2]), "v"(c.S[X][1][3]));
  GAP();
}
template <int R> DI void resc4(float f) {
  float t0, t1, t2, t3;
  asm volatile("v_accvgpr_read_b32 %0, a[%c5]\n\tv_accvgpr_read_b32 %1, a[%c6]\n\tv_accvgpr_read_b32 %2, a[%c7]\n\tv_accvgpr_read_b32 %3, a[%c8]\n\t"
               "v_mul_f32 %0, %4, %0\n\tv_mul_f32 %1, %4, %1\n\tv_mul_f32 %2, %4, %2\n\tv_mul_f32 %3, %4, %3\n\t"
               "v_accvgpr_write_b32 a[%c5], %0\n\tv_accvgpr_write_b32 a[%c6], %1\n\tv_accvgpr_write_b32 a[%c7], %2\n\tv_accvgpr_write_b32 a[%c8], %3"
               : "=&v"(t0), "=&v"(t1), "=&v"(t2), "=&v"(t3) : "v"(f), "i"(R), "i"(R + 1), "i"(R + 2), "i"(R + 3) : W4_ACLOB);
}
template <int... Is> DI void resc_all(W4C& c, iseq<Is...>) { (resc4<4 * Is>(c.fct[Is & 3]), ...); }
template <int X, bool PEEL, bool ZERO1, int... Ms> DI void phaseB_(W4C& c, const int t, iseq<Ms...>) {
  (gapB<X, PEEL, ZERO1, Ms>(c, t), ...);
  if constexpr (!PEEL) {
    if (__builtin_expect(c.pend, 0)) {
      asm volatile("s_nop 7\n\ts_nop 7" ::: W4_ACLOB);
      resc_all(c, make_iseq<32>{});
#pragma unroll
      for (int j = 0; j < 4; ++j) { c.LS[j] = c.LS[j] * c.fct[j]; asm volatile("" : "+v"(c.LS[j])); }
      asm volatile("s_nop 1" ::: W4_ACLOB);
      c.pend = false;
    }
  }
  asm volatile("s_waitcnt lgkmcnt(0)" ::: "memory");
  GAP();
}
template <int X, bool PEEL, bool ZERO1> DI void phaseB(W4C& c, const int t) { phaseB_<X, PEEL, ZERO1>(c, t, make_iseq<NGB>{}); }
template <int... Is> DI void k_reads0(W4C& c, iseq<Is...>) { (k_read<Is, 0>(c), ...); }
template <int... Is> DI void tr_reads1(W4C& c, iseq<Is...>) { (tr_read<Is, 1>(c), ...); }
template <int... Is> DI void drain_exp(W4C& c, iseq<Is...>) { (op_exp<1, 1, Is>(c), ...); }
template <int... Is> DI void drain_cvt0(W4C& c, iseq<Is...>) { (op_cvt<1, 0, Is>(c), ...); }
template <int... Is> DI void drain_cvt1(W4C& c, iseq<Is...>) { (op_cvt<1, 1, Is>(c), ...); }
template <int... Ms> DI void pv_all(W4C& c, iseq<Ms...>) { (pv_mfma<Ms, false>(c), ...); }
template <int R0, int R1> DI u32x4 acc_pack8(float f) {
  float o[8];
  asm volatile("v_accvgpr_read_b32 %0, a[%c8]\n\tv_accvgpr_read_b32 %1, a[%c9]\n\tv_accvgpr_read_b32 %2, a[%c10]\n\tv_accvgpr_read_b32 %3, a[%c11]\n\t"
               "v_accvgpr_read_b32 %4, a[%c12]\n\tv_accvgpr_read_b32 %5, a[%c13]\n\tv_accvgpr_read_b32 %6, a[%c14]\n\tv_accvgpr_read_b32 %7, a[%c15]"
               : "=v"(o[0]), "=v"(o[1]), "=v"(o[2]), "=v"(o[3]), "=v"(o[4]), "=v"(o[5]), "=v"(o[6]), "=v"(o[7])
               : "i"(R0), "i"(R0 + 1), "i"(R0 + 2), "i"(R0 + 3), "i"(R1), "i"(R1 + 1), "i"(R1 + 2), "i"(R1 + 3));
  u32x4 w = {cvtpk(o[0] * f, o[1] * f), cvtpk(o[2] * f, o[3] * f), cvtpk(o[4] * f, o[5] * f), cvtpk(o[6] * f, o[7] * f)};
  return w;
}
template <int I> DI void q_conv1(float C) {
  constexpr int j = I >> 2, s = I & 3, aq = AQ_(j, s);
  const u32x4 w = acc_pack8<AO_(2 * s, j), AO_(2 * s + 1, j)>(C);
  asm volatile("v_accvgpr_write_b32 a[%c4], %0\n\tv_accvgpr_write_b32 a[%c5], %1\n\tv_accvgpr_write_b32 a[%c6], %2\n\tv_accvgpr_write_b32 a[%c7], %3"
               :: "v"(w[0]), "v"(w[1]), "v"(w[2]), "v"(w[3]), "i"(aq), "i"(aq + 1), "i"(aq + 2), "i"(aq + 3) : W4_ACLOB_HI);
}
template <int... Is> DI void q_conv(float C, iseq<Is...>) { (q_conv1<Is>(C), ...); }
template <int I> DI void o_store1(__amdgpu_buffer_rsrc_t srd, unsigned vo, const float (&il)[4]) {
  constexpr int j = I >> 2, u = I & 3;
  const u32x4 w = acc_pack8<AO_(2 * u, j), AO_(2 * u + 1, j)>(il[j]);
  __builtin_amdgcn_raw_buffer_store_b128(w, srd, vo + j * (128u * 1024u) + u * 1024, 0, WT);
}
template <int G, int I> DI void fin_step(W4C& c, __amdgpu_buffer_rsrc_t srd, unsigned vo, const float (&il)[4]) {
  if constexpr (G < 4) { pv_mfma<32 * (I >> 3) + 4 * (2 * G + ((I >> 2) & 1)) + (I & 3), false>(c); GAP(); }
  if constexpr (G > 0 && (I & 3) == 3) { o_store1<4 * (I >> 2) + (G - 1)>(srd, vo, il); GAP(); }
}
template <int G, int... Is> DI void fin_group(W4C& c, __amdgpu_buffer_rsrc_t srd, unsigned vo, const float (&il)[4], iseq<Is...>) { (fin_step<G, Is>(c, srd, vo, il), ...); }

using QCfg = GCfg<16, 4, 64, 8, false, 1, 6, 2, 1, 1, true>;
constexpr int SHM_ATTN = 0x10000 + 6 * 16384;

__global__ __launch_bounds__(256, 1) void k_attn(const u16* __restrict__ H1f, const u16* __restrict__ Wq, const u16* __restrict__ Kc, const u16* __restrict__ Vc, u16* __restrict__ Of,
                                                        const u16* __restrict__ Wo, const u16* __restrict__ Wg, const u16* __restrict__ Wu, const u16* __restrict__ Wd, const u16* __restrict__ Wm) {

  extern __shared__ __attribute__((aligned(16))) char lds[];
  const unsigned tid = threadIdx.x, wid = rfl(tid >> 6);
  const int bid = blockIdx.x, xcd = bid & 7, j = bid >> 3;
  const int head = 4 * xcd + (j & 3), y = j >> 2, kvh = head >> 3;
  const unsigned ldsb = (unsigned)(size_t)lds;
  {
    const unsigned lane = tid & 63u;
    GState<QCfg> g;
    const int mb0 = y * 16 + wid * 4;
    constexpr unsigned mbstride = 1024u * 32u;
    g.srdA = mk_srd((const char*)H1f + (size_t)mb0 * mbstride, 4 * mbstride);
#pragma unroll
    for (int m = 0; m < 4; ++m) g.voffA[m] = lane * 16u + m * mbstride;
    constexpr unsigned ldb = 4096u * 2u;
    g.srdB = mk_srd((const char*)Wq + (size_t)head * 256, 1024u * ldb - (unsigned)head * 256u);
    g_setup<QCfg, true>(g, ldsb + QGEMM_LDS, lane, wid, ldb);
    { const unsigned gw = blockIdx.x * 4u + wid;
      g.tch[0] = (unsigned long long)(size_t)((const char*)Wq + (size_t)gw * 4096); g.tch[1] = g.tch[0] + (4096u * 1024u);
      g.tch[2] = (unsigned long long)(size_t)((gw < 512u ? (const char*)Kc : (const char*)Vc) + (size_t)(gw & 511u) * 4096);
      g.tch_off = lane * 64u; }
    g.srdK = mk_srd((const char*)Kc + (size_t)kvh * (2048u * 256u), 2048u * 256u);
    g.srdV = mk_srd((const char*)Vc + (size_t)kvh * (2048u * 256u), 2048u * 256u);
    g.kvm0 = rfl(ldsb + KRING + 4096u * wid); g.kvso = rfl(KRS * 16u * wid);
#pragma unroll
    for (int q = 0; q < 4; ++q) g.vk[q] = KRS * (lane >> 4) + 16u * ((lane & 15u) ^ (4u * q + (lane >> 4)));
    g.vv = KRS * ((lane >> 1) & 7u) + 32u * (lane >> 4) + 16u * (lane & 1u);
    gemm_run<QCfg>(g, 16);
  }

  unsigned lane; asm volatile("v_mbcnt_lo_u32_b32 %0, -1, 0\n\tv_mbcnt_hi_u32_b32 %0, -1, %0" : "=v"(lane));
  constexpr int T = 32;
  W4C c;
  { const unsigned r15 = lane & 15u, g4 = lane >> 4;
#pragma unroll
    for (int s = 0; s < 4; ++s) c.vkb[s] = ldsb + KRING + 256u * r15 + 16u * ((4u * s + g4) ^ r15);
    c.vvb = ldsb + VRING + (g4 >> 1) * 2048u + (4u * (g4 & 1u) + (r15 >> 2)) * 32u + 8u * (lane & 3u);
#pragma unroll
    for (int q = 0; q < 4; ++q) {
      c.vksrc[q] = KRS * g4 + 16u * (r15 ^ (4u * q + g4));
      c.vvsrc[q] = KRS * ((lane >> 1) & 7u) + 32u * g4 + 16u * (lane & 1u) + KRS * 8u * (q >> 1) + 128u * (q & 1) + (3072u - 1024u * q);
    } }
  c.m0k = rfl(ldsb + KRING + 4096u * wid); c.m0v = rfl(ldsb + VRING + 4096u * wid);
  c.sofk = rfl(KRS * 16u * wid); c.sofv = rfl(KRS * 16u * wid) - 3072u;
  c.srdK = mk_srd((const char*)Kc + (size_t)kvh * (2048u * 256u), 2048u * 256u);
  c.srdV = mk_srd((const char*)Vc + (size_t)kvh * (2048u * 256u), 2048u * 256u);
  {
    const unsigned gw = blockIdx.x * 4u + wid;
    const char* p;
    const unsigned c2 = gw + 1024u, c2c = c2 < 1792u ? c2 : gw;
    if (lane == 0) p = (const char*)Wo + (size_t)gw * 4096; else if (lane == 1) p = (const char*)Wo + (size_t)c2 * 4096;
    else if (lane == 2) p = (const char*)Wg + (size_t)gw * 4096; else if (lane == 3) p = (const char*)Wg + (size_t)c2c * 4096;
    else if (lane == 4) p = (const char*)Wu + (size_t)gw * 4096; else if (lane == 5) p = (const char*)Wu + (size_t)c2c * 4096;
    else if (lane == 6) p = (const char*)Wd + (size_t)gw * 4096; else if (lane == 7) p = (const char*)Wd + (size_t)c2c * 4096;
    else if (lane < 11) p = (const char*)Wm + (size_t)(gw + 1024u * (lane - 8u)) * 4096;
    else p = (const char*)Wo + (size_t)gw * 4096;
    c.pf_lo = (unsigned)(size_t)p; c.pf_hi = (unsigned)((size_t)p >> 32);
    c.pf_off = lane * 64u; c.pf_m0 = rfl(ldsb + QGEMM_LDS + 256u * wid);
  }
  asm volatile("s_barrier" ::: "memory");
  k_reads0(c, make_iseq<16>{});
  q_conv(0.12751743082f, make_iseq<16>{});
  asm volatile("s_waitcnt lgkmcnt(0)\n\ts_nop 1" ::: "memory");
  c.pend = false; c.ones = (u32x4){0x3f803f80u, 0x3f803f80u, 0x3f803f80u, 0x3f803f80u}; asm volatile("" : "+v"(c.ones));
#pragma unroll
  for (int j = 0; j < 4; ++j) { c.LS[j] = (f32x4){0.f, 0.f, 0.f, 0.f}; asm volatile("" : "+v"(c.LS[j])); }

  phaseA<0, true>(c, 0);  phaseB<0, true, false>(c, 0);
  phaseA<1, false>(c, 1); phaseB<1, false, true>(c, 1);
  for (int t = 2; t < T; t += 2) {
    phaseA<0, false>(c, t);     phaseB<0, false, false>(c, t);
    phaseA<1, false>(c, t + 1); phaseB<1, false, false>(c, t + 1);
  }

  float il[4];
  {
    tr_reads1(c, make_iseq<32>{});
    drain_exp(c, make_iseq<32>{});
    drain_cvt0(c, make_iseq<16>{});
    drain_cvt1(c, make_iseq<16>{});
    asm volatile("s_waitcnt lgkmcnt(0)" : W4_VLO :: "memory"); asm volatile("" : W4_VUP :: "memory");
    GAP();
    v_concat(c);
    asm volatile("s_nop 1" ::: "memory");
    ls_mfma<0, 0>(c); ls_mfma<1, 0>(c); ls_mfma<2, 0>(c); ls_mfma<3, 0>(c); ls_mfma<0, 1>(c); ls_mfma<1, 1>(c); ls_mfma<2, 1>(c); ls_mfma<3, 1>(c);
    unsigned lb; asm volatile("v_mbcnt_lo_u32_b32 %0, -1, 0\n\tv_mbcnt_hi_u32_b32 %0, -1, %0" : "=v"(lb));
    const unsigned vo = ((unsigned)(y * 16 + wid * 4) * 128u + (unsigned)head * 4u) * 1024u + lb * 16u;
    const __amdgpu_buffer_rsrc_t srdO = mk_srd(Of, 16u << 20);
    GAP();
    fin_group<0>(c, srdO, vo, il, make_iseq<16>{});
    asm volatile("" : "+v"(c.LS[0]), "+v"(c.LS[1]), "+v"(c.LS[2]), "+v"(c.LS[3]));
#pragma unroll
    for (int j = 0; j < 4; ++j) il[j] = __builtin_amdgcn_rcpf(c.LS[j][0]);
    GAP();
    fin_group<1>(c, srdO, vo, il, make_iseq<16>{});
    fin_group<2>(c, srdO, vo, il, make_iseq<16>{});
    fin_group<3>(c, srdO, vo, il, make_iseq<16>{});
    asm volatile("s_nop 7\n\ts_nop 1" ::: "memory");
    fin_group<4>(c, srdO, vo, il, make_iseq<16>{});
  }

}
template <int N> DI void touch(const char* p, int stride) {
  unsigned d[N];
#pragma unroll
  for (int i = 0; i < N; ++i) d[i] = *(const unsigned*)(p + (size_t)i * stride);
#pragma unroll
  for (int i = 0; i < N; ++i) asm volatile("" :: "v"(d[i]));
}

template <int NT, int KL> DI void mod_body(const u16* __restrict__ c, const u16* __restrict__ wmod, float* __restrict__ dst, int cb, int kh, float* red  ) {
  const int tid = threadIdx.x, cg = tid & 7, kg = tid >> 3, wv = tid >> 6;
  constexpr int NG = NT / 8, NI = KL / NG;
  u32x4 w[NI]; u16 cv[NI];
#pragma unroll
  for (int i = 0; i < NI; ++i) { const int k = kh * KL + kg + NG * i; w[i] = *(const u32x4*)(wmod + (size_t)k * 6144 + cb * 64 + cg * 8); cv[i] = c[k]; }
  float acc[8] = {0, 0, 0, 0, 0, 0, 0, 0};
#pragma unroll
  for (int i = 0; i < NI; ++i) {
    const float xx = bf2f(cv[i]), s = rbf(xx / (1.f + __expf(-xx)));
#pragma unroll
    for (int jj = 0; jj < 4; ++jj) { acc[2 * jj] += s * __uint_as_float(w[i][jj] << 16); acc[2 * jj + 1] += s * __uint_as_float(w[i][jj] & 0xffff0000u); }
  }
#pragma unroll
  for (int jj = 0; jj < 8; ++jj) { float a = acc[jj]; a += __shfl_xor(a, 8); a += __shfl_xor(a, 16); a += __shfl_xor(a, 32); acc[jj] = a; }
  if ((tid & 63) < 8) {
#pragma unroll
    for (int jj = 0; jj < 8; ++jj) red[wv * 64 + cg * 8 + jj] = acc[jj];
  }
  __syncthreads();
  if (tid < 64) { float s = 0;
#pragma unroll
    for (int g = 0; g < NT / 64; ++g) s += red[g * 64 + tid];
    dst[tid] = s; }
}
__global__ __launch_bounds__(512) void k_pre(const u16* __restrict__ c, const u16* __restrict__ wmod, float* __restrict__ modA, const u16* __restrict__ x, unsigned* __restrict__ flg) {
  __shared__ float red[8 * 64];
  const int b = blockIdx.x;

  unsigned dx = 0;
  if (threadIdx.x == 0) flg[b * 64] = 0u;
  if (threadIdx.x < 256) dx = *(const unsigned*)((const char*)x + (size_t)b * 16384 + threadIdx.x * 64);
  mod_body<512, 128>(c, wmod, modA + (b & 7) * 2048 + (b >> 3) * 64, b >> 3, b & 7, red);
  asm volatile("" :: "v"(dx));

}
DI f32x4 modv(const float* __restrict__ modp, int n) { const f32x4 a = *(const f32x4*)(modp + n), b = *(const f32x4*)(modp + 6144 + n); f32x4 o; for (int e = 0; e < 4; ++e) o[e] = rbf(a[e] + b[e]); return o; }
DI void unpack8(const u32x4& w, float* v) {
#pragma unroll
  for (int j = 0; j < 4; ++j) { v[2 * j] = __uint_as_float(w[j] << 16); v[2 * j + 1] = __uint_as_float(w[j] & 0xffff0000u); }
}
DI u32x4 pack8(const float* o) { return (u32x4){cvtpk(o[0], o[1]), cvtpk(o[2], o[3]), cvtpk(o[4], o[5]), cvtpk(o[6], o[7])}; }

DI void rbf2(float& a, float& b) { const unsigned w = cvtpk(a, b); a = __uint_as_float(w << 16); b = __uint_as_float(w & 0xffff0000u); }
DI void rbf8(float* v) { rbf2(v[0], v[1]); rbf2(v[2], v[3]); rbf2(v[4], v[5]); rbf2(v[6], v[7]); }
DI void modv8(const float* __restrict__ modp, int n, float* o) {
  const f32x4 a0 = *(const f32x4*)(modp + n), b0 = *(const f32x4*)(modp + 6144 + n), a1 = *(const f32x4*)(modp + n + 8), b1 = *(const f32x4*)(modp + 6144 + n + 8);
#pragma unroll
  for (int e = 0; e < 4; ++e) { o[e] = a0[e] + b0[e]; o[4 + e] = a1[e] + b1[e]; }
  rbf8(o);
}
template <int MODE>
__global__ __launch_bounds__(1024) void k_ln(const u16* __restrict__ x, const u16* __restrict__ Pf, const float* __restrict__ modp, u16* __restrict__ xf, u16* __restrict__ hf) {
  __shared__ float red[2][16][8];
  __shared__ __attribute__((aligned(16))) float msum[MODE == 0 ? 2048 : 4];
  const int tid = threadIdx.x;

  const int r = tid & 7, c = tid >> 3, h = c & 1, wid = tid >> 6;
  const int row = blockIdx.x * 8 + r, mb = row >> 5, ml = row & 31;
  const size_t fo = ((size_t)mb * 64 + (c >> 1)) * 512 + (h * 32 + ml) * 8;
  const int n = 16 * (c >> 1) + 4 * h;
  float v[8], sh[8], sc[8];
  if (MODE == 0) {
    const u32x4 xw = *(const u32x4*)(x + (size_t)row * 1024 + 8 * c);
    { f32x2 a = {0.f, 0.f};
#pragma unroll
      for (int ks = 0; ks < 8; ++ks) a += *(const f32x2*)(modp + ks * 2048 + 2 * tid);
      float a0 = a[0], a1 = a[1]; rbf2(a0, a1); *(f32x2*)(msum + 2 * tid) = (f32x2){a0, a1}; }
    const unsigned s0 = h ? xw[0] : xw[2], s1 = h ? xw[1] : xw[3];
    const unsigned g0 = __shfl_xor(s0, 8), g1 = __shfl_xor(s1, 8);
    const u32x4 f = h ? (u32x4){g0, g1, xw[2], xw[3]} : (u32x4){xw[0], xw[1], g0, g1};
    __builtin_amdgcn_raw_buffer_store_b128(f, mk_srd(xf, 4u << 20), (unsigned)fo * 2u, 0, WT);
    unpack8(f, v);
  } else {
    const u32x4 xw = *(const u32x4*)(xf + fo), p0 = *(const u32x4*)(Pf + fo), p1 = *(const u32x4*)(Pf + fo + (size_t)2048 * 1024);
    float ga[8], a[8], b[8];
    modv8(modp, 3072 + n, sh); modv8(modp, 4096 + n, sc); modv8(modp, 2048 + n, ga);
    unpack8(xw, v); unpack8(p0, a); unpack8(p1, b);
#pragma unroll
    for (int j = 0; j < 8; ++j) a[j] += b[j];
    rbf8(a);
#pragma unroll
    for (int j = 0; j < 8; ++j) a[j] *= ga[j];
    rbf8(a);
#pragma unroll
    for (int j = 0; j < 8; ++j) v[j] += a[j];
    const u32x4 w = pack8(v);
    __builtin_amdgcn_raw_buffer_store_b128(w, mk_srd(xf, 4u << 20), (unsigned)fo * 2u, 0, WT);
    unpack8(w, v);
  }
  float s = ((v[0] + v[1]) + (v[2] + v[3])) + ((v[4] + v[5]) + (v[6] + v[7]));
  s += __shfl_xor(s, 8); s += __shfl_xor(s, 16); s += __shfl_xor(s, 32);
  if ((tid & 63) < 8) red[0][wid][r] = s;
  __syncthreads();
  if (MODE == 0) {
    const f32x4 a0 = *(const f32x4*)(msum + n), a1 = *(const f32x4*)(msum + n + 8), b0 = *(const f32x4*)(msum + 1024 + n), b1 = *(const f32x4*)(msum + 1024 + n + 8);
#pragma unroll
    for (int e = 0; e < 4; ++e) { sh[e] = a0[e]; sh[4 + e] = a1[e]; sc[e] = b0[e]; sc[4 + e] = b1[e]; }
  }
  float mean = 0;
#pragma unroll
  for (int w = 0; w < 16; ++w) mean += red[0][w][r];
  mean *= (1.f / 1024.f);
  float qv = 0;
#pragma unroll
  for (int j = 0; j < 8; ++j) { const float d = v[j] - mean; qv += d * d; }
  qv += __shfl_xor(qv, 8); qv += __shfl_xor(qv, 16); qv += __shfl_xor(qv, 32);
  if ((tid & 63) < 8) red[1][wid][r] = qv;
  __syncthreads();
  float var = 0;
#pragma unroll
  for (int w = 0; w < 16; ++w) var += red[1][w][r];
  var = rbf(var * (1.f / 1024.f));
  const float mb16 = rbf(mean), rsd = 1.f / rbf(sqrtf(rbf(var + 1e-5f)));
#pragma unroll
  for (int j = 0; j < 8; ++j) { v[j] -= mb16; sc[j] += 1.f; }
  rbf8(v); rbf8(sc);
#pragma unroll
  for (int j = 0; j < 8; ++j) v[j] *= rsd;
  rbf8(v);
#pragma unroll
  for (int j = 0; j < 8; ++j) v[j] *= sc[j];
  rbf8(v);
#pragma unroll
  for (int j = 0; j < 8; ++j) v[j] += sh[j];
  const unsigned ho = (((unsigned)(row >> 4) * 32u + (unsigned)(c >> 2)) * 1024u + (unsigned)(c & 3) * 256u + (unsigned)(row & 15) * 16u);
  __builtin_amdgcn_raw_buffer_store_b128(pack8(v), mk_srd(hf, 4u << 20), ho, 0, WT);

}
template <int KORD> using SCfgT = GCfg<16, 4, 32, 8, false, KORD, 4, 4, 1, 1>;
using SCfg = SCfgT<1>;
template <int K, bool FIN, int NT0>
__global__ __launch_bounds__(256, 2) void k_gemm_sk(const u16* __restrict__ Af, const u16* __restrict__ B, u16* __restrict__ Pf,
                                                    const u16* __restrict__ cvec, const u16* __restrict__ wmod, float* __restrict__ modp,
                                                    const u16* __restrict__ x1f, u16* __restrict__ out, unsigned* __restrict__ flg) {
  using C = SCfgT<K == 4096 ? 0 : 1>;
  extern __shared__ __attribute__((aligned(16))) char lds[];

  if (K == 4096 && blockIdx.x >= 256) { const int b = blockIdx.x - 256; mod_body<256, 512>(cvec, wmod, modp + (b & 1) * 6144 + (32 + (b >> 1)) * 64, 32 + (b >> 1), b & 1, (float*)lds); return; }
  constexpr int N = 1024;
  const unsigned tid = threadIdx.x, lane = tid & 63u, wid = rfl(tid >> 6);
  const int bid = blockIdx.x, xcd = bid & 7, j = bid >> 3;
  const int kh = xcd & 1, mg = xcd >> 1;
  const int mt = mg * 4 + (j & 3), nt = j >> 2;
  const int k0 = kh ? NT0 * 64 : 0, klen = kh ? K - NT0 * 64 : NT0 * 64;
  GState<C> g;
  const int mb0 = mt * 4 + wid;
  constexpr unsigned mbstride = (unsigned)K * 32u;
  g.srdA = mk_srd((const char*)Af + (size_t)(2 * mb0) * mbstride + (size_t)k0 * 32, mbstride + (unsigned)klen * 32u);
  g.voffA[0] = lane * 16u; g.voffA[1] = lane * 16u + mbstride;
  constexpr unsigned ldb = (unsigned)N * 2u;
  g.srdB = mk_srd((const char*)B + (size_t)k0 * ldb + (size_t)nt * 256, (unsigned)klen * ldb - (unsigned)nt * 256u);
  g_setup<C>(g, (unsigned)(size_t)lds, lane, wid, ldb);
  gemm_run<C>(g, klen / 64);
  const unsigned h = (lane >> 4) & 1u, hi = lane >> 5;
  const unsigned fo = (((unsigned)mb0 * 64u + (unsigned)nt * 8u + hi) * 512u + (h * 32u + (lane & 15u)) * 8u) * 2u;
  float acc[4][2][8];
#pragma unroll
  for (int i = 0; i < 4; ++i)
#pragma unroll
    for (int m = 0; m < 2; ++m)
#pragma unroll
      for (int e = 0; e < 4; ++e) {
        auto sw = __builtin_amdgcn_permlane32_swap(__float_as_uint(g.acc[2 * i][m][e]), __float_as_uint(g.acc[2 * i + 1][m][e]), false, false);
        acc[i][m][e] = __uint_as_float(sw[0]); acc[i][m][4 + e] = __uint_as_float(sw[1]);
      }
  if constexpr (!FIN) {
    const __amdgpu_buffer_rsrc_t srdP = mk_srd(Pf, 8u << 20);
#pragma unroll
    for (int i = 0; i < 4; ++i)
#pragma unroll
      for (int m = 0; m < 2; ++m) __builtin_amdgcn_raw_buffer_store_b128(pack8(acc[i][m]), srdP, (unsigned)kh * (4u << 20) + fo + i * 2048 + m * 256, 0, WT);
  } else {
    const __amdgpu_buffer_rsrc_t srdP = mk_srd(Pf, 4u << 20);
    unsigned* flag = flg + (128 + mt * 8 + nt) * 64;
    if (kh == 0) {
#pragma unroll
      for (int i = 0; i < 4; ++i)
#pragma unroll
        for (int m = 0; m < 2; ++m) __builtin_amdgcn_raw_buffer_store_b128(pack8(acc[i][m]), srdP, fo + i * 2048 + m * 256, 0, 16);
      asm volatile("s_waitcnt vmcnt(0)" ::: "memory");
      __syncthreads();
      if (tid == 0) __hip_atomic_store(flag, 1u, __ATOMIC_RELAXED, __HIP_MEMORY_SCOPE_AGENT);
    } else {
      u32x4 xw[4][2]; float gb[4][8];
#pragma unroll
      for (int i = 0; i < 4; ++i) { xw[i][0] = *(const u32x4*)((const char*)x1f + fo + i * 2048); xw[i][1] = *(const u32x4*)((const char*)x1f + fo + i * 2048 + 256);
        modv8(modp, 5120 + nt * 128 + 16 * (2 * i + hi) + 4 * h, gb[i]); }
      if (wid == 0) {
        for (unsigned spins = 0; spins < (1u << 22); ++spins) {
          if ((unsigned)__builtin_amdgcn_readfirstlane(__hip_atomic_load(flag, __ATOMIC_RELAXED, __HIP_MEMORY_SCOPE_AGENT)) != 0u) break;
          __builtin_amdgcn_s_sleep(1);
        }
      }
      asm volatile("" ::: "memory"); __builtin_amdgcn_s_barrier(); asm volatile("" ::: "memory");
      u32x4 pw[4][2];
#pragma unroll
      for (int i = 0; i < 4; ++i)
#pragma unroll
        for (int m = 0; m < 2; ++m) pw[i][m] = __builtin_amdgcn_raw_buffer_load_b128(srdP, fo + i * 2048 + m * 256, 0, 16);
      const unsigned oo = (((unsigned)mb0 * 32u + (lane & 15u)) * 1024u + (unsigned)nt * 128u + 16u * hi + 8u * h) * 2u;
      const __amdgpu_buffer_rsrc_t srdOut = mk_srd(out, 4u << 20);
#pragma unroll
      for (int i = 0; i < 4; ++i)
#pragma unroll
        for (int m = 0; m < 2; ++m) {
          float v[8], a[8];
          unpack8(xw[i][m], v); unpack8(pw[i][m], a);
#pragma unroll
          for (int jj = 0; jj < 8; ++jj) a[jj] += acc[i][m][jj];
          rbf8(a);
#pragma unroll
          for (int jj = 0; jj < 8; ++jj) a[jj] *= gb[i][jj];
          rbf8(a);
#pragma unroll
          for (int jj = 0; jj < 8; ++jj) v[jj] += a[jj];
          const u32x4 w = pack8(v);
          const unsigned s0 = h ? w[0] : w[2], s1 = h ? w[1] : w[3];
          const unsigned g0 = __shfl_xor(s0, 16), g1 = __shfl_xor(s1, 16);
          const u32x4 f = h ? (u32x4){g0, g1, w[2], w[3]} : (u32x4){w[0], w[1], g0, g1};
          __builtin_amdgcn_raw_buffer_store_b128(f, srdOut, oo + 64u * i + 32768u * m, 0, WT);
        }
    }
  }

}
#define SK2_NT0 28
DI float silu_mul(float g, float u) { return g * __builtin_amdgcn_rcpf(1.f + __builtin_amdgcn_exp2f(g * -1.4426950408889634f)) * u; }
#define U_NS 3
#define U_BP 3
using UCfg = GCfg<16, 8, 32, 14, true, 1, U_NS, 2, 1, U_BP>;
__global__ __launch_bounds__(512, 2) void k_gemm_gu(const u16* __restrict__ Af, const u16* __restrict__ Wg, const u16* __restrict__ Wu, u16* __restrict__ Act) {
  using C = UCfg;
  extern __shared__ __attribute__((aligned(16))) char lds[];
  constexpr int K = 1024, F = 3584;

  const unsigned tid = threadIdx.x, wid = rfl(tid >> 6);
  const int bid = blockIdx.x, xcd = bid & 7, j = bid >> 3;
  const int mt = (xcd & 1) * 4 + (j & 3), nt = (xcd >> 1) * 8 + (j >> 2);
  GState<C> g;
  const int mb0 = mt * 8 + wid;
  {
    const unsigned lane = tid & 63u;
    constexpr unsigned mbstride = (unsigned)K * 32u;
    g.srdA = mk_srd((const char*)Af + (size_t)(2 * mb0) * mbstride, 2 * mbstride);
    g.voffA[0] = lane * 16u; g.voffA[1] = lane * 16u + mbstride;
    constexpr unsigned ldb = F * 2u;
    const bool up = wid >= 4;
    g.srdB = mk_srd((const char*)(up ? Wu : Wg) + (size_t)nt * 224, (unsigned)K * ldb - (unsigned)nt * 224u);
    g_setup<C>(g, (unsigned)(size_t)lds, lane, wid, ldb);
  }
  gemm_run<C>(g, K / 64);
  unsigned lane; asm volatile("v_mbcnt_lo_u32_b32 %0, -1, 0\n\tv_mbcnt_hi_u32_b32 %0, -1, %0" : "=v"(lane));
  const __amdgpu_buffer_rsrc_t srdO = mk_srd(Act, 2048u * F * 2u);
  const unsigned vo = ((unsigned)mb0 * 2u + (lane >> 5)) * (unsigned)((F >> 5) * 1024) + (lane & 31u) * 16u;
#pragma unroll
  for (int n = 0; n < 7; ++n) {
    unsigned q[2][2];
#pragma unroll
    for (int m = 0; m < 2; ++m) {
      float o[4];
#pragma unroll
      for (int e = 0; e < 4; ++e) o[e] = silu_mul(g.acc[2 * n][m][e], g.acc[2 * n + 1][m][e]);
      q[m][0] = cvtpk(o[0], o[1]); q[m][1] = cvtpk(o[2], o[3]);
    }
    auto s0 = __builtin_amdgcn_permlane32_swap(q[0][0], q[1][0], false, false); auto s1 = __builtin_amdgcn_permlane32_swap(q[0][1], q[1][1], false, false);
    const unsigned c16 = (unsigned)(nt * 7 + n);
    __builtin_amdgcn_raw_buffer_store_b128((u32x4){s0[0], s1[0], s0[1], s1[1]}, srdO, vo + (c16 >> 1) * 1024u + (c16 & 1u) * 512u, 0, WT);
  }

}

extern "C" void kernel_launch(void* const* d_in, const int* in_sizes, int n_in, void* d_out, int out_size, void* d_ws, size_t ws_size, hipStream_t stream) {
  const u16* x = (const u16*)d_in[0]; const u16* kc = (const u16*)d_in[1]; const u16* vc = (const u16*)d_in[2];
  const u16* wq = (const u16*)d_in[3]; const u16* wo = (const u16*)d_in[4];
  const u16* wg = (const u16*)d_in[6]; const u16* wu = (const u16*)d_in[7]; const u16* wd = (const u16*)d_in[8];
  const u16* c = (const u16*)d_in[9]; const u16* wmod = (const u16*)d_in[10];
  u16* out = (u16*)d_out;
  char* ws = (char*)d_ws;
  float* mod = (float*)ws;
  u16* hf = (u16*)(ws + (1 << 20));
  u16* x1f = (u16*)(ws + (8 << 20));
  u16* of = (u16*)(ws + (16 << 20));
  u16* P = (u16*)(ws + (32 << 20));
  u16* act = (u16*)(ws + (64 << 20));
  (void)hipFuncSetAttribute((const void*)k_attn, hipFuncAttributeMaxDynamicSharedMemorySize, SHM_ATTN);
  (void)hipFuncSetAttribute((const void*)k_gemm_sk<4096, false, 32>, hipFuncAttributeMaxDynamicSharedMemorySize, SCfg::LDSB);
  (void)hipFuncSetAttribute((const void*)k_gemm_sk<3584, true, SK2_NT0>, hipFuncAttributeMaxDynamicSharedMemorySize, SCfg::LDSB);
  (void)hipFuncSetAttribute((const void*)k_gemm_gu, hipFuncAttributeMaxDynamicSharedMemorySize, UCfg::LDSB);
  float* modA = (float*)(ws + (256 << 10));
  unsigned* flg = (unsigned*)(ws + (512 << 10));
  u16* P2 = (u16*)(ws + (48 << 20));
  k_pre<<<256, 512, 0, stream>>>(c, wmod, modA, x, flg);
  k_ln<0><<<256, 1024, 0, stream>>>(x, nullptr, modA, x1f, hf);
  k_attn<<<256, 256, SHM_ATTN, stream>>>(hf, wq, kc, vc, of, wo, wg, wu, wd, wmod);
  k_gemm_sk<4096, false, 32><<<256 + 128, 256, SCfg::LDSB, stream>>>(of, wo, P, c, wmod, mod, nullptr, nullptr, nullptr);
  k_ln<1><<<256, 1024, 0, stream>>>(x, P, mod, x1f, hf);
  k_gemm_gu<<<256, 512, UCfg::LDSB, stream>>>(hf, wg, wu, act);
  k_gemm_sk<3584, true, SK2_NT0><<<256, 256, SCfg::LDSB, stream>>>(act, wd, P2, nullptr, nullptr, mod, x1f, out, flg);
}
```
